```python
import torch

def _softmax(x, axis=-1):
    return torch.softmax(x, dim=axis)
def _gelu(x):
    return torch.nn.functional.gelu(x, approximate="tanh")
def _silu(x):
    return torch.nn.functional.silu(x)
def _sigmoid(x):
    return torch.sigmoid(x)
def _layernorm(x, axis=-1, eps=1e-5):
    m = x.mean(dim=axis, keepdim=True)
    v = x.var(dim=axis, keepdim=True, unbiased=False)
    return (x - m) / torch.sqrt(v + eps)
def _scatter_add(tgt, idx, src, axis=0):
    return tgt.clone().index_add_(axis, idx, src)
def _segment_sum(src, seg, n):
    out = torch.zeros((n,) + src.shape[1:], dtype=src.dtype)
    return out.index_add_(0, seg, src)
def _pad(x, pw):
    flat = tuple(v for pair in reversed(pw) for v in pair)
    return torch.nn.functional.pad(x, flat)

kernel_name = 'gated_mix__softmax_full_mha__mixer_token_mlp__prompt'

def setup_inputs():

    _w = (lambda a: (lambda t: t.to(torch.bfloat16) if t.is_floating_point() else t)(torch.as_tensor(a)))
    def _A_setup():
        B, S, D = 1, 2048, 512
        return {
            "x": _w((lambda *s: torch.randn(*s, dtype=torch.bfloat16) * 0.05)(B, S, D)),
            "wqkv": _w((lambda *s: torch.randn(*s, dtype=torch.bfloat16) * 0.8)(D, 3 * D)),
            "wo": _w((lambda *s: torch.randn(*s, dtype=torch.bfloat16) * 0.05)(D, D)),
        }
    _A_in = _A_setup()

    def _B_setup():
        B, S, D, F = 1, 2048, 512, 1376
        return {
            "x": _w((lambda *s: torch.randn(*s, dtype=torch.bfloat16) * 0.05)(B, S, D)),
            "w1": _w((lambda *s: torch.randn(*s, dtype=torch.bfloat16) * 0.8)(S, F) * (S ** -0.5)),
            "b1": _w((lambda *s: torch.randn(*s, dtype=torch.bfloat16) * 12.8)(F) * 0.02),
            "w2": _w((lambda *s: torch.randn(*s, dtype=torch.bfloat16) * 0.05)(F, S) * (F ** -0.5)),
            "b2": _w((lambda *s: torch.randn(*s, dtype=torch.bfloat16) * 0.8)(S) * 0.02),
        }
    _B_in = _B_setup()

    return {
        "A_x": _A_in["x"],
        "A_wqkv": _A_in["wqkv"],
        "A_wo": _A_in["wo"],
        "B_x": _B_in["x"],
        "B_w1": _B_in["w1"],
        "B_b1": _B_in["b1"],
        "B_w2": _B_in["w2"],
        "B_b2": _B_in["b2"],
        "gate_w": _w((lambda *s: torch.randn(*s, dtype=torch.bfloat16) * 0.05)(512) * 16.0),
    }

def reference(A_x, A_wqkv, A_wo, B_x, B_w1, B_b1, B_w2, B_b2, gate_w):
    def _A_ref(x, wqkv, wo):
        B, S, D = x.shape
        H = 32; Dh = D // H
        qkv = (lambda x, *d: x.permute(*d))((x @ wqkv).reshape(B, S, 3, H, Dh), 2, 0, 3, 1, 4)
        q, k, v = qkv[0], qkv[1], qkv[2]
        scale = Dh ** -0.5
        scores = torch.einsum("bhqd,bhkd->bhqk", q, k) * scale
        p = _softmax(scores, axis=-1)
        o = torch.einsum("bhqk,bhkd->bhqd", p, v)
        return (lambda x, *d: x.permute(*d))(o, 0, 2, 1, 3).reshape(B, S, D) @ wo

    def _B_ref(x, w1, b1, w2, b2):
        B, S, D = x.shape
        h = _layernorm(x, axis=-1)
        u = torch.einsum("bsd,sf->bdf", h, w1) + b1
        u = _gelu(u)
        y = torch.einsum("bdf,fs->bds", u, w2) + b2
        return (lambda x, *d: x.permute(*d))(y, 0, 2, 1)

    x = A_x
    g = _sigmoid((x * gate_w).sum(-1, keepdims=True))
    a = _A_ref(x, A_wqkv, A_wo)
    b = _B_ref(x, B_w1, B_b1, B_w2, B_b2)
    return g * a + (1 - g) * b
```

```cpp
#include <hip/hip_runtime.h>
#include <hip/hip_bf16.h>
typedef __hip_bfloat16 bf16;
__device__ inline float b2f(bf16 x){return __bfloat162float(x);}
constexpr int S=2048,D=512,F=1376,H=32,DH=16;

__global__ void k_qkv(const bf16* x,const bf16* w,float* qkv){
  int n=blockIdx.x*blockDim.x+threadIdx.x; int s=blockIdx.y;
  float a=0; for(int k=0;k<D;k++) a+=b2f(x[s*D+k])*b2f(w[k*1536+n]);
  qkv[s*1536+n]=a;
}
__global__ void k_ln(const bf16* x,const bf16* gw,float* h,float* g){
  int s=blockIdx.x; __shared__ float r[3][64];
  int t=threadIdx.x; float s1=0,s3=0;
  for(int k=t;k<D;k+=64){float v=b2f(x[s*D+k]); s1+=v; s3+=v*b2f(gw[k]);}
  for(int o=32;o;o>>=1){s1+=__shfl_xor(s1,o); s3+=__shfl_xor(s3,o);}
  float m=s1/D; float s2=0;
  for(int k=t;k<D;k+=64){float v=b2f(x[s*D+k])-m; s2+=v*v;}
  for(int o=32;o;o>>=1){s2+=__shfl_xor(s2,o);}
  float rs=rsqrtf(s2/D+1e-5f);
  for(int k=t;k<D;k+=64) h[s*D+k]=(b2f(x[s*D+k])-m)*rs;
  if(t==0) g[s]=1.f/(1.f+__expf(-s3));
}
__global__ void k_attn(const float* qkv,float* o){
  int s=blockIdx.x*blockDim.x+threadIdx.x; int h=blockIdx.y;
  float q[DH]; for(int d=0;d<DH;d++) q[d]=qkv[s*1536+h*DH+d]*0.25f;
  float m=-1e30f,l=0,acc[DH]; for(int d=0;d<DH;d++) acc[d]=0;
  for(int k=0;k<S;k++){
    float sc=0; for(int d=0;d<DH;d++) sc+=q[d]*qkv[k*1536+512+h*DH+d];
    float mn=fmaxf(m,sc); float f=__expf(m-mn); float p=__expf(sc-mn);
    l=l*f+p; for(int d=0;d<DH;d++) acc[d]=acc[d]*f+p*qkv[k*1536+1024+h*DH+d];
    m=mn;
  }
  for(int d=0;d<DH;d++) o[s*D+h*DH+d]=acc[d]/l;
}
__global__ void k_u(const float* h,const bf16* w1,const bf16* b1,float* u){
  int f=blockIdx.x*blockDim.x+threadIdx.x; int d=blockIdx.y; if(f>=F) return;
  float a=0; for(int s=0;s<S;s++) a+=h[s*D+d]*b2f(w1[s*F+f]);
  a+=b2f(b1[f]);
  float t=tanhf(0.7978845608f*(a+0.044715f*a*a*a));
  u[d*F+f]=0.5f*a*(1.f+t);
}
__global__ void k_out(const float* o,const bf16* wo,const float* u,const bf16* w2,const bf16* b2,const float* g,bf16* out){
  int s=blockIdx.x*blockDim.x+threadIdx.x; int d=blockIdx.y;
  float a=0; for(int c=0;c<D;c++) a+=o[s*D+c]*b2f(wo[c*D+d]);
  float y=0; for(int f=0;f<F;f++) y+=u[d*F+f]*b2f(w2[f*S+s]);
  y+=b2f(b2[s]);
  out[s*D+d]=__float2bfloat16(g[s]*a+(1.f-g[s])*y);
}
extern "C" void kernel_launch(void* const* d_in, const int* in_sizes, int n_in, void* d_out, int out_size, void* d_ws, size_t ws_size, hipStream_t stream) {
  const bf16* x=(const bf16*)d_in[0]; const bf16* wqkv=(const bf16*)d_in[1]; const bf16* wo=(const bf16*)d_in[2];
  const bf16* w1=(const bf16*)d_in[4]; const bf16* b1=(const bf16*)d_in[5]; const bf16* w2=(const bf16*)d_in[6]; const bf16* b2=(const bf16*)d_in[7]; const bf16* gw=(const bf16*)d_in[8];
  float* ws=(float*)d_ws; float* qkv=ws; float* h=qkv+S*1536; float* g=h+S*D; float* o=g+S; float* u=o+S*D;
  k_qkv<<<dim3(6,S),256,0,stream>>>(x,wqkv,qkv);
  k_ln<<<S,64,0,stream>>>(x,gw,h,g);
  k_attn<<<dim3(S/64,H),64,0,stream>>>(qkv,o);
  k_u<<<dim3(6,D),256,0,stream>>>(h,w1,b1,u);
  k_out<<<dim3(S/256,D),256,0,stream>>>(o,wo,u,w2,b2,g,(bf16*)d_out);
}
```

```cpp
#include <hip/hip_runtime.h>
#include <hip/hip_bf16.h>
#include <stdint.h>

typedef __attribute__((ext_vector_type(8))) short bf16x8;
typedef __attribute__((ext_vector_type(4))) short s16x4;
typedef __attribute__((ext_vector_type(16))) float f32x16;
typedef __attribute__((ext_vector_type(4))) float f32x4;
typedef __attribute__((ext_vector_type(4))) unsigned u32x4;
typedef __attribute__((ext_vector_type(2))) unsigned u32x2;
#define LAS __attribute__((address_space(3)))
#define GAS __attribute__((address_space(1)))
#define DI __device__ __forceinline__
#define MFMA32(a, b, c) __builtin_amdgcn_mfma_f32_32x32x16_bf16((a), (b), (c), 0, 0, 0)

constexpr int S_ = 2048, D_ = 512, F_ = 1376;
constexpr size_t WS_CNT = 0;
constexpr size_t WS_G = 8192;
constexpr size_t WS_KN = WS_G + 8192;
constexpr size_t WS_Q = 65536;
constexpr size_t WS_K = WS_Q + (2u << 20);
constexpr size_t WS_V = WS_K + (2u << 20);
constexpr size_t WS_H = WS_V + (2u << 20);
constexpr size_t WS_U = WS_H + (2u << 20);
constexpr size_t WS_O = WS_U + (2u << 20);
constexpr size_t WS_B = WS_O + (2u << 20);

#define ST_DECL
#define ST(i)
#define ST_OUT(k)
DI unsigned cvtpk(float lo, float hi) {
    unsigned r;
    asm("v_cvt_pk_bf16_f32 %0, %1, %2" : "=v"(r) : "v"(lo), "v"(hi));
    return r;
}
DI float bflo(unsigned u) { return __uint_as_float(u << 16); }
DI float bfhi(unsigned u) { return __uint_as_float(u & 0xffff0000u); }
DI s16x4 trread(LAS const char* p) {
    typedef short v4i16_t __attribute__((ext_vector_type(4)));
    return __builtin_bit_cast(s16x4, __builtin_amdgcn_ds_read_tr16_b64_v4i16((LAS v4i16_t*)p));
}
DI void glds(unsigned lds, unsigned voff, const void* sbase) {
    asm volatile("s_mov_b32 m0, %0\n\ts_nop 0\n\tglobal_load_lds_dwordx4 %1, %2" ::"s"(lds), "v"(voff), "s"(sbase) : "memory");
}
template <int CTRL> DI float dpp(float v) {
    return __int_as_float(__builtin_amdgcn_update_dpp(0, __float_as_int(v), CTRL, 0xf, 0xf, true));
}
DI float rl(float v, int l) { return __int_as_float(__builtin_amdgcn_readlane(__float_as_int(v), l)); }
DI float wave_sum(float v) {
    v += dpp<0xB1>(v); v += dpp<0x4E>(v); v += dpp<0x141>(v); v += dpp<0x140>(v);
    return (rl(v, 0) + rl(v, 16)) + (rl(v, 32) + rl(v, 48));
}
DI float wave_max(float v) {
    v = fmaxf(v, dpp<0xB1>(v)); v = fmaxf(v, dpp<0x4E>(v)); v = fmaxf(v, dpp<0x141>(v)); v = fmaxf(v, dpp<0x140>(v));
    return fmaxf(fmaxf(rl(v, 0), rl(v, 16)), fmaxf(rl(v, 32), rl(v, 48)));
}
DI float swap32_add(float v) {
    auto rr = __builtin_amdgcn_permlane32_swap(__float_as_uint(v), __float_as_uint(v), false, false);
    return __uint_as_float(rr[0]) + __uint_as_float(rr[1]);
}
DI float swap32_max(float v) {
    auto rr = __builtin_amdgcn_permlane32_swap(__float_as_uint(v), __float_as_uint(v), false, false);
    return fmaxf(__uint_as_float(rr[0]), __uint_as_float(rr[1]));
}

constexpr int K1_STAGE = 28672, K1_LN = 5 * K1_STAGE, K1_LDS = K1_LN + 8192;

template <int NT>
DI void k1_body(const char* x, const char* wqkv, const char* gw, char* ws, char* lds, int m, int p) {
    const int tid = threadIdx.x, lane = tid & 63, r = lane & 31, h2 = lane >> 5;
    const int w = __builtin_amdgcn_readfirstlane(tid >> 6), mrow = w & 3, kh = w >> 2;
    const unsigned lds0 = (unsigned)(uintptr_t)lds;
    ST_DECL; ST(0);
    u32x4 xr, gr;
    {
        const char* xrow = x + (size_t)(128 * m + 8 * p + w) * 1024;
        asm volatile("global_load_dwordx4 %0, %1, %2" : "=v"(xr) : "v"(lane * 16), "s"(xrow) : "memory");
        asm volatile("global_load_dwordx4 %0, %1, %2" : "=v"(gr) : "v"(lane * 16), "s"(gw) : "memory");
    }
    unsigned vx[2], vw[2];
#pragma unroll
    for (int ii = 0; ii < 2; ++ii) {
        const int row = 16 * w + 8 * ii + (lane >> 3);
        vx[ii] = row * 1024 + (((lane & 7) ^ ((row >> 1) & 7)) << 4);
        const int o = 16 * lane + 1024 * ii;
        vw[ii] = (8 * w + o / 192) * 3072 + (o % 192);
    }
    const char* xb = x + (size_t)m * 131072;
    const char* wb = wqkv + p * 192;
    const int rot = (p + 2 * m) & 7;
    auto issue = [&](int s, int slot) {
        const int kc = (s + rot) & 7;
        const unsigned sb = lds0 + slot * K1_STAGE;
        const char* xs = xb + kc * 128;
        const char* wsrc = wb + kc * 196608;
        glds(sb + w * 2048, vx[0], xs);
        glds(sb + w * 2048 + 1024, vx[1], xs);
        glds(sb + 16384 + w * 1536, vw[0], wsrc);
        if (lane < 32) glds(sb + 16384 + w * 1536 + 1024, vw[1], wsrc);
    };
#pragma unroll
    for (int s = 0; s < 5; ++s) issue(s, s);
    ST(1);
    asm volatile("s_waitcnt vmcnt(20)" : "+v"(xr), "+v"(gr)::"memory");
    ST(2);
    {
        float v[8], gg[8];
#pragma unroll
        for (int i = 0; i < 4; ++i) { v[2 * i] = bflo(xr[i]); v[2 * i + 1] = bfhi(xr[i]); gg[2 * i] = bflo(gr[i]); gg[2 * i + 1] = bfhi(gr[i]); }
        float s1 = 0.f, s3 = 0.f;
#pragma unroll
        for (int i = 0; i < 8; ++i) { s1 += v[i]; s3 += v[i] * gg[i]; }
        const float mu = wave_sum(s1) * (1.f / 512.f);
        s3 = wave_sum(s3);
        float s2 = 0.f;
#pragma unroll
        for (int i = 0; i < 8; ++i) { v[i] -= mu; s2 += v[i] * v[i]; }
        const float rs = __builtin_amdgcn_rsqf(wave_sum(s2) * (1.f / 512.f) + 1e-5f);
        u32x4 hv;
#pragma unroll
        for (int i = 0; i < 4; ++i) hv[i] = cvtpk(v[2 * i] * rs, v[2 * i + 1] * rs);
        *(LAS u32x4*)((LAS char*)lds + K1_LN + w * 1024 + lane * 16) = hv;
        const float g = __builtin_amdgcn_rcpf(1.f + __builtin_amdgcn_exp2f(-1.4426950408889634f * s3));
        if (lane == 0) ((GAS float*)(ws + WS_G))[128 * m + 8 * p + w] = g;
    }
    f32x16 acc[3];
#pragma unroll
    for (int i = 0; i < 3; ++i) acc[i] = f32x16{};
    const int sw = (r >> 1) & 7;
    const int xa0 = (32 * mrow + r) * 128 + (((4 * kh + h2) ^ sw) << 4);
    const int xa1 = (32 * mrow + r) * 128 + (((4 * kh + 2 + h2) ^ sw) << 4);
    const int wa = 16384 + (32 * kh + 8 * h2 + ((lane & 15) >> 2)) * 192 + ((lane >> 4) & 1) * 32 + (lane & 3) * 8;
    int slot = 0;
#pragma clang loop unroll(disable)
    for (int s = 0; s < 8; ++s) {
        if (s == 0) asm volatile("s_waitcnt vmcnt(17) lgkmcnt(0)\n\ts_barrier" ::: "memory");
        else if (s < 5) asm volatile("s_waitcnt vmcnt(14) lgkmcnt(0)\n\ts_barrier" ::: "memory");
        else if (s == 5) asm volatile("s_waitcnt vmcnt(8) lgkmcnt(0)\n\ts_barrier" ::: "memory");
        else if (s == 6) asm volatile("s_waitcnt vmcnt(4) lgkmcnt(0)\n\ts_barrier" ::: "memory");
        else asm volatile("s_waitcnt vmcnt(0) lgkmcnt(0)\n\ts_barrier" ::: "memory");
        if (s == 0) ST(3);
        if (s == 4) ST(4);
        if (s == 7) ST(5);
        if (s == 0) {
            LAS const unsigned short* lb = (LAS const unsigned short*)((LAS char*)lds + K1_LN) + tid;
            u32x4 hv;
#pragma unroll
            for (int i = 0; i < 4; ++i) hv[i] = (unsigned)lb[(2 * i) * 512] | ((unsigned)lb[(2 * i + 1) * 512] << 16);
            const int kap = 16 * m + p;
            *(GAS u32x4*)(ws + WS_H + (size_t)((tid >> 5) * 128 + (kap >> 1)) * 1024 + (kap & 1) * 512 + (tid & 31) * 16) = hv;
            asm volatile("" ::: "memory");
        } else if (s < 4) {
            issue(s + 4, slot == 0 ? 4 : slot - 1);
        }
        LAS const char* sb = (LAS const char*)lds + slot * K1_STAGE;
        bf16x8 xf[2], wf[2][3];
#pragma unroll
        for (int st = 0; st < 2; ++st) {
            xf[st] = *(LAS const bf16x8*)(sb + (st ? xa1 : xa0));
#pragma unroll
            for (int i = 0; i < 3; ++i) {
                const s16x4 lo = trread(sb + wa + st * 3072 + i * 64), hi = trread(sb + wa + st * 3072 + i * 64 + 768);
                wf[st][i] = __builtin_shufflevector(lo, hi, 0, 1, 2, 3, 4, 5, 6, 7);
            }
        }
        __builtin_amdgcn_sched_barrier(0);
#pragma unroll
        for (int st = 0; st < 2; ++st)
#pragma unroll
            for (int i = 0; i < 3; ++i) {
                if (i < NT) acc[i] = MFMA32(wf[st][i], xf[st], acc[i]);
                else acc[i] = MFMA32(xf[st], wf[st][i], acc[i]);
            }
        slot = (slot == 4) ? 0 : slot + 1;
    }
    asm volatile("s_waitcnt lgkmcnt(0)\n\ts_barrier" ::: "memory");
    LAS char* ex = (LAS char*)lds + w * 6144 + lane * 16;
    LAS const char* exr = (LAS const char*)lds + (w ^ 4) * 6144 + lane * 16;
    float fin[3][8];
    if (kh == 0) {
#pragma unroll
        for (int q = 0; q < 6; ++q) { const int e = 24 + 4 * q; *(LAS f32x4*)(ex + q * 1024) = f32x4{acc[e >> 4][e & 15], acc[e >> 4][(e & 15) + 1], acc[e >> 4][(e & 15) + 2], acc[e >> 4][(e & 15) + 3]}; }
#pragma unroll
        for (int e = 0; e < 24; ++e) fin[e >> 3][e & 7] = acc[e >> 4][e & 15];
    } else {
#pragma unroll
        for (int q = 0; q < 6; ++q) { const int e = 4 * q; *(LAS f32x4*)(ex + q * 1024) = f32x4{acc[e >> 4][e & 15], acc[e >> 4][(e & 15) + 1], acc[e >> 4][(e & 15) + 2], acc[e >> 4][(e & 15) + 3]}; }
#pragma unroll
        for (int e = 0; e < 24; ++e) fin[e >> 3][e & 7] = acc[(e + 24) >> 4][(e + 24) & 15];
    }
    asm volatile("s_waitcnt lgkmcnt(0)\n\ts_barrier" ::: "memory");
#pragma unroll
    for (int q = 0; q < 6; ++q) {
        const f32x4 t = *(LAS const f32x4*)(exr + q * 1024);
#pragma unroll
        for (int i = 0; i < 4; ++i) fin[q >> 1][(q & 1) * 4 + i] += t[i];
    }
    const int tile = 4 * m + mrow;
    ST(6);
#pragma unroll
    for (int uu = 0; uu < 3; ++uu) {
        const int u = 3 * kh + uu, cb = 3 * p + (u >> 1), half = u & 1, type = cb >> 4, hp = cb & 15;
        const float sc = (type == 0) ? 0.25f * 1.4426950408889634f : 1.f;
        u32x4 o;
#pragma unroll
        for (int i = 0; i < 4; ++i) o[i] = cvtpk(fin[uu][2 * i] * sc, fin[uu][2 * i + 1] * sc);
        if (type < 2) {
            const int head = 2 * hp + half;
            *(GAS u32x4*)(ws + (type ? WS_K : WS_Q) + (size_t)(head * 64 + tile) * 1024 + lane * 16) = o;
            if (type == 1) {
                float n2 = 0.f;
#pragma unroll
                for (int i = 0; i < 4; ++i) { const float a = bflo(o[i]), b = bfhi(o[i]); n2 += a * a + b * b; }
                n2 = wave_max(swap32_add(n2));
                if (lane == 0) ((GAS float*)(ws + WS_KN))[head * 64 + tile] = n2;
            }
        } else {
            const int head = 2 * hp + (r >> 4);
            *(GAS u32x4*)(ws + WS_V + (size_t)(head * 64 + tile) * 1024 + half * 512 + h2 * 256 + (r & 15) * 16) = o;
        }
    }
    ST(7); ST_OUT(0);
}

__global__ __launch_bounds__(512, 2) void k1(const char* x, const char* wqkv, const char* gw, char* ws) {
    extern __shared__ __attribute__((aligned(16))) char lds[];
    const int b = blockIdx.x, j = b & 7, i = b >> 3;
    const int m = 4 * (j >> 1) + (i & 3), p = 8 * (j & 1) + (i >> 2);
    if (b == 0 && threadIdx.x < 32) ((GAS unsigned*)(ws + WS_CNT))[threadIdx.x * 64] = 0u;
    if (p < 10) k1_body<3>(x, wqkv, gw, ws, lds, m, p);
    else if (p == 10) k1_body<2>(x, wqkv, gw, ws, lds, m, p);
    else k1_body<0>(x, wqkv, gw, ws, lds, m, p);
}

#define EXP(a) "v_exp_f32 v" #a ", v" #a "\n\t"
#define CVT(d, a, b) "v_cvt_pk_bf16_f32 v" #d ", v" #a ", v" #b "\n\t"
#define ATILE(c0, c1, c2, c3, c4, c5, c6, c7, c8, c9, c10, c11, c12, c13, c14, c15, SN, KN, KC, VC0, VN0, VP1, KOFF, VOFF0, VOFF1, PRE) \
    "s_waitcnt lgkmcnt(0)\n\t" EXP(c0) EXP(c1)                                                           \
    "v_mfma_f32_32x32x16_bf16 v[80:95], " VP1 ", v[100:103], v[80:95]\n\t"                               \
    EXP(c2) EXP(c3) EXP(c4) EXP(c5)                                                                      \
    "v_mfma_f32_32x32x16_bf16 " SN ", " KN ", v[24:27], v[32:47]\n\t"                                    \
    PRE                                                                                                  \
    "ds_read_b128 " KC ", v28 offset:" #KOFF "\n\t"                                                      \
    "s_mov_b64 exec, s[40:41]\n\t"                                                                       \
    "ds_read_b128 " VN0 ", v29 offset:" #VOFF0 "\n\t"                                                    \
    "ds_read_b128 " VP1 ", v29 offset:" #VOFF1 "\n\t"                                                    \
    "s_mov_b64 exec, -1\n\t"                                                                             \
    EXP(c6) EXP(c7) CVT(96, c0, c1) CVT(97, c2, c3) CVT(98, c4, c5) CVT(99, c6, c7) EXP(c8) EXP(c9)      \
    "v_mfma_f32_32x32x16_bf16 v[80:95], " VC0 ", v[96:99], v[80:95]\n\t"                                 \
    EXP(c10) EXP(c11) EXP(c12) EXP(c13) EXP(c14) EXP(c15)                                                \
    CVT(100, c8, c9) CVT(101, c10, c11) CVT(102, c12, c13) CVT(103, c14, c15)
#define ATILE_E(KOFF, VOFF0, VOFF1, PRE) \
    ATILE(48, 49, 50, 51, 52, 53, 54, 55, 56, 57, 58, 59, 60, 61, 62, 63, "v[64:79]", "v[108:111]", "v[104:107]", "v[112:115]", "v[120:123]", "v[124:127]", KOFF, VOFF0, VOFF1, PRE)
#define ATILE_O(KOFF, VOFF0, VOFF1, PRE) \
    ATILE(64, 65, 66, 67, 68, 69, 70, 71, 72, 73, 74, 75, 76, 77, 78, 79, "v[48:63]", "v[104:107]", "v[108:111]", "v[120:123]", "v[112:115]", "v[116:119]", KOFF, VOFF0, VOFF1, PRE)
#define MOV4(a, b, c, d, s) "v_mov_b32 v" #a ", " s "\n\tv_mov_b32 v" #b ", " s "\n\tv_mov_b32 v" #c ", " s "\n\tv_mov_b32 v" #d ", " s "\n\t"
#define AISSUE                                                   \
    "s_add_u32 m0, s44, s45\n\t"                                 \
    "s_add_u32 s44, s44, 0x4000\n\t"                             \
    "global_load_lds_dwordx4 v30, s[36:37]\n\t"                  \
    "s_add_u32 m0, m0, 0x2000\n\t"                               \
    "s_and_b32 s44, s44, 0xffff\n\t"                             \
    "global_load_lds_dwordx4 v30, s[38:39]\n\t"                  \
    "s_add_u32 s36, s36, 0x2000\n\t"                             \
    "s_addc_u32 s37, s37, 0\n\t"                                 \
    "s_add_u32 s38, s38, 0x2000\n\t"                             \
    "s_addc_u32 s39, s39, 0\n\t"

DI f32x16 attn_pass(u32x4 q, float negm, const char* ksrc, const char* vsrc, unsigned lds0, int w, int lane) {
    f32x16 o;
    const unsigned kl = lds0 + lane * 16;
    const unsigned vl = lds0 + 8192 + (lane >> 5) * 256 + (lane & 15) * 16;
    const unsigned ones = ((lane & 31) == 16 || (lane & 31) == 20) ? 0x3f803f80u : 0u;
    const unsigned ldsw = lds0 + w * 1024;
    asm volatile(
        "s_mov_b64 s[36:37], %[ks]\n\t"
        "s_mov_b64 s[38:39], %[vs]\n\t"
        "s_mov_b32 s45, %[ldsw]\n\t"
        "s_mov_b32 s44, 0\n\t"
        "v_mov_b32 v30, %[l16]\n\t"
        "s_mov_b32 s40, 0xffff\n\t"
        "s_mov_b32 s41, 0xffff\n\t"
        AISSUE AISSUE AISSUE
        MOV4(32, 33, 34, 35, "%[nm]") MOV4(36, 37, 38, 39, "%[nm]") MOV4(40, 41, 42, 43, "%[nm]") MOV4(44, 45, 46, 47, "%[nm]")
        MOV4(80, 81, 82, 83, "0") MOV4(84, 85, 86, 87, "0") MOV4(88, 89, 90, 91, "0") MOV4(92, 93, 94, 95, "0")
        MOV4(100, 101, 102, 103, "0")
        MOV4(112, 113, 114, 115, "%[on]") MOV4(116, 117, 118, 119, "%[on]") MOV4(120, 121, 122, 123, "%[on]") MOV4(124, 125, 126, 127, "%[on]")
        "v_mov_b32 v28, %[kl]\n\t"
        "v_mov_b32 v29, %[vl]\n\t"
        "s_mov_b32 s42, 0\n\t"
        "s_mov_b32 s43, 0\n\t"
        "s_waitcnt vmcnt(4)\n\t"
        "s_barrier\n\t"
        "ds_read_b128 v[104:107], v28\n\t"
        "ds_read_b128 v[108:111], v28 offset:1024\n\t"
        "s_mov_b64 exec, s[40:41]\n\t"
        "ds_read_b128 v[112:115], v29\n\t"
        "ds_read_b128 v[116:119], v29 offset:512\n\t"
        "s_mov_b64 exec, -1\n\t"
        "s_waitcnt lgkmcnt(3)\n\t"
        "v_mfma_f32_32x32x16_bf16 v[48:63], v[104:107], v[24:27], v[32:47]\n\t"
        "s_nop 15\n\t"
        "LA_loop%=:\n\t"
        ATILE_E(2048, 1024, 1536, "")
        ATILE_O(3072, 2048, 2560, "")
        ATILE_E(4096, 3072, 3584, "")
        ATILE_O(5120, 4096, 4608, "")
        ATILE_E(6144, 5120, 5632, "")
        ATILE_O(7168, 6144, 6656, "")
        ATILE_E(0, 7168, 7680,
                "s_add_u32 s43, s43, 0x4000\n\t"
                "s_and_b32 s43, s43, 0xffff\n\t"
                "s_cmp_lt_u32 s42, 6\n\t"
                "s_waitcnt vmcnt(2)\n\t"
                "s_cbranch_scc1 LA_w%=\n\t"
                "s_waitcnt vmcnt(0)\n\t"
                "LA_w%=:\n\t"
                "s_barrier\n\t"
                "v_add_u32 v28, s43, %[kl]\n\t"
                "s_cmp_lt_u32 s42, 5\n\t"
                "s_cbranch_scc0 LA_n%=\n\t"
                AISSUE
                "LA_n%=:\n\t")
        ATILE_O(1024, 0, 512, "v_add_u32 v29, s43, %[vl]\n\t")
        "s_add_u32 s42, s42, 1\n\t"
        "s_cmp_lt_u32 s42, 8\n\t"
        "s_cbranch_scc1 LA_loop%=\n\t"
        "s_waitcnt lgkmcnt(0)\n\t"
        "s_nop 1\n\t"
        "v_mfma_f32_32x32x16_bf16 v[80:95], v[124:127], v[100:103], v[80:95]\n\t"
        "s_nop 15\n\t"
        : "={v[80:95]}"(o)
        : "{v[24:27]}"(q), [nm] "v"(negm), [ks] "s"(ksrc), [vs] "s"(vsrc), [ldsw] "s"(ldsw), [l16] "v"(lane * 16), [kl] "v"(kl), [vl] "v"(vl), [on] "v"(ones)
        : "memory", "scc", "s36", "s37", "s38", "s39", "s40", "s41", "s42", "s43", "s44", "s45", "v28", "v29", "v30",
          "v32", "v33", "v34", "v35", "v36", "v37", "v38", "v39", "v40", "v41", "v42", "v43", "v44", "v45", "v46", "v47",
          "v48", "v49", "v50", "v51", "v52", "v53", "v54", "v55", "v56", "v57", "v58", "v59", "v60", "v61", "v62", "v63",
          "v64", "v65", "v66", "v67", "v68", "v69", "v70", "v71", "v72", "v73", "v74", "v75", "v76", "v77", "v78", "v79",
          "v96", "v97", "v98", "v99", "v100", "v101", "v102", "v103", "v104", "v105", "v106", "v107", "v108", "v109", "v110", "v111",
          "v112", "v113", "v114", "v115", "v116", "v117", "v118", "v119", "v120", "v121", "v122", "v123", "v124", "v125", "v126", "v127");
    return o;
}

DI void attn_role(char* ws, char* lds, int a) {
    const int tid = threadIdx.x, lane = tid & 63;
    const int w = __builtin_amdgcn_readfirstlane(tid >> 6);
    const int head = 4 * (a & 7) + ((a >> 3) & 3), qt = 8 * (a >> 5) + w;
    const unsigned lds0 = (unsigned)(uintptr_t)lds;
    ST_DECL; ST(0);
    const u32x4 q = *(GAS const u32x4*)(ws + WS_Q + (size_t)(head * 64 + qt) * 1024 + lane * 16);
    const float kn = wave_max(((GAS const float*)(ws + WS_KN))[head * 64 + lane]);
    float qn = 0.f;
#pragma unroll
    for (int i = 0; i < 4; ++i) { const float x = bflo(q[i]), y = bfhi(q[i]); qn += x * x + y * y; }
    qn = swap32_add(qn);
    float negm = -1.001f * __builtin_amdgcn_sqrtf(qn * kn);
    const char* ksrc = ws + WS_K + (size_t)head * 65536 + w * 1024;
    const char* vsrc = ws + WS_V + (size_t)head * 65536 + w * 1024;
    LAS unsigned* vote = (LAS unsigned*)((LAS char*)lds + 65536);
#pragma clang loop unroll(disable)
    for (int att = 0;; ++att) {
        ST(1);
        const f32x16 o = attn_pass(q, negm, ksrc, vsrc, lds0, w, lane);
        ST(2);
        const float l = o[8];
        const float rinv = __builtin_amdgcn_rcpf(l);
        u32x4 ov;
#pragma unroll
        for (int i = 0; i < 4; ++i) ov[i] = cvtpk(o[2 * i] * rinv, o[2 * i + 1] * rinv);
        asm volatile("global_store_dwordx4 %0, %1, %2\n\ts_nop 1" ::"v"(lane * 16), "v"(ov), "s"(ws + WS_O + (size_t)(qt * 32 + head) * 1024) : "memory");
        asm volatile("" : "+s"(att));
        if (att) break;
        const bool bad = !__all(l > 5.4e-20f && l < 1e30f);
        if (lane == 0) vote[w] = bad;
        __syncthreads();
        const u32x4 v0 = *(LAS const u32x4*)vote, v1 = *(LAS const u32x4*)(vote + 4);
        if (!__builtin_amdgcn_readfirstlane(v0[0] | v0[1] | v0[2] | v0[3] | v1[0] | v1[1] | v1[2] | v1[3])) break;
        float mx = -INFINITY;
        const char* kt = ws + WS_K + (size_t)head * 65536 + lane * 16;
#pragma clang loop unroll(disable)
        for (int t = 0; t < 64; ++t) {
            const bf16x8 kf = *(GAS const bf16x8*)(kt + t * 1024);
            const f32x16 s = MFMA32(kf, __builtin_bit_cast(bf16x8, q), f32x16{});
#pragma unroll
            for (int i = 0; i < 16; ++i) mx = fmaxf(mx, s[i]);
        }
        negm = -swap32_max(mx);
    }
    ST(3); ST_OUT(1);
}

#define BMM_WA(ACC, WF, DF) "v_mfma_f32_32x32x16_bf16 " ACC ", " WF ", " DF ", " ACC "\n\t"
#define BMM_WB(ACC, WF, DF) "v_mfma_f32_32x32x16_bf16 " ACC ", " DF ", " WF ", " ACC "\n\t"
#define BISSUE(SLOT, D0, D1, SC)                                   \
    "s_mul_i32 s46, s42, %[wst]\n\t"                                \
    "s_lshl_b32 s47, s42, 10\n\t"                                   \
    "s_add_u32 s36, %[wlo], s46\n\t"                                \
    "s_addc_u32 s37, %[whi], 0\n\t"                                 \
    "s_add_u32 s38, %[dlo], s47\n\t"                                \
    "s_addc_u32 s39, %[dhi], 0\n\t"                                 \
    "s_add_u32 m0, %[ldsw], " #SLOT "*2048\n\t"                     \
    "s_add_u32 s42, s42, 1\n\t"                                     \
    "global_load_lds_dwordx4 %[g0], s[36:37]\n\t"                   \
    "s_add_u32 m0, m0, 1024\n\t"                                    \
    "s_cmp_eq_u32 s42, %[n]\n\t"                                    \
    "global_load_lds_dwordx4 %[g1], s[36:37]\n\t"                   \
    "s_cselect_b32 s42, 0, s42\n\t"                                 \
    "global_load_dwordx4 " D0 ", %[d0], s[38:39]" SC "\n\t"         \
    "global_load_dwordx4 " D1 ", %[d1], s[38:39]" SC "\n\t"
#define BSTEP(SLOT, D0, D1, MM, TD, SC)                                              \
    "s_cmp_lt_u32 s43, 4\n\t"                                                         \
    "s_waitcnt vmcnt(12)\n\t"                                                         \
    "s_cbranch_scc0 LB_w" #SLOT "%=\n\t"                                              \
    "s_waitcnt vmcnt(0)\n\t"                                                          \
    "LB_w" #SLOT "%=:\n\t"                                                            \
    "ds_read_b64_tr_b16 v[24:25], %[t0] offset:" #SLOT "*2048\n\t"                    \
    "ds_read_b64_tr_b16 v[26:27], %[t0] offset:" #SLOT "*2048+" #TD "\n\t"            \
    "ds_read_b64_tr_b16 v[28:29], %[t1] offset:" #SLOT "*2048\n\t"                    \
    "ds_read_b64_tr_b16 v[30:31], %[t1] offset:" #SLOT "*2048+" #TD "\n\t"            \
    "s_waitcnt lgkmcnt(2)\n\t"                                                        \
    MM("v[64:79]", "v[24:27]", D0) MM("v[80:95]", "v[24:27]", D1)                     \
    "s_waitcnt lgkmcnt(0)\n\t"                                                        \
    MM("v[96:111]", "v[28:31]", D0) MM("v[112:127]", "v[28:31]", D1)                  \
    "s_cmp_lt_u32 s43, 5\n\t"                                                         \
    "s_cbranch_scc1 LB_n" #SLOT "%=\n\t"                                              \
    BISSUE(SLOT, D0, D1, SC)                                                          \
    "LB_n" #SLOT "%=:\n\t"                                                            \
    "s_sub_u32 s43, s43, 1\n\t"                                                       \
    "s_cmp_eq_u32 s43, 0\n\t"                                                         \
    "s_cbranch_scc1 LB_end%=\n\t"
#define BGEMM_ASM(MM, TD, SC)                                                                                     \
    asm volatile(                                                                                                 \
        "s_mov_b32 s42, %[rot]\n\t"                                                                               \
        "s_mov_b32 s43, %[n]\n\t"                                                                                 \
        BISSUE(0, "v[32:35]", "v[36:39]", SC) BISSUE(1, "v[40:43]", "v[44:47]", SC)                               \
        BISSUE(2, "v[48:51]", "v[52:55]", SC) BISSUE(3, "v[56:59]", "v[60:63]", SC)                               \
        "LB_loop%=:\n\t"                                                                                          \
        BSTEP(0, "v[32:35]", "v[36:39]", MM, TD, SC) BSTEP(1, "v[40:43]", "v[44:47]", MM, TD, SC)                 \
        BSTEP(2, "v[48:51]", "v[52:55]", MM, TD, SC) BSTEP(3, "v[56:59]", "v[60:63]", MM, TD, SC)                 \
        "s_branch LB_loop%=\n\t"                                                                                  \
        "LB_end%=:\n\t"                                                                                           \
        "s_nop 15\n\t"                                                                                            \
        : "+{v[64:79]}"(a0), "+{v[80:95]}"(a1), "+{v[96:111]}"(a2), "+{v[112:127]}"(a3)                           \
        : [rot] "s"(rot), [n] "s"(n), [wst] "s"(wst), [wlo] "s"((unsigned)(uintptr_t)wsrc), [whi] "s"((unsigned)((uintptr_t)wsrc >> 32)), \
          [dlo] "s"((unsigned)(uintptr_t)dsrc), [dhi] "s"((unsigned)((uintptr_t)dsrc >> 32)), [ldsw] "s"(ldsw),   \
          [g0] "v"(g0), [g1] "v"(g1), [d0] "v"(d0), [d1] "v"(d1), [t0] "v"(t0), [t1] "v"(t1)                      \
        : "memory", "scc", "s36", "s37", "s38", "s39", "s42", "s43", "s46", "s47",                                \
          "v24", "v25", "v26", "v27", "v28", "v29", "v30", "v31",                                                 \
          "v32", "v33", "v34", "v35", "v36", "v37", "v38", "v39", "v40", "v41", "v42", "v43", "v44", "v45", "v46", "v47", \
          "v48", "v49", "v50", "v51", "v52", "v53", "v54", "v55", "v56", "v57", "v58", "v59", "v60", "v61", "v62", "v63")

template <int MODE>
DI void bgemm(f32x16& a0, f32x16& a1, f32x16& a2, f32x16& a3, const char* wsrc, int wrow, const char* dsrc, int dblk,
              int n, int rot, unsigned ldsw, int lane, bool narrow) {
    const int row = lane >> 3;
    int sl = (lane & 7) ^ (((row >> 1) & 1) << 2);
    if (narrow) sl &= 3;
    const unsigned g0 = row * wrow + sl * 16, g1 = g0 + 8 * wrow;
    const unsigned d0 = lane * 16, d1 = d0 + dblk;
    const int q = (lane & 15) >> 2, h2 = lane >> 5;
    const unsigned tb = ldsw + ((MODE == 0 ? 8 : 4) * h2 + q) * 128 + ((lane >> 4) & 1) * 32 + (lane & 3) * 8;
    const unsigned t0 = tb + ((q >> 1) & 1) * 64, t1 = tb + (((q >> 1) & 1) ^ 1) * 64;
    const unsigned wst = 16 * wrow;
    if (MODE == 0) BGEMM_ASM(BMM_WA, 512, "");
    else if (MODE == 1) BGEMM_ASM(BMM_WB, 1024, " sc1");
    else BGEMM_ASM(BMM_WA, 1024, "");
}

DI f32x4 sub4(const f32x16& a, int g) { return f32x4{a[4 * g], a[4 * g + 1], a[4 * g + 2], a[4 * g + 3]}; }

template <int SB, class FN>
DI void reduce_ds_round(const f32x16& b0, const f32x16& b1, char* lds, int w, int lane, FN fn) {
    const int r = lane & 31, h2 = lane >> 5, tid = threadIdx.x;
    LAS char* wr = (LAS char*)lds + w * 8192 + r * 256;
#pragma unroll
    for (int g = 0; g < 4; ++g) {
        *(LAS f32x4*)(wr + (((2 * g + h2) ^ (r & 7)) << 4)) = sub4(b0, g);
        *(LAS f32x4*)(wr + (((8 + 2 * g + h2) ^ (r & 7)) << 4)) = sub4(b1, g);
    }
    __syncthreads();
    const int s = tid >> 4;
    LAS const char* rd = (LAS const char*)lds + s * 256 + (((tid & 15) ^ (s & 7)) << 4);
    f32x4 v = *(LAS const f32x4*)rd;
#pragma unroll
    for (int k = 1; k < 8; ++k) v += *(LAS const f32x4*)(rd + k * 8192);
    fn(32 * SB + s, 4 * (tid & 15), v);
}

DI float gelu_tanh(float u) {
    const float z = u * (1.f + 0.044715f * u * u) * (-2.f * 0.7978845608028654f * 1.4426950408889634f);
    return u * __builtin_amdgcn_rcpf(1.f + __builtin_amdgcn_exp2f(z));
}

DI void mixer_role(const char* w1, const char* b1, const char* w2, const char* b2, char* ws, char* lds, int bi) {
    const int tid = threadIdx.x, lane = tid & 63, h2 = lane >> 5;
    const int w = __builtin_amdgcn_readfirstlane(tid >> 6);
    const unsigned ldsw = (unsigned)(uintptr_t)lds + w * 8192;
    const int j = bi & 7, i = bi >> 3;
    GAS unsigned* cnt = (GAS unsigned*)(ws + WS_CNT);
    __builtin_amdgcn_s_setprio(3);
    ST_DECL; ST(0);
    if (i < 22) {
        const int L = (j >> 1) * 22 + i, ft = L >> 2, dt = 4 * (j & 1) + (L & 3);
        const int db = w & 1, fb = (w >> 1) & 1, sp = w >> 2;
        const bool live = (ft < 21 || fb == 0);
        const int f0 = 64 * ft + 32 * fb + 16 * sp + 4 * h2;
        u32x2 bl = {0u, 0u}, bh = {0u, 0u};
        if (live) { bl = *(GAS const u32x2*)(b1 + f0 * 2); bh = *(GAS const u32x2*)(b1 + f0 * 2 + 16); }
        f32x16 a0 = {}, a1 = {}, a2 = {}, a3 = {};
        bgemm<0>(a0, a1, a2, a3, w1 + (size_t)w * 256 * 2752 + ft * 128, 2752, ws + WS_H + (size_t)(2 * dt * 128 + 16 * w) * 1024, 131072,
                 16, (2 * dt + 5 * ft) & 15, ldsw, lane, ft == 21);
        ST(1);
        LAS char* wr = (LAS char*)lds + w * 8192 + lane * 16;
        LAS const char* rd = (LAS const char*)lds + w * 1024 + lane * 16;
        f32x4 v[2];
#pragma unroll
        for (int rho = 0; rho < 2; ++rho) {
            if (rho) __syncthreads();
#pragma unroll
            for (int k = 0; k < 8; ++k) {
                const int blk = 2 * ((k >> 1) & 1) + (k & 1), g = 2 * (k >> 2) + rho;
                *(LAS f32x4*)(wr + k * 1024) = sub4(blk == 0 ? a0 : blk == 1 ? a1 : blk == 2 ? a2 : a3, g);
            }
            __syncthreads();
            f32x4 t = *(LAS const f32x4*)rd;
#pragma unroll
            for (int k = 1; k < 8; ++k) t += *(LAS const f32x4*)(rd + k * 8192);
            v[rho] = t;
        }
        u32x4 o;
        o[0] = cvtpk(gelu_tanh(v[0][0] + bflo(bl[0])), gelu_tanh(v[0][1] + bfhi(bl[0])));
        o[1] = cvtpk(gelu_tanh(v[0][2] + bflo(bl[1])), gelu_tanh(v[0][3] + bfhi(bl[1])));
        o[2] = cvtpk(gelu_tanh(v[1][0] + bflo(bh[0])), gelu_tanh(v[1][1] + bfhi(bh[0])));
        o[3] = cvtpk(gelu_tanh(v[1][2] + bflo(bh[1])), gelu_tanh(v[1][3] + bfhi(bh[1])));
        if (live) {
            const char* dst = ws + WS_U + (size_t)((2 * dt + db) * 86 + 4 * ft + 2 * fb + sp) * 1024;
            asm volatile("global_store_dwordx4 %0, %1, %2 sc1\n\ts_nop 1" ::"v"(lane * 16), "v"(o), "s"(dst) : "memory");
        }
        asm volatile("s_waitcnt vmcnt(0)" ::: "memory");
        __syncthreads();
        if (tid == 0) __hip_atomic_fetch_add(cnt + dt * 64, 1u, __ATOMIC_RELAXED, __HIP_MEMORY_SCOPE_AGENT);
    }
    ST(2);
    const int st = 8 * (j >> 1) + (i & 7), dt = 4 * (j & 1) + (i >> 3);
    if (w == 0) {
        for (unsigned spin = 0; spin < (1u << 22); ++spin) {
            if (__builtin_amdgcn_readfirstlane(__hip_atomic_load(cnt + dt * 64, __ATOMIC_RELAXED, __HIP_MEMORY_SCOPE_AGENT)) >= 22u) break;
            __builtin_amdgcn_s_sleep(4);
        }
    }
    __syncthreads();
    ST(3);
    const int n = w < 6 ? 11 : 10, start = w < 6 ? 11 * w : 10 * w + 6;
    f32x16 a0 = {}, a1 = {}, a2 = {}, a3 = {};
    bgemm<1>(a0, a1, a2, a3, w2 + (size_t)start * 16 * 4096 + st * 128, 4096, ws + WS_U + (size_t)(2 * dt * 86 + start) * 1024, 86 * 1024,
             n, (st + 3 * dt) & 7, ldsw, lane, false);
    auto fin = [&](int s, int d, f32x4 v) {
        const int sg = 64 * st + s;
        const float g1 = 1.f - ((GAS const float*)(ws + WS_G))[sg];
        const float bb = bflo(((GAS const unsigned short*)b2)[sg]);
        u32x2 o = {cvtpk((v[0] + bb) * g1, (v[1] + bb) * g1), cvtpk((v[2] + bb) * g1, (v[3] + bb) * g1)};
        *(GAS u32x2*)(ws + WS_B + (size_t)sg * 1024 + (64 * dt + d) * 2) = o;
    };
    ST(4);
    reduce_ds_round<0>(a0, a1, lds, w, lane, fin);
    __syncthreads();
    reduce_ds_round<1>(a2, a3, lds, w, lane, fin);
    ST(5); ST_OUT(1);
}

constexpr int K2_LDS = 65536 + 256;
__global__ __launch_bounds__(512, 4) void k2(const char* w1, const char* b1, const char* w2, const char* b2, char* ws) {
    extern __shared__ __attribute__((aligned(16))) char lds[];
    if (blockIdx.x < 256) attn_role(ws, lds, blockIdx.x);
    else mixer_role(w1, b1, w2, b2, ws, lds, blockIdx.x - 256);
}

__global__ __launch_bounds__(512, 4) void k3(const char* wo, char* ws, char* out) {
    extern __shared__ __attribute__((aligned(16))) char lds[];
    const int tid = threadIdx.x, lane = tid & 63;
    const int w = __builtin_amdgcn_readfirstlane(tid >> 6);
    const unsigned ldsw = (unsigned)(uintptr_t)lds + w * 8192;
    const int j = blockIdx.x & 7, i = blockIdx.x >> 3;
    const int st = 4 * j + (i & 3), dt = i >> 2;
    ST_DECL; ST(0);
    f32x16 a0 = {}, a1 = {}, a2 = {}, a3 = {};
    bgemm<2>(a0, a1, a2, a3, wo + (size_t)w * 64 * 1024 + dt * 128, 1024, ws + WS_O + (size_t)(2 * st * 32 + 4 * w) * 1024, 32768,
             4, 0, ldsw, lane, false);
    auto fin = [&](int s, int d, f32x4 v) {
        const int sg = 64 * st + s;
        const float g = ((GAS const float*)(ws + WS_G))[sg];
        const u32x2 bp = *(GAS const u32x2*)(ws + WS_B + (size_t)sg * 1024 + (64 * dt + d) * 2);
        u32x2 o = {cvtpk(g * v[0] + bflo(bp[0]), g * v[1] + bfhi(bp[0])), cvtpk(g * v[2] + bflo(bp[1]), g * v[3] + bfhi(bp[1]))};
        *(GAS u32x2*)(out + (size_t)sg * 1024 + (64 * dt + d) * 2) = o;
    };
    ST(1);
    reduce_ds_round<0>(a0, a2, lds, w, lane, fin);
    __syncthreads();
    reduce_ds_round<1>(a1, a3, lds, w, lane, fin);
    ST(2); ST_OUT(2);
}

extern "C" void kernel_launch(void* const* d_in, const int* in_sizes, int n_in, void* d_out, int out_size, void* d_ws, size_t ws_size,
                              hipStream_t stream) {
    const char* x = (const char*)d_in[0];
    const char* wqkv = (const char*)d_in[1];
    const char* wo = (const char*)d_in[2];
    const char* w1 = (const char*)d_in[4];
    const char* b1 = (const char*)d_in[5];
    const char* w2 = (const char*)d_in[6];
    const char* b2 = (const char*)d_in[7];
    const char* gw = (const char*)d_in[8];
    char* ws = (char*)d_ws;
    (void)hipFuncSetAttribute((const void*)k1, hipFuncAttributeMaxDynamicSharedMemorySize, K1_LDS);
    (void)hipFuncSetAttribute((const void*)k2, hipFuncAttributeMaxDynamicSharedMemorySize, K2_LDS);
    (void)hipFuncSetAttribute((const void*)k3, hipFuncAttributeMaxDynamicSharedMemorySize, 65536);
    k1<<<256, 512, K1_LDS, stream>>>(x, wqkv, gw, ws);
    k2<<<512, 512, K2_LDS, stream>>>(w1, b1, w2, b2, ws);
    k3<<<256, 512, 65536, stream>>>(wo, ws, (char*)d_out);
}
```

```cpp
#include <hip/hip_runtime.h>
#include <hip/hip_bf16.h>
#include <stdint.h>

typedef __attribute__((ext_vector_type(8))) short bf16x8;
typedef __attribute__((ext_vector_type(4))) short s16x4;
typedef __attribute__((ext_vector_type(16))) float f32x16;
typedef __attribute__((ext_vector_type(4))) float f32x4;
typedef __attribute__((ext_vector_type(2))) float f32x2;
typedef __attribute__((ext_vector_type(4))) unsigned u32x4;
typedef __attribute__((ext_vector_type(2))) unsigned u32x2;
#define LAS __attribute__((address_space(3)))
#define GAS __attribute__((address_space(1)))
#define DI __device__ __forceinline__
#define MFMA32(a, b, c) __builtin_amdgcn_mfma_f32_32x32x16_bf16((a), (b), (c), 0, 0, 0)

#define QSLEEP 24
#define LSLEEP 32
constexpr int S_ = 2048, D_ = 512, F_ = 1376;
constexpr size_t WS_EPOCH = 0;
constexpr size_t WS_G = 4096;
constexpr size_t WS_KN = WS_G + 8192;
constexpr size_t WS_FL = 98304;
constexpr size_t WS_FQ = 32768;
constexpr size_t WS_FU = 65536;
constexpr size_t WS_FA = 393216;
constexpr size_t WS_FO = 131072;
constexpr size_t WS_Q = 1u << 20;
constexpr size_t WS_K = WS_Q + (2u << 20);
constexpr size_t WS_V = WS_K + (2u << 20);
constexpr size_t WS_H = WS_V + (2u << 20);
constexpr size_t WS_U = WS_H + (2u << 20);
constexpr size_t WS_O = WS_U + (2u << 20);

#define ST_DECL
#define ST_ARG
#define ST_PASS
#define ST(i)
#define ST_OUT(k)
DI unsigned cvtpk(float lo, float hi) {
    typedef float f32x2_t __attribute__((ext_vector_type(2)));
    typedef __bf16 bf16x2_t __attribute__((ext_vector_type(2)));
    return __builtin_bit_cast(unsigned, __builtin_convertvector(f32x2_t{lo, hi}, bf16x2_t));
}
DI float bflo(unsigned u) { return __uint_as_float(u << 16); }
DI float bfhi(unsigned u) { return __uint_as_float(u & 0xffff0000u); }
DI s16x4 trread(LAS const char* p) {
    typedef short v4i16_t __attribute__((ext_vector_type(4)));
    return __builtin_bit_cast(s16x4, __builtin_amdgcn_ds_read_tr16_b64_v4i16((LAS v4i16_t*)p));
}
DI void glds(unsigned lds, unsigned voff, const void* sbase) {
    asm volatile("s_mov_b32 m0, %0\n\ts_nop 0\n\tglobal_load_lds_dwordx4 %1, %2" ::"s"(lds), "v"(voff), "s"(sbase) : "memory");
}
template <int CTRL> DI float dpp(float v) {
    return __int_as_float(__builtin_amdgcn_update_dpp(0, __float_as_int(v), CTRL, 0xf, 0xf, true));
}
DI float rl(float v, int l) { return __int_as_float(__builtin_amdgcn_readlane(__float_as_int(v), l)); }
DI float wave_sum(float v) {
    v += dpp<0xB1>(v); v += dpp<0x4E>(v); v += dpp<0x141>(v); v += dpp<0x140>(v);
    return (rl(v, 0) + rl(v, 16)) + (rl(v, 32) + rl(v, 48));
}
DI float wave_max(float v) {
    v = fmaxf(v, dpp<0xB1>(v)); v = fmaxf(v, dpp<0x4E>(v)); v = fmaxf(v, dpp<0x141>(v)); v = fmaxf(v, dpp<0x140>(v));
    return fmaxf(fmaxf(rl(v, 0), rl(v, 16)), fmaxf(rl(v, 32), rl(v, 48)));
}
DI float swap32_add(float v) {
    auto rr = __builtin_amdgcn_permlane32_swap(__float_as_uint(v), __float_as_uint(v), false, false);
    return __uint_as_float(rr[0]) + __uint_as_float(rr[1]);
}
DI float swap32_max(float v) {
    auto rr = __builtin_amdgcn_permlane32_swap(__float_as_uint(v), __float_as_uint(v), false, false);
    return fmaxf(__uint_as_float(rr[0]), __uint_as_float(rr[1]));
}

DI int lane_id() {
    int l;
    asm volatile("v_mbcnt_lo_u32_b32 %0, -1, 0\n\tv_mbcnt_hi_u32_b32 %0, -1, %0" : "=v"(l));
    __builtin_assume(l >= 0 && l < 64);
    return l;
}
DI void st16(const char* sbase, unsigned voff, u32x4 v) {
    asm volatile("global_store_dwordx4 %0, %1, %2 sc1\n\ts_nop 1" ::"v"(voff), "v"(v), "s"(sbase) : "memory");
}
DI void st4(const char* p, unsigned v) {
    unsigned t0, t1;
    asm volatile("s_nop 1\n\tv_mov_b32 %0, %2\n\tv_mov_b32 %1, 0\n\ts_mov_b64 exec, 1\n\tglobal_store_dword %1, %0, %3 sc1\n\ts_mov_b64 exec, -1"
                 : "=&v"(t0), "=&v"(t1) : "s"(v), "s"(p) : "memory");
}
DI unsigned uni(float v) { return __builtin_amdgcn_readfirstlane(__float_as_uint(v)); }
DI unsigned ld4(const char* p) { return __hip_atomic_load((GAS const unsigned*)p, __ATOMIC_RELAXED, __HIP_MEMORY_SCOPE_AGENT); }
DI void drain() { asm volatile("s_waitcnt vmcnt(0)" ::: "memory"); }
DI void wgbar() { asm volatile("s_waitcnt lgkmcnt(0)\n\ts_barrier" ::: "memory"); }
template <int SLEEP>
DI void wait_flags(const char* sbase, unsigned voff, unsigned tag) {
#pragma clang loop unroll(disable)
    for (unsigned spin = 0; spin < (1u << 20); ++spin) {
        unsigned f;
        asm volatile("global_load_dword %0, %1, %2 sc1\n\ts_waitcnt vmcnt(0)" : "=v"(f) : "v"(voff), "s"(sbase) : "memory");
        if (__all(f == tag)) break;
        __builtin_amdgcn_s_sleep(SLEEP);
    }
}

constexpr int QW0 = 49152, QCNT = 73728;
DI void qkv_loader(const char* x, const char* wqkv, int m, int p, int lw ST_ARG) {
    const int lane = lane_id();
    unsigned vx[4], vw[3];
#pragma unroll
    for (int ii = 0; ii < 4; ++ii) {
        const int row = 32 * lw + 8 * ii + (lane >> 3);
        vx[ii] = row * 1024 + (((lane & 7) ^ ((row >> 1) & 7)) << 4);
    }
#pragma unroll
    for (int ii = 0; ii < 3; ++ii) {
        const int o = 16 * lane + 1024 * ii;
        vw[ii] = (16 * lw + o / 192) * 3072 + (o % 192);
    }
    const char* xb = x + (size_t)m * 131072;
    const char* wb = wqkv + p * 192;
    const int rot = (p + 2 * m) & 7;
    auto isx = [&](int s, int slot) {
        const char* xs = xb + ((s + rot) & 7) * 128;
#pragma unroll
        for (int ii = 0; ii < 4; ++ii) glds(slot * 16384 + lw * 4096 + ii * 1024, vx[ii], xs);
    };
    auto isw = [&](int s) {
        const char* wsrc = wb + ((s + rot) & 7) * 196608;
#pragma unroll
        for (int ii = 0; ii < 3; ++ii) glds(QW0 + (s & 1) * 12288 + lw * 3072 + ii * 1024, vw[ii], wsrc);
    };
    isw(0); isx(0, 0); isw(1); isx(1, 1); isx(2, 2);
    *(LAS unsigned*)QCNT = 0u;
    asm volatile("s_waitcnt vmcnt(11) lgkmcnt(0)\n\ts_barrier" ::: "memory");
    ST(7);
    int slot = 0;
#pragma clang loop unroll(disable)
    for (int s = 1; s < 6; ++s) {
        asm volatile("s_waitcnt vmcnt(4)\n\ts_barrier" ::: "memory");
        isw(s + 1); isx(s + 2, slot);
        slot = slot == 2 ? 0 : slot + 1;
    }
    asm volatile("s_waitcnt vmcnt(4)\n\ts_barrier" ::: "memory");
    isw(7);
    asm volatile("s_waitcnt vmcnt(0)\n\ts_barrier" ::: "memory");
}

template <int NT>
DI void qkv_body(char* ws, char* lds, int m, int p, int w, unsigned tag ST_ARG) {
    const int lane = lane_id(), r = lane & 31, h2 = lane >> 5;
    f32x16 acc[3];
#pragma unroll
    for (int i = 0; i < 3; ++i) acc[i] = f32x16{};
    const int sw = (r >> 1) & 7;
    int xa[4];
#pragma unroll
    for (int st = 0; st < 4; ++st) xa[st] = (32 * w + r) * 128 + (((2 * st + h2) ^ sw) << 4);
    const int wa = QW0 + (8 * h2 + ((lane & 15) >> 2)) * 192 + ((lane >> 4) & 1) * 32 + (lane & 3) * 8;
    bf16x8 xf[4], wf[2][3];
    auto rdw = [&](LAS const char* wsl, int st, int i) {
        const s16x4 lo = trread(wsl + wa + st * 3072 + i * 64), hi = trread(wsl + wa + st * 3072 + i * 64 + 768);
        wf[st & 1][i] = __builtin_shufflevector(lo, hi, 0, 1, 2, 3, 4, 5, 6, 7);
    };
    auto mm1 = [&](int st, int i) {
        if (i < NT) acc[i] = MFMA32(wf[st & 1][i], xf[st], acc[i]);
        else acc[i] = MFMA32(xf[st], wf[st & 1][i], acc[i]);
        __builtin_amdgcn_sched_barrier(0);
    };
    auto step = [&](int st, LAS const char* xsl, LAS const char* wsl, bool bar) {
        const int sn = (st + 2) & 3;
        mm1(st, 0);
        if (bar) wgbar();
        xf[sn] = *(LAS const bf16x8*)(xsl + xa[sn]); rdw(wsl, sn, 0); __builtin_amdgcn_sched_barrier(0);
        mm1(st, 1);
        rdw(wsl, sn, 1); __builtin_amdgcn_sched_barrier(0);
        mm1(st, 2);
        rdw(wsl, sn, 2); __builtin_amdgcn_sched_barrier(0);
    };
    wgbar();
    LAS const char* xsl = (LAS const char*)lds;
    LAS const char* wsl = (LAS const char*)lds;
#pragma unroll
    for (int st = 0; st < 2; ++st) { xf[st] = *(LAS const bf16x8*)(xsl + xa[st]); rdw(wsl, st, 0); rdw(wsl, st, 1); rdw(wsl, st, 2); }
    __builtin_amdgcn_sched_barrier(0);
#pragma clang loop unroll(disable)
    for (int s = 0; s < 7; ++s) {
        step(0, xsl, wsl, false); step(1, xsl, wsl, false);
        xsl = (xsl == (LAS const char*)lds + 32768) ? (LAS const char*)lds : xsl + 16384;
        wsl = (LAS const char*)lds + ((s & 1) ? 0 : 12288);
        step(2, xsl, wsl, true); step(3, xsl, wsl, false);
    }
    step(0, xsl, wsl, false); step(1, xsl, wsl, false);
    mm1(2, 0); mm1(2, 1); mm1(2, 2); mm1(3, 0); mm1(3, 1); mm1(3, 2);
    ST(4);
    const int tile = 4 * m + w;
    float kn[2 * NT + 1];
#pragma unroll
    for (int u = 0; u < 6; ++u) {
        const int cb = 3 * p + (u >> 1), half = u & 1, type = cb >> 4, hp = cb & 15;
        const float sc = (type == 0) ? 0.25f * 1.4426950408889634f : 1.f;
        u32x4 o;
#pragma unroll
        for (int i = 0; i < 4; ++i) o[i] = cvtpk(acc[u >> 1][8 * half + 2 * i] * sc, acc[u >> 1][8 * half + 2 * i + 1] * sc);
        if ((u >> 1) < NT) {
            st16(ws + (type ? WS_K : WS_Q) + (size_t)((2 * hp + half) * 64 + tile) * 1024, lane * 16, o);
            float n2 = 0.f;
#pragma unroll
            for (int i = 0; i < 4; ++i) { const float a = bflo(o[i]), b = bfhi(o[i]); n2 += a * a + b * b; }
            kn[u] = n2;
        } else {
            st16(ws + WS_V + (size_t)(2 * hp * 64 + tile) * 1024 + half * 512, (r >> 4) * 65536 + h2 * 256 + (r & 15) * 16, o);
        }
    }
#pragma unroll
    for (int u = 0; u < 2 * NT; ++u) {
        const int cb = 3 * p + (u >> 1);
        if ((cb >> 4) == 1) {
            const float n2 = wave_max(swap32_add(kn[u]));
            st4(ws + WS_KN + ((2 * (cb & 15) + (u & 1)) * 64 + tile) * 4, uni(n2));
        }
    }
    drain();
    ST(7);
    unsigned arr = 0;
    if (lane == 0) arr = __hip_atomic_fetch_add((LAS unsigned*)QCNT, 1u, __ATOMIC_RELAXED, __HIP_MEMORY_SCOPE_WORKGROUP);
    if (__builtin_amdgcn_readfirstlane(arr) == 3) st4(ws + WS_FQ + (p * 16 + m) * 128, tag);
}

#define EXP(a) "v_exp_f32 v" #a ", v" #a "\n\t"
#define CVT(d, a, b) "v_cvt_pk_bf16_f32 v" #d ", v" #a ", v" #b "\n\t"
#define MF(D, A, B, C) "v_mfma_f32_32x32x16_bf16 " D ", " A ", " B ", " C "\n\t"
#define ASTEP(c0, c1, c2, c3, c4, c5, c6, c7, c8, c9, c10, c11, c12, c13, c14, c15, M1, M2, CL, M3)                  \
    EXP(c0) EXP(c1) M1 EXP(c2) EXP(c3) EXP(c4) EXP(c5) M2 CL                                                          \
    EXP(c6) EXP(c7) CVT(96, c0, c1) CVT(97, c2, c3) CVT(98, c4, c5) CVT(99, c6, c7) EXP(c8) EXP(c9) M3                \
    EXP(c10) EXP(c11) EXP(c12) EXP(c13) EXP(c14) EXP(c15)                                                             \
    CVT(100, c8, c9) CVT(101, c10, c11) CVT(102, c12, c13) CVT(103, c14, c15)
#define R_OA "v[64:79]"
#define R_OB "v[80:95]"
#define R_SA "v[32:47]"
#define R_SB "v[48:63]"
#define R_NM "v[16:31]"
#define ATILE(KC, KN, V0C, V1C, V0N, V1N, KOFF, VOFF0, VOFF1, PRE)                                                    \
    ASTEP(32, 33, 34, 35, 36, 37, 38, 39, 40, 41, 42, 43, 44, 45, 46, 47,                                             \
          MF(R_OB, V1N, "v[100:103]", R_OB), MF(R_SB, KC, "v[8:11]", R_NM),                                                   \
          "s_waitcnt lgkmcnt(0)\n\t" PRE                                                                              \
          "ds_read_b128 " KC ", v12 offset:" #KOFF "\n\t"                                                             \
          "s_mov_b64 exec, s[40:41]\n\t"                                                                              \
          "ds_read_b128 " V0N ", v13 offset:" #VOFF0 "\n\t"                                                           \
          "ds_read_b128 " V1N ", v13 offset:" #VOFF1 "\n\t"                                                           \
          "s_mov_b64 exec, -1\n\t",                                                                                   \
          MF(R_OA, V0C, "v[96:99]", R_OA))                                                                                \
    ASTEP(48, 49, 50, 51, 52, 53, 54, 55, 56, 57, 58, 59, 60, 61, 62, 63,                                             \
          MF(R_OA, V1C, "v[100:103]", R_OA), MF(R_SA, KN, "v[4:7]", R_NM), "", MF(R_OB, V0C, "v[96:99]", R_OB))
#define ATILE_E(KOFF, VOFF0, VOFF1, PRE) ATILE("v[104:107]", "v[108:111]", "v[112:115]", "v[116:119]", "v[120:123]", "v[124:127]", KOFF, VOFF0, VOFF1, PRE)
#define ATILE_O(KOFF, VOFF0, VOFF1, PRE) ATILE("v[108:111]", "v[104:107]", "v[120:123]", "v[124:127]", "v[112:115]", "v[116:119]", KOFF, VOFF0, VOFF1, PRE)
#define MOV4(a, b, c, d, s) "v_mov_b32 v" #a ", " s "\n\tv_mov_b32 v" #b ", " s "\n\tv_mov_b32 v" #c ", " s "\n\tv_mov_b32 v" #d ", " s "\n\t"
#define AISSUE(VOFF)                                             \
    "s_add_u32 m0, %[ro], %[lw]\n\t"                             \
    "s_add_u32 %[ro], %[ro], 0x4000\n\t"                         \
    "global_load_lds_dwordx4 " VOFF ", %[ks] sc1\n\t"                \
    "s_add_u32 m0, m0, 0x2000\n\t"                               \
    "s_and_b32 %[ro], %[ro], 0xffff\n\t"                         \
    "global_load_lds_dwordx4 " VOFF ", %[vs] sc1\n\t"                \
    "v_add_u32 " VOFF ", 0x2000, " VOFF "\n\t"

#define TM(n)
#define TACC
#define TOUT
#define TCLB
DI unsigned attn_issue0(const char* ksrc, const char* vsrc, unsigned lw, int lane) {
    unsigned go = lane * 16, ro = 0;
    asm volatile(AISSUE("%[go]") AISSUE("%[go]") AISSUE("%[go]")
                 : [go] "+v"(go), [ro] "+s"(ro) : [ks] "s"(ksrc), [vs] "s"(vsrc), [lw] "s"(lw) : "memory", "scc");
    return go;
}
DI void attn_pass(f32x16& oa, f32x16& ob, u32x4 qa, u32x4 qb, float negm, unsigned go, const char* ksrc, const char* vsrc, unsigned lw, int kh ST_ARG) {
    unsigned ro = 0xc000;
    asm volatile(
        "s_mov_b32 s40, 0xffff\n\t"
        "s_mov_b32 s41, 0xffff\n\t"
        "v_and_b32 v12, 0x3ff, v14\n\t"
        "v_and_b32 v13, 0xf0, v12\n\t"
        "v_lshrrev_b32 v15, 1, v12\n\t"
        "v_and_b32 v15, 0x100, v15\n\t"
        "v_add3_u32 v13, v13, v15, %[kv]\n\t"
        "v_and_b32 v15, 0x1b0, v12\n\t"
        "v_cmp_eq_u32 vcc, 0x100, v15\n\t"
        "v_add_u32 v12, %[kk], v12\n\t"
        "v_mov_b32 v15, 0x3f803f80\n\t"
        "v_cndmask_b32 v15, 0, v15, vcc\n\t"
        MOV4(16, 17, 18, 19, "%[nm]") MOV4(20, 21, 22, 23, "%[nm]") MOV4(24, 25, 26, 27, "%[nm]") MOV4(28, 29, 30, 31, "%[nm]")
        MOV4(64, 65, 66, 67, "0") MOV4(68, 69, 70, 71, "0") MOV4(72, 73, 74, 75, "0") MOV4(76, 77, 78, 79, "0")
        MOV4(80, 81, 82, 83, "0") MOV4(84, 85, 86, 87, "0") MOV4(88, 89, 90, 91, "0") MOV4(92, 93, 94, 95, "0")
        MOV4(100, 101, 102, 103, "0")
        MOV4(112, 113, 114, 115, "v15") MOV4(116, 117, 118, 119, "v15") MOV4(120, 121, 122, 123, "v15") MOV4(124, 125, 126, 127, "v15")
        "s_mov_b32 s42, 0\n\t"
        "s_waitcnt vmcnt(4)\n\t"
        "s_barrier\n\t"
        "s_setprio 0\n\t"
        "ds_read_b128 v[104:107], v12\n\t"
        "ds_read_b128 v[108:111], v12 offset:1024\n\t"
        "s_mov_b64 exec, s[40:41]\n\t"
        "ds_read_b128 v[112:115], v13\n\t"
        "ds_read_b128 v[116:119], v13 offset:512\n\t"
        "s_mov_b64 exec, -1\n\t"
        "s_waitcnt lgkmcnt(3)\n\t"
        MF(R_SA, "v[104:107]", "v[4:7]", R_NM)
        "s_nop 15\n\t"
        "LA_loop%=:\n\t"
        ATILE_E(2048, 1024, 1536, "")
        ATILE_O(3072, 2048, 2560, "")
        ATILE_E(0, 3072, 3584,
                TM(48)
                "s_cmp_lt_u32 s42, 6\n\t"
                "s_waitcnt vmcnt(2)\n\t"
                "s_cbranch_scc1 LA_w%=\n\t"
                "s_waitcnt vmcnt(0)\n\t"
                "LA_w%=:\n\t"
                TM(50)
                "s_barrier\n\t"
                TM(52)
                "v_add_u32 v12, 0x4000, v12\n\t"
                "s_cmp_lt_u32 s42, 5\n\t"
                "v_and_b32 v12, 0xffff, v12\n\t"
                "s_cbranch_scc0 LA_n%=\n\t"
                "s_setprio 3\n\t"
                AISSUE("v14")
                "s_setprio 0\n\t"
                "LA_n%=:\n\t" TM(54) TACC)
        ATILE_O(1024, 0, 512, "v_add_u32 v13, 0x4000, v13\n\ts_add_u32 s42, s42, 1\n\tv_and_b32 v13, 0xffff, v13\n\ts_cmp_lt_u32 s42, 8\n\t")
        "s_cbranch_scc1 LA_loop%=\n\t"
        "s_waitcnt lgkmcnt(0)\n\t"
        "s_nop 1\n\t"
        MF(R_OB, "v[124:127]", "v[100:103]", R_OB)
        "s_nop 15\n\t"
        : "={v[64:79]}"(oa), "={v[80:95]}"(ob), "+{v14}"(go), [ro] "+s"(ro) TOUT
        : "{v[4:7]}"(qa), "{v[8:11]}"(qb), [nm] "v"(negm), [ks] "s"(ksrc), [vs] "s"(vsrc), [lw] "s"(lw), [kk] "s"(4096 * kh), [kv] "s"(8192 + 4096 * kh)
        : "memory", "scc", "vcc", TCLB "s40", "s41", "s42", "v12", "v13", "v15",
          "v16", "v17", "v18", "v19", "v20", "v21", "v22", "v23", "v24", "v25", "v26", "v27", "v28", "v29", "v30", "v31",
          "v32", "v33", "v34", "v35", "v36", "v37", "v38", "v39", "v40", "v41", "v42", "v43", "v44", "v45", "v46", "v47",
          "v48", "v49", "v50", "v51", "v52", "v53", "v54", "v55", "v56", "v57", "v58", "v59", "v60", "v61", "v62", "v63",
          "v96", "v97", "v98", "v99", "v100", "v101", "v102", "v103", "v104", "v105", "v106", "v107", "v108", "v109", "v110", "v111",
          "v112", "v113", "v114", "v115", "v116", "v117", "v118", "v119", "v120", "v121", "v122", "v123", "v124", "v125", "v126", "v127");
}

DI float sumsq8(u32x4 q) {
    float n = 0.f;
#pragma unroll
    for (int i = 0; i < 4; ++i) { const float x = bflo(q[i]), y = bfhi(q[i]); n += x * x + y * y; }
    return swap32_add(n);
}

DI void attn_role(const char* x, const char* wqkv, char* ws, char* lds, int a, int w) {
    const int kh = w >> 2;
    const int head = 4 * (a & 7) + ((a >> 3) & 3), qt = 8 * (a >> 5) + 2 * (w & 3);
    ST_DECL; ST(0);
    __builtin_amdgcn_s_setprio(3);
    const unsigned tag = __builtin_amdgcn_readfirstlane(ld4(ws + WS_EPOCH)) * 0x9E3779B1u + 0x7F4A7C15u;
    {
        const int m = 4 * ((a & 7) >> 1) + ((a >> 3) & 3), p = 8 * (a & 1) + (a >> 5);
        if (w >= 4) {
            qkv_loader(x, wqkv, m, p, w - 4 ST_PASS);
            ST(4);
            if (w == 4) {
                const int lane = lane_id(), hp = head >> 1;
                const int fp = lane < 16 ? (16 + hp) / 3 : lane < 32 ? (32 + hp) / 3 : hp / 3;
                const int fm = lane < 32 ? (lane & 15) : 2 * (a >> 5) + (lane & 1);
                __builtin_amdgcn_s_sleep(QSLEEP);
                wait_flags<4>(ws + WS_FQ, (fp * 16 + fm) * 128, tag);
            }
        } else if (p < 10) qkv_body<3>(ws, lds, m, p, w, tag ST_PASS);
        else if (p == 10) qkv_body<2>(ws, lds, m, p, w, tag ST_PASS);
        else qkv_body<0>(ws, lds, m, p, w, tag ST_PASS);
        ST(5);
        wgbar();
        ST(6);
    }
    const char* ksrc = ws + WS_K + (size_t)head * 65536 + w * 1024;
    const char* vsrc = ws + WS_V + (size_t)head * 65536 + w * 1024;
    const char* qsrc = ws + WS_Q + (size_t)(head * 64 + qt) * 1024;
    u32x4 qa, qb;
    float negm;
    unsigned go;
    {
        const int lane = lane_id();
        float kn;
        asm volatile("global_load_dwordx4 %0, %3, %5 sc1\n\tglobal_load_dwordx4 %1, %3, %5 offset:1024 sc1\n\tglobal_load_dword %2, %4, %6 sc1"
                     : "=&v"(qa), "=&v"(qb), "=&v"(kn) : "v"(lane * 16), "v"(lane * 4), "s"(qsrc), "s"(ws + WS_KN + head * 256) : "memory");
        go = attn_issue0(ksrc, vsrc, w * 1024, lane);
        asm volatile("s_waitcnt vmcnt(6)" : "+v"(qa), "+v"(qb), "+v"(kn)::"memory");
        if (w == 0) st4(ws + WS_FA + a * 128, tag);
        negm = -1.001f * __builtin_amdgcn_sqrtf(fmaxf(sumsq8(qa), sumsq8(qb)) * wave_max(kn));
    }
#pragma clang loop unroll(disable)
    for (int att = 0;; ++att) {
        f32x16 oa, ob;
        ST(1);
        attn_pass(oa, ob, qa, qb, negm, go, ksrc, vsrc, w * 1024, kh ST_PASS);
        ST(2);
        const int lane = lane_id();
        LAS char* ex = (LAS char*)lds + 16384 + w * 4096 + lane * 16;
        LAS const char* exr = (LAS const char*)lds + 16384 + (w ^ 4) * 4096 + lane * 16;
        if (kh) { const f32x16 t = oa; oa = ob; ob = t; }
        *(LAS f32x4*)ex = f32x4{ob[0], ob[1], ob[2], ob[3]};
        *(LAS f32x4*)(ex + 1024) = f32x4{ob[4], ob[5], ob[6], ob[7]};
        const bool bad = !__all(oa[8] > 5.4e-20f && oa[8] < 1e30f && ob[8] > 5.4e-20f && ob[8] < 1e30f);
        *(LAS f32x4*)(ex + 2048) = f32x4{ob[8], 0.f, 0.f, 0.f};
        if (lane == 0) ((LAS unsigned*)((LAS char*)lds + 65536))[w] = bad;
        wgbar();
        const f32x4 t0 = *(LAS const f32x4*)exr, t1 = *(LAS const f32x4*)(exr + 1024), t2 = *(LAS const f32x4*)(exr + 2048);
        const u32x4 v0 = *(LAS const u32x4*)((LAS char*)lds + 65536), v1 = *(LAS const u32x4*)((LAS char*)lds + 65536 + 16);
        const float rinv = __builtin_amdgcn_rcpf(oa[8] + t2[0]);
        u32x4 ov;
        ov[0] = cvtpk((oa[0] + t0[0]) * rinv, (oa[1] + t0[1]) * rinv);
        ov[1] = cvtpk((oa[2] + t0[2]) * rinv, (oa[3] + t0[3]) * rinv);
        ov[2] = cvtpk((oa[4] + t1[0]) * rinv, (oa[5] + t1[1]) * rinv);
        ov[3] = cvtpk((oa[6] + t1[2]) * rinv, (oa[7] + t1[3]) * rinv);
        const bool redo = att == 0 && __builtin_amdgcn_readfirstlane(v0[0] | v0[1] | v0[2] | v0[3] | v1[0] | v1[1] | v1[2] | v1[3]);
        if (att == 0 ? !redo : att == 1 + kh) {
            st16(ws + WS_O + (size_t)((qt + kh) * 32 + head) * 1024, lane * 16, ov);
            drain();
            st4(ws + WS_FO + ((qt + kh) * 32 + head) * 128, tag);
        }
        asm volatile("" : "+s"(att));
        if (att == 2 || (att == 0 && !redo)) break;
        wgbar();
        asm volatile("global_load_dwordx4 %0, %1, %2 sc1\n\ts_waitcnt vmcnt(0)" : "=v"(qa) : "v"(lane * 16), "s"(qsrc + att * 1024) : "memory");
        qb = qa;
        float mx = -INFINITY;
        const char* kt = ws + WS_K + (size_t)head * 65536;
#pragma clang loop unroll(disable)
        for (int t = 0; t < 64; ++t) {
            bf16x8 kf;
            asm volatile("global_load_dwordx4 %0, %1, %2 sc1\n\ts_waitcnt vmcnt(0)" : "=v"(kf) : "v"(lane * 16), "s"(kt + t * 1024) : "memory");
            const f32x16 s = MFMA32(kf, __builtin_bit_cast(bf16x8, qa), f32x16{});
#pragma unroll
            for (int i = 0; i < 16; ++i) mx = fmaxf(mx, s[i]);
        }
        negm = -swap32_max(mx);
        go = attn_issue0(ksrc, vsrc, w * 1024, lane);
    }
    ST(3); ST_OUT(0);
}

#define BS1 "s_sleep 1\n\t"
#define BS2 "s_sleep 1\n\t"
#define SMM_WA(WF, DF) MF("v[64:79]", WF, DF, "v[64:79]")
#define SMM_WB(WF, DF) MF("v[64:79]", DF, WF, "v[64:79]")
#define SISSUE(SLOT)                                                       \
    "s_add_u32 m0, %[lo], " #SLOT "*16384\n\t"                              \
    "s_add_u32 s45, s42, %[wst]\n\t"                                        \
    "buffer_load_dwordx4 %[g0], %[rw], s42 offen lds\n\t"                   \
    "s_add_u32 s46, s43, 4096\n\t"                                          \
    "s_add_u32 m0, %[lo], " #SLOT "*16384+8192\n\t"                         \
    "s_cmp_eq_u32 s43, %[de]\n\t"                                           \
    "buffer_load_dwordx4 %[d0], %[rd], s43 offen sc1 lds\n\t"               \
    "s_cselect_b32 s42, 0, s45\n\t"                                         \
    "s_cselect_b32 s43, 0, s46\n\t"
#define SSTAGE(SLOT, VM, ISS, RW0, RW1, RD, RW2, RW3, RE, PW0, PD0, PW1, PD1, MM, TD) \
    "s_waitcnt vmcnt(" #VM ") lgkmcnt(0)\n\t"                                         \
    "s_barrier\n\t"                                                                   \
    "ds_read_b64_tr_b16 " RW0 ", %[t] offset:" #SLOT "*16384\n\t"                     \
    "ds_read_b64_tr_b16 " RW1 ", %[t] offset:" #SLOT "*16384+" #TD "\n\t"             \
    "ds_read_b128 " RD ", %[dl] offset:" #SLOT "*16384\n\t"                           \
    "ds_read_b64_tr_b16 " RW2 ", %[t] offset:" #SLOT "*16384+2048\n\t"                \
    "ds_read_b64_tr_b16 " RW3 ", %[t] offset:" #SLOT "*16384+2048+" #TD "\n\t"        \
    "ds_read_b128 " RE ", %[dl] offset:" #SLOT "*16384+1024\n\t"                      \
    "s_cbranch_vccz 3f\n\t"                                                           \
    BS1                                                                               \
    "3:\n\t"                                                                          \
    MM(PW0, PD0) ISS BS2 MM(PW1, PD1)
#define SST_E(SLOT, VM, ISS, MM, TD) SSTAGE(SLOT, VM, ISS, "v[32:33]", "v[34:35]", "v[36:39]", "v[40:41]", "v[42:43]", "v[44:47]", \
                                            "v[48:51]", "v[52:55]", "v[56:59]", "v[60:63]", MM, TD)
#define SST_O(SLOT, VM, ISS, MM, TD) SSTAGE(SLOT, VM, ISS, "v[48:49]", "v[50:51]", "v[52:55]", "v[56:57]", "v[58:59]", "v[60:63]", \
                                            "v[32:35]", "v[36:39]", "v[40:43]", "v[44:47]", MM, TD)
#define SGEMM_ASM(MM, TD, NLOOP, TAIL)                                                                            \
    asm volatile(                                                                                                 \
        "s_mul_i32 s42, %[rot], %[wst]\n\t"                                                                       \
        "s_lshl_b32 s43, %[rot], 12\n\t"                                                                          \
        "s_mov_b32 s44, " #NLOOP "\n\t"                                                                           \
        "s_mov_b32 vcc_lo, %[hi]\n\ts_mov_b32 vcc_hi, 0\n\t"                                                                                \
        SISSUE(0) SISSUE(1) SISSUE(2)                                                                             \
        MOV4(64, 65, 66, 67, "0") MOV4(68, 69, 70, 71, "0") MOV4(72, 73, 74, 75, "0") MOV4(76, 77, 78, 79, "0")   \
        MOV4(48, 49, 50, 51, "0") MOV4(52, 53, 54, 55, "0") MOV4(56, 57, 58, 59, "0") MOV4(60, 61, 62, 63, "0")   \
        "LS_loop%=:\n\t"                                                                                          \
        SST_E(0, 4, SISSUE(3), MM, TD) SST_O(1, 4, SISSUE(0), MM, TD)                                             \
        SST_E(2, 4, SISSUE(1), MM, TD) SST_O(3, 4, SISSUE(2), MM, TD)                                             \
        "s_sub_u32 s44, s44, 1\n\t"                                                                               \
        "s_cmp_lg_u32 s44, 0\n\t"                                                                                 \
        "s_cbranch_scc1 LS_loop%=\n\t"                                                                            \
        TAIL                                                                                                      \
        "s_waitcnt lgkmcnt(0)\n\t"                                                                                \
        MM("v[48:51]", "v[52:55]") MM("v[56:59]", "v[60:63]")                                                     \
        "s_nop 15\n\t"                                                                                            \
        : "={v[64:79]}"(acc)                                                                                      \
        : [rot] "s"(rot), [wst] "s"(wst), [rw] "s"(rw), [rd] "s"(rd), [lo] "s"(w * 1024), [de] "s"((n - 1) * 4096), \
          [hi] "s"(hi), [g0] "v"(g0), [d0] "v"(d0), [t] "v"(t), [dl] "v"(dl)                                      \
        : "memory", "scc", "vcc", "s42", "s43", "s44", "s45", "s46",                                                            \
          "v32", "v33", "v34", "v35", "v36", "v37", "v38", "v39", "v40", "v41", "v42", "v43", "v44", "v45", "v46", "v47", \
          "v48", "v49", "v50", "v51", "v52", "v53", "v54", "v55", "v56", "v57", "v58", "v59", "v60", "v61", "v62", "v63")
DI u32x4 srd(const char* base, unsigned bytes) {
    return u32x4{(unsigned)(uintptr_t)base, (unsigned)((uintptr_t)base >> 32) & 0xffffu, bytes, 0x00020000u};
}

template <int MODE>
DI f32x16 sgemm(const char* wbase, unsigned wbytes, int wrow, const char* dbase, int dblk, int rot, int w) {
    const int lane = lane_id(), row = lane >> 3, wb = w & 1, kh = (w >> 1) & 1, db = w >> 2;
    const unsigned g0 = (8 * w + row) * wrow + (((lane & 7) ^ (((row >> 1) & 1) << 2)) << 4);
    const unsigned d0 = (w >> 2) * dblk + (w & 3) * 1024 + lane * 16;
    const u32x4 rw = srd(wbase, wbytes), rd = srd(dbase, 2 * dblk);
    const int q = (lane & 15) >> 2, h2 = lane >> 5;
    const unsigned t = (32 * kh + (MODE == 0 ? 8 : 4) * h2 + q) * 128 + ((wb ^ ((q >> 1) & 1)) << 6) + ((lane >> 4) & 1) * 32 + (lane & 3) * 8;
    const unsigned dl = 8192 + (db * 4 + 2 * kh) * 1024 + lane * 16;
    const unsigned wst = 64 * wrow;
    const int hi = db;
    const int n = MODE == 0 ? 32 : 22;
    f32x16 acc;
    if (MODE == 0)
        SGEMM_ASM(SMM_WA, 512, 7, SST_E(0, 4, SISSUE(3), SMM_WA, 512) SST_O(1, 4, "", SMM_WA, 512) SST_E(2, 2, "", SMM_WA, 512) SST_O(3, 0, "", SMM_WA, 512));
    else
        SGEMM_ASM(SMM_WB, 1024, 4, SST_E(0, 4, SISSUE(3), SMM_WB, 1024) SST_O(1, 4, SISSUE(0), SMM_WB, 1024) SST_E(2, 4, SISSUE(1), SMM_WB, 1024)
                                   SST_O(3, 4, "", SMM_WB, 1024) SST_E(0, 2, "", SMM_WB, 1024) SST_O(1, 0, "", SMM_WB, 1024));
    return acc;
}
DI void pair_sum(const f32x16& acc, float (&v)[8], char* lds, int w) {
    const int lane = lane_id(), kh = (w >> 1) & 1;
    LAS char* ex = (LAS char*)lds + 65536 + w * 2048 + lane * 16;
    LAS const char* exr = (LAS const char*)lds + 65536 + (w ^ 2) * 2048 + lane * 16;
    if (kh) {
        *(LAS f32x4*)ex = f32x4{acc[0], acc[1], acc[2], acc[3]}; *(LAS f32x4*)(ex + 1024) = f32x4{acc[4], acc[5], acc[6], acc[7]};
#pragma unroll
        for (int k = 0; k < 8; ++k) v[k] = acc[8 + k];
    } else {
        *(LAS f32x4*)ex = f32x4{acc[8], acc[9], acc[10], acc[11]}; *(LAS f32x4*)(ex + 1024) = f32x4{acc[12], acc[13], acc[14], acc[15]};
#pragma unroll
        for (int k = 0; k < 8; ++k) v[k] = acc[k];
    }
    wgbar();
    const f32x4 t0 = *(LAS const f32x4*)exr, t1 = *(LAS const f32x4*)(exr + 1024);
#pragma unroll
    for (int k = 0; k < 4; ++k) { v[k] += t0[k]; v[4 + k] += t1[k]; }
}

DI float gelu_tanh(float u) {
    const float z = u * (1.f + 0.044715f * u * u) * (-2.f * 0.7978845608028654f * 1.4426950408889634f);
    return u * __builtin_amdgcn_rcpf(1.f + __builtin_amdgcn_exp2f(z));
}

#define WRD(R0, R1, R2, R3, STEP)                                                    \
    "ds_read_b64_tr_b16 " R0 ", %[t0] offset:" #STEP "*2048\n\t"                      \
    "ds_read_b64_tr_b16 " R1 ", %[t0] offset:" #STEP "*2048+1024\n\t"                 \
    "ds_read_b64_tr_b16 " R2 ", %[t1] offset:" #STEP "*2048\n\t"                      \
    "ds_read_b64_tr_b16 " R3 ", %[t1] offset:" #STEP "*2048+1024\n\t"
#define WRD0(S) WRD("v[32:33]", "v[34:35]", "v[36:37]", "v[38:39]", S)
#define WRD1(S) WRD("v[40:41]", "v[42:43]", "v[44:45]", "v[46:47]", S)
#define WRD2(S) WRD("v[48:49]", "v[50:51]", "v[52:53]", "v[54:55]", S)
#define WRD3(S) WRD("v[56:57]", "v[58:59]", "v[60:61]", "v[62:63]", S)
#define WMM(WAIT, W0, W1, O) "s_waitcnt " WAIT "\n\t" MF("v[96:111]", W0, O, "v[96:111]") MF("v[112:127]", W1, O, "v[112:127]")
#define OLD(R, V, OFF) "global_load_dwordx4 " R ", " V ", %[o] offset:" #OFF " sc1\n\t"
DI void wo_gemm(f32x16& a0, f32x16& a1, f32x4& k0, f32x4& k1, float& k2, const char* osrc, int w) {
    const int lane = lane_id();
    const int q = (lane & 15) >> 2, h2 = lane >> 5;
    const unsigned tb = (w >> 1) * 16384 + (4 * h2 + q) * 128 + ((lane >> 4) & 1) * 32 + (lane & 3) * 8;
    const unsigned t0 = tb + ((q >> 1) & 1) * 64, t1 = tb + (((q >> 1) & 1) ^ 1) * 64;
    const unsigned d0 = lane * 16, d1 = d0 + 4096;
    asm volatile(
        OLD("v[64:67]", "%[d0]", 0) OLD("v[68:71]", "%[d0]", 1024) OLD("v[72:75]", "%[d0]", 2048) OLD("v[76:79]", "%[d0]", 3072)
        OLD("v[80:83]", "%[d1]", 0) OLD("v[84:87]", "%[d1]", 1024) OLD("v[88:91]", "%[d1]", 2048) OLD("v[92:95]", "%[d1]", 3072)
        WRD0(0) WRD1(1) WRD2(2) WRD3(3)
        "s_waitcnt vmcnt(7) lgkmcnt(12)\n\t"
        MF("v[96:111]", "v[32:35]", "v[64:67]", "0") MF("v[112:127]", "v[36:39]", "v[64:67]", "0")
        WRD0(4)
        WMM("vmcnt(6) lgkmcnt(12)", "v[40:43]", "v[44:47]", "v[68:71]") WRD1(5)
        WMM("vmcnt(5) lgkmcnt(12)", "v[48:51]", "v[52:55]", "v[72:75]") WRD2(6)
        WMM("vmcnt(4) lgkmcnt(12)", "v[56:59]", "v[60:63]", "v[76:79]") WRD3(7)
        WMM("vmcnt(3) lgkmcnt(12)", "v[32:35]", "v[36:39]", "v[80:83]")
        WMM("vmcnt(2) lgkmcnt(0)", "v[40:43]", "v[44:47]", "v[84:87]")
        "s_barrier\n\t"
        WMM("vmcnt(1)", "v[48:51]", "v[52:55]", "v[88:91]")
        WMM("vmcnt(0)", "v[56:59]", "v[60:63]", "v[92:95]")
        "s_nop 15\n\t"
        : "={v[96:111]}"(a0), "={v[112:127]}"(a1),
          "+{v[12:15]}"(k0), "+{v[16:19]}"(k1), "+{v20}"(k2)
        : [o] "s"(osrc), [d0] "v"(d0), [d1] "v"(d1), [t0] "v"(t0), [t1] "v"(t1)
        : "memory",
          "v32", "v33", "v34", "v35", "v36", "v37", "v38", "v39", "v40", "v41", "v42", "v43", "v44", "v45", "v46", "v47",
          "v48", "v49", "v50", "v51", "v52", "v53", "v54", "v55", "v56", "v57", "v58", "v59", "v60", "v61", "v62", "v63",
          "v64", "v65", "v66", "v67", "v68", "v69", "v70", "v71", "v72", "v73", "v74", "v75", "v76", "v77", "v78", "v79",
          "v80", "v81", "v82", "v83", "v84", "v85", "v86", "v87", "v88", "v89", "v90", "v91", "v92", "v93", "v94", "v95");
}
DI f32x4 sub4(const f32x16& a, int g) { return f32x4{a[4 * g], a[4 * g + 1], a[4 * g + 2], a[4 * g + 3]}; }

DI void mixer_role(const char* x, const char* gw, const char* wo, const char* w1, const char* b1, const char* w2, const char* b2,
                   char* ws, char* out, char* lds, int bi, int w) {
    const int j = bi & 7, i = bi >> 3;
    __builtin_amdgcn_s_setprio(3);
    ST_DECL; ST(0);
    const unsigned tag = __builtin_amdgcn_readfirstlane(ld4(ws + WS_EPOCH)) * 0x9E3779B1u + 0x7F4A7C15u;
    {
        const int lane = lane_id(), h2 = lane >> 5, tid = 64 * w + lane;
        const u32x4 xr = *(GAS const u32x4*)(x + (size_t)(8 * bi + w) * 1024 + lane * 16);
        const u32x4 gr = *(GAS const u32x4*)(gw + lane * 16);
        float v[8], gg[8];
#pragma unroll
        for (int k = 0; k < 4; ++k) { v[2 * k] = bflo(xr[k]); v[2 * k + 1] = bfhi(xr[k]); gg[2 * k] = bflo(gr[k]); gg[2 * k + 1] = bfhi(gr[k]); }
        float s1 = 0.f, s3 = 0.f;
#pragma unroll
        for (int k = 0; k < 8; ++k) { s1 += v[k]; s3 += v[k] * gg[k]; }
        const float mu = wave_sum(s1) * (1.f / 512.f);
        s3 = wave_sum(s3);
        float s2 = 0.f;
#pragma unroll
        for (int k = 0; k < 8; ++k) { v[k] -= mu; s2 += v[k] * v[k]; }
        const float rs = __builtin_amdgcn_rsqf(wave_sum(s2) * (1.f / 512.f) + 1e-5f);
        u32x4 hv;
#pragma unroll
        for (int k = 0; k < 4; ++k) hv[k] = cvtpk(v[2 * k] * rs, v[2 * k + 1] * rs);
        *(LAS u32x4*)((LAS char*)lds + w * 1024 + lane * 16) = hv;
        const float g = __builtin_amdgcn_rcpf(1.f + __builtin_amdgcn_exp2f(-1.4426950408889634f * s3));
        st4(ws + WS_G + (8 * bi + w) * 4, uni(g));
        wgbar();
        LAS const unsigned short* lb = (LAS const unsigned short*)lds + tid;
#pragma unroll
        for (int k = 0; k < 4; ++k) hv[k] = (unsigned)lb[(2 * k) * 512] | ((unsigned)lb[(2 * k + 1) * 512] << 16);
        st16(ws + WS_H + (size_t)(2 * w * 128 + (bi >> 1)) * 1024 + (bi & 1) * 512, h2 * 131072 + (lane & 31) * 16, hv);
        drain();
        wgbar();
        if (w == 0) st4(ws + WS_FL + bi * 128, tag);
    }
    __builtin_amdgcn_s_setprio(2);
    ST(6);
    const int wb = w & 1, kh = (w >> 1) & 1, db = w >> 2;
    if (i < 22) {
        const int L = (j >> 1) * 22 + i, ft = L >> 2, dt = 4 * (j & 1) + (L & 3);
        if (w == 0) { __builtin_amdgcn_s_sleep(LSLEEP); wait_flags<8>(ws + WS_FA + bi * 128, 0, tag); }
        if (w >= 4) { __builtin_amdgcn_s_sleep(LSLEEP); wait_flags<8>(ws + WS_FL + (w - 4) * 8192, lane_id() * 128, tag); }
        wgbar();
        ST(7);
        const f32x16 acc = sgemm<0>(w1 + ft * 128, 2048 * 2752 - ft * 128, 2752, ws + WS_H + (size_t)2 * dt * 131072, 131072, (4 * dt + 10 * ft) & 31, w);
        ST(1);
        const bool live = (ft < 21 || wb == 0);
        const int lane = lane_id(), f0 = 64 * ft + 32 * wb + 16 * kh + 4 * (lane >> 5);
        u32x2 bl = {0u, 0u}, bh = {0u, 0u};
        if (live) { bl = *(GAS const u32x2*)(b1 + f0 * 2); bh = *(GAS const u32x2*)(b1 + f0 * 2 + 16); }
        float v[8];
        pair_sum(acc, v, lds, w);
        u32x4 o;
        o[0] = cvtpk(gelu_tanh(v[0] + bflo(bl[0])), gelu_tanh(v[1] + bfhi(bl[0])));
        o[1] = cvtpk(gelu_tanh(v[2] + bflo(bl[1])), gelu_tanh(v[3] + bfhi(bl[1])));
        o[2] = cvtpk(gelu_tanh(v[4] + bflo(bh[0])), gelu_tanh(v[5] + bfhi(bh[0])));
        o[3] = cvtpk(gelu_tanh(v[6] + bflo(bh[1])), gelu_tanh(v[7] + bfhi(bh[1])));
        if (live) st16(ws + WS_U + (size_t)((2 * dt + db) * 86 + 4 * ft + 2 * wb + kh) * 1024, lane * 16, o);
        drain();
        wgbar();
        if (w == 0) st4(ws + WS_FU + (dt * 32 + ft) * 128, tag);
    }
    ST(2);
    const int st = 8 * (j >> 1) + (i & 7), dt = 4 * (j & 1) + (i >> 3);
    if (w == 0) { const int lane = lane_id(); wait_flags<8>(ws + WS_FU + dt * 4096, (lane < 22 ? lane : 0) * 128, tag); }
    wgbar();
    ST(3);
    f32x4 bp0, bp1;
    float g;
    {
        const f32x16 acc = sgemm<1>(w2 + st * 128, 1376 * 4096 - st * 128, 4096, ws + WS_U + (size_t)2 * dt * 86 * 1024, 86 * 1024, (2 * st + 5 * dt) & 15, w);
        ST(4);
        const int sg = 64 * st + 32 * wb + (lane_id() & 31);
        g = __uint_as_float(ld4(ws + WS_G + sg * 4));
        const float bb = bflo(((GAS const unsigned short*)b2)[sg]);
        float v[8];
        pair_sum(acc, v, lds, w);
        const int lane = lane_id(), row = lane >> 3;
        const unsigned g0 = row * 1024 + (((lane & 7) ^ (((row >> 1) & 1) << 2)) << 4);
        const char* wsrc = wo + (size_t)w * 65536 + dt * 128;
        bp0 = (f32x4{v[0], v[1], v[2], v[3]} + bb) * (1.f - g);
        bp1 = (f32x4{v[4], v[5], v[6], v[7]} + bb) * (1.f - g);
        asm volatile("" : "+v"(bp0), "+v"(bp1));
#pragma unroll
        for (int k = 0; k < 8; ++k) glds(w * 8192 + k * 1024, g0, wsrc + k * 8192);
    }
    if (bi == 0 && w == 1) {
#pragma clang loop unroll(disable)
        for (int k = 0; k < 4; ++k) {
            const int lane = lane_id();
            wait_flags<8>(ws + WS_FQ + k * 8192, lane * 128, tag);
            wait_flags<8>(ws + WS_FL + k * 8192, lane * 128, tag);
        }
        st4(ws + WS_EPOCH, tag);
    }
    if (w == 0) {
        __builtin_amdgcn_s_setprio(0);
        wait_flags<4>(ws + WS_FO + st * 8192, lane_id() * 128, tag);
        __builtin_amdgcn_s_setprio(2);
    }
    drain();
    wgbar();
    ST(5);
    f32x16 a0, a1;
    wo_gemm(a0, a1, bp0, bp1, g, ws + WS_O + (size_t)((2 * st + wb) * 32 + 8 * (w >> 1)) * 1024, w);
    {
        const int lane = lane_id(), kq = w >> 1;
        LAS char* ex = (LAS char*)lds + w * 8192 + lane * 16;
#pragma unroll
        for (int k = 0; k < 4; ++k) { *(LAS f32x4*)(ex + k * 1024) = sub4(a0, k); *(LAS f32x4*)(ex + 4096 + k * 1024) = sub4(a1, k); }
        wgbar();
        LAS const char* exr = (LAS const char*)lds + wb * 8192 + kq * 2048 + lane * 16;
        f32x4 s0 = *(LAS const f32x4*)exr, s1 = *(LAS const f32x4*)(exr + 1024);
#pragma unroll
        for (int k = 1; k < 4; ++k) { s0 += *(LAS const f32x4*)(exr + k * 16384); s1 += *(LAS const f32x4*)(exr + k * 16384 + 1024); }
        s0 = s0 * g + bp0;
        s1 = s1 * g + bp1;
        const auto r0 = __builtin_amdgcn_permlane32_swap(cvtpk(s0[0], s0[1]), cvtpk(s1[0], s1[1]), false, false);
        const auto r1 = __builtin_amdgcn_permlane32_swap(cvtpk(s0[2], s0[3]), cvtpk(s1[2], s1[3]), false, false);
        *(GAS u32x4*)(out + (size_t)(64 * st + 32 * wb) * 1024 + (64 * dt + 16 * kq) * 2 + (lane & 31) * 1024 + (lane >> 5) * 16) = u32x4{r0[0], r1[0], r0[1], r1[1]};
    }
    ST_OUT(0);
}

constexpr int MK_LDS = 81920;
__global__ __launch_bounds__(512, 4) void mk(const char* x, const char* wqkv, const char* wo, const char* w1, const char* b1,
                                             const char* w2, const char* b2, const char* gw, char* ws, char* out) {
    extern __shared__ __attribute__((aligned(16))) char lds[];
    const int w = __builtin_amdgcn_readfirstlane(threadIdx.x >> 6);
    if (blockIdx.x < 256) attn_role(x, wqkv, ws, lds, blockIdx.x, w);
    else mixer_role(x, gw, wo, w1, b1, w2, b2, ws, out, lds, blockIdx.x - 256, w);
}

extern "C" void kernel_launch(void* const* d_in, const int* in_sizes, int n_in, void* d_out, int out_size, void* d_ws, size_t ws_size,
                              hipStream_t stream) {
    (void)hipFuncSetAttribute((const void*)mk, hipFuncAttributeMaxDynamicSharedMemorySize, MK_LDS);
    mk<<<512, 512, MK_LDS, stream>>>((const char*)d_in[0], (const char*)d_in[1], (const char*)d_in[2], (const char*)d_in[4],
                                     (const char*)d_in[5], (const char*)d_in[6], (const char*)d_in[7], (const char*)d_in[8],
                                     (char*)d_ws, (char*)d_out);
}
```

```cpp
#include <hip/hip_runtime.h>
#include <hip/hip_bf16.h>
#include <stdint.h>

typedef __attribute__((ext_vector_type(8))) short bf16x8;
typedef __attribute__((ext_vector_type(4))) short s16x4;
typedef __attribute__((ext_vector_type(16))) float f32x16;
typedef __attribute__((ext_vector_type(4))) float f32x4;
typedef __attribute__((ext_vector_type(2))) float f32x2;
typedef __attribute__((ext_vector_type(4))) unsigned u32x4;
typedef __attribute__((ext_vector_type(2))) unsigned u32x2;
#define LAS __attribute__((address_space(3)))
#define GAS __attribute__((address_space(1)))
#define DI __device__ __forceinline__
#define MFMA32(a, b, c) __builtin_amdgcn_mfma_f32_32x32x16_bf16((a), (b), (c), 0, 0, 0)

#define QSLEEP 24
#define LSLEEP 32
#define PF_QKV 0
#define PACE 4
#define PFMIN 2
#define GATE 0
#define PF_W1 0
#define PF_W2 0
#define PF_U 0
#define ROT1 (((dt1 + ft) & 1) * 16)
#define ROT2 (((st + dt) & 1) * 11)
#define QROT 6
constexpr int S_ = 2048, D_ = 512, F_ = 1376;
constexpr size_t WS_EPOCH = 0;
constexpr size_t WS_G = 4096;
constexpr size_t WS_KN = WS_G + 8192;
constexpr size_t WS_FL = 98304;
constexpr size_t WS_FQ = 32768;
constexpr size_t WS_FU = 65536;
constexpr size_t WS_FA = 393216;
constexpr size_t WS_FO = 131072;
constexpr size_t WS_Q = 1u << 20;
constexpr size_t WS_K = WS_Q + (2u << 20);
constexpr size_t WS_V = WS_K + (2u << 20);
constexpr size_t WS_H = WS_V + (2u << 20);
constexpr size_t WS_U = WS_H + (2u << 20);
constexpr size_t WS_O = WS_U + (2u << 20);

#define ST_DECL
#define ST_ARG
#define ST_PASS
#define ST(i)
#define ST_OUT(k)
DI unsigned cvtpk(float lo, float hi) {
    typedef float f32x2_t __attribute__((ext_vector_type(2)));
    typedef __bf16 bf16x2_t __attribute__((ext_vector_type(2)));
    return __builtin_bit_cast(unsigned, __builtin_convertvector(f32x2_t{lo, hi}, bf16x2_t));
}
DI float bflo(unsigned u) { return __uint_as_float(u << 16); }
DI float bfhi(unsigned u) { return __uint_as_float(u & 0xffff0000u); }
DI s16x4 trread(LAS const char* p) {
    typedef short v4i16_t __attribute__((ext_vector_type(4)));
    return __builtin_bit_cast(s16x4, __builtin_amdgcn_ds_read_tr16_b64_v4i16((LAS v4i16_t*)p));
}
DI void glds(unsigned lds, unsigned voff, const void* sbase) {
    asm volatile("s_mov_b32 m0, %0\n\ts_nop 0\n\tglobal_load_lds_dwordx4 %1, %2" ::"s"(lds), "v"(voff), "s"(sbase) : "memory");
}
template <int CTRL> DI float dpp(float v) {
    return __int_as_float(__builtin_amdgcn_update_dpp(0, __float_as_int(v), CTRL, 0xf, 0xf, true));
}
DI float rl(float v, int l) { return __int_as_float(__builtin_amdgcn_readlane(__float_as_int(v), l)); }
DI float wave_sum(float v) {
    v += dpp<0xB1>(v); v += dpp<0x4E>(v); v += dpp<0x141>(v); v += dpp<0x140>(v);
    return (rl(v, 0) + rl(v, 16)) + (rl(v, 32) + rl(v, 48));
}
DI float wave_max(float v) {
    v = fmaxf(v, dpp<0xB1>(v)); v = fmaxf(v, dpp<0x4E>(v)); v = fmaxf(v, dpp<0x141>(v)); v = fmaxf(v, dpp<0x140>(v));
    return fmaxf(fmaxf(rl(v, 0), rl(v, 16)), fmaxf(rl(v, 32), rl(v, 48)));
}
DI float swap32_add(float v) {
    auto rr = __builtin_amdgcn_permlane32_swap(__float_as_uint(v), __float_as_uint(v), false, false);
    return __uint_as_float(rr[0]) + __uint_as_float(rr[1]);
}
DI float swap32_max(float v) {
    auto rr = __builtin_amdgcn_permlane32_swap(__float_as_uint(v), __float_as_uint(v), false, false);
    return fmaxf(__uint_as_float(rr[0]), __uint_as_float(rr[1]));
}

DI int lane_id() {
    int l;
    asm volatile("v_mbcnt_lo_u32_b32 %0, -1, 0\n\tv_mbcnt_hi_u32_b32 %0, -1, %0" : "=v"(l));
    __builtin_assume(l >= 0 && l < 64);
    return l;
}
DI void st16(const char* sbase, unsigned voff, u32x4 v) {
    asm volatile("global_store_dwordx4 %0, %1, %2 sc1\n\ts_nop 1" ::"v"(voff), "v"(v), "s"(sbase) : "memory");
}
DI void st4(const char* p, unsigned v) {
    unsigned t0, t1;
    asm volatile("s_nop 1\n\tv_mov_b32 %0, %2\n\tv_mov_b32 %1, 0\n\ts_mov_b64 exec, 1\n\tglobal_store_dword %1, %0, %3 sc1\n\ts_mov_b64 exec, -1"
                 : "=&v"(t0), "=&v"(t1) : "s"(v), "s"(p) : "memory");
}
DI unsigned uni(float v) { return __builtin_amdgcn_readfirstlane(__float_as_uint(v)); }
DI unsigned ld4(const char* p) { return __hip_atomic_load((GAS const unsigned*)p, __ATOMIC_RELAXED, __HIP_MEMORY_SCOPE_AGENT); }
DI void touch(const char* sbase, unsigned voff) {
    asm volatile("s_mov_b32 m0, 0x13e00\n\ts_nop 0\n\tglobal_load_lds_dword %0, %1 sc1" ::"v"(voff), "s"(sbase) : "memory");
}
DI void drain() { asm volatile("s_waitcnt vmcnt(0)" ::: "memory"); }
DI void wgbar() { asm volatile("s_waitcnt lgkmcnt(0)\n\ts_barrier" ::: "memory"); }
template <int SLEEP>
DI void wait_flags(const char* sbase, unsigned voff, unsigned tag) {
#pragma clang loop unroll(disable)
    for (unsigned spin = 0; spin < (1u << 20); ++spin) {
        unsigned f;
        asm volatile("global_load_dword %0, %1, %2 sc1\n\ts_waitcnt vmcnt(0)" : "=v"(f) : "v"(voff), "s"(sbase) : "memory");
        if (__all(f == tag)) break;
        __builtin_amdgcn_s_sleep(SLEEP);
    }
}

constexpr int QW0 = 49152, QCNT = 73728;
DI void qkv_loader(const char* x, const char* wqkv, int m, int p, int lw ST_ARG) {
    const int lane = lane_id();
    unsigned vx[4], vw[3];
#pragma unroll
    for (int ii = 0; ii < 4; ++ii) {
        const int row = 32 * lw + 8 * ii + (lane >> 3);
        vx[ii] = row * 1024 + (((lane & 7) ^ ((row >> 1) & 7)) << 4);
    }
#pragma unroll
    for (int ii = 0; ii < 3; ++ii) {
        const int o = 16 * lane + 1024 * ii;
        vw[ii] = (16 * lw + o / 192) * 3072 + (o % 192);
    }
    const char* xb = x + (size_t)m * 131072;
    const char* wb = wqkv + p * 192;
    const int rot = (p + 2 * m) & QROT;
    auto isx = [&](int s, int slot) {
        const char* xs = xb + ((s + rot) & 7) * 128;
#pragma unroll
        for (int ii = 0; ii < 4; ++ii) glds(slot * 16384 + lw * 4096 + ii * 1024, vx[ii], xs);
    };
    auto isw = [&](int s) {
        const char* wsrc = wb + ((s + rot) & 7) * 196608;
#pragma unroll
        for (int ii = 0; ii < 3; ++ii) glds(QW0 + (s & 1) * 12288 + lw * 3072 + ii * 1024, vw[ii], wsrc);
    };
    isw(0); isx(0, 0); isw(1); isx(1, 1); isx(2, 2);
    *(LAS unsigned*)QCNT = 0u;
    asm volatile("s_waitcnt vmcnt(11) lgkmcnt(0)\n\ts_barrier" ::: "memory");
    ST(7);
    int slot = 0;
#pragma clang loop unroll(disable)
    for (int s = 1; s < 6; ++s) {
        asm volatile("s_waitcnt vmcnt(4)\n\ts_barrier" ::: "memory");
        isw(s + 1); isx(s + 2, slot);
        slot = slot == 2 ? 0 : slot + 1;
    }
    asm volatile("s_waitcnt vmcnt(4)\n\ts_barrier" ::: "memory");
    isw(7);
    asm volatile("s_waitcnt vmcnt(0)\n\ts_barrier" ::: "memory");
}

template <int NT>
DI void qkv_body(char* ws, char* lds, int m, int p, int w, unsigned tag ST_ARG) {
    const int lane = lane_id(), r = lane & 31, h2 = lane >> 5;
    f32x16 acc[3];
#pragma unroll
    for (int i = 0; i < 3; ++i) acc[i] = f32x16{};
    const int sw = (r >> 1) & 7;
    int xa[4];
#pragma unroll
    for (int st = 0; st < 4; ++st) xa[st] = (32 * w + r) * 128 + (((2 * st + h2) ^ sw) << 4);
    const int wa = QW0 + (8 * h2 + ((lane & 15) >> 2)) * 192 + ((lane >> 4) & 1) * 32 + (lane & 3) * 8;
    bf16x8 xf[4], wf[2][3];
    auto rdw = [&](LAS const char* wsl, int st, int i) {
        const s16x4 lo = trread(wsl + wa + st * 3072 + i * 64), hi = trread(wsl + wa + st * 3072 + i * 64 + 768);
        wf[st & 1][i] = __builtin_shufflevector(lo, hi, 0, 1, 2, 3, 4, 5, 6, 7);
    };
    auto mm1 = [&](int st, int i) {
        if (i < NT) acc[i] = MFMA32(wf[st & 1][i], xf[st], acc[i]);
        else acc[i] = MFMA32(xf[st], wf[st & 1][i], acc[i]);
        __builtin_amdgcn_sched_barrier(0);
    };
    auto step = [&](int st, LAS const char* xsl, LAS const char* wsl, bool bar) {
        const int sn = (st + 2) & 3;
        mm1(st, 0);
        if (bar) wgbar();
        xf[sn] = *(LAS const bf16x8*)(xsl + xa[sn]); rdw(wsl, sn, 0); __builtin_amdgcn_sched_barrier(0);
        mm1(st, 1);
        rdw(wsl, sn, 1); __builtin_amdgcn_sched_barrier(0);
        mm1(st, 2);
        rdw(wsl, sn, 2); __builtin_amdgcn_sched_barrier(0);
    };
    wgbar();
    LAS const char* xsl = (LAS const char*)lds;
    LAS const char* wsl = (LAS const char*)lds;
#pragma unroll
    for (int st = 0; st < 2; ++st) { xf[st] = *(LAS const bf16x8*)(xsl + xa[st]); rdw(wsl, st, 0); rdw(wsl, st, 1); rdw(wsl, st, 2); }
    __builtin_amdgcn_sched_barrier(0);
#pragma clang loop unroll(disable)
    for (int s = 0; s < 7; ++s) {
        step(0, xsl, wsl, false); step(1, xsl, wsl, false);
        xsl = (xsl == (LAS const char*)lds + 32768) ? (LAS const char*)lds : xsl + 16384;
        wsl = (LAS const char*)lds + ((s & 1) ? 0 : 12288);
        step(2, xsl, wsl, true); step(3, xsl, wsl, false);
    }
    step(0, xsl, wsl, false); step(1, xsl, wsl, false);
    mm1(2, 0); mm1(2, 1); mm1(2, 2); mm1(3, 0); mm1(3, 1); mm1(3, 2);
    ST(4);
    const int tile = 4 * m + w;
#pragma unroll
    for (int u = 0; u < 6; ++u) {
        const int cb = 3 * p + (u >> 1), half = u & 1, type = cb >> 4, hp = cb & 15;
        const float sc = (type == 0) ? 0.25f * 1.4426950408889634f : 1.f;
        u32x4 o;
#pragma unroll
        for (int i = 0; i < 4; ++i) o[i] = cvtpk(acc[u >> 1][8 * half + 2 * i] * sc, acc[u >> 1][8 * half + 2 * i + 1] * sc);
        if ((u >> 1) < NT) {
            st16(ws + (type ? WS_K : WS_Q) + (size_t)((2 * hp + half) * 64 + tile) * 1024, lane * 16, o);
        } else {
            st16(ws + WS_V + (size_t)(2 * hp * 64 + tile) * 1024 + half * 512, (r >> 4) * 65536 + h2 * 256 + (r & 15) * 16, o);
        }
    }
    drain();
    ST(7);
    unsigned arr = 0;
    if (lane == 0) arr = __hip_atomic_fetch_add((LAS unsigned*)QCNT, 1u, __ATOMIC_RELAXED, __HIP_MEMORY_SCOPE_WORKGROUP);
    if (__builtin_amdgcn_readfirstlane(arr) == 3) st4(ws + WS_FQ + (p * 16 + m) * 128, tag);
}

#define EXP(a) "v_exp_f32 v" #a ", v" #a "\n\t"
#define CVT(d, a, b) "v_cvt_pk_bf16_f32 v" #d ", v" #a ", v" #b "\n\t"
#define MF(D, A, B, C) "v_mfma_f32_32x32x16_bf16 " D ", " A ", " B ", " C "\n\t"
#define ASTEP(c0, c1, c2, c3, c4, c5, c6, c7, c8, c9, c10, c11, c12, c13, c14, c15, M1, M2, CL, M3)                  \
    EXP(c0) EXP(c1) M1 EXP(c2) EXP(c3) EXP(c4) EXP(c5) M2 CL                                                          \
    EXP(c6) EXP(c7) CVT(96, c0, c1) CVT(97, c2, c3) CVT(98, c4, c5) CVT(99, c6, c7) EXP(c8) EXP(c9) M3                \
    EXP(c10) EXP(c11) EXP(c12) EXP(c13) EXP(c14) EXP(c15)                                                             \
    CVT(100, c8, c9) CVT(101, c10, c11) CVT(102, c12, c13) CVT(103, c14, c15)
#define R_OA "v[64:79]"
#define R_OB "v[80:95]"
#define R_SA "v[32:47]"
#define R_SB "v[48:63]"
#define R_NM "v[16:31]"
#define ATILE(KC, KN, V0C, V1C, V0N, V1N, KOFF, VOFF0, VOFF1, PRE)                                                    \
    ASTEP(32, 33, 34, 35, 36, 37, 38, 39, 40, 41, 42, 43, 44, 45, 46, 47,                                             \
          MF(R_OB, V1N, "v[100:103]", R_OB), MF(R_SB, KC, "v[8:11]", R_NM),                                           \
          "s_waitcnt lgkmcnt(0)\n\t" PRE                                                                              \
          "ds_read_b128 " KC ", v12 offset:" #KOFF "\n\t"                                                             \
          "ds_read_b128 " V0N ", v13 offset:" #VOFF0 "\n\t"                                                           \
          "ds_read_b128 " V1N ", v13 offset:" #VOFF1 "\n\t",                                                          \
          MF(R_OA, V0C, "v[96:99]", R_OA))                                                                            \
    ASTEP(48, 49, 50, 51, 52, 53, 54, 55, 56, 57, 58, 59, 60, 61, 62, 63,                                             \
          MF(R_OA, V1C, "v[100:103]", R_OA), MF(R_SA, KN, "v[4:7]", R_NM), "", MF(R_OB, V0C, "v[96:99]", R_OB))
#define ATILE_E(KOFF, VOFF0, VOFF1, PRE) ATILE("v[104:107]", "v[108:111]", "v[112:115]", "v[116:119]", "v[120:123]", "v[124:127]", KOFF, VOFF0, VOFF1, PRE)
#define ATILE_O(KOFF, VOFF0, VOFF1, PRE) ATILE("v[108:111]", "v[104:107]", "v[120:123]", "v[124:127]", "v[112:115]", "v[116:119]", KOFF, VOFF0, VOFF1, PRE)
#define MOV4(a, b, c, d, s) "v_mov_b32 v" #a ", " s "\n\tv_mov_b32 v" #b ", " s "\n\tv_mov_b32 v" #c ", " s "\n\tv_mov_b32 v" #d ", " s "\n\t"
#define AISSUE                                                   \
    "s_add_u32 m0, s44, %[lw]\n\t"                               \
    "s_add_u32 s44, s44, 0x4000\n\t"                             \
    "buffer_load_dwordx4 v14, %[rk], s43 offen sc1 lds\n\t"      \
    "s_add_u32 m0, m0, 0x2000\n\t"                               \
    "s_and_b32 s44, s44, 0xffff\n\t"                             \
    "buffer_load_dwordx4 v14, %[rv], s43 offen sc1 lds\n\t"      \
    "s_add_u32 s43, s43, 0x2000\n\t"
DI u32x4 srd(const char* base, unsigned bytes) {
    return u32x4{(unsigned)(uintptr_t)base, (unsigned)((uintptr_t)base >> 32) & 0xffffu, bytes, 0x00020000u};
}
constexpr int A_ONES = 65536, A_VOTE = 69632 + 512, A_LSUM = 71680;
struct AttnRegs { f32x16 oa, ob, nm; u32x4 p1, v1; unsigned ka, va, go; };
DI void attn_prep(AttnRegs& R, float negm, int w) {
    const int lane = lane_id(), r = lane & 31, kh = w >> 2;
    R.ka = 4096 * kh + lane * 16;
    R.va = r < 16 ? 8192 + 4096 * kh + (lane >> 5) * 256 + r * 16 : A_ONES + (r == 16 ? 0 : r == 20 ? 64 : (r > 20 && r < 28) ? 80 : 16);
    R.go = w * 1024 + lane * 16;
#pragma unroll
    for (int i = 0; i < 16; ++i) { R.oa[i] = 0.f; R.ob[i] = 0.f; R.nm[i] = negm; }
    R.p1 = u32x4{0u, 0u, 0u, 0u};
    R.v1 = u32x4{0u, 0u, 0u, 0u};
    asm volatile("" : "+{v[64:79]}"(R.oa), "+{v[80:95]}"(R.ob), "+{v[16:31]}"(R.nm), "+{v[100:103]}"(R.p1), "+{v[124:127]}"(R.v1),
                      "+{v12}"(R.ka), "+{v13}"(R.va), "+{v14}"(R.go));
}
DI void attn_pass(AttnRegs& R, const char* qa, const char* qb, const char* ksrc, const char* vsrc, const char* go_flag, unsigned tag, int w) {
    const u32x4 rk = srd(ksrc, 65536), rv = srd(vsrc, 65536);
    asm volatile(
        "v_and_b32 v15, 0x3ff, v14\n\t"
        "s_mov_b32 s43, 0\n\t"
        "s_mov_b32 s44, 0\n\t"
        AISSUE
        "global_load_dwordx4 v[4:7], v15, %[qa] sc1\n\t"
        "global_load_dwordx4 v[8:11], v15, %[qb] sc1\n\t"
        AISSUE AISSUE
        "s_mov_b32 s40, 0xffff\n\t"
        "s_mov_b32 s41, 0xffff\n\t"
        "s_mov_b32 s42, 8\n\t"
        "s_waitcnt vmcnt(4)\n\t"
        "s_barrier\n\t"
        "ds_read_b128 v[104:107], v12\n\t"
        "ds_read_b128 v[108:111], v12 offset:1024\n\t"
        "ds_read_b128 v[112:115], v13\n\t"
        "ds_read_b128 v[116:119], v13 offset:512\n\t"
        "s_cmp_lg_u32 %[lw], 0\n\t"
        "s_cbranch_scc1 LA_g%=\n\t"
        "v_mov_b32 v15, %[tag]\n\t"
        "v_mov_b32 v32, 0\n\t"
        "s_mov_b64 exec, 1\n\t"
        "global_store_dword v32, v15, %[gf] sc1\n\t"
        "s_mov_b64 exec, -1\n\t"
        "LA_g%=:\n\t"
        "s_setprio 0\n\t"
        "s_waitcnt lgkmcnt(3)\n\t"
        MF(R_SA, "v[104:107]", "v[4:7]", R_NM)
        "s_nop 15\n\t"
        "LA_loop%=:\n\t"
        ATILE_E(2048, 1024, 1536, "")
        ATILE_O(3072, 2048, 2560, "")
        ATILE_E(0, 3072, 3584,
                "s_waitcnt vmcnt(2)\n\t"
                "s_barrier\n\t"
                "s_setprio 3\n\t"
                "v_add_u32 v12, 0x4000, v12\n\t"
                AISSUE
                "v_and_b32 v12, 0xffff, v12\n\t"
                "s_setprio 0\n\t")
        ATILE_O(1024, 0, 512,
                "s_mov_b64 exec, s[40:41]\n\t"
                "v_add_u32 v13, 0x4000, v13\n\t"
                "s_sub_u32 s42, s42, 1\n\t"
                "v_and_b32 v13, 0xffff, v13\n\t"
                "s_mov_b64 exec, -1\n\t"
                "s_cmp_lg_u32 s42, 0\n\t")
        "s_cbranch_scc1 LA_loop%=\n\t"
        "s_waitcnt lgkmcnt(0)\n\t"
        "s_setprio 3\n\t"
        MF(R_OB, "v[124:127]", "v[100:103]", R_OB)
        "s_nop 15\n\t"
        : "+{v[64:79]}"(R.oa), "+{v[80:95]}"(R.ob), "+{v[100:103]}"(R.p1), "+{v[124:127]}"(R.v1), "+{v12}"(R.ka), "+{v13}"(R.va)
        : "{v[16:31]}"(R.nm), "{v14}"(R.go), [qa] "s"(qa), [qb] "s"(qb), [rk] "s"(rk), [rv] "s"(rv), [lw] "s"(w * 1024), [tag] "s"(tag), [gf] "s"(go_flag)
        : "memory", "scc", "s40", "s41", "s42", "s43", "s44", "v4", "v5", "v6", "v7", "v8", "v9", "v10", "v11", "v15",
          "v32", "v33", "v34", "v35", "v36", "v37", "v38", "v39", "v40", "v41", "v42", "v43", "v44", "v45", "v46", "v47",
          "v48", "v49", "v50", "v51", "v52", "v53", "v54", "v55", "v56", "v57", "v58", "v59", "v60", "v61", "v62", "v63",
          "v96", "v97", "v98", "v99", "v104", "v105", "v106", "v107", "v108", "v109", "v110", "v111",
          "v112", "v113", "v114", "v115", "v116", "v117", "v118", "v119", "v120", "v121", "v122", "v123");
}

DI void attn_role(const char* x, const char* wqkv, char* ws, char* lds, int a, int w) {
    const int kh = w >> 2;
    const int head = 4 * (a & 7) + ((a >> 3) & 3), qt = 8 * (a >> 5) + 2 * (w & 3);
    ST_DECL; ST(0);
    __builtin_amdgcn_s_setprio(3);
    const unsigned tag = __builtin_amdgcn_readfirstlane(ld4(ws + WS_EPOCH)) * 0x9E3779B1u + 0x7F4A7C15u;
    {
        const int m = 4 * ((a & 7) >> 1) + ((a >> 3) & 3), p = 8 * (a & 1) + (a >> 5);
        if (w >= 4) {
            qkv_loader(x, wqkv, m, p, w - 4 ST_PASS);
            ST(4);
            if (w == 4) {
                const int lane = lane_id(), hp = head >> 1;
                const int fp = lane < 16 ? (16 + hp) / 3 : lane < 32 ? (32 + hp) / 3 : hp / 3;
                const int fm = lane < 32 ? (lane & 15) : 2 * (a >> 5) + (lane & 1);
                __builtin_amdgcn_s_sleep(QSLEEP);
                wait_flags<4>(ws + WS_FQ, (fp * 16 + fm) * 128, tag);
            }
        } else if (p < 10) qkv_body<3>(ws, lds, m, p, w, tag ST_PASS);
        else if (p == 10) qkv_body<2>(ws, lds, m, p, w, tag ST_PASS);
        else qkv_body<0>(ws, lds, m, p, w, tag ST_PASS);
    }
    AttnRegs R;
    attn_prep(R, 0.f, w);
    ST(5);
    wgbar();
    ST(6);
    {
        const int o = w * 512 + lane_id() * 8;
        const unsigned one = ((o & 0x1b0) == 0) ? 0x3f803f80u : 0u;
        *(LAS u32x2*)((LAS char*)lds + A_ONES + o) = u32x2{one, one};
        asm volatile("s_waitcnt lgkmcnt(0)" ::: "memory");
    }
    const char* ksrc = ws + WS_K + (size_t)head * 65536;
    const char* qa = ws + WS_Q + (size_t)(head * 64 + qt + kh) * 1024;
    const char* qb = ws + WS_Q + (size_t)(head * 64 + qt + 1 - kh) * 1024;
#pragma clang loop unroll(disable)
    for (int att = 0;; ++att) {
        ST(1);
        attn_pass(R, qa, qb, ksrc, ksrc + (WS_V - WS_K), ws + WS_FA + a * 128, tag, w);
        ST(2);
        const int lane = lane_id();
        const f32x16 oa = R.oa, ob = R.ob;
        LAS char* ex = (LAS char*)lds + w * 2048 + lane * 16;
        LAS const char* exr = (LAS const char*)lds + (w ^ 4) * 2048 + lane * 16;
        *(LAS f32x4*)ex = f32x4{ob[0], ob[1], ob[2], ob[3]};
        *(LAS f32x4*)(ex + 1024) = f32x4{ob[4], ob[5], ob[6], ob[7]};
        *(LAS float*)((LAS char*)lds + A_LSUM + w * 256 + lane * 4) = ob[8];
        float big = fmaxf(oa[8], ob[8]) * 2.7e8f;
#pragma unroll
        for (int i = 0; i < 8; ++i) big = fmaxf(fmaxf(big, __builtin_fabsf(oa[i])), __builtin_fabsf(ob[i]));
        const bool bad = !__all(fminf(oa[8], ob[8]) > 8.1e-28f && big < 1.7e38f);
        if (lane == 0) ((LAS unsigned*)((LAS char*)lds + A_VOTE))[w] = bad;
        wgbar();
        const f32x4 t0 = *(LAS const f32x4*)exr, t1 = *(LAS const f32x4*)(exr + 1024);
        const float t2 = *(LAS const float*)((LAS char*)lds + A_LSUM + (w ^ 4) * 256 + lane * 4);
        const u32x4 v0 = *(LAS const u32x4*)((LAS char*)lds + A_VOTE), v1 = *(LAS const u32x4*)((LAS char*)lds + A_VOTE + 16);
        const float rinv = __builtin_amdgcn_rcpf(oa[8] + t2);
        u32x4 ov;
        ov[0] = cvtpk((oa[0] + t0[0]) * rinv, (oa[1] + t0[1]) * rinv);
        ov[1] = cvtpk((oa[2] + t0[2]) * rinv, (oa[3] + t0[3]) * rinv);
        ov[2] = cvtpk((oa[4] + t1[0]) * rinv, (oa[5] + t1[1]) * rinv);
        ov[3] = cvtpk((oa[6] + t1[2]) * rinv, (oa[7] + t1[3]) * rinv);
        const bool redo = att == 0 && __builtin_amdgcn_readfirstlane(v0[0] | v0[1] | v0[2] | v0[3] | v1[0] | v1[1] | v1[2] | v1[3]);
        if (att == 0 ? !redo : att == 1 + kh) {
            st16(ws + WS_O + (size_t)((qt + kh) * 32 + head) * 1024, lane * 16, ov);
            drain();
            st4(ws + WS_FO + ((qt + kh) * 32 + head) * 128, tag);
        }
        asm volatile("" : "+s"(att));
        if (att == 2 || (att == 0 && !redo)) break;
        drain();
        wgbar();
        qa = qb = ws + WS_Q + (size_t)(head * 64 + qt + att) * 1024;
        bf16x8 qf;
        asm volatile("global_load_dwordx4 %0, %1, %2 sc1\n\ts_waitcnt vmcnt(0)" : "=v"(qf) : "v"(lane * 16), "s"(qa) : "memory");
        float mx = -INFINITY;
#pragma clang loop unroll(disable)
        for (int t = 0; t < 64; ++t) {
            bf16x8 kf;
            asm volatile("global_load_dwordx4 %0, %1, %2 sc1\n\ts_waitcnt vmcnt(0)" : "=v"(kf) : "v"(lane * 16), "s"(ksrc + t * 1024) : "memory");
            const f32x16 s = MFMA32(kf, qf, f32x16{});
#pragma unroll
            for (int i = 0; i < 16; ++i) mx = fmaxf(mx, s[i]);
        }
        attn_prep(R, -swap32_max(mx), w);
    }
    ST(3); ST_OUT(0);
}

#define BG1 "s_sleep 1\n\t"
#define BG2 "s_sleep 1\n\t"
#define BG3 "s_sleep 1\n\t"
#define SISSUE                                                             \
    "s_mov_b32 m0, s44\n\t"                                                 \
    "s_add_u32 s45, %[ow], %[wst]\n\t"                                      \
    "buffer_load_dwordx4 %[g0], %[rw], %[ow] offen lds\n\t"                 \
    "buffer_load_dwordx4 %[g1], %[rw], %[ow] offen offset:1024 lds\n\t"     \
    "s_add_u32 m0, s44, 8192\n\t"                                           \
    "s_add_u32 s46, %[od], 4096\n\t"                                        \
    "buffer_load_dwordx4 %[d0], %[rd], %[od] offen sc1 lds\n\t"             \
    "buffer_load_dwordx4 %[d0], %[rd], %[od] offen offset:1024 sc1 lds\n\t" \
    "s_add_u32 s44, s44, 0x4000\n\t"                                        \
    "s_and_b32 s44, s44, 0xffff\n\t"                                        \
    "s_cmp_eq_u32 %[od], %[de]\n\t"                                         \
    "s_cselect_b32 %[ow], 0, s45\n\t"                                       \
    "s_cselect_b32 %[od], 0, s46\n\t"
struct SG { u32x4 rw, rd; unsigned wst, de, ow, od, a, b, c; };
DI void sgemm_load(SG& G, int n, int lw) {
    asm volatile(
        "s_mov_b32 s44, %[lo]\n\t"
        "s_sub_u32 s47, %[n], 3\n\t"
        SISSUE SISSUE SISSUE
        "LL_loop%=:\n\t"
        "s_waitcnt vmcnt(8)\n\t"
        "s_barrier\n\t"
        SISSUE
        "s_sub_u32 s47, s47, 1\n\t"
        "s_cmp_lg_u32 s47, 0\n\t"
        "s_cbranch_scc1 LL_loop%=\n\t"
        "s_waitcnt vmcnt(8)\n\ts_barrier\n\t"
        "s_waitcnt vmcnt(4)\n\ts_barrier\n\t"
        "s_waitcnt vmcnt(0)\n\ts_barrier\n\t"
        : [ow] "+s"(G.ow), [od] "+s"(G.od)
        : [wst] "s"(G.wst), [rw] "s"(G.rw), [rd] "s"(G.rd), [lo] "s"(lw * 2048), [de] "s"(G.de), [n] "s"(n), [g0] "v"(G.a), [g1] "v"(G.b), [d0] "v"(G.c)
        : "memory", "scc", "s44", "s45", "s46", "s47");
}
#define SRD3(B, SLOT, K, TD)                                                                         \
    "ds_read_b64_tr_b16 v[" #B ":" #B "+1], %[t] offset:" #SLOT "*16384+" #K "*2048\n\t"              \
    "ds_read_b64_tr_b16 v[" #B "+2:" #B "+3], %[t] offset:" #SLOT "*16384+" #K "*2048+" #TD "\n\t"    \
    "ds_read_b128 v[" #B "+4:" #B "+7], %[dl] offset:" #SLOT "*16384+" #K "*1024\n\t"
#define SSTAGE(SLOT, MM, TD, N0, N1, N2, N3, P0, P1, P2, P3)                          \
    "s_waitcnt lgkmcnt(0)\n\t"                                                        \
    "s_barrier\n\t"                                                                   \
    MM(P0) SRD3(N0, SLOT, 0, TD) SRD3(N1, SLOT, 1, TD) BG1                            \
    MM(P1) SRD3(N2, SLOT, 2, TD) SRD3(N3, SLOT, 3, TD) BG2                            \
    MM(P2) BG3 MM(P3)
#define SST_E(SLOT, MM, TD) SSTAGE(SLOT, MM, TD, 32, 40, 48, 56, 96, 104, 112, 120)
#define SST_O(SLOT, MM, TD) SSTAGE(SLOT, MM, TD, 96, 104, 112, 120, 32, 40, 48, 56)
#define SMM_NONE(P) ""
#define SMM_WA(P) MF("v[64:79]", "v[" #P ":" #P "+3]", "v[" #P "+4:" #P "+7]", "v[64:79]")
#define SMM_WB(P) MF("v[80:95]", "v[" #P "+4:" #P "+7]", "v[" #P ":" #P "+3]", "v[80:95]")
#define SGEMM_ASM(ACC, MM, TD, NLOOP, TAIL)                                                                       \
    asm volatile(                                                                                                 \
        "s_mov_b32 s44, " #NLOOP "\n\t"                                                                           \
        "s_barrier\n\t"                                                                                           \
        SRD3(32, 0, 0, TD) SRD3(40, 0, 1, TD) SRD3(48, 0, 2, TD) SRD3(56, 0, 3, TD)                               \
        "LS_loop%=:\n\t"                                                                                          \
        SST_O(1, MM, TD) SST_E(2, MM, TD) SST_O(3, MM, TD) SST_E(0, MM, TD)                                       \
        "s_sub_u32 s44, s44, 1\n\t"                                                                               \
        "s_cmp_lg_u32 s44, 0\n\t"                                                                                 \
        "s_cbranch_scc1 LS_loop%=\n\t"                                                                            \
        TAIL                                                                                                      \
        "s_waitcnt lgkmcnt(0)\n\t"                                                                                \
        MM(96) BG1 MM(104) BG1 MM(112) BG1 MM(120)                                                                \
        "s_nop 15\n\t"                                                                                            \
        : ACC(acc)                                                                                                \
        : [t] "v"(G.a), [dl] "v"(G.b)                                                                             \
        : "memory", "scc", "s44",                                                                                 \
          "v32", "v33", "v34", "v35", "v36", "v37", "v38", "v39", "v40", "v41", "v42", "v43", "v44", "v45", "v46", "v47", \
          "v48", "v49", "v50", "v51", "v52", "v53", "v54", "v55", "v56", "v57", "v58", "v59", "v60", "v61", "v62", "v63", \
          "v96", "v97", "v98", "v99", "v100", "v101", "v102", "v103", "v104", "v105", "v106", "v107", "v108", "v109", "v110", "v111", \
          "v112", "v113", "v114", "v115", "v116", "v117", "v118", "v119", "v120", "v121", "v122", "v123", "v124", "v125", "v126", "v127")

template <int MODE>
DI SG sgemm_setup(const char* wbase, unsigned wbytes, int wrow, const char* dbase, int dblk, int rot, int w) {
    const int lane = lane_id(), row = lane >> 3, lw = w & 3;
    const int q = (lane & 15) >> 2, h2 = lane >> 5;
    SG G;
    G.rw = srd(wbase, wbytes); G.rd = srd(dbase, 2 * dblk);
    G.wst = 64 * wrow; G.de = (MODE == 0 ? 31 : 21) * 4096; G.ow = rot * G.wst; G.od = rot * 4096;
    if (w >= 4) {
        G.a = (16 * lw + row) * wrow + (((lane & 7) ^ (((row >> 1) & 1) << 2)) << 4);
        G.b = G.a + 8 * wrow - 1024;
        G.c = (lw >> 1) * dblk + (lw & 1) * 2048 + lane * 16;
    } else {
        G.a = ((MODE == 0 ? 8 : 4) * h2 + q) * 128 + (((lw & 1) ^ ((q >> 1) & 1)) << 6) + ((lane >> 4) & 1) * 32 + (lane & 3) * 8;
        G.b = 8192 + (lw >> 1) * 4096 + lane * 16;
        G.c = 0;
    }
    asm volatile("" : "+v"(G.a), "+v"(G.b), "+v"(G.c));
    return G;
}
#define ACC0 "+{v[64:79]}"
#define ACC1 "+{v[80:95]}"
template <int MODE>
DI void sgemm_run(f32x16& acc, SG& G) {
    if (MODE == 0) SGEMM_ASM(ACC0, SMM_WA, 512, 7, SST_O(1, SMM_WA, 512) SST_E(2, SMM_WA, 512) SST_O(3, SMM_WA, 512));
    else SGEMM_ASM(ACC1, SMM_WB, 1024, 5, SST_O(1, SMM_WB, 1024));
}
DI f32x4 sub4(const f32x16& a, int g) { return f32x4{a[4 * g], a[4 * g + 1], a[4 * g + 2], a[4 * g + 3]}; }

DI unsigned gelu2(float a, float b) {
    constexpr float c1 = -2.f * 0.7978845608028654f * 1.4426950408889634f, c2 = c1 * 0.044715f;
    const f32x2 u = {a, b};
    const f32x2 z = u * (u * u * c2 + c1);
    const f32x2 d = f32x2{__builtin_amdgcn_exp2f(z[0]), __builtin_amdgcn_exp2f(z[1])} + 1.f;
    const f32x2 o = u * f32x2{__builtin_amdgcn_rcpf(d[0]), __builtin_amdgcn_rcpf(d[1])};
    return cvtpk(o[0], o[1]);
}

#define WRD(R0, R1, R2, R3, STEP)                                                    \
    "ds_read_b64_tr_b16 " R0 ", %[t0] offset:" #STEP "*2048\n\t"                      \
    "ds_read_b64_tr_b16 " R1 ", %[t0] offset:" #STEP "*2048+1024\n\t"                 \
    "ds_read_b64_tr_b16 " R2 ", %[t1] offset:" #STEP "*2048\n\t"                      \
    "ds_read_b64_tr_b16 " R3 ", %[t1] offset:" #STEP "*2048+1024\n\t"
#define WRD0(S) WRD("v[32:33]", "v[34:35]", "v[36:37]", "v[38:39]", S)
#define WRD1(S) WRD("v[40:41]", "v[42:43]", "v[44:45]", "v[46:47]", S)
#define WRD2(S) WRD("v[48:49]", "v[50:51]", "v[52:53]", "v[54:55]", S)
#define WRD3(S) WRD("v[56:57]", "v[58:59]", "v[60:61]", "v[62:63]", S)
#define WMM(WAIT, W0, W1, O) "s_waitcnt " WAIT "\n\t" MF("v[96:111]", W0, O, "v[96:111]") MF("v[112:127]", W1, O, "v[112:127]")
#define OLD(K, R, V, OFF)                                             \
    "5:\n\t"                                                           \
    "s_bitcmp1_b32 s40, " #K "\n\t"                                    \
    "s_cbranch_scc1 6f\n\t"                                            \
    "global_load_dword v24, %[fo], %[f] sc1\n\t"                       \
    "s_sub_u32 s41, s41, 1\n\t"                                        \
    "s_cselect_b32 s42, -1, 0\n\t"                                     \
    "s_waitcnt vmcnt(0)\n\t"                                           \
    "v_cmp_eq_u32 vcc, %[tag], v24\n\t"                                \
    "s_or_b32 s40, vcc_lo, s42\n\t"                                    \
    "s_bitcmp1_b32 s40, " #K "\n\t"                                    \
    "s_cbranch_scc1 6f\n\t"                                            \
    "s_sleep 3\n\t"                                                    \
    "s_branch 5b\n\t"                                                  \
    "6:\n\t"                                                           \
    "global_load_dwordx4 " R ", " V ", %[o] offset:" #OFF " sc1\n\t"
DI void wo_gemm(f32x16& a0, f32x16& a1, f32x4& k0, f32x4& k1, float& k2, const char* osrc, const char* fsrc, unsigned tag, int w) {
    const int lane = lane_id();
    const int q = (lane & 15) >> 2, h2 = lane >> 5;
    const unsigned tb = (w >> 1) * 16384 + (4 * h2 + q) * 128 + ((lane >> 4) & 1) * 32 + (lane & 3) * 8;
    const unsigned t0 = tb + ((q >> 1) & 1) * 64, t1 = tb + (((q >> 1) & 1) ^ 1) * 64;
    const unsigned d0 = lane * 16, d1 = d0 + 4096, fo = (lane & 7) * 128;
    asm volatile(
        WRD0(0) WRD1(1) WRD2(2) WRD3(3)
        "s_mov_b32 s40, 0\n\t"
        "s_mov_b32 s41, 0x100000\n\t"
        "s_setprio 0\n\t"
        OLD(0, "v[64:67]", "%[d0]", 0) OLD(1, "v[68:71]", "%[d0]", 1024) OLD(2, "v[72:75]", "%[d0]", 2048) OLD(3, "v[76:79]", "%[d0]", 3072)
        OLD(4, "v[80:83]", "%[d1]", 0) OLD(5, "v[84:87]", "%[d1]", 1024) OLD(6, "v[88:91]", "%[d1]", 2048) OLD(7, "v[92:95]", "%[d1]", 3072)
        "s_setprio 2\n\t"
        "s_waitcnt vmcnt(7) lgkmcnt(0)\n\t"
        MF("v[96:111]", "v[32:35]", "v[64:67]", "0") MF("v[112:127]", "v[36:39]", "v[64:67]", "0")
        WRD0(4)
        WMM("vmcnt(6)", "v[40:43]", "v[44:47]", "v[68:71]") WRD1(5)
        WMM("vmcnt(5)", "v[48:51]", "v[52:55]", "v[72:75]") WRD2(6)
        WMM("vmcnt(4)", "v[56:59]", "v[60:63]", "v[76:79]") WRD3(7)
        WMM("vmcnt(3) lgkmcnt(12)", "v[32:35]", "v[36:39]", "v[80:83]")
        WMM("vmcnt(2) lgkmcnt(0)", "v[40:43]", "v[44:47]", "v[84:87]")
        WMM("vmcnt(1)", "v[48:51]", "v[52:55]", "v[88:91]")
        "s_barrier\n\t"
        WMM("vmcnt(0)", "v[56:59]", "v[60:63]", "v[92:95]")
        "s_nop 15\n\t"
        : "={v[96:111]}"(a0), "={v[112:127]}"(a1),
          "+{v[12:15]}"(k0), "+{v[16:19]}"(k1), "+{v20}"(k2)
        : [o] "s"(osrc), [f] "s"(fsrc), [tag] "s"(tag), [d0] "v"(d0), [d1] "v"(d1), [t0] "v"(t0), [t1] "v"(t1), [fo] "v"(fo)
        : "memory", "scc", "vcc", "s40", "s41", "s42", "v24",
          "v32", "v33", "v34", "v35", "v36", "v37", "v38", "v39", "v40", "v41", "v42", "v43", "v44", "v45", "v46", "v47",
          "v48", "v49", "v50", "v51", "v52", "v53", "v54", "v55", "v56", "v57", "v58", "v59", "v60", "v61", "v62", "v63",
          "v64", "v65", "v66", "v67", "v68", "v69", "v70", "v71", "v72", "v73", "v74", "v75", "v76", "v77", "v78", "v79",
          "v80", "v81", "v82", "v83", "v84", "v85", "v86", "v87", "v88", "v89", "v90", "v91", "v92", "v93", "v94", "v95");
}

DI void mixer_role(const char* x, const char* wqkv, const char* gw, const char* wo, const char* w1, const char* b1, const char* w2, const char* b2,
                   char* ws, char* out, char* lds, int bi, int w) {
    const int j = bi & 7, i = bi >> 3;
    __builtin_amdgcn_s_setprio(1);
    ST_DECL; ST(0);
    const unsigned tag = __builtin_amdgcn_readfirstlane(ld4(ws + WS_EPOCH)) * 0x9E3779B1u + 0x7F4A7C15u;
    {
        const int lane = lane_id(), h2 = lane >> 5, tid = 64 * w + lane;
        const u32x4 xr = *(GAS const u32x4*)(x + (size_t)(8 * bi + w) * 1024 + lane * 16);
        const u32x4 gr = *(GAS const u32x4*)(gw + lane * 16);
        float v[8], gg[8];
#pragma unroll
        for (int k = 0; k < 4; ++k) { v[2 * k] = bflo(xr[k]); v[2 * k + 1] = bfhi(xr[k]); gg[2 * k] = bflo(gr[k]); gg[2 * k + 1] = bfhi(gr[k]); }
        float s1 = 0.f, s3 = 0.f;
#pragma unroll
        for (int k = 0; k < 8; ++k) { s1 += v[k]; s3 += v[k] * gg[k]; }
        const float mu = wave_sum(s1) * (1.f / 512.f);
        s3 = wave_sum(s3);
        float s2 = 0.f;
#pragma unroll
        for (int k = 0; k < 8; ++k) { v[k] -= mu; s2 += v[k] * v[k]; }
        const float rs = __builtin_amdgcn_rsqf(wave_sum(s2) * (1.f / 512.f) + 1e-5f);
        u32x4 hv;
#pragma unroll
        for (int k = 0; k < 4; ++k) hv[k] = cvtpk(v[2 * k] * rs, v[2 * k + 1] * rs);
        *(LAS u32x4*)((LAS char*)lds + w * 1024 + lane * 16) = hv;
        const float g = __builtin_amdgcn_rcpf(1.f + __builtin_amdgcn_exp2f(-1.4426950408889634f * s3));
        st4(ws + WS_G + (8 * bi + w) * 4, uni(g));
        wgbar();
        LAS const unsigned short* lb = (LAS const unsigned short*)lds + tid;
#pragma unroll
        for (int k = 0; k < 4; ++k) hv[k] = (unsigned)lb[(2 * k) * 512] | ((unsigned)lb[(2 * k + 1) * 512] << 16);
        st16(ws + WS_H + (size_t)(2 * w * 128 + (bi >> 1)) * 1024 + (bi & 1) * 512, h2 * 131072 + (lane & 31) * 16, hv);
        drain();
        wgbar();
        if (w == 0) st4(ws + WS_FL + bi * 128, tag);
    }
    __builtin_amdgcn_s_setprio(2);
    ST(6);
    const int wb = w & 1, db = (w >> 1) & 1, hf = w >> 2;
    const int L = (j >> 1) * 22 + i, ft = L >> 2, dt1 = 4 * (j & 1) + (L & 3);
    const int st = 8 * (j >> 1) + (i & 7), dt = 4 * (j & 1) + (i >> 3);
    f32x16 acc1, acc2;
    SG G1, G2;
    unsigned ex, l16, og;
    {
        const int lane = lane_id();
        const bool live = i < 22 && (ft < 21 || wb == 0);
        const int f0 = 64 * ft + 32 * wb + 4 * (lane >> 5);
        u32x2 bv[4] = {};
        if (live) {
#pragma unroll
            for (int k = 0; k < 4; ++k) bv[k] = *(GAS const u32x2*)(b1 + f0 * 2 + 16 * k);
        }
        og = (64 * st + 32 * wb + (lane & 31)) * 4;
        const float bb = bflo(*(GAS const unsigned short*)(b2 + (og >> 1)));
        G1 = sgemm_setup<0>(w1 + ft * 128, 2048 * 2752 - ft * 128, 2752, ws + WS_H + (size_t)2 * dt1 * 131072, 131072, ROT1, w);
        G2 = sgemm_setup<1>(w2 + st * 128, 1376 * 4096 - st * 128, 4096, ws + WS_U + (size_t)2 * dt * 86 * 1024, 86 * 1024, ROT2, w);
        l16 = lane * 16;
        ex = 65536 + (w & 3) * 2048 + l16;
#pragma unroll
        for (int k = 0; k < 4; ++k) {
            acc1[4 * k] = bflo(bv[k][0]); acc1[4 * k + 1] = bfhi(bv[k][0]); acc1[4 * k + 2] = bflo(bv[k][1]); acc1[4 * k + 3] = bfhi(bv[k][1]);
            acc2[4 * k] = bb; acc2[4 * k + 1] = bb; acc2[4 * k + 2] = bb; acc2[4 * k + 3] = bb;
        }
        asm volatile("" : "+{v[64:79]}"(acc1), "+{v[80:95]}"(acc2), "+v"(ex), "+v"(l16), "+v"(og));
    }
    if (i < 22) {
        if (w >= 4) { __builtin_amdgcn_s_sleep(LSLEEP); wait_flags<8>(ws + WS_FL + (w - 4) * 8192, l16 * 8, tag); }
        if (w == 0) { __builtin_amdgcn_s_sleep(LSLEEP); wait_flags<4>(ws + WS_FA + bi * 128, 0, tag); }
        wgbar();
        ST(7);
        f32x4 v0, v1;
        if (w >= 4) {
            sgemm_load(G1, 32, w - 4);
            wgbar();
            v0 = *(LAS const f32x4*)(uintptr_t)ex; v1 = *(LAS const f32x4*)(uintptr_t)(ex + 1024);
        } else {
            sgemm_run<0>(acc1, G1);
            *(LAS f32x4*)(uintptr_t)ex = sub4(acc1, 2); *(LAS f32x4*)(uintptr_t)(ex + 1024) = sub4(acc1, 3);
            wgbar();
            v0 = sub4(acc1, 0); v1 = sub4(acc1, 1);
        }
        ST(1);
        const u32x4 o = {gelu2(v0[0], v0[1]), gelu2(v0[2], v0[3]), gelu2(v1[0], v1[1]), gelu2(v1[2], v1[3])};
        if (ft < 21 || wb == 0) st16(ws + WS_U + (size_t)((2 * dt1 + db) * 86 + 4 * ft + 2 * wb + hf) * 1024, l16, o);
        drain();
        wgbar();
        if (w == 0) st4(ws + WS_FU + (dt1 * 32 + ft) * 128, tag);
    }
    ST(2);
    if (bi == 255 && w == 1) {
#pragma clang loop unroll(disable)
        for (int k = 0; k < 4; ++k) {
            const int lane = lane_id();
            wait_flags<8>(ws + WS_FQ + k * 8192, lane * 128, tag);
            wait_flags<8>(ws + WS_FL + k * 8192, lane * 128, tag);
        }
        st4(ws + WS_EPOCH, tag);
    }
    if (w == 0) wait_flags<8>(ws + WS_FU + dt * 4096, (l16 < 22 * 16 ? l16 : 0) * 8, tag);
    wgbar();
    ST(3);
    f32x4 bp0, bp1;
    float g;
    {
        asm volatile("global_load_dword %0, %1, %2 sc1" : "=v"(g) : "v"(og), "s"(ws + WS_G) : "memory");
        if (w >= 4) {
            sgemm_load(G2, 22, w - 4);
            wgbar();
            bp0 = *(LAS const f32x4*)(uintptr_t)ex; bp1 = *(LAS const f32x4*)(uintptr_t)(ex + 1024);
        } else {
            sgemm_run<1>(acc2, G2);
            *(LAS f32x4*)(uintptr_t)ex = sub4(acc2, 2); *(LAS f32x4*)(uintptr_t)(ex + 1024) = sub4(acc2, 3);
            wgbar();
            bp0 = sub4(acc2, 0); bp1 = sub4(acc2, 1);
        }
        asm volatile("s_waitcnt vmcnt(0)" : "+v"(g)::"memory");
        ST(4);
        bp0 *= 1.f - g;
        bp1 *= 1.f - g;
        asm volatile("" : "+v"(bp0), "+v"(bp1));
        const int lane = lane_id(), row = lane >> 3;
        const unsigned g0 = row * 1024 + (((lane & 7) ^ (((row >> 1) & 1) << 2)) << 4);
        const char* wsrc = wo + (size_t)w * 65536 + dt * 128;
#pragma unroll
        for (int k = 0; k < 8; ++k) glds(w * 8192 + k * 1024, g0, wsrc + k * 8192);
    }
    drain();
    wgbar();
    ST(5);
    f32x16 a0, a1;
    wo_gemm(a0, a1, bp0, bp1, g, ws + WS_O + (size_t)((2 * st + wb) * 32 + 8 * (w >> 1)) * 1024,
            ws + WS_FO + ((2 * st + wb) * 32 + 8 * (w >> 1)) * 128, tag, w);
    {
        const int lane = lane_id(), kq = 2 * db + hf;
        LAS char* ex = (LAS char*)lds + w * 8192 + lane * 16;
#pragma unroll
        for (int k = 0; k < 4; ++k) { *(LAS f32x4*)(ex + k * 1024) = sub4(a0, k); *(LAS f32x4*)(ex + 4096 + k * 1024) = sub4(a1, k); }
        wgbar();
        LAS const char* exr = (LAS const char*)lds + wb * 8192 + kq * 2048 + lane * 16;
        f32x4 s0 = *(LAS const f32x4*)exr, s1 = *(LAS const f32x4*)(exr + 1024);
#pragma unroll
        for (int k = 1; k < 4; ++k) { s0 += *(LAS const f32x4*)(exr + k * 16384); s1 += *(LAS const f32x4*)(exr + k * 16384 + 1024); }
        s0 = s0 * g + bp0;
        s1 = s1 * g + bp1;
        const auto r0 = __builtin_amdgcn_permlane32_swap(cvtpk(s0[0], s0[1]), cvtpk(s1[0], s1[1]), false, false);
        const auto r1 = __builtin_amdgcn_permlane32_swap(cvtpk(s0[2], s0[3]), cvtpk(s1[2], s1[3]), false, false);
        *(GAS u32x4*)(out + (size_t)(64 * st + 32 * wb) * 1024 + (64 * dt + 16 * kq) * 2 + (lane & 31) * 1024 + (lane >> 5) * 16) = u32x4{r0[0], r1[0], r0[1], r1[1]};
    }
    ST_OUT(0);
}

constexpr int MK_LDS = 81920;
__global__ __launch_bounds__(512, 4) void mk(const char* x, const char* wqkv, const char* wo, const char* w1, const char* b1,
                                             const char* w2, const char* b2, const char* gw, char* ws, char* out) {
    extern __shared__ __attribute__((aligned(16))) char lds[];
    const int w = __builtin_amdgcn_readfirstlane(threadIdx.x >> 6);
    if (blockIdx.x < 256) attn_role(x, wqkv, ws, lds, blockIdx.x, w);
    else mixer_role(x, wqkv, gw, wo, w1, b1, w2, b2, ws, out, lds, blockIdx.x - 256, w);
}

extern "C" void kernel_launch(void* const* d_in, const int* in_sizes, int n_in, void* d_out, int out_size, void* d_ws, size_t ws_size,
                              hipStream_t stream) {
    (void)hipFuncSetAttribute((const void*)mk, hipFuncAttributeMaxDynamicSharedMemorySize, MK_LDS);
    mk<<<512, 512, MK_LDS, stream>>>((const char*)d_in[0], (const char*)d_in[1], (const char*)d_in[2], (const char*)d_in[4],
                                     (const char*)d_in[5], (const char*)d_in[6], (const char*)d_in[7], (const char*)d_in[8],
                                     (char*)d_ws, (char*)d_out);
}
```

```cpp
#include <hip/hip_runtime.h>
#include <hip/hip_bf16.h>
#include <stdint.h>

typedef __attribute__((ext_vector_type(8))) short bf16x8;
typedef __attribute__((ext_vector_type(4))) short s16x4;
typedef __attribute__((ext_vector_type(16))) float f32x16;
typedef __attribute__((ext_vector_type(4))) float f32x4;
typedef __attribute__((ext_vector_type(2))) float f32x2;
typedef __attribute__((ext_vector_type(4))) unsigned u32x4;
typedef __attribute__((ext_vector_type(2))) unsigned u32x2;
#define LAS __attribute__((address_space(3)))
#define GAS __attribute__((address_space(1)))
#define DI __device__ __forceinline__
#define MFMA32(a, b, c) __builtin_amdgcn_mfma_f32_32x32x16_bf16((a), (b), (c), 0, 0, 0)

#define QSLEEP 24
#define LSLEEP 32
#define PF_QKV 0
#define PACE 4
#define PFMIN 2
#define GATE 0
#define PF_W1 0
#define PF_W2 0
#define PF_U 0
#define ROT1 (((dt1 + ft) & 1) * 16)
#define ROT2 (((st + dt) & 1) * 11)
#define QROT 6
constexpr int S_ = 2048, D_ = 512, F_ = 1376;
constexpr size_t WS_EPOCH = 0;
constexpr size_t WS_G = 4096;
constexpr size_t WS_KN = WS_G + 8192;
constexpr size_t WS_FL = 98304;
constexpr size_t WS_FQ = 32768;
constexpr size_t WS_FU = 65536;
constexpr size_t WS_FA = 393216;
constexpr size_t WS_FO = 131072;
constexpr size_t WS_Q = 1u << 20;
constexpr size_t WS_K = WS_Q + (2u << 20);
constexpr size_t WS_V = WS_K + (2u << 20);
constexpr size_t WS_H = WS_V + (2u << 20);
constexpr size_t WS_U = WS_H + (2u << 20);
constexpr size_t WS_O = WS_U + (2u << 20);

#define ST_DECL
#define ST_ARG
#define ST_PASS
#define ST(i)
#define ST_OUT(k)
DI unsigned cvtpk(float lo, float hi) {
    typedef float f32x2_t __attribute__((ext_vector_type(2)));
    typedef __bf16 bf16x2_t __attribute__((ext_vector_type(2)));
    return __builtin_bit_cast(unsigned, __builtin_convertvector(f32x2_t{lo, hi}, bf16x2_t));
}
DI float bflo(unsigned u) { return __uint_as_float(u << 16); }
DI float bfhi(unsigned u) { return __uint_as_float(u & 0xffff0000u); }
DI s16x4 trread(LAS const char* p) {
    typedef short v4i16_t __attribute__((ext_vector_type(4)));
    return __builtin_bit_cast(s16x4, __builtin_amdgcn_ds_read_tr16_b64_v4i16((LAS v4i16_t*)p));
}
DI void glds(unsigned lds, unsigned voff, const void* sbase) {
    asm volatile("s_mov_b32 m0, %0\n\ts_nop 0\n\tglobal_load_lds_dwordx4 %1, %2" ::"s"(lds), "v"(voff), "s"(sbase) : "memory");
}
template <int CTRL> DI float dpp(float v) {
    return __int_as_float(__builtin_amdgcn_update_dpp(0, __float_as_int(v), CTRL, 0xf, 0xf, true));
}
DI float rl(float v, int l) { return __int_as_float(__builtin_amdgcn_readlane(__float_as_int(v), l)); }
DI float wave_sum(float v) {
    v += dpp<0xB1>(v); v += dpp<0x4E>(v); v += dpp<0x141>(v); v += dpp<0x140>(v);
    return (rl(v, 0) + rl(v, 16)) + (rl(v, 32) + rl(v, 48));
}
DI float wave_max(float v) {
    v = fmaxf(v, dpp<0xB1>(v)); v = fmaxf(v, dpp<0x4E>(v)); v = fmaxf(v, dpp<0x141>(v)); v = fmaxf(v, dpp<0x140>(v));
    return fmaxf(fmaxf(rl(v, 0), rl(v, 16)), fmaxf(rl(v, 32), rl(v, 48)));
}
DI float swap32_add(float v) {
    auto rr = __builtin_amdgcn_permlane32_swap(__float_as_uint(v), __float_as_uint(v), false, false);
    return __uint_as_float(rr[0]) + __uint_as_float(rr[1]);
}
DI float swap32_max(float v) {
    auto rr = __builtin_amdgcn_permlane32_swap(__float_as_uint(v), __float_as_uint(v), false, false);
    return fmaxf(__uint_as_float(rr[0]), __uint_as_float(rr[1]));
}

DI int lane_id() {
    int l;
    asm volatile("v_mbcnt_lo_u32_b32 %0, -1, 0\n\tv_mbcnt_hi_u32_b32 %0, -1, %0" : "=v"(l));
    __builtin_assume(l >= 0 && l < 64);
    return l;
}
DI void st16(const char* sbase, unsigned voff, u32x4 v) {
    asm volatile("global_store_dwordx4 %0, %1, %2 sc1\n\ts_nop 1" ::"v"(voff), "v"(v), "s"(sbase) : "memory");
}
DI void st4(const char* p, unsigned v) {
    unsigned t0, t1;
    asm volatile("s_nop 1\n\tv_mov_b32 %0, %2\n\tv_mov_b32 %1, 0\n\ts_mov_b64 exec, 1\n\tglobal_store_dword %1, %0, %3 sc1\n\ts_mov_b64 exec, -1"
                 : "=&v"(t0), "=&v"(t1) : "s"(v), "s"(p) : "memory");
}
DI unsigned uni(float v) { return __builtin_amdgcn_readfirstlane(__float_as_uint(v)); }
DI unsigned ld4(const char* p) { return __hip_atomic_load((GAS const unsigned*)p, __ATOMIC_RELAXED, __HIP_MEMORY_SCOPE_AGENT); }
DI void touch(const char* sbase, unsigned voff) {
    asm volatile("s_mov_b32 m0, 0x13e00\n\ts_nop 0\n\tglobal_load_lds_dword %0, %1 sc1" ::"v"(voff), "s"(sbase) : "memory");
}
DI void drain() { asm volatile("s_waitcnt vmcnt(0)" ::: "memory"); }
DI void wgbar() { asm volatile("s_waitcnt lgkmcnt(0)\n\ts_barrier" ::: "memory"); }
template <int SLEEP>
DI void wait_flags(const char* sbase, unsigned voff, unsigned tag) {
#pragma clang loop unroll(disable)
    for (unsigned spin = 0; spin < (1u << 20); ++spin) {
        unsigned f;
        asm volatile("global_load_dword %0, %1, %2 sc1\n\ts_waitcnt vmcnt(0)" : "=v"(f) : "v"(voff), "s"(sbase) : "memory");
        if (__all(f == tag)) break;
        __builtin_amdgcn_s_sleep(SLEEP);
    }
}

constexpr int QW0 = 49152, QCNT = 73728;
DI void qkv_loader(const char* x, const char* wqkv, int m, int p, int lw ST_ARG) {
    const int lane = lane_id();
    unsigned vx[4], vw[3];
#pragma unroll
    for (int ii = 0; ii < 4; ++ii) {
        const int row = 32 * lw + 8 * ii + (lane >> 3);
        vx[ii] = row * 1024 + (((lane & 7) ^ ((row >> 1) & 7)) << 4);
    }
#pragma unroll
    for (int ii = 0; ii < 3; ++ii) {
        const int o = 16 * lane + 1024 * ii;
        vw[ii] = (16 * lw + o / 192) * 3072 + (o % 192);
    }
    const char* xb = x + (size_t)m * 131072;
    const char* wb = wqkv + p * 192;
    const int rot = (p + 2 * m) & QROT;
    auto isx = [&](int s, int slot) {
        const char* xs = xb + ((s + rot) & 7) * 128;
#pragma unroll
        for (int ii = 0; ii < 4; ++ii) glds(slot * 16384 + lw * 4096 + ii * 1024, vx[ii], xs);
    };
    auto isw = [&](int s) {
        const char* wsrc = wb + ((s + rot) & 7) * 196608;
#pragma unroll
        for (int ii = 0; ii < 3; ++ii) glds(QW0 + (s & 1) * 12288 + lw * 3072 + ii * 1024, vw[ii], wsrc);
    };
    isw(0); isx(0, 0); isw(1); isx(1, 1); isx(2, 2);
    *(LAS unsigned*)QCNT = 0u;
    asm volatile("s_waitcnt vmcnt(11) lgkmcnt(0)\n\ts_barrier" ::: "memory");
    ST(7);
    int slot = 0;
#pragma clang loop unroll(disable)
    for (int s = 1; s < 6; ++s) {
        asm volatile("s_waitcnt vmcnt(4)\n\ts_barrier" ::: "memory");
        isw(s + 1); isx(s + 2, slot);
        slot = slot == 2 ? 0 : slot + 1;
    }
    asm volatile("s_waitcnt vmcnt(4)\n\ts_barrier" ::: "memory");
    isw(7);
    asm volatile("s_waitcnt vmcnt(0)\n\ts_barrier" ::: "memory");
}

template <int NT>
DI void qkv_body(char* ws, char* lds, int m, int p, int w, unsigned tag ST_ARG) {
    const int lane = lane_id(), r = lane & 31, h2 = lane >> 5;
    f32x16 acc[3];
#pragma unroll
    for (int i = 0; i < 3; ++i) acc[i] = f32x16{};
    const int sw = (r >> 1) & 7;
    int xa[4];
#pragma unroll
    for (int st = 0; st < 4; ++st) xa[st] = (32 * w + r) * 128 + (((2 * st + h2) ^ sw) << 4);
    const int wa = QW0 + (8 * h2 + ((lane & 15) >> 2)) * 192 + ((lane >> 4) & 1) * 32 + (lane & 3) * 8;
    bf16x8 xf[4], wf[2][3];
    auto rdw = [&](LAS const char* wsl, int st, int i) {
        const s16x4 lo = trread(wsl + wa + st * 3072 + i * 64), hi = trread(wsl + wa + st * 3072 + i * 64 + 768);
        wf[st & 1][i] = __builtin_shufflevector(lo, hi, 0, 1, 2, 3, 4, 5, 6, 7);
    };
    auto mm1 = [&](int st, int i) {
        if (i < NT) acc[i] = MFMA32(wf[st & 1][i], xf[st], acc[i]);
        else acc[i] = MFMA32(xf[st], wf[st & 1][i], acc[i]);
        __builtin_amdgcn_sched_barrier(0);
    };
    auto step = [&](int st, LAS const char* xsl, LAS const char* wsl, bool bar) {
        const int sn = (st + 2) & 3;
        mm1(st, 0);
        if (bar) wgbar();
        xf[sn] = *(LAS const bf16x8*)(xsl + xa[sn]); rdw(wsl, sn, 0); __builtin_amdgcn_sched_barrier(0);
        mm1(st, 1);
        rdw(wsl, sn, 1); __builtin_amdgcn_sched_barrier(0);
        mm1(st, 2);
        rdw(wsl, sn, 2); __builtin_amdgcn_sched_barrier(0);
    };
    wgbar();
    LAS const char* xsl = (LAS const char*)lds;
    LAS const char* wsl = (LAS const char*)lds;
#pragma unroll
    for (int st = 0; st < 2; ++st) { xf[st] = *(LAS const bf16x8*)(xsl + xa[st]); rdw(wsl, st, 0); rdw(wsl, st, 1); rdw(wsl, st, 2); }
    __builtin_amdgcn_sched_barrier(0);
#pragma clang loop unroll(disable)
    for (int s = 0; s < 7; ++s) {
        step(0, xsl, wsl, false); step(1, xsl, wsl, false);
        xsl = (xsl == (LAS const char*)lds + 32768) ? (LAS const char*)lds : xsl + 16384;
        wsl = (LAS const char*)lds + ((s & 1) ? 0 : 12288);
        step(2, xsl, wsl, true); step(3, xsl, wsl, false);
    }
    step(0, xsl, wsl, false); step(1, xsl, wsl, false);
    mm1(2, 0); mm1(2, 1); mm1(2, 2); mm1(3, 0); mm1(3, 1); mm1(3, 2);
    ST(4);
    const int tile = 4 * m + w;
#pragma unroll
    for (int u = 0; u < 6; ++u) {
        const int cb = 3 * p + (u >> 1), half = u & 1, type = cb >> 4, hp = cb & 15;
        const float sc = (type == 0) ? 0.25f * 1.4426950408889634f : 1.f;
        u32x4 o;
#pragma unroll
        for (int i = 0; i < 4; ++i) o[i] = cvtpk(acc[u >> 1][8 * half + 2 * i] * sc, acc[u >> 1][8 * half + 2 * i + 1] * sc);
        if ((u >> 1) < NT) {
            st16(ws + (type ? WS_K : WS_Q) + (size_t)((2 * hp + half) * 64 + tile) * 1024, lane * 16, o);
        } else {
            st16(ws + WS_V + (size_t)(2 * hp * 64 + tile) * 1024 + half * 512, (r >> 4) * 65536 + h2 * 256 + (r & 15) * 16, o);
        }
    }
    drain();
    ST(7);
    unsigned arr = 0;
    if (lane == 0) arr = __hip_atomic_fetch_add((LAS unsigned*)QCNT, 1u, __ATOMIC_RELAXED, __HIP_MEMORY_SCOPE_WORKGROUP);
    if (__builtin_amdgcn_readfirstlane(arr) == 3) st4(ws + WS_FQ + (p * 16 + m) * 128, tag);
}

#define EXP(a) "v_exp_f32 v" #a ", v" #a "\n\t"
#define CVT(d, a, b) "v_cvt_pk_bf16_f32 v" #d ", v" #a ", v" #b "\n\t"
#define MF(D, A, B, C) "v_mfma_f32_32x32x16_bf16 " D ", " A ", " B ", " C "\n\t"
#define ASTEP(c0, c1, c2, c3, c4, c5, c6, c7, c8, c9, c10, c11, c12, c13, c14, c15, M1, M2, CL, M3)                  \
    EXP(c0) EXP(c1) M1 EXP(c2) EXP(c3) EXP(c4) EXP(c5) M2 CL                                                          \
    EXP(c6) EXP(c7) CVT(96, c0, c1) CVT(97, c2, c3) CVT(98, c4, c5) CVT(99, c6, c7) EXP(c8) EXP(c9) M3                \
    EXP(c10) EXP(c11) EXP(c12) EXP(c13) EXP(c14) EXP(c15)                                                             \
    CVT(100, c8, c9) CVT(101, c10, c11) CVT(102, c12, c13) CVT(103, c14, c15)
#define R_OA "v[64:79]"
#define R_OB "v[80:95]"
#define R_SA "v[32:47]"
#define R_SB "v[48:63]"
#define R_NM "v[16:31]"
#define ATILE(KC, KN, V0C, V1C, V0N, V1N, KOFF, VOFF0, VOFF1, PRE)                                                    \
    ASTEP(32, 33, 34, 35, 36, 37, 38, 39, 40, 41, 42, 43, 44, 45, 46, 47,                                             \
          MF(R_OB, V1N, "v[100:103]", R_OB), MF(R_SB, KC, "v[8:11]", R_NM),                                           \
          "s_waitcnt lgkmcnt(0)\n\t" PRE                                                                              \
          "ds_read_b128 " KC ", v12 offset:" #KOFF "\n\t"                                                             \
          "ds_read_b128 " V0N ", v13 offset:" #VOFF0 "\n\t"                                                           \
          "ds_read_b128 " V1N ", v13 offset:" #VOFF1 "\n\t",                                                          \
          MF(R_OA, V0C, "v[96:99]", R_OA))                                                                            \
    ASTEP(48, 49, 50, 51, 52, 53, 54, 55, 56, 57, 58, 59, 60, 61, 62, 63,                                             \
          MF(R_OA, V1C, "v[100:103]", R_OA), MF(R_SA, KN, "v[4:7]", R_NM), "", MF(R_OB, V0C, "v[96:99]", R_OB))
#define ATILE_E(KOFF, VOFF0, VOFF1, PRE) ATILE("v[104:107]", "v[108:111]", "v[112:115]", "v[116:119]", "v[120:123]", "v[124:127]", KOFF, VOFF0, VOFF1, PRE)
#define ATILE_O(KOFF, VOFF0, VOFF1, PRE) ATILE("v[108:111]", "v[104:107]", "v[120:123]", "v[124:127]", "v[112:115]", "v[116:119]", KOFF, VOFF0, VOFF1, PRE)
#define MOV4(a, b, c, d, s) "v_mov_b32 v" #a ", " s "\n\tv_mov_b32 v" #b ", " s "\n\tv_mov_b32 v" #c ", " s "\n\tv_mov_b32 v" #d ", " s "\n\t"
#define AISSUE                                                   \
    "s_add_u32 m0, s44, %[lw]\n\t"                               \
    "s_add_u32 s44, s44, 0x4000\n\t"                             \
    "buffer_load_dwordx4 v14, %[rk], s43 offen sc1 lds\n\t"      \
    "s_add_u32 m0, m0, 0x2000\n\t"                               \
    "s_and_b32 s44, s44, 0xffff\n\t"                             \
    "buffer_load_dwordx4 v14, %[rv], s43 offen sc1 lds\n\t"      \
    "s_add_u32 s43, s43, 0x2000\n\t"
DI u32x4 srd(const char* base, unsigned bytes) {
    return u32x4{(unsigned)(uintptr_t)base, (unsigned)((uintptr_t)base >> 32) & 0xffffu, bytes, 0x00020000u};
}
constexpr int A_ONES = 65536, A_VOTE = 69632 + 512, A_LSUM = 71680;
struct AttnRegs { f32x16 oa, ob, nm; u32x4 p1, v1; unsigned ka, va, go; };
DI void attn_prep(AttnRegs& R, float negm, int w) {
    const int lane = lane_id(), r = lane & 31, kh = w >> 2;
    R.ka = 4096 * kh + lane * 16;
    R.va = r < 16 ? 8192 + 4096 * kh + (lane >> 5) * 256 + r * 16 : A_ONES + (r == 16 ? 0 : r == 20 ? 64 : (r > 20 && r < 28) ? 80 : 16);
    R.go = w * 1024 + lane * 16;
#pragma unroll
    for (int i = 0; i < 16; ++i) { R.oa[i] = 0.f; R.ob[i] = 0.f; R.nm[i] = negm; }
    R.p1 = u32x4{0u, 0u, 0u, 0u};
    R.v1 = u32x4{0u, 0u, 0u, 0u};
    asm volatile("" : "+{v[64:79]}"(R.oa), "+{v[80:95]}"(R.ob), "+{v[16:31]}"(R.nm), "+{v[100:103]}"(R.p1), "+{v[124:127]}"(R.v1),
                      "+{v12}"(R.ka), "+{v13}"(R.va), "+{v14}"(R.go));
}
DI void attn_pass(AttnRegs& R, const char* qa, const char* qb, const char* ksrc, const char* vsrc, const char* go_flag, unsigned tag, int w) {
    const u32x4 rk = srd(ksrc, 65536), rv = srd(vsrc, 65536);
    asm volatile(
        "v_and_b32 v15, 0x3ff, v14\n\t"
        "s_mov_b32 s43, 0\n\t"
        "s_mov_b32 s44, 0\n\t"
        AISSUE
        "global_load_dwordx4 v[4:7], v15, %[qa] sc1\n\t"
        "global_load_dwordx4 v[8:11], v15, %[qb] sc1\n\t"
        AISSUE AISSUE
        "s_mov_b32 s40, 0xffff\n\t"
        "s_mov_b32 s41, 0xffff\n\t"
        "s_mov_b32 s42, 8\n\t"
        "s_waitcnt vmcnt(4)\n\t"
        "s_barrier\n\t"
        "ds_read_b128 v[104:107], v12\n\t"
        "ds_read_b128 v[108:111], v12 offset:1024\n\t"
        "ds_read_b128 v[112:115], v13\n\t"
        "ds_read_b128 v[116:119], v13 offset:512\n\t"
        "s_cmp_lg_u32 %[lw], 0\n\t"
        "s_cbranch_scc1 LA_g%=\n\t"
        "v_mov_b32 v15, %[tag]\n\t"
        "v_mov_b32 v32, 0\n\t"
        "s_mov_b64 exec, 1\n\t"
        "global_store_dword v32, v15, %[gf] sc1\n\t"
        "s_mov_b64 exec, -1\n\t"
        "LA_g%=:\n\t"
        "s_setprio 0\n\t"
        "s_waitcnt lgkmcnt(3)\n\t"
        MF(R_SA, "v[104:107]", "v[4:7]", R_NM)
        "s_nop 15\n\t"
        "LA_loop%=:\n\t"
        ATILE_E(2048, 1024, 1536, "")
        ATILE_O(3072, 2048, 2560, "")
        ATILE_E(0, 3072, 3584,
                "s_waitcnt vmcnt(2)\n\t"
                "s_barrier\n\t"
                "s_setprio 3\n\t"
                "v_add_u32 v12, 0x4000, v12\n\t"
                AISSUE
                "v_and_b32 v12, 0xffff, v12\n\t"
                "s_setprio 0\n\t")
        ATILE_O(1024, 0, 512,
                "s_mov_b64 exec, s[40:41]\n\t"
                "v_add_u32 v13, 0x4000, v13\n\t"
                "s_sub_u32 s42, s42, 1\n\t"
                "v_and_b32 v13, 0xffff, v13\n\t"
                "s_mov_b64 exec, -1\n\t"
                "s_cmp_lg_u32 s42, 0\n\t")
        "s_cbranch_scc1 LA_loop%=\n\t"
        "s_waitcnt lgkmcnt(0)\n\t"
        "s_setprio 3\n\t"
        MF(R_OB, "v[124:127]", "v[100:103]", R_OB)
        "s_nop 15\n\t"
        : "+{v[64:79]}"(R.oa), "+{v[80:95]}"(R.ob), "+{v[100:103]}"(R.p1), "+{v[124:127]}"(R.v1), "+{v12}"(R.ka), "+{v13}"(R.va)
        : "{v[16:31]}"(R.nm), "{v14}"(R.go), [qa] "s"(qa), [qb] "s"(qb), [rk] "s"(rk), [rv] "s"(rv), [lw] "s"(w * 1024), [tag] "s"(tag), [gf] "s"(go_flag)
        : "memory", "scc", "s40", "s41", "s42", "s43", "s44", "v4", "v5", "v6", "v7", "v8", "v9", "v10", "v11", "v15",
          "v32", "v33", "v34", "v35", "v36", "v37", "v38", "v39", "v40", "v41", "v42", "v43", "v44", "v45", "v46", "v47",
          "v48", "v49", "v50", "v51", "v52", "v53", "v54", "v55", "v56", "v57", "v58", "v59", "v60", "v61", "v62", "v63",
          "v96", "v97", "v98", "v99", "v104", "v105", "v106", "v107", "v108", "v109", "v110", "v111",
          "v112", "v113", "v114", "v115", "v116", "v117", "v118", "v119", "v120", "v121", "v122", "v123");
}

DI void attn_role(const char* x, const char* wqkv, char* ws, char* lds, int a, int w) {
    const int kh = w >> 2;
    const int head = 4 * (a & 7) + ((a >> 3) & 3), qt = 8 * (a >> 5) + 2 * (w & 3);
    ST_DECL; ST(0);
    __builtin_amdgcn_s_setprio(3);
    const unsigned tag = __builtin_amdgcn_readfirstlane(ld4(ws + WS_EPOCH)) * 0x9E3779B1u + 0x7F4A7C15u;
    {
        const int m = 4 * ((a & 7) >> 1) + ((a >> 3) & 3), p = 8 * (a & 1) + (a >> 5);
        if (w >= 4) {
            qkv_loader(x, wqkv, m, p, w - 4 ST_PASS);
            ST(4);
            if (w == 4) {
                const int lane = lane_id(), hp = head >> 1;
                const int fp = lane < 16 ? (16 + hp) / 3 : lane < 32 ? (32 + hp) / 3 : hp / 3;
                const int fm = lane < 32 ? (lane & 15) : 2 * (a >> 5) + (lane & 1);
                __builtin_amdgcn_s_sleep(QSLEEP);
                wait_flags<4>(ws + WS_FQ, (fp * 16 + fm) * 128, tag);
            }
        } else if (p < 10) qkv_body<3>(ws, lds, m, p, w, tag ST_PASS);
        else if (p == 10) qkv_body<2>(ws, lds, m, p, w, tag ST_PASS);
        else qkv_body<0>(ws, lds, m, p, w, tag ST_PASS);
    }
    AttnRegs R;
    attn_prep(R, 0.f, w);
    ST(5);
    wgbar();
    ST(6);
    {
        const int o = w * 512 + lane_id() * 8;
        const unsigned one = ((o & 0x1b0) == 0) ? 0x3f803f80u : 0u;
        *(LAS u32x2*)((LAS char*)lds + A_ONES + o) = u32x2{one, one};
        asm volatile("s_waitcnt lgkmcnt(0)" ::: "memory");
    }
    const char* ksrc = ws + WS_K + (size_t)head * 65536;
    const char* qa = ws + WS_Q + (size_t)(head * 64 + qt + kh) * 1024;
    const char* qb = ws + WS_Q + (size_t)(head * 64 + qt + 1 - kh) * 1024;
#pragma clang loop unroll(disable)
    for (int att = 0;; ++att) {
        ST(1);
        attn_pass(R, qa, qb, ksrc, ksrc + (WS_V - WS_K), ws + WS_FA + a * 128, tag, w);
        ST(2);
        const int lane = lane_id();
        const f32x16 oa = R.oa, ob = R.ob;
        LAS char* ex = (LAS char*)lds + w * 2048 + lane * 16;
        LAS const char* exr = (LAS const char*)lds + (w ^ 4) * 2048 + lane * 16;
        *(LAS f32x4*)ex = f32x4{ob[0], ob[1], ob[2], ob[3]};
        *(LAS f32x4*)(ex + 1024) = f32x4{ob[4], ob[5], ob[6], ob[7]};
        *(LAS float*)((LAS char*)lds + A_LSUM + w * 256 + lane * 4) = ob[8];
        float big = fmaxf(oa[8], ob[8]) * 2.7e8f;
#pragma unroll
        for (int i = 0; i < 8; ++i) big = fmaxf(fmaxf(big, __builtin_fabsf(oa[i])), __builtin_fabsf(ob[i]));
        const bool bad = !__all(fminf(oa[8], ob[8]) > 8.1e-28f && big < 1.7e38f);
        if (lane == 0) ((LAS unsigned*)((LAS char*)lds + A_VOTE))[w] = bad;
        wgbar();
        const f32x4 t0 = *(LAS const f32x4*)exr, t1 = *(LAS const f32x4*)(exr + 1024);
        const float t2 = *(LAS const float*)((LAS char*)lds + A_LSUM + (w ^ 4) * 256 + lane * 4);
        const u32x4 v0 = *(LAS const u32x4*)((LAS char*)lds + A_VOTE), v1 = *(LAS const u32x4*)((LAS char*)lds + A_VOTE + 16);
        const float rinv = __builtin_amdgcn_rcpf(oa[8] + t2);
        u32x4 ov;
        ov[0] = cvtpk((oa[0] + t0[0]) * rinv, (oa[1] + t0[1]) * rinv);
        ov[1] = cvtpk((oa[2] + t0[2]) * rinv, (oa[3] + t0[3]) * rinv);
        ov[2] = cvtpk((oa[4] + t1[0]) * rinv, (oa[5] + t1[1]) * rinv);
        ov[3] = cvtpk((oa[6] + t1[2]) * rinv, (oa[7] + t1[3]) * rinv);
        const bool redo = att == 0 && __builtin_amdgcn_readfirstlane(v0[0] | v0[1] | v0[2] | v0[3] | v1[0] | v1[1] | v1[2] | v1[3]);
        if (att == 0 ? !redo : att == 1 + kh) {
            st16(ws + WS_O + (size_t)((qt + kh) * 32 + head) * 1024, lane * 16, ov);
            drain();
            st4(ws + WS_FO + ((qt + kh) * 32 + head) * 128, tag);
        }
        asm volatile("" : "+s"(att));
        if (att == 2 || (att == 0 && !redo)) break;
        drain();
        wgbar();
        qa = qb = ws + WS_Q + (size_t)(head * 64 + qt + att) * 1024;
        bf16x8 qf;
        asm volatile("global_load_dwordx4 %0, %1, %2 sc1\n\ts_waitcnt vmcnt(0)" : "=v"(qf) : "v"(lane * 16), "s"(qa) : "memory");
        float mx = -INFINITY;
#pragma clang loop unroll(disable)
        for (int t = 0; t < 64; ++t) {
            bf16x8 kf;
            asm volatile("global_load_dwordx4 %0, %1, %2 sc1\n\ts_waitcnt vmcnt(0)" : "=v"(kf) : "v"(lane * 16), "s"(ksrc + t * 1024) : "memory");
            const f32x16 s = MFMA32(kf, qf, f32x16{});
#pragma unroll
            for (int i = 0; i < 16; ++i) mx = fmaxf(mx, s[i]);
        }
        attn_prep(R, -swap32_max(mx), w);
    }
    ST(3); ST_OUT(0);
}

#define BG1 "s_sleep 1\n\t"
#define BG2 "s_sleep 1\n\t"
#define BG3 "s_sleep 1\n\t"
#define SISSUE                                                             \
    "s_mov_b32 m0, s44\n\t"                                                 \
    "s_add_u32 s45, %[ow], %[wst]\n\t"                                      \
    "buffer_load_dwordx4 %[g0], %[rw], %[ow] offen lds\n\t"                 \
    "buffer_load_dwordx4 %[g1], %[rw], %[ow] offen offset:1024 lds\n\t"     \
    "s_add_u32 m0, s44, 8192\n\t"                                           \
    "s_add_u32 s46, %[od], 4096\n\t"                                        \
    "buffer_load_dwordx4 %[d0], %[rd], %[od] offen sc1 lds\n\t"             \
    "buffer_load_dwordx4 %[d0], %[rd], %[od] offen offset:1024 sc1 lds\n\t" \
    "s_add_u32 s44, s44, 0x4000\n\t"                                        \
    "s_and_b32 s44, s44, 0xffff\n\t"                                        \
    "s_cmp_eq_u32 %[od], %[de]\n\t"                                         \
    "s_cselect_b32 %[ow], 0, s45\n\t"                                       \
    "s_cselect_b32 %[od], 0, s46\n\t"
struct SG { u32x4 rw, rd; unsigned wst, de, ow, od, a, b, c; };
DI void sgemm_load(SG& G, int n, int lw) {
    asm volatile(
        "s_mov_b32 s44, %[lo]\n\t"
        "s_sub_u32 s47, %[n], 3\n\t"
        SISSUE SISSUE SISSUE
        "LL_loop%=:\n\t"
        "s_waitcnt vmcnt(8)\n\t"
        "s_barrier\n\t"
        SISSUE
        "s_sub_u32 s47, s47, 1\n\t"
        "s_cmp_lg_u32 s47, 0\n\t"
        "s_cbranch_scc1 LL_loop%=\n\t"
        "s_waitcnt vmcnt(8)\n\ts_barrier\n\t"
        "s_waitcnt vmcnt(4)\n\ts_barrier\n\t"
        "s_waitcnt vmcnt(0)\n\ts_barrier\n\t"
        : [ow] "+s"(G.ow), [od] "+s"(G.od)
        : [wst] "s"(G.wst), [rw] "s"(G.rw), [rd] "s"(G.rd), [lo] "s"(lw * 2048), [de] "s"(G.de), [n] "s"(n), [g0] "v"(G.a), [g1] "v"(G.b), [d0] "v"(G.c)
        : "memory", "scc", "s44", "s45", "s46", "s47");
}
#define SRD3(B, SLOT, K, TD)                                                                         \
    "ds_read_b64_tr_b16 v[" #B ":" #B "+1], %[t] offset:" #SLOT "*16384+" #K "*2048\n\t"              \
    "ds_read_b64_tr_b16 v[" #B "+2:" #B "+3], %[t] offset:" #SLOT "*16384+" #K "*2048+" #TD "\n\t"    \
    "ds_read_b128 v[" #B "+4:" #B "+7], %[dl] offset:" #SLOT "*16384+" #K "*1024\n\t"
#define SSTAGE(SLOT, MM, TD, N0, N1, N2, N3, P0, P1, P2, P3)                          \
    "s_waitcnt lgkmcnt(0)\n\t"                                                        \
    "s_barrier\n\t"                                                                   \
    MM(P0) SRD3(N0, SLOT, 0, TD) SRD3(N1, SLOT, 1, TD) BG1                            \
    MM(P1) SRD3(N2, SLOT, 2, TD) SRD3(N3, SLOT, 3, TD) BG2                            \
    MM(P2) BG3 MM(P3)
#define SST_E(SLOT, MM, TD) SSTAGE(SLOT, MM, TD, 32, 40, 48, 56, 96, 104, 112, 120)
#define SST_O(SLOT, MM, TD) SSTAGE(SLOT, MM, TD, 96, 104, 112, 120, 32, 40, 48, 56)
#define SMM_NONE(P) ""
#define SMM_WA(P) MF("v[64:79]", "v[" #P ":" #P "+3]", "v[" #P "+4:" #P "+7]", "v[64:79]")
#define SMM_WB(P) MF("v[80:95]", "v[" #P "+4:" #P "+7]", "v[" #P ":" #P "+3]", "v[80:95]")
#define SGEMM_ASM(ACC, MM, TD, NLOOP, TAIL)                                                                       \
    asm volatile(                                                                                                 \
        "s_mov_b32 s44, " #NLOOP "\n\t"                                                                           \
        "s_barrier\n\t"                                                                                           \
        SRD3(32, 0, 0, TD) SRD3(40, 0, 1, TD) SRD3(48, 0, 2, TD) SRD3(56, 0, 3, TD)                               \
        "LS_loop%=:\n\t"                                                                                          \
        SST_O(1, MM, TD) SST_E(2, MM, TD) SST_O(3, MM, TD) SST_E(0, MM, TD)                                       \
        "s_sub_u32 s44, s44, 1\n\t"                                                                               \
        "s_cmp_lg_u32 s44, 0\n\t"                                                                                 \
        "s_cbranch_scc1 LS_loop%=\n\t"                                                                            \
        TAIL                                                                                                      \
        "s_waitcnt lgkmcnt(0)\n\t"                                                                                \
        MM(96) BG1 MM(104) BG1 MM(112) BG1 MM(120)                                                                \
        "s_nop 15\n\t"                                                                                            \
        : ACC(acc)                                                                                                \
        : [t] "v"(G.a), [dl] "v"(G.b)                                                                             \
        : "memory", "scc", "s44",                                                                                 \
          "v32", "v33", "v34", "v35", "v36", "v37", "v38", "v39", "v40", "v41", "v42", "v43", "v44", "v45", "v46", "v47", \
          "v48", "v49", "v50", "v51", "v52", "v53", "v54", "v55", "v56", "v57", "v58", "v59", "v60", "v61", "v62", "v63", \
          "v96", "v97", "v98", "v99", "v100", "v101", "v102", "v103", "v104", "v105", "v106", "v107", "v108", "v109", "v110", "v111", \
          "v112", "v113", "v114", "v115", "v116", "v117", "v118", "v119", "v120", "v121", "v122", "v123", "v124", "v125", "v126", "v127")

template <int MODE>
DI SG sgemm_setup(const char* wbase, unsigned wbytes, int wrow, const char* dbase, int dblk, int rot, int w) {
    const int lane = lane_id(), row = lane >> 3, lw = w & 3;
    const int q = (lane & 15) >> 2, h2 = lane >> 5;
    SG G;
    G.rw = srd(wbase, wbytes); G.rd = srd(dbase, 2 * dblk);
    G.wst = 64 * wrow; G.de = (MODE == 0 ? 31 : 21) * 4096; G.ow = rot * G.wst; G.od = rot * 4096;
    if (w >= 4) {
        G.a = (16 * lw + row) * wrow + (((lane & 7) ^ (((row >> 1) & 1) << 2)) << 4);
        G.b = G.a + 8 * wrow - 1024;
        G.c = (lw >> 1) * dblk + (lw & 1) * 2048 + lane * 16;
    } else {
        G.a = ((MODE == 0 ? 8 : 4) * h2 + q) * 128 + (((lw & 1) ^ ((q >> 1) & 1)) << 6) + ((lane >> 4) & 1) * 32 + (lane & 3) * 8;
        G.b = 8192 + (lw >> 1) * 4096 + lane * 16;
        G.c = 0;
    }
    asm volatile("" : "+v"(G.a), "+v"(G.b), "+v"(G.c));
    return G;
}
#define ACC0 "+{v[64:79]}"
#define ACC1 "+{v[80:95]}"
template <int MODE>
DI void sgemm_run(f32x16& acc, SG& G) {
    if (MODE == 0) SGEMM_ASM(ACC0, SMM_WA, 512, 7, SST_O(1, SMM_WA, 512) SST_E(2, SMM_WA, 512) SST_O(3, SMM_WA, 512));
    else SGEMM_ASM(ACC1, SMM_WB, 1024, 5, SST_O(1, SMM_WB, 1024));
}
DI f32x4 sub4(const f32x16& a, int g) { return f32x4{a[4 * g], a[4 * g + 1], a[4 * g + 2], a[4 * g + 3]}; }

DI unsigned gelu2(float a, float b) {
    constexpr float c1 = -2.f * 0.7978845608028654f * 1.4426950408889634f, c2 = c1 * 0.044715f;
    const f32x2 u = {a, b};
    const f32x2 z = u * (u * u * c2 + c1);
    const f32x2 d = f32x2{__builtin_amdgcn_exp2f(z[0]), __builtin_amdgcn_exp2f(z[1])} + 1.f;
    const f32x2 o = u * f32x2{__builtin_amdgcn_rcpf(d[0]), __builtin_amdgcn_rcpf(d[1])};
    return cvtpk(o[0], o[1]);
}

#define WRD(R0, R1, R2, R3, STEP)                                                    \
    "ds_read_b64_tr_b16 " R0 ", %[t0] offset:" #STEP "*2048\n\t"                      \
    "ds_read_b64_tr_b16 " R1 ", %[t0] offset:" #STEP "*2048+1024\n\t"                 \
    "ds_read_b64_tr_b16 " R2 ", %[t1] offset:" #STEP "*2048\n\t"                      \
    "ds_read_b64_tr_b16 " R3 ", %[t1] offset:" #STEP "*2048+1024\n\t"
#define WRD0(S) WRD("v[32:33]", "v[34:35]", "v[36:37]", "v[38:39]", S)
#define WRD1(S) WRD("v[40:41]", "v[42:43]", "v[44:45]", "v[46:47]", S)
#define WRD2(S) WRD("v[48:49]", "v[50:51]", "v[52:53]", "v[54:55]", S)
#define WRD3(S) WRD("v[56:57]", "v[58:59]", "v[60:61]", "v[62:63]", S)
#define WMM(WAIT, W0, W1, O) "s_waitcnt " WAIT "\n\t" MF("v[96:111]", W0, O, "v[96:111]") MF("v[112:127]", W1, O, "v[112:127]")
#define OLD(K, R, V, OFF)                                             \
    "5:\n\t"                                                           \
    "s_bitcmp1_b32 s40, " #K "\n\t"                                    \
    "s_cbranch_scc1 6f\n\t"                                            \
    "global_load_dword v24, %[fo], %[f] sc1\n\t"                       \
    "s_sub_u32 s41, s41, 1\n\t"                                        \
    "s_cselect_b32 s42, -1, 0\n\t"                                     \
    "s_waitcnt vmcnt(0)\n\t"                                           \
    "v_cmp_eq_u32 vcc, %[tag], v24\n\t"                                \
    "s_or_b32 s40, vcc_lo, s42\n\t"                                    \
    "s_bitcmp1_b32 s40, " #K "\n\t"                                    \
    "s_cbranch_scc1 6f\n\t"                                            \
    "s_sleep 3\n\t"                                                    \
    "s_branch 5b\n\t"                                                  \
    "6:\n\t"                                                           \
    "global_load_dwordx4 " R ", " V ", %[o] offset:" #OFF " sc1\n\t"
DI void wo_gemm(f32x16& a0, f32x16& a1, f32x4& k0, f32x4& k1, float& k2, const char* osrc, const char* fsrc, unsigned tag, int w) {
    const int lane = lane_id();
    const int q = (lane & 15) >> 2, h2 = lane >> 5;
    const unsigned tb = (w >> 1) * 16384 + (4 * h2 + q) * 128 + ((lane >> 4) & 1) * 32 + (lane & 3) * 8;
    const unsigned t0 = tb + ((q >> 1) & 1) * 64, t1 = tb + (((q >> 1) & 1) ^ 1) * 64;
    const unsigned d0 = lane * 16, d1 = d0 + 4096, fo = (lane & 7) * 128;
    asm volatile(
        WRD0(0) WRD1(1) WRD2(2) WRD3(3)
        "s_mov_b32 s40, 0\n\t"
        "s_mov_b32 s41, 0x100000\n\t"
        "s_setprio 0\n\t"
        OLD(0, "v[64:67]", "%[d0]", 0) OLD(1, "v[68:71]", "%[d0]", 1024) OLD(2, "v[72:75]", "%[d0]", 2048) OLD(3, "v[76:79]", "%[d0]", 3072)
        OLD(4, "v[80:83]", "%[d1]", 0) OLD(5, "v[84:87]", "%[d1]", 1024) OLD(6, "v[88:91]", "%[d1]", 2048) OLD(7, "v[92:95]", "%[d1]", 3072)
        "s_setprio 2\n\t"
        "s_waitcnt vmcnt(7) lgkmcnt(0)\n\t"
        MF("v[96:111]", "v[32:35]", "v[64:67]", "0") MF("v[112:127]", "v[36:39]", "v[64:67]", "0")
        WRD0(4)
        WMM("vmcnt(6)", "v[40:43]", "v[44:47]", "v[68:71]") WRD1(5)
        WMM("vmcnt(5)", "v[48:51]", "v[52:55]", "v[72:75]") WRD2(6)
        WMM("vmcnt(4)", "v[56:59]", "v[60:63]", "v[76:79]") WRD3(7)
        WMM("vmcnt(3) lgkmcnt(12)", "v[32:35]", "v[36:39]", "v[80:83]")
        WMM("vmcnt(2) lgkmcnt(0)", "v[40:43]", "v[44:47]", "v[84:87]")
        WMM("vmcnt(1)", "v[48:51]", "v[52:55]", "v[88:91]")
        "s_barrier\n\t"
        WMM("vmcnt(0)", "v[56:59]", "v[60:63]", "v[92:95]")
        "s_nop 15\n\t"
        : "={v[96:111]}"(a0), "={v[112:127]}"(a1),
          "+{v[12:15]}"(k0), "+{v[16:19]}"(k1), "+{v20}"(k2)
        : [o] "s"(osrc), [f] "s"(fsrc), [tag] "s"(tag), [d0] "v"(d0), [d1] "v"(d1), [t0] "v"(t0), [t1] "v"(t1), [fo] "v"(fo)
        : "memory", "scc", "vcc", "s40", "s41", "s42", "v24",
          "v32", "v33", "v34", "v35", "v36", "v37", "v38", "v39", "v40", "v41", "v42", "v43", "v44", "v45", "v46", "v47",
          "v48", "v49", "v50", "v51", "v52", "v53", "v54", "v55", "v56", "v57", "v58", "v59", "v60", "v61", "v62", "v63",
          "v64", "v65", "v66", "v67", "v68", "v69", "v70", "v71", "v72", "v73", "v74", "v75", "v76", "v77", "v78", "v79",
          "v80", "v81", "v82", "v83", "v84", "v85", "v86", "v87", "v88", "v89", "v90", "v91", "v92", "v93", "v94", "v95");
}

DI void mixer_role(const char* x, const char* wqkv, const char* gw, const char* wo, const char* w1, const char* b1, const char* w2, const char* b2,
                   char* ws, char* out, char* lds, int bi, int w) {
    const int j = bi & 7, i = bi >> 3;
    const unsigned long long rt0 = __builtin_amdgcn_s_memrealtime();
    __builtin_amdgcn_s_setprio(1);
    ST_DECL; ST(0);
    const unsigned tag = __builtin_amdgcn_readfirstlane(ld4(ws + WS_EPOCH)) * 0x9E3779B1u + 0x7F4A7C15u;
    {
        const int lane = lane_id(), h2 = lane >> 5, tid = 64 * w + lane;
        const u32x4 xr = *(GAS const u32x4*)(x + (size_t)(8 * bi + w) * 1024 + lane * 16);
        const u32x4 gr = *(GAS const u32x4*)(gw + lane * 16);
        float v[8], gg[8];
#pragma unroll
        for (int k = 0; k < 4; ++k) { v[2 * k] = bflo(xr[k]); v[2 * k + 1] = bfhi(xr[k]); gg[2 * k] = bflo(gr[k]); gg[2 * k + 1] = bfhi(gr[k]); }
        float s1 = 0.f, s3 = 0.f;
#pragma unroll
        for (int k = 0; k < 8; ++k) { s1 += v[k]; s3 += v[k] * gg[k]; }
        const float mu = wave_sum(s1) * (1.f / 512.f);
        s3 = wave_sum(s3);
        float s2 = 0.f;
#pragma unroll
        for (int k = 0; k < 8; ++k) { v[k] -= mu; s2 += v[k] * v[k]; }
        const float rs = __builtin_amdgcn_rsqf(wave_sum(s2) * (1.f / 512.f) + 1e-5f);
        u32x4 hv;
#pragma unroll
        for (int k = 0; k < 4; ++k) hv[k] = cvtpk(v[2 * k] * rs, v[2 * k + 1] * rs);
        *(LAS u32x4*)((LAS char*)lds + w * 1024 + lane * 16) = hv;
        const float g = __builtin_amdgcn_rcpf(1.f + __builtin_amdgcn_exp2f(-1.4426950408889634f * s3));
        st4(ws + WS_G + (8 * bi + w) * 4, uni(g));
        wgbar();
        LAS const unsigned short* lb = (LAS const unsigned short*)lds + tid;
#pragma unroll
        for (int k = 0; k < 4; ++k) hv[k] = (unsigned)lb[(2 * k) * 512] | ((unsigned)lb[(2 * k + 1) * 512] << 16);
        st16(ws + WS_H + (size_t)(2 * w * 128 + (bi >> 1)) * 1024 + (bi & 1) * 512, h2 * 131072 + (lane & 31) * 16, hv);
        drain();
        wgbar();
        if (w == 0) st4(ws + WS_FL + bi * 128, tag);
    }
    __builtin_amdgcn_s_setprio(2);
    ST(6);
    const int wb = w & 1, db = (w >> 1) & 1, hf = w >> 2;
    const int L = (j >> 1) * 22 + i, ft = L >> 2, dt1 = 4 * (j & 1) + (L & 3);
    const int st = 8 * (j >> 1) + (i & 7), dt = 4 * (j & 1) + (i >> 3);
    f32x16 acc1, acc2;
    SG G1, G2;
    unsigned ex, l16, og;
    {
        const int lane = lane_id();
        const bool live = i < 22 && (ft < 21 || wb == 0);
        const int f0 = 64 * ft + 32 * wb + 4 * (lane >> 5);
        u32x2 bv[4] = {};
        if (live) {
#pragma unroll
            for (int k = 0; k < 4; ++k) bv[k] = *(GAS const u32x2*)(b1 + f0 * 2 + 16 * k);
        }
        og = (64 * st + 32 * wb + (lane & 31)) * 4;
        const float bb = bflo(*(GAS const unsigned short*)(b2 + (og >> 1)));
        G1 = sgemm_setup<0>(w1 + ft * 128, 2048 * 2752 - ft * 128, 2752, ws + WS_H + (size_t)2 * dt1 * 131072, 131072, ROT1, w);
        G2 = sgemm_setup<1>(w2 + st * 128, 1376 * 4096 - st * 128, 4096, ws + WS_U + (size_t)2 * dt * 86 * 1024, 86 * 1024, ROT2, w);
        l16 = lane * 16;
        ex = 65536 + (w & 3) * 2048 + l16;
#pragma unroll
        for (int k = 0; k < 4; ++k) {
            acc1[4 * k] = bflo(bv[k][0]); acc1[4 * k + 1] = bfhi(bv[k][0]); acc1[4 * k + 2] = bflo(bv[k][1]); acc1[4 * k + 3] = bfhi(bv[k][1]);
            acc2[4 * k] = bb; acc2[4 * k + 1] = bb; acc2[4 * k + 2] = bb; acc2[4 * k + 3] = bb;
        }
        asm volatile("" : "+{v[64:79]}"(acc1), "+{v[80:95]}"(acc2), "+v"(ex), "+v"(l16), "+v"(og));
    }
    if (i < 22) {
        if (w >= 4) { __builtin_amdgcn_s_sleep(LSLEEP); wait_flags<8>(ws + WS_FL + (w - 4) * 8192, l16 * 8, tag); }
        if (w == 0) { __builtin_amdgcn_s_sleep(LSLEEP); wait_flags<4>(ws + WS_FA + bi * 128, 0, tag); }
        wgbar();
        ST(7);
        f32x4 v0, v1;
        if (w >= 4) {
            sgemm_load(G1, 32, w - 4);
            wgbar();
            v0 = *(LAS const f32x4*)(uintptr_t)ex; v1 = *(LAS const f32x4*)(uintptr_t)(ex + 1024);
        } else {
            sgemm_run<0>(acc1, G1);
            *(LAS f32x4*)(uintptr_t)ex = sub4(acc1, 2); *(LAS f32x4*)(uintptr_t)(ex + 1024) = sub4(acc1, 3);
            wgbar();
            v0 = sub4(acc1, 0); v1 = sub4(acc1, 1);
        }
        ST(1);
        const u32x4 o = {gelu2(v0[0], v0[1]), gelu2(v0[2], v0[3]), gelu2(v1[0], v1[1]), gelu2(v1[2], v1[3])};
        if (ft < 21 || wb == 0) st16(ws + WS_U + (size_t)((2 * dt1 + db) * 86 + 4 * ft + 2 * wb + hf) * 1024, l16, o);
        drain();
        wgbar();
        if (w == 0) st4(ws + WS_FU + (dt1 * 32 + ft) * 128, tag);
    }
    ST(2);
    if (bi == 255 && w == 1) {
#pragma clang loop unroll(disable)
        for (int k = 0; k < 4; ++k) {
            const int lane = lane_id();
            wait_flags<8>(ws + WS_FQ + k * 8192, lane * 128, tag);
            wait_flags<8>(ws + WS_FL + k * 8192, lane * 128, tag);
        }
        st4(ws + WS_EPOCH, tag);
    }
    if (w == 0) wait_flags<8>(ws + WS_FU + dt * 4096, (l16 < 22 * 16 ? l16 : 0) * 8, tag);
    wgbar();
    ST(3);
    f32x4 bp0, bp1;
    float g;
    {
        asm volatile("global_load_dword %0, %1, %2 sc1" : "=v"(g) : "v"(og), "s"(ws + WS_G) : "memory");
        if (w >= 4) {
            sgemm_load(G2, 22, w - 4);
            wgbar();
            bp0 = *(LAS const f32x4*)(uintptr_t)ex; bp1 = *(LAS const f32x4*)(uintptr_t)(ex + 1024);
        } else {
            sgemm_run<1>(acc2, G2);
            *(LAS f32x4*)(uintptr_t)ex = sub4(acc2, 2); *(LAS f32x4*)(uintptr_t)(ex + 1024) = sub4(acc2, 3);
            wgbar();
            bp0 = sub4(acc2, 0); bp1 = sub4(acc2, 1);
        }
        asm volatile("s_waitcnt vmcnt(0)" : "+v"(g)::"memory");
        ST(4);
        bp0 *= 1.f - g;
        bp1 *= 1.f - g;
        asm volatile("" : "+v"(bp0), "+v"(bp1));
        const int lane = lane_id(), row = lane >> 3;
        const unsigned g0 = row * 1024 + (((lane & 7) ^ (((row >> 1) & 1) << 2)) << 4);
        const char* wsrc = wo + (size_t)w * 65536 + dt * 128;
#pragma unroll
        for (int k = 0; k < 8; ++k) glds(w * 8192 + k * 1024, g0, wsrc + k * 8192);
    }
    drain();
    wgbar();
    ST(5);
    f32x16 a0, a1;
    wo_gemm(a0, a1, bp0, bp1, g, ws + WS_O + (size_t)((2 * st + wb) * 32 + 8 * (w >> 1)) * 1024,
            ws + WS_FO + ((2 * st + wb) * 32 + 8 * (w >> 1)) * 128, tag, w);
    {
        const int lane = lane_id(), kq = 2 * db + hf;
        LAS char* ex = (LAS char*)lds + w * 8192 + lane * 16;
#pragma unroll
        for (int k = 0; k < 4; ++k) { *(LAS f32x4*)(ex + k * 1024) = sub4(a0, k); *(LAS f32x4*)(ex + 4096 + k * 1024) = sub4(a1, k); }
        wgbar();
        LAS const char* exr = (LAS const char*)lds + wb * 8192 + kq * 2048 + lane * 16;
        f32x4 s0 = *(LAS const f32x4*)exr, s1 = *(LAS const f32x4*)(exr + 1024);
#pragma unroll
        for (int k = 1; k < 4; ++k) { s0 += *(LAS const f32x4*)(exr + k * 16384); s1 += *(LAS const f32x4*)(exr + k * 16384 + 1024); }
        s0 = s0 * g + bp0;
        s1 = s1 * g + bp1;
        const auto r0 = __builtin_amdgcn_permlane32_swap(cvtpk(s0[0], s0[1]), cvtpk(s1[0], s1[1]), false, false);
        const auto r1 = __builtin_amdgcn_permlane32_swap(cvtpk(s0[2], s0[3]), cvtpk(s1[2], s1[3]), false, false);
        *(GAS u32x4*)(out + (size_t)(64 * st + 32 * wb) * 1024 + (64 * dt + 16 * kq) * 2 + (lane & 31) * 1024 + (lane >> 5) * 16) = u32x4{r0[0], r1[0], r0[1], r1[1]};
    }
    while (__builtin_amdgcn_s_memrealtime() - rt0 < 5000ull) __builtin_amdgcn_s_sleep(1);
}

constexpr int MK_LDS = 81920;
__global__ __launch_bounds__(512, 4) void mk(const char* x, const char* wqkv, const char* wo, const char* w1, const char* b1,
                                             const char* w2, const char* b2, const char* gw, char* ws, char* out) {
    extern __shared__ __attribute__((aligned(16))) char lds[];
    const int w = __builtin_amdgcn_readfirstlane(threadIdx.x >> 6);
    if (blockIdx.x < 256) attn_role(x, wqkv, ws, lds, blockIdx.x, w);
    else mixer_role(x, wqkv, gw, wo, w1, b1, w2, b2, ws, out, lds, blockIdx.x - 256, w);
}

__global__ __launch_bounds__(512) void nopk(char* p) { if (p == nullptr) *p = 0; }
extern "C" void kernel_launch(void* const* d_in, const int* in_sizes, int n_in, void* d_out, int out_size, void* d_ws, size_t ws_size,
                              hipStream_t stream) {
    (void)hipFuncSetAttribute((const void*)mk, hipFuncAttributeMaxDynamicSharedMemorySize, MK_LDS);
    mk<<<512, 512, MK_LDS, stream>>>((const char*)d_in[0], (const char*)d_in[1], (const char*)d_in[2], (const char*)d_in[4],
                                     (const char*)d_in[5], (const char*)d_in[6], (const char*)d_in[7], (const char*)d_in[8],
                                     (char*)d_ws, (char*)d_out);
    nopk<<<512, 512, 0, stream>>>((char*)d_ws);
}
```

```cpp
#include <hip/hip_runtime.h>
#include <hip/hip_bf16.h>
#include <stdint.h>

typedef __attribute__((ext_vector_type(8))) short bf16x8;
typedef __attribute__((ext_vector_type(4))) short s16x4;
typedef __attribute__((ext_vector_type(16))) float f32x16;
typedef __attribute__((ext_vector_type(4))) float f32x4;
typedef __attribute__((ext_vector_type(2))) float f32x2;
typedef __attribute__((ext_vector_type(4))) unsigned u32x4;
typedef __attribute__((ext_vector_type(2))) unsigned u32x2;
#define LAS __attribute__((address_space(3)))
#define GAS __attribute__((address_space(1)))
#define DI __device__ __forceinline__
#define MFMA32(a, b, c) __builtin_amdgcn_mfma_f32_32x32x16_bf16((a), (b), (c), 0, 0, 0)

#define QSLEEP 24
#define LSLEEP 32
#define ROT1 (((dt1 + ft) & 1) * 16)
#define ROT2 (((st + dt) & 1) * 11)
#define GATEQ 16
#define QROT 6
constexpr int S_ = 2048, D_ = 512, F_ = 1376;
constexpr size_t WS_EPOCH = 0;
constexpr size_t WS_G = 4096;
constexpr size_t WS_KN = WS_G + 8192;
constexpr size_t WS_FL = 98304;
constexpr size_t WS_FQ = 32768;
constexpr size_t WS_FU = 65536;
constexpr size_t WS_FA = 393216;
constexpr size_t WS_FO = 131072;
constexpr size_t WS_Q = 1u << 20;
constexpr size_t WS_K = WS_Q + (2u << 20);
constexpr size_t WS_V = WS_K + (2u << 20);
constexpr size_t WS_H = WS_V + (2u << 20);
constexpr size_t WS_U = WS_H + (2u << 20);
constexpr size_t WS_O = WS_U + (2u << 20);

#define ST_DECL
#define ST_ARG
#define ST_PASS
#define ST(i)
#define ST_OUT(k)
DI unsigned cvtpk(float lo, float hi) {
    typedef float f32x2_t __attribute__((ext_vector_type(2)));
    typedef __bf16 bf16x2_t __attribute__((ext_vector_type(2)));
    return __builtin_bit_cast(unsigned, __builtin_convertvector(f32x2_t{lo, hi}, bf16x2_t));
}
DI float bflo(unsigned u) { return __uint_as_float(u << 16); }
DI float bfhi(unsigned u) { return __uint_as_float(u & 0xffff0000u); }
DI s16x4 trread(LAS const char* p) {
    typedef short v4i16_t __attribute__((ext_vector_type(4)));
    return __builtin_bit_cast(s16x4, __builtin_amdgcn_ds_read_tr16_b64_v4i16((LAS v4i16_t*)p));
}
DI void glds(unsigned lds, unsigned voff, const void* sbase) {
    asm volatile("s_mov_b32 m0, %0\n\ts_nop 0\n\tglobal_load_lds_dwordx4 %1, %2" ::"s"(lds), "v"(voff), "s"(sbase) : "memory");
}
template <int CTRL> DI float dpp(float v) {
    return __int_as_float(__builtin_amdgcn_update_dpp(0, __float_as_int(v), CTRL, 0xf, 0xf, true));
}
DI float rl(float v, int l) { return __int_as_float(__builtin_amdgcn_readlane(__float_as_int(v), l)); }
DI float wave_sum(float v) {
    v += dpp<0xB1>(v); v += dpp<0x4E>(v); v += dpp<0x141>(v); v += dpp<0x140>(v);
    return (rl(v, 0) + rl(v, 16)) + (rl(v, 32) + rl(v, 48));
}
DI float wave_max(float v) {
    v = fmaxf(v, dpp<0xB1>(v)); v = fmaxf(v, dpp<0x4E>(v)); v = fmaxf(v, dpp<0x141>(v)); v = fmaxf(v, dpp<0x140>(v));
    return fmaxf(fmaxf(rl(v, 0), rl(v, 16)), fmaxf(rl(v, 32), rl(v, 48)));
}
DI float swap32_add(float v) {
    auto rr = __builtin_amdgcn_permlane32_swap(__float_as_uint(v), __float_as_uint(v), false, false);
    return __uint_as_float(rr[0]) + __uint_as_float(rr[1]);
}
DI float swap32_max(float v) {
    auto rr = __builtin_amdgcn_permlane32_swap(__float_as_uint(v), __float_as_uint(v), false, false);
    return fmaxf(__uint_as_float(rr[0]), __uint_as_float(rr[1]));
}

DI int lane_id() {
    int l;
    asm volatile("v_mbcnt_lo_u32_b32 %0, -1, 0\n\tv_mbcnt_hi_u32_b32 %0, -1, %0" : "=v"(l));
    __builtin_assume(l >= 0 && l < 64);
    return l;
}
DI void st16(const char* sbase, unsigned voff, u32x4 v) {
    asm volatile("global_store_dwordx4 %0, %1, %2 sc1\n\ts_nop 1" ::"v"(voff), "v"(v), "s"(sbase) : "memory");
}
DI void st4(const char* p, unsigned v) {
    unsigned t0, t1;
    asm volatile("s_nop 1\n\tv_mov_b32 %0, %2\n\tv_mov_b32 %1, 0\n\ts_mov_b64 exec, 1\n\tglobal_store_dword %1, %0, %3 sc1\n\ts_mov_b64 exec, -1"
                 : "=&v"(t0), "=&v"(t1) : "s"(v), "s"(p) : "memory");
}
DI unsigned uni(float v) { return __builtin_amdgcn_readfirstlane(__float_as_uint(v)); }
DI unsigned ld4(const char* p) { return __hip_atomic_load((GAS const unsigned*)p, __ATOMIC_RELAXED, __HIP_MEMORY_SCOPE_AGENT); }
DI void drain() { asm volatile("s_waitcnt vmcnt(0)" ::: "memory"); }
DI void wgbar() { asm volatile("s_waitcnt lgkmcnt(0)\n\ts_barrier" ::: "memory"); }
template <int SLEEP>
DI void wait_flags(const char* sbase, unsigned voff, unsigned tag) {
#pragma clang loop unroll(disable)
    for (unsigned spin = 0; spin < (1u << 20); ++spin) {
        unsigned f;
        asm volatile("global_load_dword %0, %1, %2 sc1\n\ts_waitcnt vmcnt(0)" : "=v"(f) : "v"(voff), "s"(sbase) : "memory");
        if (__all(f == tag)) break;
        __builtin_amdgcn_s_sleep(SLEEP);
    }
}

constexpr int QW0 = 49152, QCNT = 73728;
DI void qkv_loader(const char* x, const char* wqkv, int m, int p, int lw ST_ARG) {
    const int lane = lane_id();
    unsigned vx[4], vw[3];
#pragma unroll
    for (int ii = 0; ii < 4; ++ii) {
        const int row = 32 * lw + 8 * ii + (lane >> 3);
        vx[ii] = row * 1024 + (((lane & 7) ^ ((row >> 1) & 7)) << 4);
    }
#pragma unroll
    for (int ii = 0; ii < 3; ++ii) {
        const int o = 16 * lane + 1024 * ii;
        vw[ii] = (16 * lw + o / 192) * 3072 + (o % 192);
    }
    const char* xb = x + (size_t)m * 131072;
    const char* wb = wqkv + p * 192;
    const int rot = (p + 2 * m) & QROT;
    auto isx = [&](int s, int slot) {
        const char* xs = xb + ((s + rot) & 7) * 128;
#pragma unroll
        for (int ii = 0; ii < 4; ++ii) glds(slot * 16384 + lw * 4096 + ii * 1024, vx[ii], xs);
    };
    auto isw = [&](int s) {
        const char* wsrc = wb + ((s + rot) & 7) * 196608;
#pragma unroll
        for (int ii = 0; ii < 3; ++ii) glds(QW0 + (s & 1) * 12288 + lw * 3072 + ii * 1024, vw[ii], wsrc);
    };
    isw(0); isx(0, 0); isw(1); isx(1, 1); isx(2, 2);
    *(LAS unsigned*)QCNT = 0u;
    asm volatile("s_waitcnt vmcnt(11) lgkmcnt(0)\n\ts_barrier" ::: "memory");
    ST(7);
    int slot = 0;
#pragma clang loop unroll(disable)
    for (int s = 1; s < 6; ++s) {
        asm volatile("s_waitcnt vmcnt(4)\n\ts_barrier" ::: "memory");
        isw(s + 1); isx(s + 2, slot);
        slot = slot == 2 ? 0 : slot + 1;
    }
    asm volatile("s_waitcnt vmcnt(4)\n\ts_barrier" ::: "memory");
    isw(7);
    asm volatile("s_waitcnt vmcnt(0)\n\ts_barrier" ::: "memory");
}

template <int NT>
DI void qkv_body(char* ws, char* lds, int m, int p, int w, unsigned tag ST_ARG) {
    const int lane = lane_id(), r = lane & 31, h2 = lane >> 5;
    f32x16 acc[3];
#pragma unroll
    for (int i = 0; i < 3; ++i) acc[i] = f32x16{};
    const int sw = (r >> 1) & 7;
    int xa[4];
#pragma unroll
    for (int st = 0; st < 4; ++st) xa[st] = (32 * w + r) * 128 + (((2 * st + h2) ^ sw) << 4);
    const int wa = QW0 + (8 * h2 + ((lane & 15) >> 2)) * 192 + ((lane >> 4) & 1) * 32 + (lane & 3) * 8;
    bf16x8 xf[4], wf[2][3];
    auto rdw = [&](LAS const char* wsl, int st, int i) {
        const s16x4 lo = trread(wsl + wa + st * 3072 + i * 64), hi = trread(wsl + wa + st * 3072 + i * 64 + 768);
        wf[st & 1][i] = __builtin_shufflevector(lo, hi, 0, 1, 2, 3, 4, 5, 6, 7);
    };
    auto mm1 = [&](int st, int i) {
        if (i < NT) acc[i] = MFMA32(wf[st & 1][i], xf[st], acc[i]);
        else acc[i] = MFMA32(xf[st], wf[st & 1][i], acc[i]);
        __builtin_amdgcn_sched_barrier(0);
    };
    auto step = [&](int st, LAS const char* xsl, LAS const char* wsl, bool bar) {
        const int sn = (st + 2) & 3;
        mm1(st, 0);
        if (bar) wgbar();
        xf[sn] = *(LAS const bf16x8*)(xsl + xa[sn]); rdw(wsl, sn, 0); __builtin_amdgcn_sched_barrier(0);
        mm1(st, 1);
        rdw(wsl, sn, 1); __builtin_amdgcn_sched_barrier(0);
        mm1(st, 2);
        rdw(wsl, sn, 2); __builtin_amdgcn_sched_barrier(0);
    };
    wgbar();
    LAS const char* xsl = (LAS const char*)lds;
    LAS const char* wsl = (LAS const char*)lds;
#pragma unroll
    for (int st = 0; st < 2; ++st) { xf[st] = *(LAS const bf16x8*)(xsl + xa[st]); rdw(wsl, st, 0); rdw(wsl, st, 1); rdw(wsl, st, 2); }
    __builtin_amdgcn_sched_barrier(0);
#pragma clang loop unroll(disable)
    for (int s = 0; s < 7; ++s) {
        step(0, xsl, wsl, false); step(1, xsl, wsl, false);
        xsl = (xsl == (LAS const char*)lds + 32768) ? (LAS const char*)lds : xsl + 16384;
        wsl = (LAS const char*)lds + ((s & 1) ? 0 : 12288);
        step(2, xsl, wsl, true); step(3, xsl, wsl, false);
    }
    step(0, xsl, wsl, false); step(1, xsl, wsl, false);
    mm1(2, 0); mm1(2, 1); mm1(2, 2); mm1(3, 0); mm1(3, 1); mm1(3, 2);
    ST(4);
    const int tile = 4 * m + w;
#pragma unroll
    for (int u = 0; u < 6; ++u) {
        const int cb = 3 * p + (u >> 1), half = u & 1, type = cb >> 4, hp = cb & 15;
        const float sc = (type == 0) ? 0.25f * 1.4426950408889634f : 1.f;
        u32x4 o;
#pragma unroll
        for (int i = 0; i < 4; ++i) o[i] = cvtpk(acc[u >> 1][8 * half + 2 * i] * sc, acc[u >> 1][8 * half + 2 * i + 1] * sc);
        if ((u >> 1) < NT) {
            st16(ws + (type ? WS_K : WS_Q) + (size_t)((2 * hp + half) * 64 + tile) * 1024, lane * 16, o);
        } else {
            st16(ws + WS_V + (size_t)(2 * hp * 64 + tile) * 1024 + half * 512, (r >> 4) * 65536 + h2 * 256 + (r & 15) * 16, o);
        }
    }
    drain();
    ST(7);
    unsigned arr = 0;
    if (lane == 0) arr = __hip_atomic_fetch_add((LAS unsigned*)QCNT, 1u, __ATOMIC_RELAXED, __HIP_MEMORY_SCOPE_WORKGROUP);
    if (__builtin_amdgcn_readfirstlane(arr) == 3) st4(ws + WS_FQ + (p * 16 + m) * 128, tag);
}

#define EXP(a) "v_exp_f32 v" #a ", v" #a "\n\t"
#define CVT(d, a, b) "v_cvt_pk_bf16_f32 v" #d ", v" #a ", v" #b "\n\t"
#define MF(D, A, B, C) "v_mfma_f32_32x32x16_bf16 " D ", " A ", " B ", " C "\n\t"
#define ASTEP(c0, c1, c2, c3, c4, c5, c6, c7, c8, c9, c10, c11, c12, c13, c14, c15, M1, M2, CL, M3)                  \
    EXP(c0) EXP(c1) M1 EXP(c2) EXP(c3) EXP(c4) EXP(c5) M2 CL                                                          \
    EXP(c6) EXP(c7) CVT(96, c0, c1) CVT(97, c2, c3) CVT(98, c4, c5) CVT(99, c6, c7) EXP(c8) EXP(c9) M3                \
    EXP(c10) EXP(c11) EXP(c12) EXP(c13) EXP(c14) EXP(c15)                                                             \
    CVT(100, c8, c9) CVT(101, c10, c11) CVT(102, c12, c13) CVT(103, c14, c15)
#define R_OA "v[64:79]"
#define R_OB "v[80:95]"
#define R_SA "v[32:47]"
#define R_SB "v[48:63]"
#define R_NM "v[16:31]"
#define ATILE(KC, KN, V0C, V1C, V0N, V1N, KOFF, VOFF0, VOFF1, PRE)                                                    \
    ASTEP(32, 33, 34, 35, 36, 37, 38, 39, 40, 41, 42, 43, 44, 45, 46, 47,                                             \
          MF(R_OB, V1N, "v[100:103]", R_OB), MF(R_SB, KC, "v[8:11]", R_NM),                                           \
          "s_waitcnt lgkmcnt(0)\n\t" PRE                                                                              \
          "ds_read_b128 " KC ", v12 offset:" #KOFF "\n\t"                                                             \
          "ds_read_b128 " V0N ", v13 offset:" #VOFF0 "\n\t"                                                           \
          "ds_read_b128 " V1N ", v13 offset:" #VOFF1 "\n\t",                                                          \
          MF(R_OA, V0C, "v[96:99]", R_OA))                                                                            \
    ASTEP(48, 49, 50, 51, 52, 53, 54, 55, 56, 57, 58, 59, 60, 61, 62, 63,                                             \
          MF(R_OA, V1C, "v[100:103]", R_OA), MF(R_SA, KN, "v[4:7]", R_NM), "", MF(R_OB, V0C, "v[96:99]", R_OB))
#define ATILE_E(KOFF, VOFF0, VOFF1, PRE) ATILE("v[104:107]", "v[108:111]", "v[112:115]", "v[116:119]", "v[120:123]", "v[124:127]", KOFF, VOFF0, VOFF1, PRE)
#define ATILE_O(KOFF, VOFF0, VOFF1, PRE) ATILE("v[108:111]", "v[104:107]", "v[120:123]", "v[124:127]", "v[112:115]", "v[116:119]", KOFF, VOFF0, VOFF1, PRE)
#define MOV4(a, b, c, d, s) "v_mov_b32 v" #a ", " s "\n\tv_mov_b32 v" #b ", " s "\n\tv_mov_b32 v" #c ", " s "\n\tv_mov_b32 v" #d ", " s "\n\t"
#define AISSUE                                                   \
    "s_add_u32 m0, s44, %[lw]\n\t"                               \
    "s_add_u32 s44, s44, 0x4000\n\t"                             \
    "buffer_load_dwordx4 v14, %[rk], s43 offen sc1 lds\n\t"      \
    "s_add_u32 m0, m0, 0x2000\n\t"                               \
    "s_and_b32 s44, s44, 0xffff\n\t"                             \
    "buffer_load_dwordx4 v14, %[rv], s43 offen sc1 lds\n\t"      \
    "s_add_u32 s43, s43, 0x2000\n\t"
DI u32x4 srd(const char* base, unsigned bytes) {
    return u32x4{(unsigned)(uintptr_t)base, (unsigned)((uintptr_t)base >> 32) & 0xffffu, bytes, 0x00020000u};
}
#define PR2 "s_setprio 2\n\t"
#define PR3 ""
#define PR0 "s_setprio 1\n\t"
#define PR1 "s_setprio 0\n\t"
constexpr int A_ONES = 65536, A_VOTE = 69632 + 512, A_LSUM = 71680;
struct AttnRegs { f32x16 oa, ob, nm; u32x4 p1, v1; unsigned ka, va, go; };
DI void attn_prep(AttnRegs& R, float negm, int w) {
    const int lane = lane_id(), r = lane & 31, kh = w >> 2;
    R.ka = 4096 * kh + lane * 16;
    R.va = r < 16 ? 8192 + 4096 * kh + (lane >> 5) * 256 + r * 16 : A_ONES + (r == 16 ? 0 : r == 20 ? 64 : (r > 20 && r < 28) ? 80 : 16);
    R.go = w * 1024 + lane * 16;
#pragma unroll
    for (int i = 0; i < 16; ++i) { R.oa[i] = 0.f; R.ob[i] = 0.f; R.nm[i] = negm; }
    R.p1 = u32x4{0u, 0u, 0u, 0u};
    R.v1 = u32x4{0u, 0u, 0u, 0u};
    asm volatile("" : "+{v[64:79]}"(R.oa), "+{v[80:95]}"(R.ob), "+{v[16:31]}"(R.nm), "+{v[100:103]}"(R.p1), "+{v[124:127]}"(R.v1),
                      "+{v12}"(R.ka), "+{v13}"(R.va), "+{v14}"(R.go));
}
DI void attn_pass(AttnRegs& R, const char* qa, const char* qb, const char* ksrc, const char* vsrc, const char* go_flag, unsigned tag, int w) {
    const u32x4 rk = srd(ksrc, 65536), rv = srd(vsrc, 65536);
    asm volatile(
        "v_and_b32 v15, 0x3ff, v14\n\t"
        "s_mov_b32 s43, 0\n\t"
        "s_mov_b32 s44, 0\n\t"
        AISSUE
        "global_load_dwordx4 v[4:7], v15, %[qa] sc1\n\t"
        "global_load_dwordx4 v[8:11], v15, %[qb] sc1\n\t"
        AISSUE AISSUE
        "s_mov_b32 s40, 0xffff\n\t"
        "s_mov_b32 s41, 0xffff\n\t"
        "s_mov_b32 s42, 8\n\t"
        "s_waitcnt vmcnt(4)\n\t"
        "s_barrier\n\t"
        "ds_read_b128 v[104:107], v12\n\t"
        "ds_read_b128 v[108:111], v12 offset:1024\n\t"
        "ds_read_b128 v[112:115], v13\n\t"
        "ds_read_b128 v[116:119], v13 offset:512\n\t"
        "s_cmp_lg_u32 %[lw], 0\n\t"
        "s_cbranch_scc1 LA_g%=\n\t"
        "v_mov_b32 v15, %[tag]\n\t"
        "v_mov_b32 v32, 0\n\t"
        "s_mov_b64 exec, 1\n\t"
        "global_store_dword v32, v15, %[gf] sc1\n\t"
        "s_mov_b64 exec, -1\n\t"
        "LA_g%=:\n\t"
        PR2
        "s_waitcnt lgkmcnt(3)\n\t"
        MF(R_SA, "v[104:107]", "v[4:7]", R_NM)
        "s_nop 15\n\t"
        "LA_loop%=:\n\t"
        ATILE_E(2048, 1024, 1536, PR0)
        ATILE_O(3072, 2048, 2560, PR1)
        ATILE_E(0, 3072, 3584,
                "s_waitcnt vmcnt(2)\n\t"
                "s_barrier\n\t"
                "s_setprio 3\n\t"
                "v_add_u32 v12, 0x4000, v12\n\t"
                AISSUE
                "v_and_b32 v12, 0xffff, v12\n\t"
                PR2)
        ATILE_O(1024, 0, 512,
                PR3
                "s_mov_b64 exec, s[40:41]\n\t"
                "v_add_u32 v13, 0x4000, v13\n\t"
                "s_sub_u32 s42, s42, 1\n\t"
                "v_and_b32 v13, 0xffff, v13\n\t"
                "s_mov_b64 exec, -1\n\t"
                "s_cmp_lg_u32 s42, 0\n\t")
        "s_cbranch_scc1 LA_loop%=\n\t"
        "s_waitcnt lgkmcnt(0)\n\t"
        "s_setprio 3\n\t"
        MF(R_OB, "v[124:127]", "v[100:103]", R_OB)
        "s_nop 15\n\t"
        : "+{v[64:79]}"(R.oa), "+{v[80:95]}"(R.ob), "+{v[100:103]}"(R.p1), "+{v[124:127]}"(R.v1), "+{v12}"(R.ka), "+{v13}"(R.va)
        : "{v[16:31]}"(R.nm), "{v14}"(R.go), [qa] "s"(qa), [qb] "s"(qb), [rk] "s"(rk), [rv] "s"(rv), [lw] "s"(w * 1024), [tag] "s"(tag), [gf] "s"(go_flag)
        : "memory", "scc", "s40", "s41", "s42", "s43", "s44", "v4", "v5", "v6", "v7", "v8", "v9", "v10", "v11", "v15",
          "v32", "v33", "v34", "v35", "v36", "v37", "v38", "v39", "v40", "v41", "v42", "v43", "v44", "v45", "v46", "v47",
          "v48", "v49", "v50", "v51", "v52", "v53", "v54", "v55", "v56", "v57", "v58", "v59", "v60", "v61", "v62", "v63",
          "v96", "v97", "v98", "v99", "v104", "v105", "v106", "v107", "v108", "v109", "v110", "v111",
          "v112", "v113", "v114", "v115", "v116", "v117", "v118", "v119", "v120", "v121", "v122", "v123");
}

DI void wait_qkv(const char* ws, int a, unsigned tag) {
    const int lane = lane_id(), hp = 2 * (a & 7) + ((a >> 4) & 1);
    const int fp = lane < 16 ? (16 + hp) / 3 : lane < 32 ? (32 + hp) / 3 : hp / 3;
    const int fm = lane < 32 ? (lane & 15) : 2 * (a >> 5) + (lane & 1);
    wait_flags<4>(ws + WS_FQ, (fp * 16 + fm) * 128, tag);
}
DI void attn_role(const char* x, const char* wqkv, char* ws, char* lds, int a, int w) {
    const int kh = w >> 2;
    const int head = 4 * (a & 7) + ((a >> 3) & 3), qt = 8 * (a >> 5) + 2 * (w & 3);
    ST_DECL; ST(0);
    __builtin_amdgcn_s_setprio(3);
    const unsigned tag = __builtin_amdgcn_readfirstlane(ld4(ws + WS_EPOCH)) * 0x9E3779B1u + 0x7F4A7C15u;
    {
        const int m = 4 * ((a & 7) >> 1) + ((a >> 3) & 3), p = 8 * (a & 1) + (a >> 5);
        if (w >= 4) {
            qkv_loader(x, wqkv, m, p, w - 4 ST_PASS);
            ST(4);
            if (w == 4) {
                __builtin_amdgcn_s_sleep(QSLEEP);
                wait_qkv(ws, a, tag);
            }
        } else if (p < 10) qkv_body<3>(ws, lds, m, p, w, tag ST_PASS);
        else if (p == 10) qkv_body<2>(ws, lds, m, p, w, tag ST_PASS);
        else qkv_body<0>(ws, lds, m, p, w, tag ST_PASS);
    }
    AttnRegs R;
    attn_prep(R, 0.f, w);
    ST(5);
    wgbar();
    ST(6);
    {
        const int o = w * 512 + lane_id() * 8;
        const unsigned one = ((o & 0x1b0) == 0) ? 0x3f803f80u : 0u;
        *(LAS u32x2*)((LAS char*)lds + A_ONES + o) = u32x2{one, one};
        asm volatile("s_waitcnt lgkmcnt(0)" ::: "memory");
    }
    const char* ksrc = ws + WS_K + (size_t)head * 65536;
    const char* qa = ws + WS_Q + (size_t)(head * 64 + qt + kh) * 1024;
    const char* qb = ws + WS_Q + (size_t)(head * 64 + qt + 1 - kh) * 1024;
#pragma clang loop unroll(disable)
    for (int att = 0;; ++att) {
        ST(1);
        attn_pass(R, qa, qb, ksrc, ksrc + (WS_V - WS_K), ws + WS_FA + a * 128, tag, w);
        ST(2);
        const int lane = lane_id();
        const f32x16 oa = R.oa, ob = R.ob;
        LAS char* ex = (LAS char*)lds + w * 2048 + lane * 16;
        LAS const char* exr = (LAS const char*)lds + (w ^ 4) * 2048 + lane * 16;
        *(LAS f32x4*)ex = f32x4{ob[0], ob[1], ob[2], ob[3]};
        *(LAS f32x4*)(ex + 1024) = f32x4{ob[4], ob[5], ob[6], ob[7]};
        *(LAS float*)((LAS char*)lds + A_LSUM + w * 256 + lane * 4) = ob[8];
        float big = fmaxf(oa[8], ob[8]) * 2.7e8f;
#pragma unroll
        for (int i = 0; i < 8; ++i) big = fmaxf(fmaxf(big, __builtin_fabsf(oa[i])), __builtin_fabsf(ob[i]));
        const bool bad = !__all(fminf(oa[8], ob[8]) > 8.1e-28f && big < 1.7e38f);
        if (lane == 0) ((LAS unsigned*)((LAS char*)lds + A_VOTE))[w] = bad;
        wgbar();
        const f32x4 t0 = *(LAS const f32x4*)exr, t1 = *(LAS const f32x4*)(exr + 1024);
        const float t2 = *(LAS const float*)((LAS char*)lds + A_LSUM + (w ^ 4) * 256 + lane * 4);
        const u32x4 v0 = *(LAS const u32x4*)((LAS char*)lds + A_VOTE), v1 = *(LAS const u32x4*)((LAS char*)lds + A_VOTE + 16);
        const float rinv = __builtin_amdgcn_rcpf(oa[8] + t2);
        u32x4 ov;
        ov[0] = cvtpk((oa[0] + t0[0]) * rinv, (oa[1] + t0[1]) * rinv);
        ov[1] = cvtpk((oa[2] + t0[2]) * rinv, (oa[3] + t0[3]) * rinv);
        ov[2] = cvtpk((oa[4] + t1[0]) * rinv, (oa[5] + t1[1]) * rinv);
        ov[3] = cvtpk((oa[6] + t1[2]) * rinv, (oa[7] + t1[3]) * rinv);
        const bool redo = att == 0 && __builtin_amdgcn_readfirstlane(v0[0] | v0[1] | v0[2] | v0[3] | v1[0] | v1[1] | v1[2] | v1[3]);
        if (att == 0 ? !redo : att == 1 + kh) {
            st16(ws + WS_O + (size_t)((qt + kh) * 32 + head) * 1024, lane * 16, ov);
            drain();
            st4(ws + WS_FO + ((qt + kh) * 32 + head) * 128, tag);
        }
        asm volatile("" : "+s"(att));
        if (att == 2 || (att == 0 && !redo)) break;
        drain();
        wgbar();
        qa = qb = ws + WS_Q + (size_t)(head * 64 + qt + att) * 1024;
        bf16x8 qf;
        asm volatile("global_load_dwordx4 %0, %1, %2 sc1\n\ts_waitcnt vmcnt(0)" : "=v"(qf) : "v"(lane * 16), "s"(qa) : "memory");
        float mx = -INFINITY;
#pragma clang loop unroll(disable)
        for (int t = 0; t < 64; ++t) {
            bf16x8 kf;
            asm volatile("global_load_dwordx4 %0, %1, %2 sc1\n\ts_waitcnt vmcnt(0)" : "=v"(kf) : "v"(lane * 16), "s"(ksrc + t * 1024) : "memory");
            const f32x16 s = MFMA32(kf, qf, f32x16{});
#pragma unroll
            for (int i = 0; i < 16; ++i) mx = fmaxf(mx, s[i]);
        }
        attn_prep(R, -swap32_max(mx), w);
    }
    ST(3); ST_OUT(0);
}

#define BG1 ""
#define BG2 ""
#define BG3 "s_sleep 1\n\t"
#define SISSUE                                                             \
    "s_mov_b32 m0, s44\n\t"                                                 \
    "s_add_u32 s45, %[ow], %[wst]\n\t"                                      \
    "buffer_load_dwordx4 %[g0], %[rw], %[ow] offen lds\n\t"                 \
    "buffer_load_dwordx4 %[g1], %[rw], %[ow] offen offset:1024 lds\n\t"     \
    "s_add_u32 m0, s44, 8192\n\t"                                           \
    "s_add_u32 s46, %[od], 4096\n\t"                                        \
    "buffer_load_dwordx4 %[d0], %[rd], %[od] offen sc1 lds\n\t"             \
    "buffer_load_dwordx4 %[d0], %[rd], %[od] offen offset:1024 sc1 lds\n\t" \
    "s_add_u32 s44, s44, 0x4000\n\t"                                        \
    "s_and_b32 s44, s44, 0xffff\n\t"                                        \
    "s_cmp_eq_u32 %[od], %[de]\n\t"                                         \
    "s_cselect_b32 %[ow], 0, s45\n\t"                                       \
    "s_cselect_b32 %[od], 0, s46\n\t"
struct SG { u32x4 rw, rd; unsigned wst, de, ow, od, a, b, c; };
#define WISSUE                                                              \
    "s_and_b32 s45, s44, 0xc000\n\t"                                        \
    "s_add_u32 m0, s45, %[lo2]\n\t"                                         \
    "s_lshl_b32 s45, s45, 3\n\t"                                            \
    "buffer_load_dwordx4 %[o0], %[ro], s45 offen lds\n\t"                   \
    "buffer_load_dwordx4 %[o1], %[ro], s45 offen offset:1024 lds\n\t"       \
    "buffer_load_dwordx4 %[o2], %[ro], s45 offen offset:2048 lds\n\t"       \
    "buffer_load_dwordx4 %[o3], %[ro], s45 offen offset:3072 lds\n\t"       \
    "s_add_u32 s44, s44, 0x4000\n\t"                                        \
    "s_and_b32 s44, s44, 0xffff\n\t"
template <int WO>
DI void sgemm_load(SG& G, int n, int lw, u32x4 ro, unsigned o0) {
    asm volatile(
        "s_mov_b32 s44, %[lo]\n\t"
        "s_sub_u32 s47, %[n], 3\n\t"
        SISSUE SISSUE SISSUE
        "LL_loop%=:\n\t"
        "s_waitcnt vmcnt(8)\n\t"
        "s_barrier\n\t"
        SISSUE
        "s_sub_u32 s47, s47, 1\n\t"
        "s_cmp_lg_u32 s47, 0\n\t"
        "s_cbranch_scc1 LL_loop%=\n\t"
        ".if %c[wo]\n\t"
        "s_waitcnt vmcnt(8)\n\ts_barrier\n\t" WISSUE
        "s_waitcnt vmcnt(8)\n\ts_barrier\n\t" WISSUE
        "s_waitcnt vmcnt(8)\n\ts_barrier\n\t" WISSUE
        "s_barrier\n\t" WISSUE
        ".else\n\t"
        "s_waitcnt vmcnt(8)\n\ts_barrier\n\t"
        "s_waitcnt vmcnt(4)\n\ts_barrier\n\t"
        "s_waitcnt vmcnt(0)\n\ts_barrier\n\t"
        "s_barrier\n\t"
        ".endif\n\t"
        : [ow] "+s"(G.ow), [od] "+s"(G.od)
        : [wst] "s"(G.wst), [rw] "s"(G.rw), [rd] "s"(G.rd), [lo] "s"(lw * 2048), [de] "s"(G.de), [n] "s"(n), [g0] "v"(G.a), [g1] "v"(G.b), [d0] "v"(G.c),
          [wo] "i"(WO), [ro] "s"(ro), [lo2] "s"(lw * 4096), [o0] "v"(o0), [o1] "v"(o0 + 7168), [o2] "v"(o0 + 14336), [o3] "v"(o0 + 21504)
        : "memory", "scc", "s44", "s45", "s46", "s47");
}
#define SRD3(B, SLOT, K, TD)                                                                         \
    "ds_read_b64_tr_b16 v[" #B ":" #B "+1], %[t] offset:" #SLOT "*16384+" #K "*2048\n\t"              \
    "ds_read_b64_tr_b16 v[" #B "+2:" #B "+3], %[t] offset:" #SLOT "*16384+" #K "*2048+" #TD "\n\t"    \
    "ds_read_b128 v[" #B "+4:" #B "+7], %[dl] offset:" #SLOT "*16384+" #K "*1024\n\t"
#define SSTAGE(SLOT, MM, TD, N0, N1, N2, N3, P0, P1, P2, P3)                          \
    "s_waitcnt lgkmcnt(0)\n\t"                                                        \
    "s_barrier\n\t"                                                                   \
    MM(P0) SRD3(N0, SLOT, 0, TD) SRD3(N1, SLOT, 1, TD) BG1                            \
    MM(P1) SRD3(N2, SLOT, 2, TD) SRD3(N3, SLOT, 3, TD) BG2                            \
    MM(P2) BG3 MM(P3)
#define SST_E(SLOT, MM, TD) SSTAGE(SLOT, MM, TD, 32, 40, 48, 56, 96, 104, 112, 120)
#define SST_O(SLOT, MM, TD) SSTAGE(SLOT, MM, TD, 96, 104, 112, 120, 32, 40, 48, 56)
#define SMM_NONE(P) ""
#define SMM_WA(P) MF("v[64:79]", "v[" #P ":" #P "+3]", "v[" #P "+4:" #P "+7]", "v[64:79]")
#define SMM_WB(P) MF("v[80:95]", "v[" #P "+4:" #P "+7]", "v[" #P ":" #P "+3]", "v[80:95]")
#define SGEMM_ASM(ACC, MM, TD, NLOOP, TAIL)                                                                       \
    asm volatile(                                                                                                 \
        "s_mov_b32 s44, " #NLOOP "\n\t"                                                                           \
        "s_barrier\n\t"                                                                                           \
        SRD3(32, 0, 0, TD) SRD3(40, 0, 1, TD) SRD3(48, 0, 2, TD) SRD3(56, 0, 3, TD)                               \
        "LS_loop%=:\n\t"                                                                                          \
        SST_O(1, MM, TD) SST_E(2, MM, TD) SST_O(3, MM, TD) SST_E(0, MM, TD)                                       \
        "s_sub_u32 s44, s44, 1\n\t"                                                                               \
        "s_cmp_lg_u32 s44, 0\n\t"                                                                                 \
        "s_cbranch_scc1 LS_loop%=\n\t"                                                                            \
        TAIL                                                                                                      \
        "s_waitcnt lgkmcnt(0)\n\t"                                                                                \
        MM(96) BG3 MM(104) BG3 MM(112) BG3 MM(120)                                                                \
        "s_nop 15\n\t"                                                                                            \
        : ACC(acc)                                                                                                \
        : [t] "v"(G.a), [dl] "v"(G.b)                                                                             \
        : "memory", "scc", "s44",                                                                                 \
          "v32", "v33", "v34", "v35", "v36", "v37", "v38", "v39", "v40", "v41", "v42", "v43", "v44", "v45", "v46", "v47", \
          "v48", "v49", "v50", "v51", "v52", "v53", "v54", "v55", "v56", "v57", "v58", "v59", "v60", "v61", "v62", "v63", \
          "v96", "v97", "v98", "v99", "v100", "v101", "v102", "v103", "v104", "v105", "v106", "v107", "v108", "v109", "v110", "v111", \
          "v112", "v113", "v114", "v115", "v116", "v117", "v118", "v119", "v120", "v121", "v122", "v123", "v124", "v125", "v126", "v127")

template <int MODE>
DI SG sgemm_setup(const char* wbase, unsigned wbytes, int wrow, const char* dbase, int dblk, int rot, int w) {
    const int lane = lane_id(), row = lane >> 3, lw = w & 3;
    const int q = (lane & 15) >> 2, h2 = lane >> 5;
    SG G;
    G.rw = srd(wbase, wbytes); G.rd = srd(dbase, 2 * dblk);
    G.wst = 64 * wrow; G.de = (MODE == 0 ? 31 : 21) * 4096; G.ow = rot * G.wst; G.od = rot * 4096;
    if (w >= 4) {
        G.a = (16 * lw + row) * wrow + (((lane & 7) ^ (((row >> 1) & 1) << 2)) << 4);
        G.b = G.a + 8 * wrow - 1024;
        G.c = (lw >> 1) * dblk + (lw & 1) * 2048 + lane * 16;
    } else {
        G.a = ((MODE == 0 ? 8 : 4) * h2 + q) * 128 + (((lw & 1) ^ ((q >> 1) & 1)) << 6) + ((lane >> 4) & 1) * 32 + (lane & 3) * 8;
        G.b = 8192 + (lw >> 1) * 4096 + lane * 16;
        G.c = 0;
    }
    asm volatile("" : "+v"(G.a), "+v"(G.b), "+v"(G.c));
    return G;
}
#define ACC0 "+{v[64:79]}"
#define ACC1 "+{v[80:95]}"
template <int MODE>
DI void sgemm_run(f32x16& acc, SG& G) {
    if (MODE == 0) SGEMM_ASM(ACC0, SMM_WA, 512, 7, SST_O(1, SMM_WA, 512) SST_E(2, SMM_WA, 512) SST_O(3, SMM_WA, 512));
    else SGEMM_ASM(ACC1, SMM_WB, 1024, 5, SST_O(1, SMM_WB, 1024));
}
DI f32x4 sub4(const f32x16& a, int g) { return f32x4{a[4 * g], a[4 * g + 1], a[4 * g + 2], a[4 * g + 3]}; }

DI unsigned gelu2(float a, float b) {
    constexpr float c1 = -2.f * 0.7978845608028654f * 1.4426950408889634f, c2 = c1 * 0.044715f;
    const f32x2 u = {a, b};
    const f32x2 z = u * (u * u * c2 + c1);
    const f32x2 d = f32x2{__builtin_amdgcn_exp2f(z[0]), __builtin_amdgcn_exp2f(z[1])} + 1.f;
    const f32x2 o = u * f32x2{__builtin_amdgcn_rcpf(d[0]), __builtin_amdgcn_rcpf(d[1])};
    return cvtpk(o[0], o[1]);
}

#define WRD(R0, R1, R2, R3, STEP)                                                    \
    "ds_read_b64_tr_b16 " R0 ", %[t0] offset:" #STEP "*2048\n\t"                      \
    "ds_read_b64_tr_b16 " R1 ", %[t0] offset:" #STEP "*2048+1024\n\t"                 \
    "ds_read_b64_tr_b16 " R2 ", %[t1] offset:" #STEP "*2048\n\t"                      \
    "ds_read_b64_tr_b16 " R3 ", %[t1] offset:" #STEP "*2048+1024\n\t"
#define WRD0(S) WRD("v[32:33]", "v[34:35]", "v[36:37]", "v[38:39]", S)
#define WRD1(S) WRD("v[40:41]", "v[42:43]", "v[44:45]", "v[46:47]", S)
#define WRD2(S) WRD("v[48:49]", "v[50:51]", "v[52:53]", "v[54:55]", S)
#define WRD3(S) WRD("v[56:57]", "v[58:59]", "v[60:61]", "v[62:63]", S)
#define WMM(WAIT, W0, W1, O) "s_waitcnt " WAIT "\n\t" MF("v[96:111]", W0, O, "v[96:111]") MF("v[112:127]", W1, O, "v[112:127]")
#define OLD(K, R, V, OFF)                                             \
    "5:\n\t"                                                           \
    "s_bitcmp1_b32 s40, " #K "\n\t"                                    \
    "s_cbranch_scc1 6f\n\t"                                            \
    "global_load_dword v24, %[fo], %[f] sc1\n\t"                       \
    "s_sub_u32 s41, s41, 1\n\t"                                        \
    "s_cselect_b32 s42, -1, 0\n\t"                                     \
    "s_waitcnt vmcnt(0)\n\t"                                           \
    "v_cmp_eq_u32 vcc, %[tag], v24\n\t"                                \
    "s_or_b32 s40, vcc_lo, s42\n\t"                                    \
    "s_bitcmp1_b32 s40, " #K "\n\t"                                    \
    "s_cbranch_scc1 6f\n\t"                                            \
    "s_sleep 3\n\t"                                                    \
    "s_branch 5b\n\t"                                                  \
    "6:\n\t"                                                           \
    "global_load_dwordx4 " R ", " V ", %[o] offset:" #OFF " sc1\n\t"
DI void wo_gemm(f32x16& a0, f32x16& a1, f32x4& k0, f32x4& k1, float& k2, const char* osrc, const char* fsrc, unsigned tag, int w) {
    const int lane = lane_id();
    const int q = (lane & 15) >> 2, h2 = lane >> 5;
    const unsigned tb = (w >> 1) * 16384 + (4 * h2 + q) * 128 + ((lane >> 4) & 1) * 32 + (lane & 3) * 8;
    const unsigned t0 = tb + ((q >> 1) & 1) * 64, t1 = tb + (((q >> 1) & 1) ^ 1) * 64;
    const unsigned d0 = lane * 16, d1 = d0 + 4096, fo = (lane & 7) * 128;
    asm volatile(
        WRD0(0) WRD1(1) WRD2(2) WRD3(3)
        "s_mov_b32 s40, 0\n\t"
        "s_mov_b32 s41, 0x100000\n\t"
        "s_setprio 0\n\t"
        OLD(0, "v[64:67]", "%[d0]", 0) OLD(1, "v[68:71]", "%[d0]", 1024) OLD(2, "v[72:75]", "%[d0]", 2048) OLD(3, "v[76:79]", "%[d0]", 3072)
        OLD(4, "v[80:83]", "%[d1]", 0) OLD(5, "v[84:87]", "%[d1]", 1024) OLD(6, "v[88:91]", "%[d1]", 2048) OLD(7, "v[92:95]", "%[d1]", 3072)
        "s_setprio 3\n\t"
        "s_waitcnt vmcnt(7) lgkmcnt(0)\n\t"
        MF("v[96:111]", "v[32:35]", "v[64:67]", "0") MF("v[112:127]", "v[36:39]", "v[64:67]", "0")
        WRD0(4)
        WMM("vmcnt(6)", "v[40:43]", "v[44:47]", "v[68:71]") WRD1(5)
        WMM("vmcnt(5)", "v[48:51]", "v[52:55]", "v[72:75]") WRD2(6)
        WMM("vmcnt(4)", "v[56:59]", "v[60:63]", "v[76:79]") WRD3(7)
        WMM("vmcnt(3) lgkmcnt(12)", "v[32:35]", "v[36:39]", "v[80:83]")
        WMM("vmcnt(2) lgkmcnt(0)", "v[40:43]", "v[44:47]", "v[84:87]")
        WMM("vmcnt(1)", "v[48:51]", "v[52:55]", "v[88:91]")
        "s_barrier\n\t"
        WMM("vmcnt(0)", "v[56:59]", "v[60:63]", "v[92:95]")
        "s_nop 15\n\t"
        : "={v[96:111]}"(a0), "={v[112:127]}"(a1),
          "+{v[12:15]}"(k0), "+{v[16:19]}"(k1), "+{v20}"(k2)
        : [o] "s"(osrc), [f] "s"(fsrc), [tag] "s"(tag), [d0] "v"(d0), [d1] "v"(d1), [t0] "v"(t0), [t1] "v"(t1), [fo] "v"(fo)
        : "memory", "scc", "vcc", "s40", "s41", "s42", "v24",
          "v32", "v33", "v34", "v35", "v36", "v37", "v38", "v39", "v40", "v41", "v42", "v43", "v44", "v45", "v46", "v47",
          "v48", "v49", "v50", "v51", "v52", "v53", "v54", "v55", "v56", "v57", "v58", "v59", "v60", "v61", "v62", "v63",
          "v64", "v65", "v66", "v67", "v68", "v69", "v70", "v71", "v72", "v73", "v74", "v75", "v76", "v77", "v78", "v79",
          "v80", "v81", "v82", "v83", "v84", "v85", "v86", "v87", "v88", "v89", "v90", "v91", "v92", "v93", "v94", "v95");
}

DI void mixer_role(const char* x, const char* wqkv, const char* gw, const char* wo, const char* w1, const char* b1, const char* w2, const char* b2,
                   char* ws, char* out, char* lds, int bi, int w) {
    const int j = bi & 7, i = bi >> 3;
    __builtin_amdgcn_s_setprio(1);
    ST_DECL; ST(0);
    const unsigned tag = __builtin_amdgcn_readfirstlane(ld4(ws + WS_EPOCH)) * 0x9E3779B1u + 0x7F4A7C15u;
    {
        const int lane = lane_id(), h2 = lane >> 5, tid = 64 * w + lane;
        const u32x4 xr = *(GAS const u32x4*)(x + (size_t)(8 * bi + w) * 1024 + lane * 16);
        const u32x4 gr = *(GAS const u32x4*)(gw + lane * 16);
        float v[8], gg[8];
#pragma unroll
        for (int k = 0; k < 4; ++k) { v[2 * k] = bflo(xr[k]); v[2 * k + 1] = bfhi(xr[k]); gg[2 * k] = bflo(gr[k]); gg[2 * k + 1] = bfhi(gr[k]); }
        float s1 = 0.f, s3 = 0.f;
#pragma unroll
        for (int k = 0; k < 8; ++k) { s1 += v[k]; s3 += v[k] * gg[k]; }
        const float mu = wave_sum(s1) * (1.f / 512.f);
        s3 = wave_sum(s3);
        float s2 = 0.f;
#pragma unroll
        for (int k = 0; k < 8; ++k) { v[k] -= mu; s2 += v[k] * v[k]; }
        const float rs = __builtin_amdgcn_rsqf(wave_sum(s2) * (1.f / 512.f) + 1e-5f);
        u32x4 hv;
#pragma unroll
        for (int k = 0; k < 4; ++k) hv[k] = cvtpk(v[2 * k] * rs, v[2 * k + 1] * rs);
        *(LAS u32x4*)((LAS char*)lds + w * 1024 + lane * 16) = hv;
        const float g = __builtin_amdgcn_rcpf(1.f + __builtin_amdgcn_exp2f(-1.4426950408889634f * s3));
        st4(ws + WS_G + (8 * bi + w) * 4, uni(g));
        wgbar();
        LAS const unsigned short* lb = (LAS const unsigned short*)lds + tid;
#pragma unroll
        for (int k = 0; k < 4; ++k) hv[k] = (unsigned)lb[(2 * k) * 512] | ((unsigned)lb[(2 * k + 1) * 512] << 16);
        st16(ws + WS_H + (size_t)(2 * w * 128 + (bi >> 1)) * 1024 + (bi & 1) * 512, h2 * 131072 + (lane & 31) * 16, hv);
        drain();
        wgbar();
        if (w == 0) st4(ws + WS_FL + bi * 128, tag);
    }
    __builtin_amdgcn_s_setprio(3);
    ST(6);
    const int wb = w & 1, db = (w >> 1) & 1, hf = w >> 2;
    const int L = (j >> 1) * 22 + i, ft = L >> 2, dt1 = 4 * (j & 1) + (L & 3);
    const int st = 8 * (j >> 1) + (i & 7), dt = 4 * (j & 1) + (i >> 3);
    f32x16 acc1, acc2;
    SG G1, G2;
    unsigned ex, l16, og, wof;
    {
        const int lane = lane_id();
        const bool live = i < 22 && (ft < 21 || wb == 0);
        const int f0 = 64 * ft + 32 * wb + 4 * (lane >> 5);
        u32x2 bv[4] = {};
        if (live) {
#pragma unroll
            for (int k = 0; k < 4; ++k) bv[k] = *(GAS const u32x2*)(b1 + f0 * 2 + 16 * k);
        }
        og = (64 * st + 32 * wb + (lane & 31)) * 4;
        const float bb = bflo(*(GAS const unsigned short*)(b2 + (og >> 1)));
        G1 = sgemm_setup<0>(w1 + ft * 128, 2048 * 2752 - ft * 128, 2752, ws + WS_H + (size_t)2 * dt1 * 131072, 131072, ROT1, w);
        G2 = sgemm_setup<1>(w2 + st * 128, 1376 * 4096 - st * 128, 4096, ws + WS_U + (size_t)2 * dt * 86 * 1024, 86 * 1024, ROT2, w);
        l16 = lane * 16;
        ex = 65536 + (w & 3) * 2048 + l16;
        wof = (32 * (w & 3) + (lane >> 3)) * 1024 + (((lane & 7) ^ (((lane >> 4) & 1) << 2)) << 4);
#pragma unroll
        for (int k = 0; k < 4; ++k) {
            acc1[4 * k] = bflo(bv[k][0]); acc1[4 * k + 1] = bfhi(bv[k][0]); acc1[4 * k + 2] = bflo(bv[k][1]); acc1[4 * k + 3] = bfhi(bv[k][1]);
            acc2[4 * k] = bb; acc2[4 * k + 1] = bb; acc2[4 * k + 2] = bb; acc2[4 * k + 3] = bb;
        }
        asm volatile("" : "+{v[64:79]}"(acc1), "+{v[80:95]}"(acc2), "+v"(ex), "+v"(l16), "+v"(og), "+v"(wof));
    }
    if (i < 22) {
        if (w >= 4) { __builtin_amdgcn_s_sleep(LSLEEP); wait_flags<8>(ws + WS_FL + (w - 4) * 8192, l16 * 8, tag); }
        if (w == 0) {
            __builtin_amdgcn_s_sleep(LSLEEP);
            wait_qkv(ws, bi, tag);
            __builtin_amdgcn_s_sleep(GATEQ);
        }
        wgbar();
        ST(7);
        f32x4 v0, v1;
        if (w >= 4) {
            sgemm_load<0>(G1, 32, w - 4, G1.rw, 0);
            v0 = *(LAS const f32x4*)(uintptr_t)ex; v1 = *(LAS const f32x4*)(uintptr_t)(ex + 1024);
        } else {
            sgemm_run<0>(acc1, G1);
            *(LAS f32x4*)(uintptr_t)ex = sub4(acc1, 2); *(LAS f32x4*)(uintptr_t)(ex + 1024) = sub4(acc1, 3);
            wgbar();
            v0 = sub4(acc1, 0); v1 = sub4(acc1, 1);
        }
        ST(1);
        const u32x4 o = {gelu2(v0[0], v0[1]), gelu2(v0[2], v0[3]), gelu2(v1[0], v1[1]), gelu2(v1[2], v1[3])};
        if (ft < 21 || wb == 0) st16(ws + WS_U + (size_t)((2 * dt1 + db) * 86 + 4 * ft + 2 * wb + hf) * 1024, l16, o);
        drain();
        wgbar();
        if (w == 0) st4(ws + WS_FU + (dt1 * 32 + ft) * 128, tag);
    }
    ST(2);
    if (bi == 255 && w == 1) {
#pragma clang loop unroll(disable)
        for (int k = 0; k < 4; ++k) {
            const int lane = lane_id();
            wait_flags<8>(ws + WS_FQ + k * 8192, lane * 128, tag);
            wait_flags<8>(ws + WS_FL + k * 8192, lane * 128, tag);
        }
        st4(ws + WS_EPOCH, tag);
    }
    if (w == 0) wait_flags<8>(ws + WS_FU + dt * 4096, (l16 < 22 * 16 ? l16 : 0) * 8, tag);
    wgbar();
    ST(3);
    f32x4 bp0, bp1;
    float g;
    {
        asm volatile("global_load_dword %0, %1, %2 sc1" : "=v"(g) : "v"(og), "s"(ws + WS_G) : "memory");
        if (w >= 4) {
            sgemm_load<1>(G2, 22, w - 4, srd(wo + dt * 128, 524288 - dt * 128), wof);
            bp0 = *(LAS const f32x4*)(uintptr_t)ex; bp1 = *(LAS const f32x4*)(uintptr_t)(ex + 1024);
        } else {
            sgemm_run<1>(acc2, G2);
            *(LAS f32x4*)(uintptr_t)ex = sub4(acc2, 2); *(LAS f32x4*)(uintptr_t)(ex + 1024) = sub4(acc2, 3);
            wgbar();
            bp0 = sub4(acc2, 0); bp1 = sub4(acc2, 1);
            drain();
        }
        asm volatile("" : "+v"(g));
        ST(4);
        bp0 *= 1.f - g;
        bp1 *= 1.f - g;
        asm volatile("" : "+v"(bp0), "+v"(bp1));
    }
    drain();
    wgbar();
    ST(5);
    f32x16 a0, a1;
    wo_gemm(a0, a1, bp0, bp1, g, ws + WS_O + (size_t)((2 * st + wb) * 32 + 8 * (w >> 1)) * 1024,
            ws + WS_FO + ((2 * st + wb) * 32 + 8 * (w >> 1)) * 128, tag, w);
    {
        const int lane = lane_id(), kq = 2 * db + hf;
        LAS char* ex = (LAS char*)lds + w * 8192 + lane * 16;
#pragma unroll
        for (int k = 0; k < 4; ++k) { *(LAS f32x4*)(ex + k * 1024) = sub4(a0, k); *(LAS f32x4*)(ex + 4096 + k * 1024) = sub4(a1, k); }
        wgbar();
        LAS const char* exr = (LAS const char*)lds + wb * 8192 + kq * 2048 + lane * 16;
        f32x4 s0 = *(LAS const f32x4*)exr, s1 = *(LAS const f32x4*)(exr + 1024);
#pragma unroll
        for (int k = 1; k < 4; ++k) { s0 += *(LAS const f32x4*)(exr + k * 16384); s1 += *(LAS const f32x4*)(exr + k * 16384 + 1024); }
        s0 = s0 * g + bp0;
        s1 = s1 * g + bp1;
        const auto r0 = __builtin_amdgcn_permlane32_swap(cvtpk(s0[0], s0[1]), cvtpk(s1[0], s1[1]), false, false);
        const auto r1 = __builtin_amdgcn_permlane32_swap(cvtpk(s0[2], s0[3]), cvtpk(s1[2], s1[3]), false, false);
        *(GAS u32x4*)(out + (size_t)(64 * st + 32 * wb) * 1024 + (64 * dt + 16 * kq) * 2 + (lane & 31) * 1024 + (lane >> 5) * 16) = u32x4{r0[0], r1[0], r0[1], r1[1]};
    }
    ST_OUT(0);
}

constexpr int MK_LDS = 81920;
__global__ __launch_bounds__(512, 4) void mk(const char* x, const char* wqkv, const char* wo, const char* w1, const char* b1,
                                             const char* w2, const char* b2, const char* gw, char* ws, char* out) {
    extern __shared__ __attribute__((aligned(16))) char lds[];
    const int w = __builtin_amdgcn_readfirstlane(threadIdx.x >> 6);
    if (blockIdx.x < 256) attn_role(x, wqkv, ws, lds, blockIdx.x, w);
    else mixer_role(x, wqkv, gw, wo, w1, b1, w2, b2, ws, out, lds, blockIdx.x - 256, w);
}

extern "C" void kernel_launch(void* const* d_in, const int* in_sizes, int n_in, void* d_out, int out_size, void* d_ws, size_t ws_size,
                              hipStream_t stream) {
    (void)hipFuncSetAttribute((const void*)mk, hipFuncAttributeMaxDynamicSharedMemorySize, MK_LDS);
    mk<<<512, 512, MK_LDS, stream>>>((const char*)d_in[0], (const char*)d_in[1], (const char*)d_in[2], (const char*)d_in[4],
                                     (const char*)d_in[5], (const char*)d_in[6], (const char*)d_in[7], (const char*)d_in[8],
                                     (char*)d_ws, (char*)d_out);
}
```

```cpp
#include <hip/hip_runtime.h>
#include <hip/hip_bf16.h>
#include <stdint.h>

typedef __attribute__((ext_vector_type(8))) short bf16x8;
typedef __attribute__((ext_vector_type(4))) short s16x4;
typedef __attribute__((ext_vector_type(16))) float f32x16;
typedef __attribute__((ext_vector_type(4))) float f32x4;
typedef __attribute__((ext_vector_type(2))) float f32x2;
typedef __attribute__((ext_vector_type(4))) unsigned u32x4;
typedef __attribute__((ext_vector_type(2))) unsigned u32x2;
#define LAS __attribute__((address_space(3)))
#define GAS __attribute__((address_space(1)))
#define DI __device__ __forceinline__
#define MFMA32(a, b, c) __builtin_amdgcn_mfma_f32_32x32x16_bf16((a), (b), (c), 0, 0, 0)

#define LSLEEP 32
#define ROT1 (((dt1 + ft) & 1) * 16)
#define ROT2 (((st + dt) & 1) * 11)
#define GATEQ 1
#define BSLEEP 0
#define LNP 0
#define BGATE 1
#define FAEARLY 1
#define QROT 4
#define QROTM 4
constexpr int S_ = 2048, D_ = 512, F_ = 1376;
constexpr size_t WS_EPOCH = 0;
constexpr size_t WS_G = 4096;
constexpr size_t WS_FL = 98304;
constexpr size_t WS_FQ = 458752;
constexpr size_t WS_FU = 65536;
constexpr size_t WS_FA = 393216;
constexpr size_t WS_FO = 131072;
constexpr size_t WS_Q = 1u << 20;
constexpr size_t WS_K = WS_Q + (2u << 20);
constexpr size_t WS_V = WS_K + (2u << 20);
constexpr size_t WS_H = WS_V + (2u << 20);
constexpr size_t WS_U = WS_H + (2u << 20);
constexpr size_t WS_O = WS_U + (2u << 20);

#define ST_DECL
#define ST_ARG
#define ST_PASS
#define ST(i)
#define ST_OUT(k)
DI unsigned cvtpk(float lo, float hi) {
    typedef float f32x2_t __attribute__((ext_vector_type(2)));
    typedef __bf16 bf16x2_t __attribute__((ext_vector_type(2)));
    return __builtin_bit_cast(unsigned, __builtin_convertvector(f32x2_t{lo, hi}, bf16x2_t));
}
DI float bflo(unsigned u) { return __uint_as_float(u << 16); }
DI float bfhi(unsigned u) { return __uint_as_float(u & 0xffff0000u); }
DI s16x4 trread(LAS const char* p) {
    typedef short v4i16_t __attribute__((ext_vector_type(4)));
    return __builtin_bit_cast(s16x4, __builtin_amdgcn_ds_read_tr16_b64_v4i16((LAS v4i16_t*)p));
}
DI void glds(unsigned lds, unsigned voff, const void* sbase) {
    asm volatile("s_mov_b32 m0, %0\n\ts_nop 0\n\tglobal_load_lds_dwordx4 %1, %2" ::"s"(lds), "v"(voff), "s"(sbase) : "memory");
}
template <int CTRL> DI float dpp(float v) {
    return __int_as_float(__builtin_amdgcn_update_dpp(0, __float_as_int(v), CTRL, 0xf, 0xf, true));
}
DI float rl(float v, int l) { return __int_as_float(__builtin_amdgcn_readlane(__float_as_int(v), l)); }
DI float wave_sum(float v) {
    v += dpp<0xB1>(v); v += dpp<0x4E>(v); v += dpp<0x141>(v); v += dpp<0x140>(v);
    return (rl(v, 0) + rl(v, 16)) + (rl(v, 32) + rl(v, 48));
}
DI float wave_max(float v) {
    v = fmaxf(v, dpp<0xB1>(v)); v = fmaxf(v, dpp<0x4E>(v)); v = fmaxf(v, dpp<0x141>(v)); v = fmaxf(v, dpp<0x140>(v));
    return fmaxf(fmaxf(rl(v, 0), rl(v, 16)), fmaxf(rl(v, 32), rl(v, 48)));
}
DI float swap32_add(float v) {
    auto rr = __builtin_amdgcn_permlane32_swap(__float_as_uint(v), __float_as_uint(v), false, false);
    return __uint_as_float(rr[0]) + __uint_as_float(rr[1]);
}
DI float swap32_max(float v) {
    auto rr = __builtin_amdgcn_permlane32_swap(__float_as_uint(v), __float_as_uint(v), false, false);
    return fmaxf(__uint_as_float(rr[0]), __uint_as_float(rr[1]));
}

DI int lane_id() {
    int l;
    asm volatile("v_mbcnt_lo_u32_b32 %0, -1, 0\n\tv_mbcnt_hi_u32_b32 %0, -1, %0" : "=v"(l));
    __builtin_assume(l >= 0 && l < 64);
    return l;
}
DI void st16(const char* sbase, unsigned voff, u32x4 v) {
    asm volatile("global_store_dwordx4 %0, %1, %2 sc1\n\ts_nop 1" ::"v"(voff), "v"(v), "s"(sbase) : "memory");
}
DI void st4(const char* p, unsigned v) {
    unsigned t0, t1;
    asm volatile("s_nop 1\n\tv_mov_b32 %0, %2\n\tv_mov_b32 %1, 0\n\ts_mov_b64 exec, 1\n\tglobal_store_dword %1, %0, %3 sc1\n\ts_mov_b64 exec, -1"
                 : "=&v"(t0), "=&v"(t1) : "s"(v), "s"(p) : "memory");
}
DI unsigned uni(float v) { return __builtin_amdgcn_readfirstlane(__float_as_uint(v)); }
DI unsigned ld4(const char* p) { return __hip_atomic_load((GAS const unsigned*)p, __ATOMIC_RELAXED, __HIP_MEMORY_SCOPE_AGENT); }
DI void drain() { asm volatile("s_waitcnt vmcnt(0)" ::: "memory"); }
DI void wgbar() { asm volatile("s_waitcnt lgkmcnt(0)\n\ts_barrier" ::: "memory"); }
template <int SLEEP>
DI void wait_flags(const char* sbase, unsigned voff, unsigned tag) {
#pragma clang loop unroll(disable)
    for (unsigned spin = 0; spin < (1u << 20); ++spin) {
        unsigned f;
        asm volatile("global_load_dword %0, %1, %2 sc1\n\ts_waitcnt vmcnt(0)" : "=v"(f) : "v"(voff), "s"(sbase) : "memory");
        if (__all(f == tag)) break;
        __builtin_amdgcn_s_sleep(SLEEP);
    }
}

DI void wait_flags256(const char* sbase, unsigned voff, unsigned tag) {
#pragma clang loop unroll(disable)
    for (unsigned spin = 0; spin < (1u << 20); ++spin) {
        unsigned f0, f1, f2, f3;
        asm volatile("global_load_dword %0, %4, %5 sc1\n\tglobal_load_dword %1, %4, %6 sc1\n\tglobal_load_dword %2, %4, %7 sc1\n\t"
                     "global_load_dword %3, %4, %8 sc1\n\ts_waitcnt vmcnt(0)"
                     : "=&v"(f0), "=&v"(f1), "=&v"(f2), "=&v"(f3)
                     : "v"(voff), "s"(sbase), "s"(sbase + 8192), "s"(sbase + 16384), "s"(sbase + 24576) : "memory");
        if (__all(f0 == tag && f1 == tag && f2 == tag && f3 == tag)) break;
        __builtin_amdgcn_s_sleep(8);
    }
}

constexpr int QCNT = 73728;
#define MF(D, A, B, C) "v_mfma_f32_32x32x16_bf16 " D ", " A ", " B ", " C "\n\t"
DI u32x4 srd(const char* base, unsigned bytes) {
    return u32x4{(unsigned)(uintptr_t)base, (unsigned)((uintptr_t)base >> 32) & 0xffffu, bytes, 0x00020000u};
}
DI void qkv_store(char* ws, const f32x16& acc, int type, int head, int tile, int lane) {
    const float sc = (type == 0) ? 0.25f * 1.4426950408889634f : 1.f;
    const char* dst = ws + WS_Q + (size_t)type * (2u << 20) + (size_t)(head * 64 + tile) * 1024;
    const unsigned vo = type < 2 ? lane * 16 : ((lane >> 4) & 1) * 65536 + (lane >> 5) * 256 + (lane & 15) * 16;
#pragma unroll
    for (int hf = 0; hf < 2; ++hf) {
        u32x4 o;
#pragma unroll
        for (int i = 0; i < 4; ++i) o[i] = cvtpk(acc[8 * hf + 2 * i] * sc, acc[8 * hf + 2 * i + 1] * sc);
        st16(dst + hf * (type < 2 ? 65536 : 512), vo, o);
    }
}
DI void publish4(const char* flag, unsigned tag, int lane) {
    drain();
    unsigned arr = 0;
    if (lane == 0) arr = __hip_atomic_fetch_add((LAS unsigned*)QCNT, 1u, __ATOMIC_RELAXED, __HIP_MEMORY_SCOPE_WORKGROUP);
    if (__builtin_amdgcn_readfirstlane(arr) == 3) st4(flag, tag);
}
DI void qkv_loader(const char* x, const char* wqkv, int row, int col, int lw, const char* hint) {
    const int lane = lane_id(), xr = 16 * lw + (lane >> 3), wr = 16 * lw + (lane >> 4);
    const unsigned x0 = xr * 1024 + (((lane & 7) ^ ((xr >> 1) & 7)) << 4), x1 = (xr + 8) * 1024 + (((lane & 7) ^ (((xr + 8) >> 1) & 7)) << 4) - 1024;
    const unsigned w0 = wr * 3072 + (((lane & 15) ^ ((wr & 3) << 2)) << 4);
    const u32x4 rx = srd(x + (size_t)row * 65536, 65536), rw = srd(wqkv + col * 256, 512 * 3072 - col * 256);
    const int rot = (row + col) * QROTM & QROT;
    auto issue = [&](int s, int slot) {
        const int kc = (s + rot) & 7;
        asm volatile("s_mov_b32 m0, %[m]\n\ts_nop 0\n\t"
                     "buffer_load_dwordx4 %[x0], %[rx], %[ox] offen lds\n\t"
                     "buffer_load_dwordx4 %[x1], %[rx], %[ox] offen offset:1024 lds\n\t"
                     "s_mov_b32 m0, %[m2]\n\ts_nop 0\n\t"
                     "buffer_load_dwordx4 %[w0], %[rw], %[ow] offen lds\n\t"
                     "buffer_load_dwordx4 %[w1], %[rw], %[ow] offen offset:1024 lds\n\t"
                     "buffer_load_dwordx4 %[w2], %[rw], %[ow] offen offset:2048 lds\n\t"
                     "buffer_load_dwordx4 %[w3], %[rw], %[ow] offen offset:3072 lds"
                     :: [m] "s"(slot * 24576 + lw * 2048), [m2] "s"(slot * 24576 + 8192 + lw * 4096), [ow] "s"(kc * 196608), [ox] "s"(kc * 128),
                        [rw] "s"(rw), [rx] "s"(rx), [x0] "v"(x0), [x1] "v"(x1),
                        [w0] "v"(w0), [w1] "v"(w0 + 11264), [w2] "v"(w0 + 22528), [w3] "v"(w0 + 33792)
                     : "memory");
    };
    issue(0, 0);
    __builtin_amdgcn_s_setprio(2);
    issue(1, 1);
    __builtin_amdgcn_s_setprio(1);
    issue(2, 2);
    __builtin_amdgcn_s_setprio(3);
    asm volatile("s_waitcnt vmcnt(12)\n\ts_barrier" ::: "memory");
    int slot = 0;
#pragma clang loop unroll(disable)
    for (int s = 3; s < 8; ++s) {
        asm volatile("s_waitcnt vmcnt(6)\n\ts_barrier" ::: "memory");
        issue(s, slot);
        slot = slot == 2 ? 0 : slot + 1;
    }
    if (lw == 1) {
        st4(hint, __builtin_amdgcn_readfirstlane(*(LAS const unsigned*)(QCNT + 4)));
        asm volatile("s_waitcnt vmcnt(7)\n\ts_barrier\n\ts_waitcnt vmcnt(1)\n\ts_barrier" ::: "memory");
    } else
    asm volatile("s_waitcnt vmcnt(6)\n\ts_barrier\n\ts_waitcnt vmcnt(0)\n\ts_barrier" ::: "memory");
}
template <bool T>
DI void qkv_body(char* ws, char* lds, int row, int col, int a, int w, unsigned tag ST_ARG) {
    const int lane = lane_id(), r = lane & 31, h2 = lane >> 5, q = (lane & 15) >> 2;
    f32x16 acc[2] = {};
    const int xa0 = (32 * (w & 1) + r) * 128 + ((h2 ^ ((r >> 1) & 7)) << 4);
    const int wa0 = 8192 + (8 * h2 + q) * 256 + (((2 * (w >> 1)) ^ q) << 6) + ((lane >> 4) & 1) * 32 + (lane & 3) * 8;
    bf16x8 xf[4], wf[2][2];
    auto rdw = [&](LAS const char* sl, int st, int i) {
        LAS const char* pw = sl + (wa0 ^ (i << 6)) + st * 4096;
        const s16x4 lo = trread(pw), hi = trread(pw + 1024);
        wf[st & 1][i] = __builtin_shufflevector(lo, hi, 0, 1, 2, 3, 4, 5, 6, 7);
    };
    auto mm1 = [&](int st, int i) {
        acc[i] = T ? MFMA32(wf[st & 1][i], xf[st], acc[i]) : MFMA32(xf[st], wf[st & 1][i], acc[i]);
        __builtin_amdgcn_sched_barrier(0);
    };
    auto step = [&](int st, LAS const char* sl, bool bar) {
        const int sn = (st + 2) & 3;
        mm1(st, 0);
        if (bar) wgbar();
        xf[sn] = *(LAS const bf16x8*)(sl + (xa0 ^ (sn << 5))); rdw(sl, sn, 0); __builtin_amdgcn_sched_barrier(0);
        mm1(st, 1);
        rdw(sl, sn, 1); __builtin_amdgcn_sched_barrier(0);
    };
    if (w == 0) *(LAS u32x2*)QCNT = u32x2{0u, tag};
    wgbar();
    LAS const char* sl = (LAS const char*)lds;
#pragma unroll
    for (int st = 0; st < 2; ++st) { xf[st] = *(LAS const bf16x8*)(sl + (xa0 ^ (st << 5))); rdw(sl, st, 0); rdw(sl, st, 1); }
    __builtin_amdgcn_sched_barrier(0);
#pragma clang loop unroll(disable)
    for (int s = 0; s < 7; ++s) {
        step(0, sl, false); step(1, sl, false);
        sl = (sl == (LAS const char*)lds + 49152) ? (LAS const char*)lds : sl + 24576;
        step(2, sl, true); step(3, sl, false);
    }
    step(0, sl, false); step(1, sl, false);
    mm1(2, 0); mm1(2, 1); mm1(3, 0); mm1(3, 1);
    ST(4);
#pragma unroll
    for (int i = 0; i < 2; ++i) qkv_store(ws, acc[i], col >> 2, 8 * (col & 3) + 4 * (w >> 1) + 2 * i, 2 * row + (w & 1), lane);
    publish4(ws + WS_FQ + a * 128, tag, lane);
    ST(7);
}
DI void qkv2_loader(const char* x, const char* wqkv, int rowt, int col, int lw) {
    const int lane = lane_id(), row = 16 * lw + (lane >> 3);
    const unsigned w0 = row * 3072 + (((lane & 7) ^ (((row >> 1) & 1) << 2)) << 4), w1 = w0 + 8 * 3072 - 1024;
    const unsigned x0 = row * 1024 + (((lane & 7) ^ ((row >> 1) & 7)) << 4), x1 = (row + 8) * 1024 + (((lane & 7) ^ (((row + 8) >> 1) & 7)) << 4) - 1024;
    const u32x4 rx = srd(x + (size_t)rowt * 65536, 65536), rw = srd(wqkv + col * 128, 512 * 3072 - col * 128);
    auto issue = [&](int s) {
        asm volatile("s_mov_b32 m0, %[m]\n\ts_nop 0\n\t"
                     "buffer_load_dwordx4 %[w0], %[rw], %[ow] offen lds\n\t"
                     "buffer_load_dwordx4 %[w1], %[rw], %[ow] offen offset:1024 lds\n\t"
                     "s_add_u32 m0, %[m], 8192\n\ts_nop 0\n\t"
                     "buffer_load_dwordx4 %[x0], %[rx], %[ox] offen lds\n\t"
                     "buffer_load_dwordx4 %[x1], %[rx], %[ox] offen offset:1024 lds"
                     :: [m] "s"((s & 3) * 16384 + lw * 2048), [ow] "s"(s * 196608), [ox] "s"(s * 128), [rw] "s"(rw), [rx] "s"(rx),
                        [w0] "v"(w0), [w1] "v"(w1), [x0] "v"(x0), [x1] "v"(x1)
                     : "memory", "scc");
    };
    issue(0); issue(1); issue(2);
#pragma clang loop unroll(disable)
    for (int s = 3; s < 8; ++s) {
        asm volatile("s_waitcnt vmcnt(8)\n\ts_barrier" ::: "memory");
        issue(s);
    }
    asm volatile("s_waitcnt vmcnt(8)\n\ts_barrier\n\ts_waitcnt vmcnt(4)\n\ts_barrier\n\ts_waitcnt vmcnt(0)\n\ts_barrier" ::: "memory");
}
#define QRD3(B, SLOT, K)                                                                             \
    "ds_read_b64_tr_b16 v[" #B ":" #B "+1], %[t] offset:" #SLOT "*16384+" #K "*2048\n\t"              \
    "ds_read_b64_tr_b16 v[" #B "+2:" #B "+3], %[t] offset:" #SLOT "*16384+" #K "*2048+512\n\t"        \
    "ds_read_b128 v[" #B "+4:" #B "+7], %[x" #K "] offset:" #SLOT "*16384\n\t"
#define QSTAGE(SLOT, MM, N0, N1, N2, N3, P0, P1, P2, P3)                              \
    "s_waitcnt lgkmcnt(0)\n\t"                                                        \
    "s_barrier\n\t"                                                                   \
    MM(P0) QRD3(N0, SLOT, 0) MM(P1) QRD3(N1, SLOT, 1) MM(P2) QRD3(N2, SLOT, 2) MM(P3) QRD3(N3, SLOT, 3)
#define QST_E(SLOT, MM) QSTAGE(SLOT, MM, 32, 40, 48, 56, 96, 104, 112, 120)
#define QST_O(SLOT, MM) QSTAGE(SLOT, MM, 96, 104, 112, 120, 32, 40, 48, 56)
#define QMT(P) MF("v[16:31]", "v[" #P ":" #P "+3]", "v[" #P "+4:" #P "+7]", "v[16:31]")
#define QMV(P) MF("v[16:31]", "v[" #P "+4:" #P "+7]", "v[" #P ":" #P "+3]", "v[16:31]")
#define QTILE_ASM(MM)                                                                                             \
    asm volatile(                                                                                                 \
        "s_barrier\n\t"                                                                                           \
        QRD3(32, 0, 0) QRD3(40, 0, 1) QRD3(48, 0, 2) QRD3(56, 0, 3)                                               \
        QST_O(1, MM) QST_E(2, MM)                                                                                 \
        "s_mov_b32 s44, 0\n\t"                                                                                    \
        "LQ_loop%=:\n\t"                                                                                          \
        QST_O(3, MM)                                                                                              \
        "s_cmp_lg_u32 s44, 0\n\t"                                                                                 \
        "s_cbranch_scc1 LQ_end%=\n\t"                                                                             \
        QST_E(0, MM) QST_O(1, MM) QST_E(2, MM)                                                                    \
        "s_mov_b32 s44, 1\n\t"                                                                                    \
        "s_branch LQ_loop%=\n\t"                                                                                  \
        "LQ_end%=:\n\t"                                                                                           \
        "s_waitcnt lgkmcnt(0)\n\t"                                                                                \
        MM(96) MM(104) MM(112) MM(120)                                                                            \
        "s_nop 15\n\t"                                                                                            \
        : "+{v[16:31]}"(acc)                                                                                      \
        : [t] "v"(t), [x0] "v"(xa), [x1] "v"(xa ^ 32), [x2] "v"(xa ^ 64), [x3] "v"(xa ^ 96)                       \
        : "memory", "scc", "s44",                                                                                 \
          "v32", "v33", "v34", "v35", "v36", "v37", "v38", "v39", "v40", "v41", "v42", "v43", "v44", "v45", "v46", "v47", \
          "v48", "v49", "v50", "v51", "v52", "v53", "v54", "v55", "v56", "v57", "v58", "v59", "v60", "v61", "v62", "v63", \
          "v96", "v97", "v98", "v99", "v100", "v101", "v102", "v103", "v104", "v105", "v106", "v107", "v108", "v109", "v110", "v111", \
          "v112", "v113", "v114", "v115", "v116", "v117", "v118", "v119", "v120", "v121", "v122", "v123", "v124", "v125", "v126", "v127")
DI void qkv2_body(char* ws, int row, int col, int bi, int w, unsigned tag) {
    const int lane = lane_id(), r = lane & 31, h2 = lane >> 5, q = (lane & 15) >> 2;
    const unsigned t = (8 * h2 + q) * 128 + (((w & 1) ^ ((q >> 1) & 1)) << 6) + ((lane >> 4) & 1) * 32 + (lane & 3) * 8;
    const unsigned xa = 8192 + (32 * (w >> 1) + r) * 128 + ((h2 ^ ((r >> 1) & 7)) << 4);
    f32x16 acc = {};
    if (col < 16) QTILE_ASM(QMT);
    else QTILE_ASM(QMV);
    qkv_store(ws, acc, col >> 3, 4 * (col & 7) + 2 * (w & 1), 2 * row + (w >> 1), lane);
    publish4(ws + WS_FQ + 32768 + bi * 128, tag, lane);
}

#define EXP(a) "v_exp_f32 v" #a ", v" #a "\n\t"
#define CVT(d, a, b) "v_cvt_pk_bf16_f32 v" #d ", v" #a ", v" #b "\n\t"
#define ASTEP(c0, c1, c2, c3, c4, c5, c6, c7, c8, c9, c10, c11, c12, c13, c14, c15, M1, M2, CL, M3)                  \
    EXP(c0) EXP(c1) M1 EXP(c2) EXP(c3) EXP(c4) EXP(c5) M2 CL                                                          \
    EXP(c6) EXP(c7) CVT(96, c0, c1) CVT(97, c2, c3) CVT(98, c4, c5) CVT(99, c6, c7) EXP(c8) EXP(c9) M3                \
    EXP(c10) EXP(c11) EXP(c12) EXP(c13) EXP(c14) EXP(c15)                                                             \
    CVT(100, c8, c9) CVT(101, c10, c11) CVT(102, c12, c13) CVT(103, c14, c15)
#define R_OA "v[64:79]"
#define R_OB "v[80:95]"
#define R_SA "v[32:47]"
#define R_SB "v[48:63]"
#define R_NM "v[16:31]"
#define ATILE(KC, KN, V0C, V1C, V0N, V1N, KOFF, VOFF0, VOFF1, PRE)                                                    \
    ASTEP(32, 33, 34, 35, 36, 37, 38, 39, 40, 41, 42, 43, 44, 45, 46, 47,                                             \
          MF(R_OB, V1N, "v[100:103]", R_OB), MF(R_SB, KC, "v[8:11]", R_NM),                                           \
          "s_waitcnt lgkmcnt(0)\n\t" PRE                                                                              \
          "ds_read_b128 " KC ", v12 offset:" #KOFF "\n\t"                                                             \
          "ds_read_b128 " V0N ", v13 offset:" #VOFF0 "\n\t"                                                           \
          "ds_read_b128 " V1N ", v13 offset:" #VOFF1 "\n\t",                                                          \
          MF(R_OA, V0C, "v[96:99]", R_OA))                                                                            \
    ASTEP(48, 49, 50, 51, 52, 53, 54, 55, 56, 57, 58, 59, 60, 61, 62, 63,                                             \
          MF(R_OA, V1C, "v[100:103]", R_OA), MF(R_SA, KN, "v[4:7]", R_NM), "", MF(R_OB, V0C, "v[96:99]", R_OB))
#define ATILE_E(KOFF, VOFF0, VOFF1, PRE) ATILE("v[104:107]", "v[108:111]", "v[112:115]", "v[116:119]", "v[120:123]", "v[124:127]", KOFF, VOFF0, VOFF1, PRE)
#define ATILE_O(KOFF, VOFF0, VOFF1, PRE) ATILE("v[108:111]", "v[104:107]", "v[120:123]", "v[124:127]", "v[112:115]", "v[116:119]", KOFF, VOFF0, VOFF1, PRE)
#define MOV4(a, b, c, d, s) "v_mov_b32 v" #a ", " s "\n\tv_mov_b32 v" #b ", " s "\n\tv_mov_b32 v" #c ", " s "\n\tv_mov_b32 v" #d ", " s "\n\t"
#define AISSUE                                                   \
    "s_add_u32 m0, s44, %[lw]\n\t"                               \
    "s_add_u32 s44, s44, 0x4000\n\t"                             \
    "buffer_load_dwordx4 v14, %[rk], s43 offen sc1 lds\n\t"      \
    "s_add_u32 m0, m0, 0x2000\n\t"                               \
    "s_and_b32 s44, s44, 0xffff\n\t"                             \
    "buffer_load_dwordx4 v14, %[rv], s43 offen sc1 lds\n\t"      \
    "s_add_u32 s43, s43, 0x2000\n\t"
#define PR2 "s_setprio 2\n\t"
#define PR3 ""
#define PR0 "s_setprio 1\n\t"
#define PR1 "s_setprio 0\n\t"
constexpr int A_ONES = 65536, A_VOTE = 69632 + 512, A_LSUM = 71680;
struct AttnRegs { f32x16 oa, ob, nm; u32x4 p1, v1; unsigned ka, va, go, fo; };
DI unsigned qkv_dep(int a, bool early) {
    const int lane = lane_id(), r = lane & 15, v = (lane >> 4) & 1, R = 4 * (a >> 5) + (lane & 3);
    const int kv = (r >> 3) + 8 * ((early ? 8 + 8 * v : 16 * v + 8 * (a & 1)) + (r & 7));
    const int qq = ((R >> 3) & 1) + 8 * ((R >> 4) * 24 + (R & 7));
    return ((a & 6) + (lane < 32 || !early ? kv : qq)) * 128;
}
DI void attn_prep(AttnRegs& R, float negm, int a, int w) {
    const int lane = lane_id(), r = lane & 31, kh = w >> 2;
    R.ka = 4096 * kh + lane * 16;
    R.va = r < 16 ? 8192 + 4096 * kh + (lane >> 5) * 256 + r * 16 : A_ONES + (r == 16 ? 0 : r == 20 ? 64 : (r > 20 && r < 28) ? 80 : 16);
    R.go = w * 1024 + lane * 16;
    R.fo = qkv_dep(a, false);
#pragma unroll
    for (int i = 0; i < 16; ++i) { R.oa[i] = 0.f; R.ob[i] = 0.f; R.nm[i] = negm; }
    R.p1 = u32x4{0u, 0u, 0u, 0u};
    R.v1 = u32x4{0u, 0u, 0u, 0u};
    asm volatile("" : "+{v[64:79]}"(R.oa), "+{v[80:95]}"(R.ob), "+{v[16:31]}"(R.nm), "+{v[100:103]}"(R.p1), "+{v[124:127]}"(R.v1),
                      "+{v12}"(R.ka), "+{v13}"(R.va), "+{v14}"(R.go), "+{v2}"(R.fo));
}
DI void attn_pass(AttnRegs& R, const char* qa, const char* qb, const char* ksrc, const char* vsrc, const char* go_flag, unsigned tag, int w) {
    const u32x4 rk = srd(ksrc, 65536), rv = srd(vsrc, 65536);
    asm volatile(
        "v_and_b32 v15, 0x3ff, v14\n\t"
        "s_mov_b32 s43, 0\n\t"
        "s_mov_b32 s44, 0\n\t"
        AISSUE
        "global_load_dwordx4 v[4:7], v15, %[qa] sc1\n\t"
        "global_load_dwordx4 v[8:11], v15, %[qb] sc1\n\t"
        AISSUE AISSUE
        "s_mov_b32 s40, 0xffff\n\t"
        "s_mov_b32 s41, 0xffff\n\t"
        "s_mov_b32 s42, 8\n\t"
        "s_waitcnt vmcnt(4)\n\t"
        "s_barrier\n\t"
        "ds_read_b128 v[104:107], v12\n\t"
        "ds_read_b128 v[108:111], v12 offset:1024\n\t"
        "ds_read_b128 v[112:115], v13\n\t"
        "ds_read_b128 v[116:119], v13 offset:512\n\t"
        PR2
        "s_waitcnt lgkmcnt(3)\n\t"
        MF(R_SA, "v[104:107]", "v[4:7]", R_NM)
        "s_nop 15\n\t"
        "LA_loop%=:\n\t"
        ATILE_E(2048, 1024, 1536, PR0)
        ATILE_O(3072, 2048, 2560, PR1)
        ATILE_E(0, 3072, 3584,
                "s_waitcnt vmcnt(2)\n\t"
                "s_cmp_eq_u32 s42, 7\n\t"
                "s_cbranch_scc0 LA_f%=\n\t"
                "s_mov_b32 s45, 0x100000\n\t"
                "LA_p%=:\n\t"
                "v_cmp_ne_u32 vcc, %[tag], v3\n\t"
                "s_sub_u32 s45, s45, 1\n\t"
                "s_cbranch_scc1 LA_f%=\n\t"
                "s_cbranch_vccz LA_f%=\n\t"
                "global_load_dword v3, v2, %[ff] sc1\n\t"
                "s_waitcnt vmcnt(0)\n\t"
                "s_branch LA_p%=\n\t"
                "LA_f%=:\n\t"
                "s_barrier\n\t"
                "s_setprio 3\n\t"
                "v_add_u32 v12, 0x4000, v12\n\t"
                "s_cmp_eq_u32 s42, 8\n\t"
                "s_cbranch_scc0 LA_h%=\n\t"
                "global_load_dword v3, v2, %[ff] sc1\n\t"
                "LA_h%=:\n\t"
                AISSUE
                "v_and_b32 v12, 0xffff, v12\n\t"
                PR2)
        ATILE_O(1024, 0, 512,
                PR3
                "s_mov_b64 exec, s[40:41]\n\t"
                "v_add_u32 v13, 0x4000, v13\n\t"
                "s_sub_u32 s42, s42, 1\n\t"
                "v_and_b32 v13, 0xffff, v13\n\t"
                "s_mov_b64 exec, -1\n\t"
                "s_cmp_lg_u32 s42, 0\n\t")
        "s_cbranch_scc1 LA_loop%=\n\t"
        "s_waitcnt lgkmcnt(0)\n\t"
        "s_setprio 3\n\t"
        MF(R_OB, "v[124:127]", "v[100:103]", R_OB)
        "s_nop 15\n\t"
        : "+{v[64:79]}"(R.oa), "+{v[80:95]}"(R.ob), "+{v[100:103]}"(R.p1), "+{v[124:127]}"(R.v1), "+{v12}"(R.ka), "+{v13}"(R.va)
        : "{v[16:31]}"(R.nm), "{v14}"(R.go), [qa] "s"(qa), [qb] "s"(qb), [rk] "s"(rk), [rv] "s"(rv), [lw] "s"(w * 1024), [tag] "s"(tag), [ff] "s"(go_flag), "{v2}"(R.fo)
        : "memory", "scc", "vcc", "s40", "s41", "s42", "s43", "s44", "s45", "v3", "v4", "v5", "v6", "v7", "v8", "v9", "v10", "v11", "v15",
          "v32", "v33", "v34", "v35", "v36", "v37", "v38", "v39", "v40", "v41", "v42", "v43", "v44", "v45", "v46", "v47",
          "v48", "v49", "v50", "v51", "v52", "v53", "v54", "v55", "v56", "v57", "v58", "v59", "v60", "v61", "v62", "v63",
          "v96", "v97", "v98", "v99", "v104", "v105", "v106", "v107", "v108", "v109", "v110", "v111",
          "v112", "v113", "v114", "v115", "v116", "v117", "v118", "v119", "v120", "v121", "v122", "v123");
}

DI void wait_qkv(const char* ws, int a, unsigned tag) { wait_flags<4>(ws + WS_FQ, qkv_dep(a, true), tag); }
DI void attn_role(const char* x, const char* wqkv, char* ws, char* lds, int a, int w) {
    const int kh = w >> 2;
    const int head = 4 * (a & 7) + ((a >> 3) & 3), qt = 8 * (a >> 5) + 2 * (w & 3);
    ST_DECL; ST(0);
    __builtin_amdgcn_s_setprio(3);
    unsigned tag;
    const int ti = a >> 3, trow = 8 * (a & 1) + (ti & 7) + (ti >= 24 ? 16 : 0), tcol = (ti >= 24 ? 0 : 4 * (ti >> 3)) + ((a & 7) >> 1);
    if (w >= 4) {
        qkv_loader(x, wqkv, trow, tcol, w - 4, ws + WS_FA + a * 128);
        tag = __builtin_amdgcn_readfirstlane(*(LAS const unsigned*)(QCNT + 4));
        if (!FAEARLY && w == 5) st4(ws + WS_FA + a * 128, tag);
        if (w == 4) wait_qkv(ws, a, tag);
    } else {
        tag = __builtin_amdgcn_readfirstlane(ld4(ws + WS_EPOCH)) * 0x9E3779B1u + 0x7F4A7C15u;
        if (tcol < 8) qkv_body<true>(ws, lds, trow, tcol, a, w, tag ST_PASS);
        else qkv_body<false>(ws, lds, trow, tcol, a, w, tag ST_PASS);
    }
    AttnRegs R;
    attn_prep(R, 0.f, a, w);
    ST(5);
    wgbar();
    ST(6);
    {
        const int o = w * 512 + lane_id() * 8;
        const unsigned one = ((o & 0x1b0) == 0) ? 0x3f803f80u : 0u;
        *(LAS u32x2*)((LAS char*)lds + A_ONES + o) = u32x2{one, one};
        asm volatile("s_waitcnt lgkmcnt(0)" ::: "memory");
    }
    const char* ksrc = ws + WS_K + (size_t)head * 65536;
    const char* qa = ws + WS_Q + (size_t)(head * 64 + qt + kh) * 1024;
    const char* qb = ws + WS_Q + (size_t)(head * 64 + qt + 1 - kh) * 1024;
#pragma clang loop unroll(disable)
    for (int att = 0;; ++att) {
        ST(1);
        attn_pass(R, qa, qb, ksrc, ksrc + (WS_V - WS_K), ws + WS_FQ + 32768, tag, w);
        ST(2);
        const int lane = lane_id();
        const f32x16 oa = R.oa, ob = R.ob;
        LAS char* ex = (LAS char*)lds + w * 2048 + lane * 16;
        LAS const char* exr = (LAS const char*)lds + (w ^ 4) * 2048 + lane * 16;
        *(LAS f32x4*)ex = f32x4{ob[0], ob[1], ob[2], ob[3]};
        *(LAS f32x4*)(ex + 1024) = f32x4{ob[4], ob[5], ob[6], ob[7]};
        *(LAS float*)((LAS char*)lds + A_LSUM + w * 256 + lane * 4) = ob[8];
        float big = fmaxf(oa[8], ob[8]) * 2.7e8f;
#pragma unroll
        for (int i = 0; i < 8; ++i) big = fmaxf(fmaxf(big, __builtin_fabsf(oa[i])), __builtin_fabsf(ob[i]));
        const bool bad = !__all(fminf(oa[8], ob[8]) > 8.1e-28f && big < 1.7e38f);
        if (lane == 0) ((LAS unsigned*)((LAS char*)lds + A_VOTE))[w] = bad;
        wgbar();
        const f32x4 t0 = *(LAS const f32x4*)exr, t1 = *(LAS const f32x4*)(exr + 1024);
        const float t2 = *(LAS const float*)((LAS char*)lds + A_LSUM + (w ^ 4) * 256 + lane * 4);
        const u32x4 v0 = *(LAS const u32x4*)((LAS char*)lds + A_VOTE), v1 = *(LAS const u32x4*)((LAS char*)lds + A_VOTE + 16);
        const float rinv = __builtin_amdgcn_rcpf(oa[8] + t2);
        u32x4 ov;
        ov[0] = cvtpk((oa[0] + t0[0]) * rinv, (oa[1] + t0[1]) * rinv);
        ov[1] = cvtpk((oa[2] + t0[2]) * rinv, (oa[3] + t0[3]) * rinv);
        ov[2] = cvtpk((oa[4] + t1[0]) * rinv, (oa[5] + t1[1]) * rinv);
        ov[3] = cvtpk((oa[6] + t1[2]) * rinv, (oa[7] + t1[3]) * rinv);
        const bool redo = att == 0 && __builtin_amdgcn_readfirstlane(v0[0] | v0[1] | v0[2] | v0[3] | v1[0] | v1[1] | v1[2] | v1[3]);
        if (att == 0 ? !redo : att == 1 + kh) {
            st16(ws + WS_O + (size_t)((qt + kh) * 32 + head) * 1024, lane * 16, ov);
            drain();
            st4(ws + WS_FO + ((qt + kh) * 32 + head) * 128, tag);
        }
        asm volatile("" : "+s"(att));
        if (att == 2 || (att == 0 && !redo)) break;
        drain();
        wgbar();
        qa = qb = ws + WS_Q + (size_t)(head * 64 + qt + att) * 1024;
        bf16x8 qf;
        asm volatile("global_load_dwordx4 %0, %1, %2 sc1\n\ts_waitcnt vmcnt(0)" : "=v"(qf) : "v"(lane * 16), "s"(qa) : "memory");
        float mx = -INFINITY;
#pragma clang loop unroll(disable)
        for (int t = 0; t < 64; ++t) {
            bf16x8 kf;
            asm volatile("global_load_dwordx4 %0, %1, %2 sc1\n\ts_waitcnt vmcnt(0)" : "=v"(kf) : "v"(lane * 16), "s"(ksrc + t * 1024) : "memory");
            const f32x16 s = MFMA32(kf, qf, f32x16{});
#pragma unroll
            for (int i = 0; i < 16; ++i) mx = fmaxf(mx, s[i]);
        }
        attn_prep(R, -swap32_max(mx), a, w);
    }
    ST(3); ST_OUT(0);
}

#define BG1 "s_sleep 1\n\t"
#define BG2 "s_sleep 1\n\t"
#define BG3 "s_sleep 1\n\t"
#define SISSUE                                                             \
    "s_mov_b32 m0, s44\n\t"                                                 \
    "s_add_u32 s45, %[ow], %[wst]\n\t"                                      \
    "buffer_load_dwordx4 %[g0], %[rw], %[ow] offen lds\n\t"                 \
    "buffer_load_dwordx4 %[g1], %[rw], %[ow] offen offset:1024 lds\n\t"     \
    "s_add_u32 m0, s44, 8192\n\t"                                           \
    "s_add_u32 s46, %[od], 4096\n\t"                                        \
    "buffer_load_dwordx4 %[d0], %[rd], %[od] offen sc1 lds\n\t"             \
    "buffer_load_dwordx4 %[d0], %[rd], %[od] offen offset:1024 sc1 lds\n\t" \
    "s_add_u32 s44, s44, 0x4000\n\t"                                        \
    "s_and_b32 s44, s44, 0xffff\n\t"                                        \
    "s_cmp_eq_u32 %[od], %[de]\n\t"                                         \
    "s_cselect_b32 %[ow], 0, s45\n\t"                                       \
    "s_cselect_b32 %[od], 0, s46\n\t"
struct SG { u32x4 rw, rd; unsigned wst, de, ow, od, a, b, c; };
#define WISSUE                                                              \
    "s_and_b32 s45, s44, 0xc000\n\t"                                        \
    "s_add_u32 m0, s45, %[lo2]\n\t"                                         \
    "s_lshl_b32 s45, s45, 3\n\t"                                            \
    "buffer_load_dwordx4 %[o0], %[ro], s45 offen lds\n\t"                   \
    "buffer_load_dwordx4 %[o1], %[ro], s45 offen offset:1024 lds\n\t"       \
    "buffer_load_dwordx4 %[o2], %[ro], s45 offen offset:2048 lds\n\t"       \
    "buffer_load_dwordx4 %[o3], %[ro], s45 offen offset:3072 lds\n\t"       \
    "s_add_u32 s44, s44, 0x4000\n\t"                                        \
    "s_and_b32 s44, s44, 0xffff\n\t"
template <int WO>
DI void sgemm_load(SG& G, int n, int lw, u32x4 ro, unsigned o0) {
    asm volatile(
        "s_mov_b32 s44, %[lo]\n\t"
        "s_sub_u32 s47, %[n], 3\n\t"
        SISSUE SISSUE SISSUE
        "LL_loop%=:\n\t"
        "s_waitcnt vmcnt(8)\n\t"
        "s_barrier\n\t"
        SISSUE
        "s_sub_u32 s47, s47, 1\n\t"
        "s_cmp_lg_u32 s47, 0\n\t"
        "s_cbranch_scc1 LL_loop%=\n\t"
        ".if %c[wo]\n\t"
        "s_waitcnt vmcnt(8)\n\ts_barrier\n\t" WISSUE
        "s_waitcnt vmcnt(8)\n\ts_barrier\n\t" WISSUE
        "s_waitcnt vmcnt(8)\n\ts_barrier\n\t" WISSUE
        "s_barrier\n\t" WISSUE
        ".else\n\t"
        "s_waitcnt vmcnt(8)\n\ts_barrier\n\t"
        "s_waitcnt vmcnt(4)\n\ts_barrier\n\t"
        "s_waitcnt vmcnt(0)\n\ts_barrier\n\t"
        "s_barrier\n\t"
        ".endif\n\t"
        : [ow] "+s"(G.ow), [od] "+s"(G.od)
        : [wst] "s"(G.wst), [rw] "s"(G.rw), [rd] "s"(G.rd), [lo] "s"(lw * 2048), [de] "s"(G.de), [n] "s"(n), [g0] "v"(G.a), [g1] "v"(G.b), [d0] "v"(G.c),
          [wo] "i"(WO), [ro] "s"(ro), [lo2] "s"(lw * 4096), [o0] "v"(o0), [o1] "v"(o0 + 7168), [o2] "v"(o0 + 14336), [o3] "v"(o0 + 21504)
        : "memory", "scc", "s44", "s45", "s46", "s47");
}
#define SRD3(B, SLOT, K, TD)                                                                         \
    "ds_read_b64_tr_b16 v[" #B ":" #B "+1], %[t] offset:" #SLOT "*16384+" #K "*2048\n\t"              \
    "ds_read_b64_tr_b16 v[" #B "+2:" #B "+3], %[t] offset:" #SLOT "*16384+" #K "*2048+" #TD "\n\t"    \
    "ds_read_b128 v[" #B "+4:" #B "+7], %[dl] offset:" #SLOT "*16384+" #K "*1024\n\t"
#define SSTAGE(SLOT, MM, TD, N0, N1, N2, N3, P0, P1, P2, P3)                          \
    "s_waitcnt lgkmcnt(0)\n\t"                                                        \
    "s_barrier\n\t"                                                                   \
    MM(P0) SRD3(N0, SLOT, 0, TD) SRD3(N1, SLOT, 1, TD) BG1                            \
    MM(P1) SRD3(N2, SLOT, 2, TD) SRD3(N3, SLOT, 3, TD) BG2                            \
    MM(P2) BG3 MM(P3)
#define SST_E(SLOT, MM, TD) SSTAGE(SLOT, MM, TD, 32, 40, 48, 56, 96, 104, 112, 120)
#define SST_O(SLOT, MM, TD) SSTAGE(SLOT, MM, TD, 96, 104, 112, 120, 32, 40, 48, 56)
#define SMM_NONE(P) ""
#define SMM_WA(P) MF("v[64:79]", "v[" #P ":" #P "+3]", "v[" #P "+4:" #P "+7]", "v[64:79]")
#define SMM_WB(P) MF("v[80:95]", "v[" #P "+4:" #P "+7]", "v[" #P ":" #P "+3]", "v[80:95]")
#define SGEMM_ASM(ACC, MM, TD, NLOOP, TAIL)                                                                       \
    asm volatile(                                                                                                 \
        "s_mov_b32 s44, " #NLOOP "\n\t"                                                                           \
        "s_barrier\n\t"                                                                                           \
        SRD3(32, 0, 0, TD) SRD3(40, 0, 1, TD) SRD3(48, 0, 2, TD) SRD3(56, 0, 3, TD)                               \
        "LS_loop%=:\n\t"                                                                                          \
        SST_O(1, MM, TD) SST_E(2, MM, TD) SST_O(3, MM, TD) SST_E(0, MM, TD)                                       \
        "s_sub_u32 s44, s44, 1\n\t"                                                                               \
        "s_cmp_lg_u32 s44, 0\n\t"                                                                                 \
        "s_cbranch_scc1 LS_loop%=\n\t"                                                                            \
        TAIL                                                                                                      \
        "s_waitcnt lgkmcnt(0)\n\t"                                                                                \
        MM(96) BG3 MM(104) BG3 MM(112) BG3 MM(120)                                                                \
        "s_nop 15\n\t"                                                                                            \
        : ACC(acc)                                                                                                \
        : [t] "v"(G.a), [dl] "v"(G.b)                                                                             \
        : "memory", "scc", "s44",                                                                                 \
          "v32", "v33", "v34", "v35", "v36", "v37", "v38", "v39", "v40", "v41", "v42", "v43", "v44", "v45", "v46", "v47", \
          "v48", "v49", "v50", "v51", "v52", "v53", "v54", "v55", "v56", "v57", "v58", "v59", "v60", "v61", "v62", "v63", \
          "v96", "v97", "v98", "v99", "v100", "v101", "v102", "v103", "v104", "v105", "v106", "v107", "v108", "v109", "v110", "v111", \
          "v112", "v113", "v114", "v115", "v116", "v117", "v118", "v119", "v120", "v121", "v122", "v123", "v124", "v125", "v126", "v127")

template <int MODE>
DI SG sgemm_setup(const char* wbase, unsigned wbytes, int wrow, const char* dbase, int dblk, int rot, int w) {
    const int lane = lane_id(), row = lane >> 3, lw = w & 3;
    const int q = (lane & 15) >> 2, h2 = lane >> 5;
    SG G;
    G.rw = srd(wbase, wbytes); G.rd = srd(dbase, 2 * dblk);
    G.wst = 64 * wrow; G.de = (MODE == 0 ? 31 : 21) * 4096; G.ow = rot * G.wst; G.od = rot * 4096;
    if (w >= 4) {
        G.a = (16 * lw + row) * wrow + (((lane & 7) ^ (((row >> 1) & 1) << 2)) << 4);
        G.b = G.a + 8 * wrow - 1024;
        G.c = (lw >> 1) * dblk + (lw & 1) * 2048 + lane * 16;
    } else {
        G.a = ((MODE == 0 ? 8 : 4) * h2 + q) * 128 + (((lw & 1) ^ ((q >> 1) & 1)) << 6) + ((lane >> 4) & 1) * 32 + (lane & 3) * 8;
        G.b = 8192 + (lw >> 1) * 4096 + lane * 16;
        G.c = 0;
    }
    asm volatile("" : "+v"(G.a), "+v"(G.b), "+v"(G.c));
    return G;
}
#define ACC0 "+{v[64:79]}"
#define ACC1 "+{v[80:95]}"
template <int MODE>
DI void sgemm_run(f32x16& acc, SG& G) {
    if (MODE == 0) SGEMM_ASM(ACC0, SMM_WA, 512, 7, SST_O(1, SMM_WA, 512) SST_E(2, SMM_WA, 512) SST_O(3, SMM_WA, 512));
    else SGEMM_ASM(ACC1, SMM_WB, 1024, 5, SST_O(1, SMM_WB, 1024));
}
DI f32x4 sub4(const f32x16& a, int g) { return f32x4{a[4 * g], a[4 * g + 1], a[4 * g + 2], a[4 * g + 3]}; }

DI unsigned gelu2(float a, float b) {
    constexpr float c1 = -2.f * 0.7978845608028654f * 1.4426950408889634f, c2 = c1 * 0.044715f;
    const f32x2 u = {a, b};
    const f32x2 z = u * (u * u * c2 + c1);
    const f32x2 d = f32x2{__builtin_amdgcn_exp2f(z[0]), __builtin_amdgcn_exp2f(z[1])} + 1.f;
    const f32x2 o = u * f32x2{__builtin_amdgcn_rcpf(d[0]), __builtin_amdgcn_rcpf(d[1])};
    return cvtpk(o[0], o[1]);
}

#define WRD(R0, R1, R2, R3, STEP)                                                    \
    "ds_read_b64_tr_b16 " R0 ", %[t0] offset:" #STEP "*2048\n\t"                      \
    "ds_read_b64_tr_b16 " R1 ", %[t0] offset:" #STEP "*2048+1024\n\t"                 \
    "ds_read_b64_tr_b16 " R2 ", %[t1] offset:" #STEP "*2048\n\t"                      \
    "ds_read_b64_tr_b16 " R3 ", %[t1] offset:" #STEP "*2048+1024\n\t"
#define WRD0(S) WRD("v[32:33]", "v[34:35]", "v[36:37]", "v[38:39]", S)
#define WRD1(S) WRD("v[40:41]", "v[42:43]", "v[44:45]", "v[46:47]", S)
#define WRD2(S) WRD("v[48:49]", "v[50:51]", "v[52:53]", "v[54:55]", S)
#define WRD3(S) WRD("v[56:57]", "v[58:59]", "v[60:61]", "v[62:63]", S)
#define WMM(WAIT, W0, W1, O) "s_waitcnt " WAIT "\n\t" MF("v[96:111]", W0, O, "v[96:111]") MF("v[112:127]", W1, O, "v[112:127]")
#define OLD(K, R, V, OFF)                                             \
    "5:\n\t"                                                           \
    "s_bitcmp1_b32 s40, " #K "\n\t"                                    \
    "s_cbranch_scc1 6f\n\t"                                            \
    "global_load_dword v24, %[fo], %[f] sc1\n\t"                       \
    "s_sub_u32 s41, s41, 1\n\t"                                        \
    "s_cselect_b32 s42, -1, 0\n\t"                                     \
    "s_waitcnt vmcnt(0)\n\t"                                           \
    "v_cmp_eq_u32 vcc, %[tag], v24\n\t"                                \
    "s_or_b32 s40, vcc_lo, s42\n\t"                                    \
    "s_bitcmp1_b32 s40, " #K "\n\t"                                    \
    "s_cbranch_scc1 6f\n\t"                                            \
    "s_sleep 3\n\t"                                                    \
    "s_branch 5b\n\t"                                                  \
    "6:\n\t"                                                           \
    "global_load_dwordx4 " R ", " V ", %[o] offset:" #OFF " sc1\n\t"
DI void wo_gemm(f32x16& a0, f32x16& a1, f32x4& k0, f32x4& k1, float& k2, const char* osrc, const char* fsrc, unsigned tag, int w) {
    const int lane = lane_id();
    const int q = (lane & 15) >> 2, h2 = lane >> 5;
    const unsigned tb = (w >> 1) * 16384 + (4 * h2 + q) * 128 + ((lane >> 4) & 1) * 32 + (lane & 3) * 8;
    const unsigned t0 = tb + ((q >> 1) & 1) * 64, t1 = tb + (((q >> 1) & 1) ^ 1) * 64;
    const unsigned d0 = lane * 16, d1 = d0 + 4096, fo = (lane & 7) * 128;
    asm volatile(
        WRD0(0) WRD1(1) WRD2(2) WRD3(3)
        "s_mov_b32 s40, 0\n\t"
        "s_mov_b32 s41, 0x100000\n\t"
        "s_setprio 0\n\t"
        OLD(0, "v[64:67]", "%[d0]", 0) OLD(1, "v[68:71]", "%[d0]", 1024) OLD(2, "v[72:75]", "%[d0]", 2048) OLD(3, "v[76:79]", "%[d0]", 3072)
        OLD(4, "v[80:83]", "%[d1]", 0) OLD(5, "v[84:87]", "%[d1]", 1024) OLD(6, "v[88:91]", "%[d1]", 2048) OLD(7, "v[92:95]", "%[d1]", 3072)
        "s_setprio 3\n\t"
        "s_waitcnt vmcnt(7) lgkmcnt(0)\n\t"
        MF("v[96:111]", "v[32:35]", "v[64:67]", "0") MF("v[112:127]", "v[36:39]", "v[64:67]", "0")
        WRD0(4)
        WMM("vmcnt(6)", "v[40:43]", "v[44:47]", "v[68:71]") WRD1(5)
        WMM("vmcnt(5)", "v[48:51]", "v[52:55]", "v[72:75]") WRD2(6)
        WMM("vmcnt(4)", "v[56:59]", "v[60:63]", "v[76:79]") WRD3(7)
        WMM("vmcnt(3) lgkmcnt(12)", "v[32:35]", "v[36:39]", "v[80:83]")
        WMM("vmcnt(2) lgkmcnt(0)", "v[40:43]", "v[44:47]", "v[84:87]")
        WMM("vmcnt(1)", "v[48:51]", "v[52:55]", "v[88:91]")
        "s_barrier\n\t"
        WMM("vmcnt(0)", "v[56:59]", "v[60:63]", "v[92:95]")
        "s_nop 15\n\t"
        : "={v[96:111]}"(a0), "={v[112:127]}"(a1),
          "+{v[12:15]}"(k0), "+{v[16:19]}"(k1), "+{v20}"(k2)
        : [o] "s"(osrc), [f] "s"(fsrc), [tag] "s"(tag), [d0] "v"(d0), [d1] "v"(d1), [t0] "v"(t0), [t1] "v"(t1), [fo] "v"(fo)
        : "memory", "scc", "vcc", "s40", "s41", "s42", "v24",
          "v32", "v33", "v34", "v35", "v36", "v37", "v38", "v39", "v40", "v41", "v42", "v43", "v44", "v45", "v46", "v47",
          "v48", "v49", "v50", "v51", "v52", "v53", "v54", "v55", "v56", "v57", "v58", "v59", "v60", "v61", "v62", "v63",
          "v64", "v65", "v66", "v67", "v68", "v69", "v70", "v71", "v72", "v73", "v74", "v75", "v76", "v77", "v78", "v79",
          "v80", "v81", "v82", "v83", "v84", "v85", "v86", "v87", "v88", "v89", "v90", "v91", "v92", "v93", "v94", "v95");
}

DI void mixer_role(const char* x, const char* wqkv, const char* gw, const char* wo, const char* w1, const char* b1, const char* w2, const char* b2,
                   char* ws, char* out, char* lds, int bi, int w) {
    const int j = bi & 7, i = bi >> 3;
    __builtin_amdgcn_s_setprio(LNP);
    ST_DECL; ST(0);
    const unsigned ep = ld4(ws + WS_EPOCH);
    unsigned tag;
    {
        const int lane = lane_id(), h2 = lane >> 5, tid = 64 * w + lane;
        const u32x4 xr = *(GAS const u32x4*)(x + (size_t)(8 * bi + w) * 1024 + lane * 16);
        const u32x4 gr = *(GAS const u32x4*)(gw + lane * 16);
        tag = __builtin_amdgcn_readfirstlane(ep) * 0x9E3779B1u + 0x7F4A7C15u;
        float v[8], gg[8];
#pragma unroll
        for (int k = 0; k < 4; ++k) { v[2 * k] = bflo(xr[k]); v[2 * k + 1] = bfhi(xr[k]); gg[2 * k] = bflo(gr[k]); gg[2 * k + 1] = bfhi(gr[k]); }
        float s1 = 0.f, s3 = 0.f;
#pragma unroll
        for (int k = 0; k < 8; ++k) { s1 += v[k]; s3 += v[k] * gg[k]; }
        const float mu = wave_sum(s1) * (1.f / 512.f);
        s3 = wave_sum(s3);
        float s2 = 0.f;
#pragma unroll
        for (int k = 0; k < 8; ++k) { v[k] -= mu; s2 += v[k] * v[k]; }
        const float rs = __builtin_amdgcn_rsqf(wave_sum(s2) * (1.f / 512.f) + 1e-5f);
        u32x4 hv;
#pragma unroll
        for (int k = 0; k < 4; ++k) hv[k] = cvtpk(v[2 * k] * rs, v[2 * k + 1] * rs);
        *(LAS u32x4*)((LAS char*)lds + w * 1024 + lane * 16) = hv;
        const float g = __builtin_amdgcn_rcpf(1.f + __builtin_amdgcn_exp2f(-1.4426950408889634f * s3));
        st4(ws + WS_G + (8 * bi + w) * 4, uni(g));
        wgbar();
        LAS const unsigned short* lb = (LAS const unsigned short*)lds + tid;
#pragma unroll
        for (int k = 0; k < 4; ++k) hv[k] = (unsigned)lb[(2 * k) * 512] | ((unsigned)lb[(2 * k + 1) * 512] << 16);
        st16(ws + WS_H + (size_t)(2 * w * 128 + (bi >> 1)) * 1024 + (bi & 1) * 512, h2 * 131072 + (lane & 31) * 16, hv);
        drain();
        wgbar();
        if (w == 0) st4(ws + WS_FL + bi * 128, tag);
    }
    __builtin_amdgcn_s_setprio(3);
    const int wb = w & 1, db = (w >> 1) & 1, hf = w >> 2;
    const int L = (j >> 1) * 22 + i, ft = L >> 2, dt1 = 4 * (j & 1) + (L & 3);
    const int st = 8 * (j >> 1) + (i & 7), dt = 4 * (j & 1) + (i >> 3);
    SG G1, G2;
    unsigned ex, l16, og, wof;
    {
        const int lane = lane_id();
        og = (64 * st + 32 * wb + (lane & 31)) * 4;
        G1 = sgemm_setup<0>(w1 + ft * 128, 2048 * 2752 - ft * 128, 2752, ws + WS_H + (size_t)2 * dt1 * 131072, 131072, ROT1, w);
        G2 = sgemm_setup<1>(w2 + st * 128, 1376 * 4096 - st * 128, 4096, ws + WS_U + (size_t)2 * dt * 86 * 1024, 86 * 1024, ROT2, w);
        l16 = lane * 16;
        ex = 65536 + (w & 3) * 2048 + l16;
        wof = (32 * (w & 3) + (lane >> 3)) * 1024 + (((lane & 7) ^ (((lane >> 4) & 1) << 2)) << 4);
        asm volatile("" : "+v"(ex), "+v"(l16), "+v"(og), "+v"(wof));
    }
    {
        const int row = 16 + 8 * (j & 1) + (i & 7), col = 8 + 8 * (i >> 4) + 2 * (j >> 1) + ((i >> 3) & 1);
        if (w == 0) {
            *(LAS unsigned*)QCNT = 0u;
            wait_flags<2>(ws + WS_FA + bi * 128, 0, tag);
        }
        wgbar();
        if (w >= 4) qkv2_loader(x, wqkv, row, col, w - 4);
        else qkv2_body(ws, row, col, bi, w, tag);
    }
    ST(6);
    if (i < 22) {
        if (w >= 4) wait_flags<8>(ws + WS_FL + (w - 4) * 8192, l16 * 8, tag);
        if (w == 4) wait_qkv(ws, bi, tag);
        if (BSLEEP) __builtin_amdgcn_s_sleep(BSLEEP);
        wgbar();
        ST(7);
        f32x4 v0, v1;
        if (w >= 4) {
            sgemm_load<0>(G1, 32, w - 4, G1.rw, 0);
            v0 = *(LAS const f32x4*)(uintptr_t)ex; v1 = *(LAS const f32x4*)(uintptr_t)(ex + 1024);
        } else {
            f32x16 acc1;
            const char* bsrc = b1 + (64 * ft + (ft < 21 ? 32 * wb : 0)) * 2 + (l16 >> 9) * 8;
#pragma unroll
            for (int k = 0; k < 4; ++k) {
                const u32x2 bv = *(GAS const u32x2*)(bsrc + 16 * k);
                acc1[4 * k] = bflo(bv[0]); acc1[4 * k + 1] = bfhi(bv[0]); acc1[4 * k + 2] = bflo(bv[1]); acc1[4 * k + 3] = bfhi(bv[1]);
            }
            sgemm_run<0>(acc1, G1);
            *(LAS f32x4*)(uintptr_t)ex = sub4(acc1, 2); *(LAS f32x4*)(uintptr_t)(ex + 1024) = sub4(acc1, 3);
            wgbar();
            v0 = sub4(acc1, 0); v1 = sub4(acc1, 1);
        }
        ST(1);
        const u32x4 o = {gelu2(v0[0], v0[1]), gelu2(v0[2], v0[3]), gelu2(v1[0], v1[1]), gelu2(v1[2], v1[3])};
        if (ft < 21 || wb == 0) st16(ws + WS_U + (size_t)((2 * dt1 + db) * 86 + 4 * ft + 2 * wb + hf) * 1024, l16, o);
        drain();
        wgbar();
        if (w == 0) st4(ws + WS_FU + (dt1 * 32 + ft) * 128, tag);
    }
    ST(2);
    if (bi == 255 && w == 1) {
        wait_flags256(ws + WS_FQ, l16 * 8, tag);
        wait_flags256(ws + WS_FL, l16 * 8, tag);
        st4(ws + WS_EPOCH, tag);
    }
    if (w == 0) wait_flags<8>(ws + WS_FU + dt * 4096, (l16 < 22 * 16 ? l16 : 0) * 8, tag);
    wgbar();
    ST(3);
    f32x4 bp0, bp1;
    float g;
    {
        asm volatile("global_load_dword %0, %1, %2 sc1" : "=v"(g) : "v"(og), "s"(ws + WS_G) : "memory");
        if (w >= 4) {
            sgemm_load<1>(G2, 22, w - 4, srd(wo + dt * 128, 524288 - dt * 128), wof);
            bp0 = *(LAS const f32x4*)(uintptr_t)ex; bp1 = *(LAS const f32x4*)(uintptr_t)(ex + 1024);
        } else {
            const float bb = bflo(*(GAS const unsigned short*)(b2 + (og >> 1)));
            f32x16 acc2 = {bb, bb, bb, bb, bb, bb, bb, bb, bb, bb, bb, bb, bb, bb, bb, bb};
            sgemm_run<1>(acc2, G2);
            *(LAS f32x4*)(uintptr_t)ex = sub4(acc2, 2); *(LAS f32x4*)(uintptr_t)(ex + 1024) = sub4(acc2, 3);
            wgbar();
            bp0 = sub4(acc2, 0); bp1 = sub4(acc2, 1);
            drain();
        }
        asm volatile("" : "+v"(g));
        ST(4);
        bp0 *= 1.f - g;
        bp1 *= 1.f - g;
        asm volatile("" : "+v"(bp0), "+v"(bp1));
    }
    drain();
    wgbar();
    ST(5);
    f32x16 a0, a1;
    wo_gemm(a0, a1, bp0, bp1, g, ws + WS_O + (size_t)((2 * st + wb) * 32 + 8 * (w >> 1)) * 1024,
            ws + WS_FO + ((2 * st + wb) * 32 + 8 * (w >> 1)) * 128, tag, w);
    {
        const int lane = lane_id(), kq = 2 * db + hf;
        LAS char* ex = (LAS char*)lds + w * 8192 + lane * 16;
#pragma unroll
        for (int k = 0; k < 4; ++k) { *(LAS f32x4*)(ex + k * 1024) = sub4(a0, k); *(LAS f32x4*)(ex + 4096 + k * 1024) = sub4(a1, k); }
        wgbar();
        LAS const char* exr = (LAS const char*)lds + wb * 8192 + kq * 2048 + lane * 16;
        f32x4 s0 = *(LAS const f32x4*)exr, s1 = *(LAS const f32x4*)(exr + 1024);
#pragma unroll
        for (int k = 1; k < 4; ++k) { s0 += *(LAS const f32x4*)(exr + k * 16384); s1 += *(LAS const f32x4*)(exr + k * 16384 + 1024); }
        s0 = s0 * g + bp0;
        s1 = s1 * g + bp1;
        const auto r0 = __builtin_amdgcn_permlane32_swap(cvtpk(s0[0], s0[1]), cvtpk(s1[0], s1[1]), false, false);
        const auto r1 = __builtin_amdgcn_permlane32_swap(cvtpk(s0[2], s0[3]), cvtpk(s1[2], s1[3]), false, false);
        *(GAS u32x4*)(out + (size_t)(64 * st + 32 * wb) * 1024 + (64 * dt + 16 * kq) * 2 + (lane & 31) * 1024 + (lane >> 5) * 16) = u32x4{r0[0], r1[0], r0[1], r1[1]};
    }
    ST_OUT(0);
}

constexpr int MK_LDS = 81920;
__global__ __launch_bounds__(512, 4) void mk(const char* x, const char* wqkv, const char* wo, const char* w1, const char* b1,
                                             const char* w2, const char* b2, const char* gw, char* ws, char* out) {
    extern __shared__ __attribute__((aligned(16))) char lds[];
    const int w = __builtin_amdgcn_readfirstlane(threadIdx.x >> 6);
    if (blockIdx.x < 256) attn_role(x, wqkv, ws, lds, blockIdx.x, w);
    else mixer_role(x, wqkv, gw, wo, w1, b1, w2, b2, ws, out, lds, blockIdx.x - 256, w);
}

extern "C" void kernel_launch(void* const* d_in, const int* in_sizes, int n_in, void* d_out, int out_size, void* d_ws, size_t ws_size,
                              hipStream_t stream) {
    (void)hipFuncSetAttribute((const void*)mk, hipFuncAttributeMaxDynamicSharedMemorySize, MK_LDS);
    mk<<<512, 512, MK_LDS, stream>>>((const char*)d_in[0], (const char*)d_in[1], (const char*)d_in[2], (const char*)d_in[4],
                                     (const char*)d_in[5], (const char*)d_in[6], (const char*)d_in[7], (const char*)d_in[8],
                                     (char*)d_ws, (char*)d_out);
}
```

```cpp
#include <hip/hip_runtime.h>
#include <hip/hip_bf16.h>
#include <stdint.h>

typedef __attribute__((ext_vector_type(8))) short bf16x8;
typedef __attribute__((ext_vector_type(4))) short s16x4;
typedef __attribute__((ext_vector_type(16))) float f32x16;
typedef __attribute__((ext_vector_type(4))) float f32x4;
typedef __attribute__((ext_vector_type(2))) float f32x2;
typedef __attribute__((ext_vector_type(4))) unsigned u32x4;
typedef __attribute__((ext_vector_type(2))) unsigned u32x2;
#define LAS __attribute__((address_space(3)))
#define GAS __attribute__((address_space(1)))
#define DI __device__ __forceinline__
#define MFMA32(a, b, c) __builtin_amdgcn_mfma_f32_32x32x16_bf16((a), (b), (c), 0, 0, 0)

#define QSLEEP 24
#define LSLEEP 32
#define ROT1 (((dt1 + ft) & 1) * 16)
#define ROT2 (((st + dt) & 1) * 11)
#define GATEQ 16
#define LNP 0
#define QROT 4
constexpr int S_ = 2048, D_ = 512, F_ = 1376;
constexpr size_t WS_EPOCH = 0;
constexpr size_t WS_G = 4096;
constexpr size_t WS_KN = WS_G + 8192;
constexpr size_t WS_FL = 98304;
constexpr size_t WS_FQ = 32768;
constexpr size_t WS_FU = 65536;
constexpr size_t WS_FA = 393216;
constexpr size_t WS_FO = 131072;
constexpr size_t WS_Q = 1u << 20;
constexpr size_t WS_K = WS_Q + (2u << 20);
constexpr size_t WS_V = WS_K + (2u << 20);
constexpr size_t WS_H = WS_V + (2u << 20);
constexpr size_t WS_U = WS_H + (2u << 20);
constexpr size_t WS_O = WS_U + (2u << 20);

#define ST_DECL
#define ST_ARG
#define ST_PASS
#define ST(i)
#define ST_OUT(k)
DI unsigned cvtpk(float lo, float hi) {
    typedef float f32x2_t __attribute__((ext_vector_type(2)));
    typedef __bf16 bf16x2_t __attribute__((ext_vector_type(2)));
    return __builtin_bit_cast(unsigned, __builtin_convertvector(f32x2_t{lo, hi}, bf16x2_t));
}
DI float bflo(unsigned u) { return __uint_as_float(u << 16); }
DI float bfhi(unsigned u) { return __uint_as_float(u & 0xffff0000u); }
DI s16x4 trread(LAS const char* p) {
    typedef short v4i16_t __attribute__((ext_vector_type(4)));
    return __builtin_bit_cast(s16x4, __builtin_amdgcn_ds_read_tr16_b64_v4i16((LAS v4i16_t*)p));
}
DI void glds(unsigned lds, unsigned voff, const void* sbase) {
    asm volatile("s_mov_b32 m0, %0\n\ts_nop 0\n\tglobal_load_lds_dwordx4 %1, %2" ::"s"(lds), "v"(voff), "s"(sbase) : "memory");
}
template <int CTRL> DI float dpp(float v) {
    return __int_as_float(__builtin_amdgcn_update_dpp(0, __float_as_int(v), CTRL, 0xf, 0xf, true));
}
DI float rl(float v, int l) { return __int_as_float(__builtin_amdgcn_readlane(__float_as_int(v), l)); }
DI float wave_sum(float v) {
    v += dpp<0xB1>(v); v += dpp<0x4E>(v); v += dpp<0x141>(v); v += dpp<0x140>(v);
    return (rl(v, 0) + rl(v, 16)) + (rl(v, 32) + rl(v, 48));
}
DI float wave_max(float v) {
    v = fmaxf(v, dpp<0xB1>(v)); v = fmaxf(v, dpp<0x4E>(v)); v = fmaxf(v, dpp<0x141>(v)); v = fmaxf(v, dpp<0x140>(v));
    return fmaxf(fmaxf(rl(v, 0), rl(v, 16)), fmaxf(rl(v, 32), rl(v, 48)));
}
DI float swap32_add(float v) {
    auto rr = __builtin_amdgcn_permlane32_swap(__float_as_uint(v), __float_as_uint(v), false, false);
    return __uint_as_float(rr[0]) + __uint_as_float(rr[1]);
}
DI float swap32_max(float v) {
    auto rr = __builtin_amdgcn_permlane32_swap(__float_as_uint(v), __float_as_uint(v), false, false);
    return fmaxf(__uint_as_float(rr[0]), __uint_as_float(rr[1]));
}

DI int lane_id() {
    int l;
    asm volatile("v_mbcnt_lo_u32_b32 %0, -1, 0\n\tv_mbcnt_hi_u32_b32 %0, -1, %0" : "=v"(l));
    __builtin_assume(l >= 0 && l < 64);
    return l;
}
DI void st16(const char* sbase, unsigned voff, u32x4 v) {
    asm volatile("global_store_dwordx4 %0, %1, %2 sc1\n\ts_nop 1" ::"v"(voff), "v"(v), "s"(sbase) : "memory");
}
DI void st4(const char* p, unsigned v) {
    unsigned t0, t1;
    asm volatile("s_nop 1\n\tv_mov_b32 %0, %2\n\tv_mov_b32 %1, 0\n\ts_mov_b64 exec, 1\n\tglobal_store_dword %1, %0, %3 sc1\n\ts_mov_b64 exec, -1"
                 : "=&v"(t0), "=&v"(t1) : "s"(v), "s"(p) : "memory");
}
DI unsigned uni(float v) { return __builtin_amdgcn_readfirstlane(__float_as_uint(v)); }
DI unsigned ld4(const char* p) { return __hip_atomic_load((GAS const unsigned*)p, __ATOMIC_RELAXED, __HIP_MEMORY_SCOPE_AGENT); }
DI void drain() { asm volatile("s_waitcnt vmcnt(0)" ::: "memory"); }
DI void wgbar() { asm volatile("s_waitcnt lgkmcnt(0)\n\ts_barrier" ::: "memory"); }
template <int SLEEP>
DI void wait_flags(const char* sbase, unsigned voff, unsigned tag) {
#pragma clang loop unroll(disable)
    for (unsigned spin = 0; spin < (1u << 20); ++spin) {
        unsigned f;
        asm volatile("global_load_dword %0, %1, %2 sc1\n\ts_waitcnt vmcnt(0)" : "=v"(f) : "v"(voff), "s"(sbase) : "memory");
        if (__all(f == tag)) break;
        __builtin_amdgcn_s_sleep(SLEEP);
    }
}

constexpr int QW0 = 49152, QCNT = 73728;
DI void qkv_loader(const char* x, const char* wqkv, int m, int p, int lw ST_ARG) {
    const int lane = lane_id();
    unsigned vx[4], vw[3];
#pragma unroll
    for (int ii = 0; ii < 4; ++ii) {
        const int row = 32 * lw + 8 * ii + (lane >> 3);
        vx[ii] = row * 1024 + (((lane & 7) ^ ((row >> 1) & 7)) << 4);
    }
#pragma unroll
    for (int ii = 0; ii < 3; ++ii) {
        const int o = 16 * lane + 1024 * ii;
        vw[ii] = (16 * lw + o / 192) * 3072 + (o % 192);
    }
    const char* xb = x + (size_t)m * 131072;
    const char* wb = wqkv + p * 192;
    const int rot = (p + 2 * m) & QROT;
    auto isx = [&](int s, int slot) {
        const char* xs = xb + ((s + rot) & 7) * 128;
#pragma unroll
        for (int ii = 0; ii < 4; ++ii) glds(slot * 16384 + lw * 4096 + ii * 1024, vx[ii], xs);
    };
    auto isw = [&](int s) {
        const char* wsrc = wb + ((s + rot) & 7) * 196608;
#pragma unroll
        for (int ii = 0; ii < 3; ++ii) glds(QW0 + (s & 1) * 12288 + lw * 3072 + ii * 1024, vw[ii], wsrc);
    };
    isw(0); isx(0, 0);
    __builtin_amdgcn_s_setprio(2);
    isw(1); isx(1, 1);
    __builtin_amdgcn_s_setprio(1);
    isx(2, 2);
    asm volatile("s_waitcnt vmcnt(11)\n\ts_barrier" ::: "memory");
    ST(7);
    int slot = 0;
#pragma clang loop unroll(disable)
    for (int s = 1; s < 6; ++s) {
        asm volatile("s_waitcnt vmcnt(4)\n\ts_barrier" ::: "memory");
        __builtin_amdgcn_s_setprio(3);
        isw(s + 1);
        __builtin_amdgcn_s_setprio(2);
        isx(s + 2, slot);
        slot = slot == 2 ? 0 : slot + 1;
    }
    asm volatile("s_waitcnt vmcnt(4)\n\ts_barrier" ::: "memory");
    __builtin_amdgcn_s_setprio(3);
    isw(7);
    asm volatile("s_waitcnt vmcnt(0)\n\ts_barrier" ::: "memory");
}

template <int NT>
DI void qkv_body(char* ws, char* lds, int m, int p, int w, unsigned tag ST_ARG) {
    const int lane = lane_id(), r = lane & 31, h2 = lane >> 5;
    f32x16 acc[3];
#pragma unroll
    for (int i = 0; i < 3; ++i) acc[i] = f32x16{};
    const int sw = (r >> 1) & 7;
    int xa[4];
#pragma unroll
    for (int st = 0; st < 4; ++st) xa[st] = (32 * w + r) * 128 + (((2 * st + h2) ^ sw) << 4);
    const int wa = QW0 + (8 * h2 + ((lane & 15) >> 2)) * 192 + ((lane >> 4) & 1) * 32 + (lane & 3) * 8;
    bf16x8 xf[4], wf[2][3];
    auto rdw = [&](LAS const char* wsl, int st, int i) {
        const s16x4 lo = trread(wsl + wa + st * 3072 + i * 64), hi = trread(wsl + wa + st * 3072 + i * 64 + 768);
        wf[st & 1][i] = __builtin_shufflevector(lo, hi, 0, 1, 2, 3, 4, 5, 6, 7);
    };
    auto mm1 = [&](int st, int i) {
        if (i < NT) acc[i] = MFMA32(wf[st & 1][i], xf[st], acc[i]);
        else acc[i] = MFMA32(xf[st], wf[st & 1][i], acc[i]);
        __builtin_amdgcn_sched_barrier(0);
    };
    auto step = [&](int st, LAS const char* xsl, LAS const char* wsl, bool bar) {
        const int sn = (st + 2) & 3;
        mm1(st, 0);
        if (bar) wgbar();
        xf[sn] = *(LAS const bf16x8*)(xsl + xa[sn]); rdw(wsl, sn, 0); __builtin_amdgcn_sched_barrier(0);
        mm1(st, 1);
        rdw(wsl, sn, 1); __builtin_amdgcn_sched_barrier(0);
        mm1(st, 2);
        rdw(wsl, sn, 2); __builtin_amdgcn_sched_barrier(0);
    };
    if (w == 0) *(LAS u32x2*)QCNT = u32x2{0u, tag};
    wgbar();
    LAS const char* xsl = (LAS const char*)lds;
    LAS const char* wsl = (LAS const char*)lds;
#pragma unroll
    for (int st = 0; st < 2; ++st) { xf[st] = *(LAS const bf16x8*)(xsl + xa[st]); rdw(wsl, st, 0); rdw(wsl, st, 1); rdw(wsl, st, 2); }
    __builtin_amdgcn_sched_barrier(0);
#pragma clang loop unroll(disable)
    for (int s = 0; s < 7; ++s) {
        step(0, xsl, wsl, false); step(1, xsl, wsl, false);
        xsl = (xsl == (LAS const char*)lds + 32768) ? (LAS const char*)lds : xsl + 16384;
        wsl = (LAS const char*)lds + ((s & 1) ? 0 : 12288);
        step(2, xsl, wsl, true); step(3, xsl, wsl, false);
    }
    step(0, xsl, wsl, false); step(1, xsl, wsl, false);
    mm1(2, 0); mm1(2, 1); mm1(2, 2); mm1(3, 0); mm1(3, 1); mm1(3, 2);
    ST(4);
    const int tile = 4 * m + w;
#pragma unroll
    for (int u = 0; u < 6; ++u) {
        const int cb = 3 * p + (u >> 1), half = u & 1, type = cb >> 4, hp = cb & 15;
        const float sc = (type == 0) ? 0.25f * 1.4426950408889634f : 1.f;
        u32x4 o;
#pragma unroll
        for (int i = 0; i < 4; ++i) o[i] = cvtpk(acc[u >> 1][8 * half + 2 * i] * sc, acc[u >> 1][8 * half + 2 * i + 1] * sc);
        if ((u >> 1) < NT) {
            st16(ws + (type ? WS_K : WS_Q) + (size_t)((2 * hp + half) * 64 + tile) * 1024, lane * 16, o);
        } else {
            st16(ws + WS_V + (size_t)(2 * hp * 64 + tile) * 1024 + half * 512, (r >> 4) * 65536 + h2 * 256 + (r & 15) * 16, o);
        }
    }
    drain();
    ST(7);
    unsigned arr = 0;
    if (lane == 0) arr = __hip_atomic_fetch_add((LAS unsigned*)QCNT, 1u, __ATOMIC_RELAXED, __HIP_MEMORY_SCOPE_WORKGROUP);
    if (__builtin_amdgcn_readfirstlane(arr) == 3) st4(ws + WS_FQ + (p * 16 + m) * 128, tag);
}

#define EXP(a) "v_exp_f32 v" #a ", v" #a "\n\t"
#define CVT(d, a, b) "v_cvt_pk_bf16_f32 v" #d ", v" #a ", v" #b "\n\t"
#define MF(D, A, B, C) "v_mfma_f32_32x32x16_bf16 " D ", " A ", " B ", " C "\n\t"
#define ASTEP(c0, c1, c2, c3, c4, c5, c6, c7, c8, c9, c10, c11, c12, c13, c14, c15, M1, M2, CL, M3)                  \
    EXP(c0) EXP(c1) M1 EXP(c2) EXP(c3) EXP(c4) EXP(c5) M2 CL                                                          \
    EXP(c6) EXP(c7) CVT(96, c0, c1) CVT(97, c2, c3) CVT(98, c4, c5) CVT(99, c6, c7) EXP(c8) EXP(c9) M3                \
    EXP(c10) EXP(c11) EXP(c12) EXP(c13) EXP(c14) EXP(c15)                                                             \
    CVT(100, c8, c9) CVT(101, c10, c11) CVT(102, c12, c13) CVT(103, c14, c15)
#define R_OA "v[64:79]"
#define R_OB "v[80:95]"
#define R_SA "v[32:47]"
#define R_SB "v[48:63]"
#define R_NM "v[16:31]"
#define ATILE(KC, KN, V0C, V1C, V0N, V1N, KOFF, VOFF0, VOFF1, PRE)                                                    \
    ASTEP(32, 33, 34, 35, 36, 37, 38, 39, 40, 41, 42, 43, 44, 45, 46, 47,                                             \
          MF(R_OB, V1N, "v[100:103]", R_OB), MF(R_SB, KC, "v[8:11]", R_NM),                                           \
          "s_waitcnt lgkmcnt(0)\n\t" PRE                                                                              \
          "ds_read_b128 " KC ", v12 offset:" #KOFF "\n\t"                                                             \
          "ds_read_b128 " V0N ", v13 offset:" #VOFF0 "\n\t"                                                           \
          "ds_read_b128 " V1N ", v13 offset:" #VOFF1 "\n\t",                                                          \
          MF(R_OA, V0C, "v[96:99]", R_OA))                                                                            \
    ASTEP(48, 49, 50, 51, 52, 53, 54, 55, 56, 57, 58, 59, 60, 61, 62, 63,                                             \
          MF(R_OA, V1C, "v[100:103]", R_OA), MF(R_SA, KN, "v[4:7]", R_NM), "", MF(R_OB, V0C, "v[96:99]", R_OB))
#define ATILE_E(KOFF, VOFF0, VOFF1, PRE) ATILE("v[104:107]", "v[108:111]", "v[112:115]", "v[116:119]", "v[120:123]", "v[124:127]", KOFF, VOFF0, VOFF1, PRE)
#define ATILE_O(KOFF, VOFF0, VOFF1, PRE) ATILE("v[108:111]", "v[104:107]", "v[120:123]", "v[124:127]", "v[112:115]", "v[116:119]", KOFF, VOFF0, VOFF1, PRE)
#define MOV4(a, b, c, d, s) "v_mov_b32 v" #a ", " s "\n\tv_mov_b32 v" #b ", " s "\n\tv_mov_b32 v" #c ", " s "\n\tv_mov_b32 v" #d ", " s "\n\t"
#define AISSUE                                                   \
    "s_add_u32 m0, s44, %[lw]\n\t"                               \
    "s_add_u32 s44, s44, 0x4000\n\t"                             \
    "buffer_load_dwordx4 v14, %[rk], s43 offen sc1 lds\n\t"      \
    "s_add_u32 m0, m0, 0x2000\n\t"                               \
    "s_and_b32 s44, s44, 0xffff\n\t"                             \
    "buffer_load_dwordx4 v14, %[rv], s43 offen sc1 lds\n\t"      \
    "s_add_u32 s43, s43, 0x2000\n\t"
DI u32x4 srd(const char* base, unsigned bytes) {
    return u32x4{(unsigned)(uintptr_t)base, (unsigned)((uintptr_t)base >> 32) & 0xffffu, bytes, 0x00020000u};
}
#define PR2 "s_setprio 2\n\t"
#define PR3 ""
#define PR0 "s_setprio 1\n\t"
#define PR1 "s_setprio 0\n\t"
constexpr int A_ONES = 65536, A_VOTE = 69632 + 512, A_LSUM = 71680;
struct AttnRegs { f32x16 oa, ob, nm; u32x4 p1, v1; unsigned ka, va, go; };
DI void attn_prep(AttnRegs& R, float negm, int w) {
    const int lane = lane_id(), r = lane & 31, kh = w >> 2;
    R.ka = 4096 * kh + lane * 16;
    R.va = r < 16 ? 8192 + 4096 * kh + (lane >> 5) * 256 + r * 16 : A_ONES + (r == 16 ? 0 : r == 20 ? 64 : (r > 20 && r < 28) ? 80 : 16);
    R.go = w * 1024 + lane * 16;
#pragma unroll
    for (int i = 0; i < 16; ++i) { R.oa[i] = 0.f; R.ob[i] = 0.f; R.nm[i] = negm; }
    R.p1 = u32x4{0u, 0u, 0u, 0u};
    R.v1 = u32x4{0u, 0u, 0u, 0u};
    asm volatile("" : "+{v[64:79]}"(R.oa), "+{v[80:95]}"(R.ob), "+{v[16:31]}"(R.nm), "+{v[100:103]}"(R.p1), "+{v[124:127]}"(R.v1),
                      "+{v12}"(R.ka), "+{v13}"(R.va), "+{v14}"(R.go));
}
DI void attn_pass(AttnRegs& R, const char* qa, const char* qb, const char* ksrc, const char* vsrc, const char* go_flag, unsigned tag, int w) {
    const u32x4 rk = srd(ksrc, 65536), rv = srd(vsrc, 65536);
    asm volatile(
        "v_and_b32 v15, 0x3ff, v14\n\t"
        "s_mov_b32 s43, 0\n\t"
        "s_mov_b32 s44, 0\n\t"
        AISSUE
        "global_load_dwordx4 v[4:7], v15, %[qa] sc1\n\t"
        "global_load_dwordx4 v[8:11], v15, %[qb] sc1\n\t"
        AISSUE AISSUE
        "s_mov_b32 s40, 0xffff\n\t"
        "s_mov_b32 s41, 0xffff\n\t"
        "s_mov_b32 s42, 8\n\t"
        "s_waitcnt vmcnt(4)\n\t"
        "s_barrier\n\t"
        "ds_read_b128 v[104:107], v12\n\t"
        "ds_read_b128 v[108:111], v12 offset:1024\n\t"
        "ds_read_b128 v[112:115], v13\n\t"
        "ds_read_b128 v[116:119], v13 offset:512\n\t"
        "s_cmp_lg_u32 %[lw], 0\n\t"
        "s_cbranch_scc1 LA_g%=\n\t"
        "v_mov_b32 v15, %[tag]\n\t"
        "v_mov_b32 v32, 0\n\t"
        "s_mov_b64 exec, 1\n\t"
        "global_store_dword v32, v15, %[gf] sc1\n\t"
        "s_mov_b64 exec, -1\n\t"
        "LA_g%=:\n\t"
        PR2
        "s_waitcnt lgkmcnt(3)\n\t"
        MF(R_SA, "v[104:107]", "v[4:7]", R_NM)
        "s_nop 15\n\t"
        "LA_loop%=:\n\t"
        ATILE_E(2048, 1024, 1536, PR0)
        ATILE_O(3072, 2048, 2560, PR1)
        ATILE_E(0, 3072, 3584,
                "s_waitcnt vmcnt(2)\n\t"
                "s_barrier\n\t"
                "s_setprio 3\n\t"
                "v_add_u32 v12, 0x4000, v12\n\t"
                AISSUE
                "v_and_b32 v12, 0xffff, v12\n\t"
                PR2)
        ATILE_O(1024, 0, 512,
                PR3
                "s_mov_b64 exec, s[40:41]\n\t"
                "v_add_u32 v13, 0x4000, v13\n\t"
                "s_sub_u32 s42, s42, 1\n\t"
                "v_and_b32 v13, 0xffff, v13\n\t"
                "s_mov_b64 exec, -1\n\t"
                "s_cmp_lg_u32 s42, 0\n\t")
        "s_cbranch_scc1 LA_loop%=\n\t"
        "s_waitcnt lgkmcnt(0)\n\t"
        "s_setprio 3\n\t"
        MF(R_OB, "v[124:127]", "v[100:103]", R_OB)
        "s_nop 15\n\t"
        : "+{v[64:79]}"(R.oa), "+{v[80:95]}"(R.ob), "+{v[100:103]}"(R.p1), "+{v[124:127]}"(R.v1), "+{v12}"(R.ka), "+{v13}"(R.va)
        : "{v[16:31]}"(R.nm), "{v14}"(R.go), [qa] "s"(qa), [qb] "s"(qb), [rk] "s"(rk), [rv] "s"(rv), [lw] "s"(w * 1024), [tag] "s"(tag), [gf] "s"(go_flag)
        : "memory", "scc", "s40", "s41", "s42", "s43", "s44", "v4", "v5", "v6", "v7", "v8", "v9", "v10", "v11", "v15",
          "v32", "v33", "v34", "v35", "v36", "v37", "v38", "v39", "v40", "v41", "v42", "v43", "v44", "v45", "v46", "v47",
          "v48", "v49", "v50", "v51", "v52", "v53", "v54", "v55", "v56", "v57", "v58", "v59", "v60", "v61", "v62", "v63",
          "v96", "v97", "v98", "v99", "v104", "v105", "v106", "v107", "v108", "v109", "v110", "v111",
          "v112", "v113", "v114", "v115", "v116", "v117", "v118", "v119", "v120", "v121", "v122", "v123");
}

DI void wait_qkv(const char* ws, int a, unsigned tag) {
    const int lane = lane_id(), hp = 2 * (a & 7) + ((a >> 4) & 1);
    const int fp = lane < 16 ? (16 + hp) / 3 : lane < 32 ? (32 + hp) / 3 : hp / 3;
    const int fm = lane < 32 ? (lane & 15) : 2 * (a >> 5) + (lane & 1);
    wait_flags<4>(ws + WS_FQ, (fp * 16 + fm) * 128, tag);
}
DI void attn_role(const char* x, const char* wqkv, char* ws, char* lds, int a, int w) {
    const int kh = w >> 2;
    const int head = 4 * (a & 7) + ((a >> 3) & 3), qt = 8 * (a >> 5) + 2 * (w & 3);
    ST_DECL; ST(0);
    __builtin_amdgcn_s_setprio(3);
    unsigned tag = 0;
    {
        const int m = 4 * ((a & 7) >> 1) + ((a >> 3) & 3), p = 8 * (a & 1) + (a >> 5);
        if (w < 4) tag = __builtin_amdgcn_readfirstlane(ld4(ws + WS_EPOCH)) * 0x9E3779B1u + 0x7F4A7C15u;
        if (w >= 4) {
            qkv_loader(x, wqkv, m, p, w - 4 ST_PASS);
            tag = __builtin_amdgcn_readfirstlane(*(LAS const unsigned*)(QCNT + 4));
            ST(4);
            if (w == 4) {
                __builtin_amdgcn_s_sleep(QSLEEP);
                wait_qkv(ws, a, tag);
            }
        } else if (p < 10) qkv_body<3>(ws, lds, m, p, w, tag ST_PASS);
        else if (p == 10) qkv_body<2>(ws, lds, m, p, w, tag ST_PASS);
        else qkv_body<0>(ws, lds, m, p, w, tag ST_PASS);
    }
    AttnRegs R;
    attn_prep(R, 0.f, w);
    ST(5);
    wgbar();
    ST(6);
    {
        const int o = w * 512 + lane_id() * 8;
        const unsigned one = ((o & 0x1b0) == 0) ? 0x3f803f80u : 0u;
        *(LAS u32x2*)((LAS char*)lds + A_ONES + o) = u32x2{one, one};
        asm volatile("s_waitcnt lgkmcnt(0)" ::: "memory");
    }
    const char* ksrc = ws + WS_K + (size_t)head * 65536;
    const char* qa = ws + WS_Q + (size_t)(head * 64 + qt + kh) * 1024;
    const char* qb = ws + WS_Q + (size_t)(head * 64 + qt + 1 - kh) * 1024;
#pragma clang loop unroll(disable)
    for (int att = 0;; ++att) {
        ST(1);
        attn_pass(R, qa, qb, ksrc, ksrc + (WS_V - WS_K), ws + WS_FA + a * 128, tag, w);
        ST(2);
        const int lane = lane_id();
        const f32x16 oa = R.oa, ob = R.ob;
        LAS char* ex = (LAS char*)lds + w * 2048 + lane * 16;
        LAS const char* exr = (LAS const char*)lds + (w ^ 4) * 2048 + lane * 16;
        *(LAS f32x4*)ex = f32x4{ob[0], ob[1], ob[2], ob[3]};
        *(LAS f32x4*)(ex + 1024) = f32x4{ob[4], ob[5], ob[6], ob[7]};
        *(LAS float*)((LAS char*)lds + A_LSUM + w * 256 + lane * 4) = ob[8];
        float big = fmaxf(oa[8], ob[8]) * 2.7e8f;
#pragma unroll
        for (int i = 0; i < 8; ++i) big = fmaxf(fmaxf(big, __builtin_fabsf(oa[i])), __builtin_fabsf(ob[i]));
        const bool bad = !__all(fminf(oa[8], ob[8]) > 8.1e-28f && big < 1.7e38f);
        if (lane == 0) ((LAS unsigned*)((LAS char*)lds + A_VOTE))[w] = bad;
        wgbar();
        const f32x4 t0 = *(LAS const f32x4*)exr, t1 = *(LAS const f32x4*)(exr + 1024);
        const float t2 = *(LAS const float*)((LAS char*)lds + A_LSUM + (w ^ 4) * 256 + lane * 4);
        const u32x4 v0 = *(LAS const u32x4*)((LAS char*)lds + A_VOTE), v1 = *(LAS const u32x4*)((LAS char*)lds + A_VOTE + 16);
        const float rinv = __builtin_amdgcn_rcpf(oa[8] + t2);
        u32x4 ov;
        ov[0] = cvtpk((oa[0] + t0[0]) * rinv, (oa[1] + t0[1]) * rinv);
        ov[1] = cvtpk((oa[2] + t0[2]) * rinv, (oa[3] + t0[3]) * rinv);
        ov[2] = cvtpk((oa[4] + t1[0]) * rinv, (oa[5] + t1[1]) * rinv);
        ov[3] = cvtpk((oa[6] + t1[2]) * rinv, (oa[7] + t1[3]) * rinv);
        const bool redo = att == 0 && __builtin_amdgcn_readfirstlane(v0[0] | v0[1] | v0[2] | v0[3] | v1[0] | v1[1] | v1[2] | v1[3]);
        if (att == 0 ? !redo : att == 1 + kh) {
            st16(ws + WS_O + (size_t)((qt + kh) * 32 + head) * 1024, lane * 16, ov);
            drain();
            st4(ws + WS_FO + ((qt + kh) * 32 + head) * 128, tag);
        }
        asm volatile("" : "+s"(att));
        if (att == 2 || (att == 0 && !redo)) break;
        drain();
        wgbar();
        qa = qb = ws + WS_Q + (size_t)(head * 64 + qt + att) * 1024;
        bf16x8 qf;
        asm volatile("global_load_dwordx4 %0, %1, %2 sc1\n\ts_waitcnt vmcnt(0)" : "=v"(qf) : "v"(lane * 16), "s"(qa) : "memory");
        float mx = -INFINITY;
#pragma clang loop unroll(disable)
        for (int t = 0; t < 64; ++t) {
            bf16x8 kf;
            asm volatile("global_load_dwordx4 %0, %1, %2 sc1\n\ts_waitcnt vmcnt(0)" : "=v"(kf) : "v"(lane * 16), "s"(ksrc + t * 1024) : "memory");
            const f32x16 s = MFMA32(kf, qf, f32x16{});
#pragma unroll
            for (int i = 0; i < 16; ++i) mx = fmaxf(mx, s[i]);
        }
        attn_prep(R, -swap32_max(mx), w);
    }
    ST(3); ST_OUT(0);
}

#define BG1 "s_sleep 1\n\t"
#define BG2 "s_sleep 1\n\t"
#define BG3 "s_sleep 1\n\t"
#define SISSUE                                                             \
    "s_mov_b32 m0, s44\n\t"                                                 \
    "s_add_u32 s45, %[ow], %[wst]\n\t"                                      \
    "buffer_load_dwordx4 %[g0], %[rw], %[ow] offen lds\n\t"                 \
    "buffer_load_dwordx4 %[g1], %[rw], %[ow] offen offset:1024 lds\n\t"     \
    "s_add_u32 m0, s44, 8192\n\t"                                           \
    "s_add_u32 s46, %[od], 4096\n\t"                                        \
    "buffer_load_dwordx4 %[d0], %[rd], %[od] offen sc1 lds\n\t"             \
    "buffer_load_dwordx4 %[d0], %[rd], %[od] offen offset:1024 sc1 lds\n\t" \
    "s_add_u32 s44, s44, 0x4000\n\t"                                        \
    "s_and_b32 s44, s44, 0xffff\n\t"                                        \
    "s_cmp_eq_u32 %[od], %[de]\n\t"                                         \
    "s_cselect_b32 %[ow], 0, s45\n\t"                                       \
    "s_cselect_b32 %[od], 0, s46\n\t"
struct SG { u32x4 rw, rd; unsigned wst, de, ow, od, a, b, c; };
#define WISSUE                                                              \
    "s_and_b32 s45, s44, 0xc000\n\t"                                        \
    "s_add_u32 m0, s45, %[lo2]\n\t"                                         \
    "s_lshl_b32 s45, s45, 3\n\t"                                            \
    "buffer_load_dwordx4 %[o0], %[ro], s45 offen lds\n\t"                   \
    "buffer_load_dwordx4 %[o1], %[ro], s45 offen offset:1024 lds\n\t"       \
    "buffer_load_dwordx4 %[o2], %[ro], s45 offen offset:2048 lds\n\t"       \
    "buffer_load_dwordx4 %[o3], %[ro], s45 offen offset:3072 lds\n\t"       \
    "s_add_u32 s44, s44, 0x4000\n\t"                                        \
    "s_and_b32 s44, s44, 0xffff\n\t"
template <int WO>
DI void sgemm_load(SG& G, int n, int lw, u32x4 ro, unsigned o0) {
    asm volatile(
        "s_mov_b32 s44, %[lo]\n\t"
        "s_sub_u32 s47, %[n], 3\n\t"
        SISSUE SISSUE SISSUE
        "LL_loop%=:\n\t"
        "s_waitcnt vmcnt(8)\n\t"
        "s_barrier\n\t"
        SISSUE
        "s_sub_u32 s47, s47, 1\n\t"
        "s_cmp_lg_u32 s47, 0\n\t"
        "s_cbranch_scc1 LL_loop%=\n\t"
        ".if %c[wo]\n\t"
        "s_waitcnt vmcnt(8)\n\ts_barrier\n\t" WISSUE
        "s_waitcnt vmcnt(8)\n\ts_barrier\n\t" WISSUE
        "s_waitcnt vmcnt(8)\n\ts_barrier\n\t" WISSUE
        "s_barrier\n\t" WISSUE
        ".else\n\t"
        "s_waitcnt vmcnt(8)\n\ts_barrier\n\t"
        "s_waitcnt vmcnt(4)\n\ts_barrier\n\t"
        "s_waitcnt vmcnt(0)\n\ts_barrier\n\t"
        "s_barrier\n\t"
        ".endif\n\t"
        : [ow] "+s"(G.ow), [od] "+s"(G.od)
        : [wst] "s"(G.wst), [rw] "s"(G.rw), [rd] "s"(G.rd), [lo] "s"(lw * 2048), [de] "s"(G.de), [n] "s"(n), [g0] "v"(G.a), [g1] "v"(G.b), [d0] "v"(G.c),
          [wo] "i"(WO), [ro] "s"(ro), [lo2] "s"(lw * 4096), [o0] "v"(o0), [o1] "v"(o0 + 7168), [o2] "v"(o0 + 14336), [o3] "v"(o0 + 21504)
        : "memory", "scc", "s44", "s45", "s46", "s47");
}
#define SRD3(B, SLOT, K, TD)                                                                         \
    "ds_read_b64_tr_b16 v[" #B ":" #B "+1], %[t] offset:" #SLOT "*16384+" #K "*2048\n\t"              \
    "ds_read_b64_tr_b16 v[" #B "+2:" #B "+3], %[t] offset:" #SLOT "*16384+" #K "*2048+" #TD "\n\t"    \
    "ds_read_b128 v[" #B "+4:" #B "+7], %[dl] offset:" #SLOT "*16384+" #K "*1024\n\t"
#define SSTAGE(SLOT, MM, TD, N0, N1, N2, N3, P0, P1, P2, P3)                          \
    "s_waitcnt lgkmcnt(0)\n\t"                                                        \
    "s_barrier\n\t"                                                                   \
    MM(P0) SRD3(N0, SLOT, 0, TD) SRD3(N1, SLOT, 1, TD) BG1                            \
    MM(P1) SRD3(N2, SLOT, 2, TD) SRD3(N3, SLOT, 3, TD) BG2                            \
    MM(P2) BG3 MM(P3)
#define SST_E(SLOT, MM, TD) SSTAGE(SLOT, MM, TD, 32, 40, 48, 56, 96, 104, 112, 120)
#define SST_O(SLOT, MM, TD) SSTAGE(SLOT, MM, TD, 96, 104, 112, 120, 32, 40, 48, 56)
#define SMM_NONE(P) ""
#define SMM_WA(P) MF("v[64:79]", "v[" #P ":" #P "+3]", "v[" #P "+4:" #P "+7]", "v[64:79]")
#define SMM_WB(P) MF("v[80:95]", "v[" #P "+4:" #P "+7]", "v[" #P ":" #P "+3]", "v[80:95]")
#define SGEMM_ASM(ACC, MM, TD, NLOOP, TAIL)                                                                       \
    asm volatile(                                                                                                 \
        "s_mov_b32 s44, " #NLOOP "\n\t"                                                                           \
        "s_barrier\n\t"                                                                                           \
        SRD3(32, 0, 0, TD) SRD3(40, 0, 1, TD) SRD3(48, 0, 2, TD) SRD3(56, 0, 3, TD)                               \
        "LS_loop%=:\n\t"                                                                                          \
        SST_O(1, MM, TD) SST_E(2, MM, TD) SST_O(3, MM, TD) SST_E(0, MM, TD)                                       \
        "s_sub_u32 s44, s44, 1\n\t"                                                                               \
        "s_cmp_lg_u32 s44, 0\n\t"                                                                                 \
        "s_cbranch_scc1 LS_loop%=\n\t"                                                                            \
        TAIL                                                                                                      \
        "s_waitcnt lgkmcnt(0)\n\t"                                                                                \
        MM(96) BG3 MM(104) BG3 MM(112) BG3 MM(120)                                                                \
        "s_nop 15\n\t"                                                                                            \
        : ACC(acc)                                                                                                \
        : [t] "v"(G.a), [dl] "v"(G.b)                                                                             \
        : "memory", "scc", "s44",                                                                                 \
          "v32", "v33", "v34", "v35", "v36", "v37", "v38", "v39", "v40", "v41", "v42", "v43", "v44", "v45", "v46", "v47", \
          "v48", "v49", "v50", "v51", "v52", "v53", "v54", "v55", "v56", "v57", "v58", "v59", "v60", "v61", "v62", "v63", \
          "v96", "v97", "v98", "v99", "v100", "v101", "v102", "v103", "v104", "v105", "v106", "v107", "v108", "v109", "v110", "v111", \
          "v112", "v113", "v114", "v115", "v116", "v117", "v118", "v119", "v120", "v121", "v122", "v123", "v124", "v125", "v126", "v127")

template <int MODE>
DI SG sgemm_setup(const char* wbase, unsigned wbytes, int wrow, const char* dbase, int dblk, int rot, int w) {
    const int lane = lane_id(), row = lane >> 3, lw = w & 3;
    const int q = (lane & 15) >> 2, h2 = lane >> 5;
    SG G;
    G.rw = srd(wbase, wbytes); G.rd = srd(dbase, 2 * dblk);
    G.wst = 64 * wrow; G.de = (MODE == 0 ? 31 : 21) * 4096; G.ow = rot * G.wst; G.od = rot * 4096;
    if (w >= 4) {
        G.a = (16 * lw + row) * wrow + (((lane & 7) ^ (((row >> 1) & 1) << 2)) << 4);
        G.b = G.a + 8 * wrow - 1024;
        G.c = (lw >> 1) * dblk + (lw & 1) * 2048 + lane * 16;
    } else {
        G.a = ((MODE == 0 ? 8 : 4) * h2 + q) * 128 + (((lw & 1) ^ ((q >> 1) & 1)) << 6) + ((lane >> 4) & 1) * 32 + (lane & 3) * 8;
        G.b = 8192 + (lw >> 1) * 4096 + lane * 16;
        G.c = 0;
    }
    asm volatile("" : "+v"(G.a), "+v"(G.b), "+v"(G.c));
    return G;
}
#define ACC0 "+{v[64:79]}"
#define ACC1 "+{v[80:95]}"
template <int MODE>
DI void sgemm_run(f32x16& acc, SG& G) {
    if (MODE == 0) SGEMM_ASM(ACC0, SMM_WA, 512, 7, SST_O(1, SMM_WA, 512) SST_E(2, SMM_WA, 512) SST_O(3, SMM_WA, 512));
    else SGEMM_ASM(ACC1, SMM_WB, 1024, 5, SST_O(1, SMM_WB, 1024));
}
DI f32x4 sub4(const f32x16& a, int g) { return f32x4{a[4 * g], a[4 * g + 1], a[4 * g + 2], a[4 * g + 3]}; }

DI unsigned gelu2(float a, float b) {
    constexpr float c1 = -2.f * 0.7978845608028654f * 1.4426950408889634f, c2 = c1 * 0.044715f;
    const f32x2 u = {a, b};
    const f32x2 z = u * (u * u * c2 + c1);
    const f32x2 d = f32x2{__builtin_amdgcn_exp2f(z[0]), __builtin_amdgcn_exp2f(z[1])} + 1.f;
    const f32x2 o = u * f32x2{__builtin_amdgcn_rcpf(d[0]), __builtin_amdgcn_rcpf(d[1])};
    return cvtpk(o[0], o[1]);
}

#define WRD(R0, R1, R2, R3, STEP)                                                    \
    "ds_read_b64_tr_b16 " R0 ", %[t0] offset:" #STEP "*2048\n\t"                      \
    "ds_read_b64_tr_b16 " R1 ", %[t0] offset:" #STEP "*2048+1024\n\t"                 \
    "ds_read_b64_tr_b16 " R2 ", %[t1] offset:" #STEP "*2048\n\t"                      \
    "ds_read_b64_tr_b16 " R3 ", %[t1] offset:" #STEP "*2048+1024\n\t"
#define WRD0(S) WRD("v[32:33]", "v[34:35]", "v[36:37]", "v[38:39]", S)
#define WRD1(S) WRD("v[40:41]", "v[42:43]", "v[44:45]", "v[46:47]", S)
#define WRD2(S) WRD("v[48:49]", "v[50:51]", "v[52:53]", "v[54:55]", S)
#define WRD3(S) WRD("v[56:57]", "v[58:59]", "v[60:61]", "v[62:63]", S)
#define WMM(WAIT, W0, W1, O) "s_waitcnt " WAIT "\n\t" MF("v[96:111]", W0, O, "v[96:111]") MF("v[112:127]", W1, O, "v[112:127]")
#define OLD(K, R, V, OFF)                                             \
    "5:\n\t"                                                           \
    "s_bitcmp1_b32 s40, " #K "\n\t"                                    \
    "s_cbranch_scc1 6f\n\t"                                            \
    "global_load_dword v24, %[fo], %[f] sc1\n\t"                       \
    "s_sub_u32 s41, s41, 1\n\t"                                        \
    "s_cselect_b32 s42, -1, 0\n\t"                                     \
    "s_waitcnt vmcnt(0)\n\t"                                           \
    "v_cmp_eq_u32 vcc, %[tag], v24\n\t"                                \
    "s_or_b32 s40, vcc_lo, s42\n\t"                                    \
    "s_bitcmp1_b32 s40, " #K "\n\t"                                    \
    "s_cbranch_scc1 6f\n\t"                                            \
    "s_sleep 3\n\t"                                                    \
    "s_branch 5b\n\t"                                                  \
    "6:\n\t"                                                           \
    "global_load_dwordx4 " R ", " V ", %[o] offset:" #OFF " sc1\n\t"
DI void wo_gemm(f32x16& a0, f32x16& a1, f32x4& k0, f32x4& k1, float& k2, const char* osrc, const char* fsrc, unsigned tag, int w) {
    const int lane = lane_id();
    const int q = (lane & 15) >> 2, h2 = lane >> 5;
    const unsigned tb = (w >> 1) * 16384 + (4 * h2 + q) * 128 + ((lane >> 4) & 1) * 32 + (lane & 3) * 8;
    const unsigned t0 = tb + ((q >> 1) & 1) * 64, t1 = tb + (((q >> 1) & 1) ^ 1) * 64;
    const unsigned d0 = lane * 16, d1 = d0 + 4096, fo = (lane & 7) * 128;
    asm volatile(
        WRD0(0) WRD1(1) WRD2(2) WRD3(3)
        "s_mov_b32 s40, 0\n\t"
        "s_mov_b32 s41, 0x100000\n\t"
        "s_setprio 0\n\t"
        OLD(0, "v[64:67]", "%[d0]", 0) OLD(1, "v[68:71]", "%[d0]", 1024) OLD(2, "v[72:75]", "%[d0]", 2048) OLD(3, "v[76:79]", "%[d0]", 3072)
        OLD(4, "v[80:83]", "%[d1]", 0) OLD(5, "v[84:87]", "%[d1]", 1024) OLD(6, "v[88:91]", "%[d1]", 2048) OLD(7, "v[92:95]", "%[d1]", 3072)
        "s_setprio 3\n\t"
        "s_waitcnt vmcnt(7) lgkmcnt(0)\n\t"
        MF("v[96:111]", "v[32:35]", "v[64:67]", "0") MF("v[112:127]", "v[36:39]", "v[64:67]", "0")
        WRD0(4)
        WMM("vmcnt(6)", "v[40:43]", "v[44:47]", "v[68:71]") WRD1(5)
        WMM("vmcnt(5)", "v[48:51]", "v[52:55]", "v[72:75]") WRD2(6)
        WMM("vmcnt(4)", "v[56:59]", "v[60:63]", "v[76:79]") WRD3(7)
        WMM("vmcnt(3) lgkmcnt(12)", "v[32:35]", "v[36:39]", "v[80:83]")
        WMM("vmcnt(2) lgkmcnt(0)", "v[40:43]", "v[44:47]", "v[84:87]")
        WMM("vmcnt(1)", "v[48:51]", "v[52:55]", "v[88:91]")
        "s_barrier\n\t"
        WMM("vmcnt(0)", "v[56:59]", "v[60:63]", "v[92:95]")
        "s_nop 15\n\t"
        : "={v[96:111]}"(a0), "={v[112:127]}"(a1),
          "+{v[12:15]}"(k0), "+{v[16:19]}"(k1), "+{v20}"(k2)
        : [o] "s"(osrc), [f] "s"(fsrc), [tag] "s"(tag), [d0] "v"(d0), [d1] "v"(d1), [t0] "v"(t0), [t1] "v"(t1), [fo] "v"(fo)
        : "memory", "scc", "vcc", "s40", "s41", "s42", "v24",
          "v32", "v33", "v34", "v35", "v36", "v37", "v38", "v39", "v40", "v41", "v42", "v43", "v44", "v45", "v46", "v47",
          "v48", "v49", "v50", "v51", "v52", "v53", "v54", "v55", "v56", "v57", "v58", "v59", "v60", "v61", "v62", "v63",
          "v64", "v65", "v66", "v67", "v68", "v69", "v70", "v71", "v72", "v73", "v74", "v75", "v76", "v77", "v78", "v79",
          "v80", "v81", "v82", "v83", "v84", "v85", "v86", "v87", "v88", "v89", "v90", "v91", "v92", "v93", "v94", "v95");
}

DI void mixer_role(const char* x, const char* wqkv, const char* gw, const char* wo, const char* w1, const char* b1, const char* w2, const char* b2,
                   char* ws, char* out, char* lds, int bi, int w) {
    const int j = bi & 7, i = bi >> 3;
    __builtin_amdgcn_s_setprio(LNP);
    ST_DECL; ST(0);
    const unsigned ep = ld4(ws + WS_EPOCH);
    unsigned tag;
    {
        const int lane = lane_id(), h2 = lane >> 5, tid = 64 * w + lane;
        const u32x4 xr = *(GAS const u32x4*)(x + (size_t)(8 * bi + w) * 1024 + lane * 16);
        const u32x4 gr = *(GAS const u32x4*)(gw + lane * 16);
        tag = __builtin_amdgcn_readfirstlane(ep) * 0x9E3779B1u + 0x7F4A7C15u;
        float v[8], gg[8];
#pragma unroll
        for (int k = 0; k < 4; ++k) { v[2 * k] = bflo(xr[k]); v[2 * k + 1] = bfhi(xr[k]); gg[2 * k] = bflo(gr[k]); gg[2 * k + 1] = bfhi(gr[k]); }
        float s1 = 0.f, s3 = 0.f;
#pragma unroll
        for (int k = 0; k < 8; ++k) { s1 += v[k]; s3 += v[k] * gg[k]; }
        const float mu = wave_sum(s1) * (1.f / 512.f);
        s3 = wave_sum(s3);
        float s2 = 0.f;
#pragma unroll
        for (int k = 0; k < 8; ++k) { v[k] -= mu; s2 += v[k] * v[k]; }
        const float rs = __builtin_amdgcn_rsqf(wave_sum(s2) * (1.f / 512.f) + 1e-5f);
        u32x4 hv;
#pragma unroll
        for (int k = 0; k < 4; ++k) hv[k] = cvtpk(v[2 * k] * rs, v[2 * k + 1] * rs);
        *(LAS u32x4*)((LAS char*)lds + w * 1024 + lane * 16) = hv;
        const float g = __builtin_amdgcn_rcpf(1.f + __builtin_amdgcn_exp2f(-1.4426950408889634f * s3));
        st4(ws + WS_G + (8 * bi + w) * 4, uni(g));
        wgbar();
        LAS const unsigned short* lb = (LAS const unsigned short*)lds + tid;
#pragma unroll
        for (int k = 0; k < 4; ++k) hv[k] = (unsigned)lb[(2 * k) * 512] | ((unsigned)lb[(2 * k + 1) * 512] << 16);
        st16(ws + WS_H + (size_t)(2 * w * 128 + (bi >> 1)) * 1024 + (bi & 1) * 512, h2 * 131072 + (lane & 31) * 16, hv);
        drain();
        wgbar();
        if (w == 0) st4(ws + WS_FL + bi * 128, tag);
    }
    __builtin_amdgcn_s_setprio(3);
    ST(6);
    const int wb = w & 1, db = (w >> 1) & 1, hf = w >> 2;
    const int L = (j >> 1) * 22 + i, ft = L >> 2, dt1 = 4 * (j & 1) + (L & 3);
    const int st = 8 * (j >> 1) + (i & 7), dt = 4 * (j & 1) + (i >> 3);
    f32x16 acc1, acc2;
    SG G1, G2;
    unsigned ex, l16, og, wof;
    {
        const int lane = lane_id();
        const bool live = i < 22 && (ft < 21 || wb == 0);
        const int f0 = 64 * ft + 32 * wb + 4 * (lane >> 5);
        u32x2 bv[4] = {};
        if (live) {
#pragma unroll
            for (int k = 0; k < 4; ++k) bv[k] = *(GAS const u32x2*)(b1 + f0 * 2 + 16 * k);
        }
        og = (64 * st + 32 * wb + (lane & 31)) * 4;
        const float bb = bflo(*(GAS const unsigned short*)(b2 + (og >> 1)));
        G1 = sgemm_setup<0>(w1 + ft * 128, 2048 * 2752 - ft * 128, 2752, ws + WS_H + (size_t)2 * dt1 * 131072, 131072, ROT1, w);
        G2 = sgemm_setup<1>(w2 + st * 128, 1376 * 4096 - st * 128, 4096, ws + WS_U + (size_t)2 * dt * 86 * 1024, 86 * 1024, ROT2, w);
        l16 = lane * 16;
        ex = 65536 + (w & 3) * 2048 + l16;
        wof = (32 * (w & 3) + (lane >> 3)) * 1024 + (((lane & 7) ^ (((lane >> 4) & 1) << 2)) << 4);
#pragma unroll
        for (int k = 0; k < 4; ++k) {
            acc1[4 * k] = bflo(bv[k][0]); acc1[4 * k + 1] = bfhi(bv[k][0]); acc1[4 * k + 2] = bflo(bv[k][1]); acc1[4 * k + 3] = bfhi(bv[k][1]);
            acc2[4 * k] = bb; acc2[4 * k + 1] = bb; acc2[4 * k + 2] = bb; acc2[4 * k + 3] = bb;
        }
        asm volatile("" : "+{v[64:79]}"(acc1), "+{v[80:95]}"(acc2), "+v"(ex), "+v"(l16), "+v"(og), "+v"(wof));
    }
    if (i < 22) {
        if (w >= 4) { __builtin_amdgcn_s_sleep(LSLEEP); wait_flags<8>(ws + WS_FL + (w - 4) * 8192, l16 * 8, tag); }
        if (w == 0) {
            __builtin_amdgcn_s_sleep(LSLEEP);
            wait_qkv(ws, bi, tag);
            __builtin_amdgcn_s_sleep(GATEQ);
        }
        wgbar();
        ST(7);
        f32x4 v0, v1;
        if (w >= 4) {
            sgemm_load<0>(G1, 32, w - 4, G1.rw, 0);
            v0 = *(LAS const f32x4*)(uintptr_t)ex; v1 = *(LAS const f32x4*)(uintptr_t)(ex + 1024);
        } else {
            sgemm_run<0>(acc1, G1);
            *(LAS f32x4*)(uintptr_t)ex = sub4(acc1, 2); *(LAS f32x4*)(uintptr_t)(ex + 1024) = sub4(acc1, 3);
            wgbar();
            v0 = sub4(acc1, 0); v1 = sub4(acc1, 1);
        }
        ST(1);
        const u32x4 o = {gelu2(v0[0], v0[1]), gelu2(v0[2], v0[3]), gelu2(v1[0], v1[1]), gelu2(v1[2], v1[3])};
        if (ft < 21 || wb == 0) st16(ws + WS_U + (size_t)((2 * dt1 + db) * 86 + 4 * ft + 2 * wb + hf) * 1024, l16, o);
        drain();
        wgbar();
        if (w == 0) st4(ws + WS_FU + (dt1 * 32 + ft) * 128, tag);
    }
    ST(2);
    if (bi == 255 && w == 1) {
#pragma clang loop unroll(disable)
        for (int k = 0; k < 4; ++k) {
            const int lane = lane_id();
            wait_flags<8>(ws + WS_FQ + k * 8192, lane * 128, tag);
            wait_flags<8>(ws + WS_FL + k * 8192, lane * 128, tag);
        }
        st4(ws + WS_EPOCH, tag);
    }
    if (w == 0) wait_flags<8>(ws + WS_FU + dt * 4096, (l16 < 22 * 16 ? l16 : 0) * 8, tag);
    wgbar();
    ST(3);
    f32x4 bp0, bp1;
    float g;
    {
        asm volatile("global_load_dword %0, %1, %2 sc1" : "=v"(g) : "v"(og), "s"(ws + WS_G) : "memory");
        if (w >= 4) {
            sgemm_load<1>(G2, 22, w - 4, srd(wo + dt * 128, 524288 - dt * 128), wof);
            bp0 = *(LAS const f32x4*)(uintptr_t)ex; bp1 = *(LAS const f32x4*)(uintptr_t)(ex + 1024);
        } else {
            sgemm_run<1>(acc2, G2);
            *(LAS f32x4*)(uintptr_t)ex = sub4(acc2, 2); *(LAS f32x4*)(uintptr_t)(ex + 1024) = sub4(acc2, 3);
            wgbar();
            bp0 = sub4(acc2, 0); bp1 = sub4(acc2, 1);
            drain();
        }
        asm volatile("" : "+v"(g));
        ST(4);
        bp0 *= 1.f - g;
        bp1 *= 1.f - g;
        asm volatile("" : "+v"(bp0), "+v"(bp1));
    }
    drain();
    wgbar();
    ST(5);
    f32x16 a0, a1;
    wo_gemm(a0, a1, bp0, bp1, g, ws + WS_O + (size_t)((2 * st + wb) * 32 + 8 * (w >> 1)) * 1024,
            ws + WS_FO + ((2 * st + wb) * 32 + 8 * (w >> 1)) * 128, tag, w);
    {
        const int lane = lane_id(), kq = 2 * db + hf;
        LAS char* ex = (LAS char*)lds + w * 8192 + lane * 16;
#pragma unroll
        for (int k = 0; k < 4; ++k) { *(LAS f32x4*)(ex + k * 1024) = sub4(a0, k); *(LAS f32x4*)(ex + 4096 + k * 1024) = sub4(a1, k); }
        wgbar();
        LAS const char* exr = (LAS const char*)lds + wb * 8192 + kq * 2048 + lane * 16;
        f32x4 s0 = *(LAS const f32x4*)exr, s1 = *(LAS const f32x4*)(exr + 1024);
#pragma unroll
        for (int k = 1; k < 4; ++k) { s0 += *(LAS const f32x4*)(exr + k * 16384); s1 += *(LAS const f32x4*)(exr + k * 16384 + 1024); }
        s0 = s0 * g + bp0;
        s1 = s1 * g + bp1;
        const auto r0 = __builtin_amdgcn_permlane32_swap(cvtpk(s0[0], s0[1]), cvtpk(s1[0], s1[1]), false, false);
        const auto r1 = __builtin_amdgcn_permlane32_swap(cvtpk(s0[2], s0[3]), cvtpk(s1[2], s1[3]), false, false);
        *(GAS u32x4*)(out + (size_t)(64 * st + 32 * wb) * 1024 + (64 * dt + 16 * kq) * 2 + (lane & 31) * 1024 + (lane >> 5) * 16) = u32x4{r0[0], r1[0], r0[1], r1[1]};
    }
    ST_OUT(0);
}

constexpr int MK_LDS = 81920;
__global__ __launch_bounds__(512, 4) void mk(const char* x, const char* wqkv, const char* wo, const char* w1, const char* b1,
                                             const char* w2, const char* b2, const char* gw, char* ws, char* out) {
    extern __shared__ __attribute__((aligned(16))) char lds[];
    const int w = __builtin_amdgcn_readfirstlane(threadIdx.x >> 6);
    if (blockIdx.x < 256) attn_role(x, wqkv, ws, lds, blockIdx.x, w);
    else mixer_role(x, wqkv, gw, wo, w1, b1, w2, b2, ws, out, lds, blockIdx.x - 256, w);
}

extern "C" void kernel_launch(void* const* d_in, const int* in_sizes, int n_in, void* d_out, int out_size, void* d_ws, size_t ws_size,
                              hipStream_t stream) {
    (void)hipFuncSetAttribute((const void*)mk, hipFuncAttributeMaxDynamicSharedMemorySize, MK_LDS);
    mk<<<512, 512, MK_LDS, stream>>>((const char*)d_in[0], (const char*)d_in[1], (const char*)d_in[2], (const char*)d_in[4],
                                     (const char*)d_in[5], (const char*)d_in[6], (const char*)d_in[7], (const char*)d_in[8],
                                     (char*)d_ws, (char*)d_out);
}
```

```cpp
#include <hip/hip_runtime.h>
#include <hip/hip_bf16.h>
#include <stdint.h>

typedef __attribute__((ext_vector_type(8))) short bf16x8;
typedef __attribute__((ext_vector_type(4))) short s16x4;
typedef __attribute__((ext_vector_type(16))) float f32x16;
typedef __attribute__((ext_vector_type(4))) float f32x4;
typedef __attribute__((ext_vector_type(2))) float f32x2;
typedef __attribute__((ext_vector_type(4))) unsigned u32x4;
typedef __attribute__((ext_vector_type(2))) unsigned u32x2;
#define LAS __attribute__((address_space(3)))
#define GAS __attribute__((address_space(1)))
#define DI __device__ __forceinline__
#define MFMA32(a, b, c) __builtin_amdgcn_mfma_f32_32x32x16_bf16((a), (b), (c), 0, 0, 0)

#define LSLEEP 32
#define ROT1 (((dt1 + ft) & 1) * 16)
#define ROT2 (((st + dt) & 1) * 11)
#define GATEQ 1
#define BSLEEP 0
#define LNP 0
#define BGATE 1
#define FAEARLY 1
#define PFA 1
#define PFD 2
#define QROT 4
#define QROTM 4
constexpr int S_ = 2048, D_ = 512, F_ = 1376;
constexpr size_t WS_EPOCH = 0;
constexpr size_t WS_G = 4096;
constexpr size_t WS_FL = 98304;
constexpr size_t WS_FQ = 458752;
constexpr size_t WS_FU = 65536;
constexpr size_t WS_FA = 393216;
constexpr size_t WS_FO = 131072;
constexpr size_t WS_Q = 1u << 20;
constexpr size_t WS_K = WS_Q + (2u << 20);
constexpr size_t WS_V = WS_K + (2u << 20);
constexpr size_t WS_H = WS_V + (2u << 20);
constexpr size_t WS_U = WS_H + (2u << 20);
constexpr size_t WS_O = WS_U + (2u << 20);

#define ST_DECL
#define ST_ARG
#define ST_PASS
#define ST(i)
#define ST_OUT(k)
DI unsigned cvtpk(float lo, float hi) {
    typedef float f32x2_t __attribute__((ext_vector_type(2)));
    typedef __bf16 bf16x2_t __attribute__((ext_vector_type(2)));
    return __builtin_bit_cast(unsigned, __builtin_convertvector(f32x2_t{lo, hi}, bf16x2_t));
}
DI float bflo(unsigned u) { return __uint_as_float(u << 16); }
DI float bfhi(unsigned u) { return __uint_as_float(u & 0xffff0000u); }
DI s16x4 trread(LAS const char* p) {
    typedef short v4i16_t __attribute__((ext_vector_type(4)));
    return __builtin_bit_cast(s16x4, __builtin_amdgcn_ds_read_tr16_b64_v4i16((LAS v4i16_t*)p));
}
DI void glds(unsigned lds, unsigned voff, const void* sbase) {
    asm volatile("s_mov_b32 m0, %0\n\ts_nop 0\n\tglobal_load_lds_dwordx4 %1, %2" ::"s"(lds), "v"(voff), "s"(sbase) : "memory");
}
template <int CTRL> DI float dpp(float v) {
    return __int_as_float(__builtin_amdgcn_update_dpp(0, __float_as_int(v), CTRL, 0xf, 0xf, true));
}
DI float rl(float v, int l) { return __int_as_float(__builtin_amdgcn_readlane(__float_as_int(v), l)); }
DI float wave_sum(float v) {
    v += dpp<0xB1>(v); v += dpp<0x4E>(v); v += dpp<0x141>(v); v += dpp<0x140>(v);
    return (rl(v, 0) + rl(v, 16)) + (rl(v, 32) + rl(v, 48));
}
DI float wave_max(float v) {
    v = fmaxf(v, dpp<0xB1>(v)); v = fmaxf(v, dpp<0x4E>(v)); v = fmaxf(v, dpp<0x141>(v)); v = fmaxf(v, dpp<0x140>(v));
    return fmaxf(fmaxf(rl(v, 0), rl(v, 16)), fmaxf(rl(v, 32), rl(v, 48)));
}
DI float swap32_add(float v) {
    auto rr = __builtin_amdgcn_permlane32_swap(__float_as_uint(v), __float_as_uint(v), false, false);
    return __uint_as_float(rr[0]) + __uint_as_float(rr[1]);
}
DI float swap32_max(float v) {
    auto rr = __builtin_amdgcn_permlane32_swap(__float_as_uint(v), __float_as_uint(v), false, false);
    return fmaxf(__uint_as_float(rr[0]), __uint_as_float(rr[1]));
}

DI int lane_id() {
    int l;
    asm volatile("v_mbcnt_lo_u32_b32 %0, -1, 0\n\tv_mbcnt_hi_u32_b32 %0, -1, %0" : "=v"(l));
    __builtin_assume(l >= 0 && l < 64);
    return l;
}
DI void st16(const char* sbase, unsigned voff, u32x4 v) {
    asm volatile("global_store_dwordx4 %0, %1, %2 sc1\n\ts_nop 1" ::"v"(voff), "v"(v), "s"(sbase) : "memory");
}
DI void st4(const char* p, unsigned v) {
    unsigned t0, t1;
    asm volatile("s_nop 1\n\tv_mov_b32 %0, %2\n\tv_mov_b32 %1, 0\n\ts_mov_b64 exec, 1\n\tglobal_store_dword %1, %0, %3 sc1\n\ts_mov_b64 exec, -1"
                 : "=&v"(t0), "=&v"(t1) : "s"(v), "s"(p) : "memory");
}
DI unsigned uni(float v) { return __builtin_amdgcn_readfirstlane(__float_as_uint(v)); }
DI unsigned ld4(const char* p) { return __hip_atomic_load((GAS const unsigned*)p, __ATOMIC_RELAXED, __HIP_MEMORY_SCOPE_AGENT); }
DI void warm(const char* base, int nchunks, int first, unsigned l64) {
#pragma clang loop unroll(disable)
    for (int n = first; n < nchunks; n += 640) {
        const char* p = base + (size_t)((n & 1) * (nchunks >> 1) + (n >> 1)) * 4096;
        asm volatile("s_mov_b32 m0, 0x13e00\n\ts_nop 0\n\tglobal_load_lds_dword %0, %1" ::"v"(l64), "s"(p) : "memory");
    }
}
DI void drain() { asm volatile("s_waitcnt vmcnt(0)" ::: "memory"); }
DI void wgbar() { asm volatile("s_waitcnt lgkmcnt(0)\n\ts_barrier" ::: "memory"); }
template <int SLEEP>
DI void wait_flags(const char* sbase, unsigned voff, unsigned tag) {
#pragma clang loop unroll(disable)
    for (unsigned spin = 0; spin < (1u << 20); ++spin) {
        unsigned f;
        asm volatile("global_load_dword %0, %1, %2 sc1\n\ts_waitcnt vmcnt(0)" : "=v"(f) : "v"(voff), "s"(sbase) : "memory");
        if (__all(f == tag)) break;
        __builtin_amdgcn_s_sleep(SLEEP);
    }
}

DI void wait_flags256(const char* sbase, unsigned voff, unsigned tag) {
#pragma clang loop unroll(disable)
    for (unsigned spin = 0; spin < (1u << 20); ++spin) {
        unsigned f0, f1, f2, f3;
        asm volatile("global_load_dword %0, %4, %5 sc1\n\tglobal_load_dword %1, %4, %6 sc1\n\tglobal_load_dword %2, %4, %7 sc1\n\t"
                     "global_load_dword %3, %4, %8 sc1\n\ts_waitcnt vmcnt(0)"
                     : "=&v"(f0), "=&v"(f1), "=&v"(f2), "=&v"(f3)
                     : "v"(voff), "s"(sbase), "s"(sbase + 8192), "s"(sbase + 16384), "s"(sbase + 24576) : "memory");
        if (__all(f0 == tag && f1 == tag && f2 == tag && f3 == tag)) break;
        __builtin_amdgcn_s_sleep(8);
    }
}

constexpr int QCNT = 73728;
#define MF(D, A, B, C) "v_mfma_f32_32x32x16_bf16 " D ", " A ", " B ", " C "\n\t"
DI u32x4 srd(const char* base, unsigned bytes) {
    return u32x4{(unsigned)(uintptr_t)base, (unsigned)((uintptr_t)base >> 32) & 0xffffu, bytes, 0x00020000u};
}
DI void qkv_store(char* ws, const f32x16& acc, int type, int head, int tile, int lane) {
    const float sc = (type == 0) ? 0.25f * 1.4426950408889634f : 1.f;
    const char* dst = ws + WS_Q + (size_t)type * (2u << 20) + (size_t)(head * 64 + tile) * 1024;
    const unsigned vo = type < 2 ? lane * 16 : ((lane >> 4) & 1) * 65536 + (lane >> 5) * 256 + (lane & 15) * 16;
#pragma unroll
    for (int hf = 0; hf < 2; ++hf) {
        u32x4 o;
#pragma unroll
        for (int i = 0; i < 4; ++i) o[i] = cvtpk(acc[8 * hf + 2 * i] * sc, acc[8 * hf + 2 * i + 1] * sc);
        st16(dst + hf * (type < 2 ? 65536 : 512), vo, o);
    }
}
DI void publish4(const char* flag, unsigned tag, int lane) {
    drain();
    unsigned arr = 0;
    if (lane == 0) arr = __hip_atomic_fetch_add((LAS unsigned*)QCNT, 1u, __ATOMIC_RELAXED, __HIP_MEMORY_SCOPE_WORKGROUP);
    if (__builtin_amdgcn_readfirstlane(arr) == 3) st4(flag, tag);
}
DI void qkv_loader(const char* x, const char* wqkv, int row, int col, int lw, const char* hint) {
    const int lane = lane_id(), xr = 16 * lw + (lane >> 3), wr = 16 * lw + (lane >> 4);
    const unsigned x0 = xr * 1024 + (((lane & 7) ^ ((xr >> 1) & 7)) << 4), x1 = (xr + 8) * 1024 + (((lane & 7) ^ (((xr + 8) >> 1) & 7)) << 4) - 1024;
    const unsigned w0 = wr * 3072 + (((lane & 15) ^ ((wr & 3) << 2)) << 4);
    const u32x4 rx = srd(x + (size_t)row * 65536, 65536), rw = srd(wqkv + col * 256, 512 * 3072 - col * 256);
    const int rot = (row + col) * QROTM & QROT;
    auto issue = [&](int s, int slot) {
        const int kc = (s + rot) & 7;
        asm volatile("s_mov_b32 m0, %[m]\n\ts_nop 0\n\t"
                     "buffer_load_dwordx4 %[x0], %[rx], %[ox] offen lds\n\t"
                     "buffer_load_dwordx4 %[x1], %[rx], %[ox] offen offset:1024 lds\n\t"
                     "s_mov_b32 m0, %[m2]\n\ts_nop 0\n\t"
                     "buffer_load_dwordx4 %[w0], %[rw], %[ow] offen lds\n\t"
                     "buffer_load_dwordx4 %[w1], %[rw], %[ow] offen offset:1024 lds\n\t"
                     "buffer_load_dwordx4 %[w2], %[rw], %[ow] offen offset:2048 lds\n\t"
                     "buffer_load_dwordx4 %[w3], %[rw], %[ow] offen offset:3072 lds"
                     :: [m] "s"(slot * 24576 + lw * 2048), [m2] "s"(slot * 24576 + 8192 + lw * 4096), [ow] "s"(kc * 196608), [ox] "s"(kc * 128),
                        [rw] "s"(rw), [rx] "s"(rx), [x0] "v"(x0), [x1] "v"(x1),
                        [w0] "v"(w0), [w1] "v"(w0 + 11264), [w2] "v"(w0 + 22528), [w3] "v"(w0 + 33792)
                     : "memory");
    };
    issue(0, 0);
    __builtin_amdgcn_s_setprio(2);
    issue(1, 1);
    __builtin_amdgcn_s_setprio(1);
    issue(2, 2);
    __builtin_amdgcn_s_setprio(3);
    asm volatile("s_waitcnt vmcnt(12)\n\ts_barrier" ::: "memory");
    int slot = 0;
#pragma clang loop unroll(disable)
    for (int s = 3; s < 8; ++s) {
        asm volatile("s_waitcnt vmcnt(6)\n\ts_barrier" ::: "memory");
        issue(s, slot);
        slot = slot == 2 ? 0 : slot + 1;
    }
    if (lw == 1) {
        st4(hint, __builtin_amdgcn_readfirstlane(*(LAS const unsigned*)(QCNT + 4)));
        asm volatile("s_waitcnt vmcnt(7)\n\ts_barrier\n\ts_waitcnt vmcnt(1)\n\ts_barrier" ::: "memory");
    } else
    asm volatile("s_waitcnt vmcnt(6)\n\ts_barrier\n\ts_waitcnt vmcnt(0)\n\ts_barrier" ::: "memory");
}
template <bool T>
DI void qkv_body(char* ws, char* lds, int row, int col, int a, int w, unsigned tag ST_ARG) {
    const int lane = lane_id(), r = lane & 31, h2 = lane >> 5, q = (lane & 15) >> 2;
    f32x16 acc[2] = {};
    const int xa0 = (32 * (w & 1) + r) * 128 + ((h2 ^ ((r >> 1) & 7)) << 4);
    const int wa0 = 8192 + (8 * h2 + q) * 256 + (((2 * (w >> 1)) ^ q) << 6) + ((lane >> 4) & 1) * 32 + (lane & 3) * 8;
    bf16x8 xf[4], wf[2][2];
    auto rdw = [&](LAS const char* sl, int st, int i) {
        LAS const char* pw = sl + (wa0 ^ (i << 6)) + st * 4096;
        const s16x4 lo = trread(pw), hi = trread(pw + 1024);
        wf[st & 1][i] = __builtin_shufflevector(lo, hi, 0, 1, 2, 3, 4, 5, 6, 7);
    };
    auto mm1 = [&](int st, int i) {
        acc[i] = T ? MFMA32(wf[st & 1][i], xf[st], acc[i]) : MFMA32(xf[st], wf[st & 1][i], acc[i]);
        __builtin_amdgcn_sched_barrier(0);
    };
    auto step = [&](int st, LAS const char* sl, bool bar) {
        const int sn = (st + 2) & 3;
        mm1(st, 0);
        if (bar) wgbar();
        xf[sn] = *(LAS const bf16x8*)(sl + (xa0 ^ (sn << 5))); rdw(sl, sn, 0); __builtin_amdgcn_sched_barrier(0);
        mm1(st, 1);
        rdw(sl, sn, 1); __builtin_amdgcn_sched_barrier(0);
    };
    if (w == 0) *(LAS u32x2*)QCNT = u32x2{0u, tag};
    wgbar();
    LAS const char* sl = (LAS const char*)lds;
#pragma unroll
    for (int st = 0; st < 2; ++st) { xf[st] = *(LAS const bf16x8*)(sl + (xa0 ^ (st << 5))); rdw(sl, st, 0); rdw(sl, st, 1); }
    __builtin_amdgcn_sched_barrier(0);
#pragma clang loop unroll(disable)
    for (int s = 0; s < 7; ++s) {
        step(0, sl, false); step(1, sl, false);
        sl = (sl == (LAS const char*)lds + 49152) ? (LAS const char*)lds : sl + 24576;
        step(2, sl, true); step(3, sl, false);
    }
    step(0, sl, false); step(1, sl, false);
    mm1(2, 0); mm1(2, 1); mm1(3, 0); mm1(3, 1);
    ST(4);
#pragma unroll
    for (int i = 0; i < 2; ++i) qkv_store(ws, acc[i], col >> 2, 8 * (col & 3) + 4 * (w >> 1) + 2 * i, 2 * row + (w & 1), lane);
    publish4(ws + WS_FQ + a * 128, tag, lane);
    ST(7);
}
DI void qkv2_loader(const char* x, const char* wqkv, int rowt, int col, int lw) {
    const int lane = lane_id(), row = 16 * lw + (lane >> 3);
    const unsigned w0 = row * 3072 + (((lane & 7) ^ (((row >> 1) & 1) << 2)) << 4), w1 = w0 + 8 * 3072 - 1024;
    const unsigned x0 = row * 1024 + (((lane & 7) ^ ((row >> 1) & 7)) << 4), x1 = (row + 8) * 1024 + (((lane & 7) ^ (((row + 8) >> 1) & 7)) << 4) - 1024;
    const u32x4 rx = srd(x + (size_t)rowt * 65536, 65536), rw = srd(wqkv + col * 128, 512 * 3072 - col * 128);
    auto issue = [&](int s) {
        asm volatile("s_mov_b32 m0, %[m]\n\ts_nop 0\n\t"
                     "buffer_load_dwordx4 %[w0], %[rw], %[ow] offen lds\n\t"
                     "buffer_load_dwordx4 %[w1], %[rw], %[ow] offen offset:1024 lds\n\t"
                     "s_add_u32 m0, %[m], 8192\n\ts_nop 0\n\t"
                     "buffer_load_dwordx4 %[x0], %[rx], %[ox] offen lds\n\t"
                     "buffer_load_dwordx4 %[x1], %[rx], %[ox] offen offset:1024 lds"
                     :: [m] "s"((s & 3) * 16384 + lw * 2048), [ow] "s"(s * 196608), [ox] "s"(s * 128), [rw] "s"(rw), [rx] "s"(rx),
                        [w0] "v"(w0), [w1] "v"(w1), [x0] "v"(x0), [x1] "v"(x1)
                     : "memory", "scc");
    };
    issue(0); issue(1); issue(2);
#pragma clang loop unroll(disable)
    for (int s = 3; s < 8; ++s) {
        asm volatile("s_waitcnt vmcnt(8)\n\ts_barrier" ::: "memory");
        issue(s);
    }
    asm volatile("s_waitcnt vmcnt(8)\n\ts_barrier\n\ts_waitcnt vmcnt(4)\n\ts_barrier\n\ts_waitcnt vmcnt(0)\n\ts_barrier" ::: "memory");
}
#define QRD3(B, SLOT, K)                                                                             \
    "ds_read_b64_tr_b16 v[" #B ":" #B "+1], %[t] offset:" #SLOT "*16384+" #K "*2048\n\t"              \
    "ds_read_b64_tr_b16 v[" #B "+2:" #B "+3], %[t] offset:" #SLOT "*16384+" #K "*2048+512\n\t"        \
    "ds_read_b128 v[" #B "+4:" #B "+7], %[x" #K "] offset:" #SLOT "*16384\n\t"
#define QSTAGE(SLOT, MM, N0, N1, N2, N3, P0, P1, P2, P3)                              \
    "s_waitcnt lgkmcnt(0)\n\t"                                                        \
    "s_barrier\n\t"                                                                   \
    MM(P0) QRD3(N0, SLOT, 0) MM(P1) QRD3(N1, SLOT, 1) MM(P2) QRD3(N2, SLOT, 2) MM(P3) QRD3(N3, SLOT, 3)
#define QST_E(SLOT, MM) QSTAGE(SLOT, MM, 32, 40, 48, 56, 96, 104, 112, 120)
#define QST_O(SLOT, MM) QSTAGE(SLOT, MM, 96, 104, 112, 120, 32, 40, 48, 56)
#define QMT(P) MF("v[16:31]", "v[" #P ":" #P "+3]", "v[" #P "+4:" #P "+7]", "v[16:31]")
#define QMV(P) MF("v[16:31]", "v[" #P "+4:" #P "+7]", "v[" #P ":" #P "+3]", "v[16:31]")
#define QTILE_ASM(MM)                                                                                             \
    asm volatile(                                                                                                 \
        "s_barrier\n\t"                                                                                           \
        QRD3(32, 0, 0) QRD3(40, 0, 1) QRD3(48, 0, 2) QRD3(56, 0, 3)                                               \
        QST_O(1, MM) QST_E(2, MM)                                                                                 \
        "s_mov_b32 s44, 0\n\t"                                                                                    \
        "LQ_loop%=:\n\t"                                                                                          \
        QST_O(3, MM)                                                                                              \
        "s_cmp_lg_u32 s44, 0\n\t"                                                                                 \
        "s_cbranch_scc1 LQ_end%=\n\t"                                                                             \
        QST_E(0, MM) QST_O(1, MM) QST_E(2, MM)                                                                    \
        "s_mov_b32 s44, 1\n\t"                                                                                    \
        "s_branch LQ_loop%=\n\t"                                                                                  \
        "LQ_end%=:\n\t"                                                                                           \
        "s_waitcnt lgkmcnt(0)\n\t"                                                                                \
        MM(96) MM(104) MM(112) MM(120)                                                                            \
        "s_nop 15\n\t"                                                                                            \
        : "+{v[16:31]}"(acc)                                                                                      \
        : [t] "v"(t), [x0] "v"(xa), [x1] "v"(xa ^ 32), [x2] "v"(xa ^ 64), [x3] "v"(xa ^ 96)                       \
        : "memory", "scc", "s44",                                                                                 \
          "v32", "v33", "v34", "v35", "v36", "v37", "v38", "v39", "v40", "v41", "v42", "v43", "v44", "v45", "v46", "v47", \
          "v48", "v49", "v50", "v51", "v52", "v53", "v54", "v55", "v56", "v57", "v58", "v59", "v60", "v61", "v62", "v63", \
          "v96", "v97", "v98", "v99", "v100", "v101", "v102", "v103", "v104", "v105", "v106", "v107", "v108", "v109", "v110", "v111", \
          "v112", "v113", "v114", "v115", "v116", "v117", "v118", "v119", "v120", "v121", "v122", "v123", "v124", "v125", "v126", "v127")
DI void qkv2_body(char* ws, int row, int col, int bi, int w, unsigned tag) {
    const int lane = lane_id(), r = lane & 31, h2 = lane >> 5, q = (lane & 15) >> 2;
    const unsigned t = (8 * h2 + q) * 128 + (((w & 1) ^ ((q >> 1) & 1)) << 6) + ((lane >> 4) & 1) * 32 + (lane & 3) * 8;
    const unsigned xa = 8192 + (32 * (w >> 1) + r) * 128 + ((h2 ^ ((r >> 1) & 7)) << 4);
    f32x16 acc = {};
    if (col < 16) QTILE_ASM(QMT);
    else QTILE_ASM(QMV);
    qkv_store(ws, acc, col >> 3, 4 * (col & 7) + 2 * (w & 1), 2 * row + (w >> 1), lane);
    publish4(ws + WS_FQ + 32768 + bi * 128, tag, lane);
}

#define EXP(a) "v_exp_f32 v" #a ", v" #a "\n\t"
#define CVT(d, a, b) "v_cvt_pk_bf16_f32 v" #d ", v" #a ", v" #b "\n\t"
#define ASTEP(c0, c1, c2, c3, c4, c5, c6, c7, c8, c9, c10, c11, c12, c13, c14, c15, M1, M2, CL, M3)                  \
    EXP(c0) EXP(c1) M1 EXP(c2) EXP(c3) EXP(c4) EXP(c5) M2 CL                                                          \
    EXP(c6) EXP(c7) CVT(96, c0, c1) CVT(97, c2, c3) CVT(98, c4, c5) CVT(99, c6, c7) EXP(c8) EXP(c9) M3                \
    EXP(c10) EXP(c11) EXP(c12) EXP(c13) EXP(c14) EXP(c15)                                                             \
    CVT(100, c8, c9) CVT(101, c10, c11) CVT(102, c12, c13) CVT(103, c14, c15)
#define R_OA "v[64:79]"
#define R_OB "v[80:95]"
#define R_SA "v[32:47]"
#define R_SB "v[48:63]"
#define R_NM "v[16:31]"
#define ATILE(KC, KN, V0C, V1C, V0N, V1N, KOFF, VOFF0, VOFF1, PRE)                                                    \
    ASTEP(32, 33, 34, 35, 36, 37, 38, 39, 40, 41, 42, 43, 44, 45, 46, 47,                                             \
          MF(R_OB, V1N, "v[100:103]", R_OB), MF(R_SB, KC, "v[8:11]", R_NM),                                           \
          "s_waitcnt lgkmcnt(0)\n\t" PRE                                                                              \
          "ds_read_b128 " KC ", v12 offset:" #KOFF "\n\t"                                                             \
          "ds_read_b128 " V0N ", v13 offset:" #VOFF0 "\n\t"                                                           \
          "ds_read_b128 " V1N ", v13 offset:" #VOFF1 "\n\t",                                                          \
          MF(R_OA, V0C, "v[96:99]", R_OA))                                                                            \
    ASTEP(48, 49, 50, 51, 52, 53, 54, 55, 56, 57, 58, 59, 60, 61, 62, 63,                                             \
          MF(R_OA, V1C, "v[100:103]", R_OA), MF(R_SA, KN, "v[4:7]", R_NM), "", MF(R_OB, V0C, "v[96:99]", R_OB))
#define ATILE_E(KOFF, VOFF0, VOFF1, PRE) ATILE("v[104:107]", "v[108:111]", "v[112:115]", "v[116:119]", "v[120:123]", "v[124:127]", KOFF, VOFF0, VOFF1, PRE)
#define ATILE_O(KOFF, VOFF0, VOFF1, PRE) ATILE("v[108:111]", "v[104:107]", "v[120:123]", "v[124:127]", "v[112:115]", "v[116:119]", KOFF, VOFF0, VOFF1, PRE)
#define MOV4(a, b, c, d, s) "v_mov_b32 v" #a ", " s "\n\tv_mov_b32 v" #b ", " s "\n\tv_mov_b32 v" #c ", " s "\n\tv_mov_b32 v" #d ", " s "\n\t"
#define AISSUE                                                   \
    "s_add_u32 m0, s44, %[lw]\n\t"                               \
    "s_add_u32 s44, s44, 0x4000\n\t"                             \
    "buffer_load_dwordx4 v14, %[rk], s43 offen sc1 lds\n\t"      \
    "s_add_u32 m0, m0, 0x2000\n\t"                               \
    "s_and_b32 s44, s44, 0xffff\n\t"                             \
    "buffer_load_dwordx4 v14, %[rv], s43 offen sc1 lds\n\t"      \
    "s_add_u32 s43, s43, 0x2000\n\t"
#define PR2 "s_setprio 2\n\t"
#define PR3 ""
#define PR0 "s_setprio 1\n\t"
#define PR1 "s_setprio 0\n\t"
constexpr int A_ONES = 65536, A_VOTE = 69632 + 512, A_LSUM = 71680;
struct AttnRegs { f32x16 oa, ob, nm; u32x4 p1, v1; unsigned ka, va, go, fo; };
DI unsigned qkv_dep(int a, bool early) {
    const int lane = lane_id(), r = lane & 15, v = (lane >> 4) & 1, R = 4 * (a >> 5) + (lane & 3);
    const int kv = (r >> 3) + 8 * ((early ? 8 + 8 * v : 16 * v + 8 * (a & 1)) + (r & 7));
    const int qq = ((R >> 3) & 1) + 8 * ((R >> 4) * 24 + (R & 7));
    return ((a & 6) + (lane < 32 || !early ? kv : qq)) * 128;
}
DI void attn_prep(AttnRegs& R, float negm, int a, int w) {
    const int lane = lane_id(), r = lane & 31, kh = w >> 2;
    R.ka = 4096 * kh + lane * 16;
    R.va = r < 16 ? 8192 + 4096 * kh + (lane >> 5) * 256 + r * 16 : A_ONES + (r == 16 ? 0 : r == 20 ? 64 : (r > 20 && r < 28) ? 80 : 16);
    R.go = w * 1024 + lane * 16;
    R.fo = qkv_dep(a, false);
#pragma unroll
    for (int i = 0; i < 16; ++i) { R.oa[i] = 0.f; R.ob[i] = 0.f; R.nm[i] = negm; }
    R.p1 = u32x4{0u, 0u, 0u, 0u};
    R.v1 = u32x4{0u, 0u, 0u, 0u};
    asm volatile("" : "+{v[64:79]}"(R.oa), "+{v[80:95]}"(R.ob), "+{v[16:31]}"(R.nm), "+{v[100:103]}"(R.p1), "+{v[124:127]}"(R.v1),
                      "+{v12}"(R.ka), "+{v13}"(R.va), "+{v14}"(R.go), "+{v2}"(R.fo));
}
DI void attn_pass(AttnRegs& R, const char* qa, const char* qb, const char* ksrc, const char* vsrc, const char* go_flag, unsigned tag, int w) {
    const u32x4 rk = srd(ksrc, 65536), rv = srd(vsrc, 65536);
    asm volatile(
        "v_and_b32 v15, 0x3ff, v14\n\t"
        "s_mov_b32 s43, 0\n\t"
        "s_mov_b32 s44, 0\n\t"
        AISSUE
        "global_load_dwordx4 v[4:7], v15, %[qa] sc1\n\t"
        "global_load_dwordx4 v[8:11], v15, %[qb] sc1\n\t"
        AISSUE AISSUE
        "s_mov_b32 s40, 0xffff\n\t"
        "s_mov_b32 s41, 0xffff\n\t"
        "s_mov_b32 s42, 8\n\t"
        "s_waitcnt vmcnt(4)\n\t"
        "s_barrier\n\t"
        "ds_read_b128 v[104:107], v12\n\t"
        "ds_read_b128 v[108:111], v12 offset:1024\n\t"
        "ds_read_b128 v[112:115], v13\n\t"
        "ds_read_b128 v[116:119], v13 offset:512\n\t"
        PR2
        "s_waitcnt lgkmcnt(3)\n\t"
        MF(R_SA, "v[104:107]", "v[4:7]", R_NM)
        "s_nop 15\n\t"
        "LA_loop%=:\n\t"
        ATILE_E(2048, 1024, 1536, PR0)
        ATILE_O(3072, 2048, 2560, PR1)
        ATILE_E(0, 3072, 3584,
                "s_waitcnt vmcnt(2)\n\t"
                "s_cmp_eq_u32 s42, 7\n\t"
                "s_cbranch_scc0 LA_f%=\n\t"
                "s_mov_b32 s45, 0x100000\n\t"
                "LA_p%=:\n\t"
                "v_cmp_ne_u32 vcc, %[tag], v3\n\t"
                "s_sub_u32 s45, s45, 1\n\t"
                "s_cbranch_scc1 LA_f%=\n\t"
                "s_cbranch_vccz LA_f%=\n\t"
                "global_load_dword v3, v2, %[ff] sc1\n\t"
                "s_waitcnt vmcnt(0)\n\t"
                "s_branch LA_p%=\n\t"
                "LA_f%=:\n\t"
                "s_barrier\n\t"
                "s_setprio 3\n\t"
                "v_add_u32 v12, 0x4000, v12\n\t"
                "s_cmp_eq_u32 s42, 8\n\t"
                "s_cbranch_scc0 LA_h%=\n\t"
                "global_load_dword v3, v2, %[ff] sc1\n\t"
                "LA_h%=:\n\t"
                AISSUE
                "v_and_b32 v12, 0xffff, v12\n\t"
                PR2)
        ATILE_O(1024, 0, 512,
                PR3
                "s_mov_b64 exec, s[40:41]\n\t"
                "v_add_u32 v13, 0x4000, v13\n\t"
                "s_sub_u32 s42, s42, 1\n\t"
                "v_and_b32 v13, 0xffff, v13\n\t"
                "s_mov_b64 exec, -1\n\t"
                "s_cmp_lg_u32 s42, 0\n\t")
        "s_cbranch_scc1 LA_loop%=\n\t"
        "s_waitcnt lgkmcnt(0)\n\t"
        "s_setprio 3\n\t"
        MF(R_OB, "v[124:127]", "v[100:103]", R_OB)
        "s_nop 15\n\t"
        : "+{v[64:79]}"(R.oa), "+{v[80:95]}"(R.ob), "+{v[100:103]}"(R.p1), "+{v[124:127]}"(R.v1), "+{v12}"(R.ka), "+{v13}"(R.va)
        : "{v[16:31]}"(R.nm), "{v14}"(R.go), [qa] "s"(qa), [qb] "s"(qb), [rk] "s"(rk), [rv] "s"(rv), [lw] "s"(w * 1024), [tag] "s"(tag), [ff] "s"(go_flag), "{v2}"(R.fo)
        : "memory", "scc", "vcc", "s40", "s41", "s42", "s43", "s44", "s45", "v3", "v4", "v5", "v6", "v7", "v8", "v9", "v10", "v11", "v15",
          "v32", "v33", "v34", "v35", "v36", "v37", "v38", "v39", "v40", "v41", "v42", "v43", "v44", "v45", "v46", "v47",
          "v48", "v49", "v50", "v51", "v52", "v53", "v54", "v55", "v56", "v57", "v58", "v59", "v60", "v61", "v62", "v63",
          "v96", "v97", "v98", "v99", "v104", "v105", "v106", "v107", "v108", "v109", "v110", "v111",
          "v112", "v113", "v114", "v115", "v116", "v117", "v118", "v119", "v120", "v121", "v122", "v123");
}

DI void wait_qkv(const char* ws, int a, unsigned tag) { wait_flags<4>(ws + WS_FQ, qkv_dep(a, true), tag); }
DI void attn_role(const char* x, const char* wqkv, char* ws, char* lds, int a, int w) {
    const int kh = w >> 2;
    const int head = 4 * (a & 7) + ((a >> 3) & 3), qt = 8 * (a >> 5) + 2 * (w & 3);
    ST_DECL; ST(0);
    __builtin_amdgcn_s_setprio(3);
    unsigned tag;
    const int ti = a >> 3, trow = 8 * (a & 1) + (ti & 7) + (ti >= 24 ? 16 : 0), tcol = (ti >= 24 ? 0 : 4 * (ti >> 3)) + ((a & 7) >> 1);
    if (w >= 4) {
        qkv_loader(x, wqkv, trow, tcol, w - 4, ws + WS_FA + a * 128);
        tag = __builtin_amdgcn_readfirstlane(*(LAS const unsigned*)(QCNT + 4));
        if (!FAEARLY && w == 5) st4(ws + WS_FA + a * 128, tag);
        if (w == 4) wait_qkv(ws, a, tag);
    } else {
        tag = __builtin_amdgcn_readfirstlane(ld4(ws + WS_EPOCH)) * 0x9E3779B1u + 0x7F4A7C15u;
        if (tcol < 8) qkv_body<true>(ws, lds, trow, tcol, a, w, tag ST_PASS);
        else qkv_body<false>(ws, lds, trow, tcol, a, w, tag ST_PASS);
    }
    AttnRegs R;
    attn_prep(R, 0.f, a, w);
    ST(5);
    wgbar();
    ST(6);
    {
        const int o = w * 512 + lane_id() * 8;
        const unsigned one = ((o & 0x1b0) == 0) ? 0x3f803f80u : 0u;
        *(LAS u32x2*)((LAS char*)lds + A_ONES + o) = u32x2{one, one};
        asm volatile("s_waitcnt lgkmcnt(0)" ::: "memory");
    }
    const char* ksrc = ws + WS_K + (size_t)head * 65536;
    const char* qa = ws + WS_Q + (size_t)(head * 64 + qt + kh) * 1024;
    const char* qb = ws + WS_Q + (size_t)(head * 64 + qt + 1 - kh) * 1024;
#pragma clang loop unroll(disable)
    for (int att = 0;; ++att) {
        ST(1);
        attn_pass(R, qa, qb, ksrc, ksrc + (WS_V - WS_K), ws + WS_FQ + 32768, tag, w);
        ST(2);
        const int lane = lane_id();
        const f32x16 oa = R.oa, ob = R.ob;
        LAS char* ex = (LAS char*)lds + w * 2048 + lane * 16;
        LAS const char* exr = (LAS const char*)lds + (w ^ 4) * 2048 + lane * 16;
        *(LAS f32x4*)ex = f32x4{ob[0], ob[1], ob[2], ob[3]};
        *(LAS f32x4*)(ex + 1024) = f32x4{ob[4], ob[5], ob[6], ob[7]};
        *(LAS float*)((LAS char*)lds + A_LSUM + w * 256 + lane * 4) = ob[8];
        float big = fmaxf(oa[8], ob[8]) * 2.7e8f;
#pragma unroll
        for (int i = 0; i < 8; ++i) big = fmaxf(fmaxf(big, __builtin_fabsf(oa[i])), __builtin_fabsf(ob[i]));
        const bool bad = !__all(fminf(oa[8], ob[8]) > 8.1e-28f && big < 1.7e38f);
        if (lane == 0) ((LAS unsigned*)((LAS char*)lds + A_VOTE))[w] = bad;
        wgbar();
        const f32x4 t0 = *(LAS const f32x4*)exr, t1 = *(LAS const f32x4*)(exr + 1024);
        const float t2 = *(LAS const float*)((LAS char*)lds + A_LSUM + (w ^ 4) * 256 + lane * 4);
        const u32x4 v0 = *(LAS const u32x4*)((LAS char*)lds + A_VOTE), v1 = *(LAS const u32x4*)((LAS char*)lds + A_VOTE + 16);
        const float rinv = __builtin_amdgcn_rcpf(oa[8] + t2);
        u32x4 ov;
        ov[0] = cvtpk((oa[0] + t0[0]) * rinv, (oa[1] + t0[1]) * rinv);
        ov[1] = cvtpk((oa[2] + t0[2]) * rinv, (oa[3] + t0[3]) * rinv);
        ov[2] = cvtpk((oa[4] + t1[0]) * rinv, (oa[5] + t1[1]) * rinv);
        ov[3] = cvtpk((oa[6] + t1[2]) * rinv, (oa[7] + t1[3]) * rinv);
        const bool redo = att == 0 && __builtin_amdgcn_readfirstlane(v0[0] | v0[1] | v0[2] | v0[3] | v1[0] | v1[1] | v1[2] | v1[3]);
        if (att == 0 ? !redo : att == 1 + kh) {
            st16(ws + WS_O + (size_t)((qt + kh) * 32 + head) * 1024, lane * 16, ov);
            drain();
            st4(ws + WS_FO + ((qt + kh) * 32 + head) * 128, tag);
        }
        asm volatile("" : "+s"(att));
        if (att == 2 || (att == 0 && !redo)) break;
        drain();
        wgbar();
        qa = qb = ws + WS_Q + (size_t)(head * 64 + qt + att) * 1024;
        bf16x8 qf;
        asm volatile("global_load_dwordx4 %0, %1, %2 sc1\n\ts_waitcnt vmcnt(0)" : "=v"(qf) : "v"(lane * 16), "s"(qa) : "memory");
        float mx = -INFINITY;
#pragma clang loop unroll(disable)
        for (int t = 0; t < 64; ++t) {
            bf16x8 kf;
            asm volatile("global_load_dwordx4 %0, %1, %2 sc1\n\ts_waitcnt vmcnt(0)" : "=v"(kf) : "v"(lane * 16), "s"(ksrc + t * 1024) : "memory");
            const f32x16 s = MFMA32(kf, qf, f32x16{});
#pragma unroll
            for (int i = 0; i < 16; ++i) mx = fmaxf(mx, s[i]);
        }
        attn_prep(R, -swap32_max(mx), a, w);
    }
    ST(3); ST_OUT(0);
}

#define BG1 "s_sleep 1\n\t"
#define BG2 "s_sleep 1\n\t"
#define BG3 "s_sleep 1\n\t"
#define SISSUE                                                             \
    "s_mov_b32 m0, s44\n\t"                                                 \
    "s_add_u32 s45, %[ow], %[wst]\n\t"                                      \
    "buffer_load_dwordx4 %[g0], %[rw], %[ow] offen lds\n\t"                 \
    "buffer_load_dwordx4 %[g1], %[rw], %[ow] offen offset:1024 lds\n\t"     \
    "s_add_u32 m0, s44, 8192\n\t"                                           \
    "s_add_u32 s46, %[od], 4096\n\t"                                        \
    "buffer_load_dwordx4 %[d0], %[rd], %[od] offen sc1 lds\n\t"             \
    "buffer_load_dwordx4 %[d0], %[rd], %[od] offen offset:1024 sc1 lds\n\t" \
    "s_add_u32 s44, s44, 0x4000\n\t"                                        \
    "s_and_b32 s44, s44, 0xffff\n\t"                                        \
    "s_cmp_eq_u32 %[od], %[de]\n\t"                                         \
    "s_cselect_b32 %[ow], 0, s45\n\t"                                       \
    "s_cselect_b32 %[od], 0, s46\n\t"
struct SG { u32x4 rw, rd; unsigned wst, de, ow, od, a, b, c; };
#define WISSUE                                                              \
    "s_and_b32 s45, s44, 0xc000\n\t"                                        \
    "s_add_u32 m0, s45, %[lo2]\n\t"                                         \
    "s_lshl_b32 s45, s45, 3\n\t"                                            \
    "buffer_load_dwordx4 %[o0], %[ro], s45 offen lds\n\t"                   \
    "buffer_load_dwordx4 %[o1], %[ro], s45 offen offset:1024 lds\n\t"       \
    "buffer_load_dwordx4 %[o2], %[ro], s45 offen offset:2048 lds\n\t"       \
    "buffer_load_dwordx4 %[o3], %[ro], s45 offen offset:3072 lds\n\t"       \
    "s_add_u32 s44, s44, 0x4000\n\t"                                        \
    "s_and_b32 s44, s44, 0xffff\n\t"
template <int WO>
DI void sgemm_load(SG& G, int n, int lw, u32x4 ro, unsigned o0) {
    asm volatile(
        "s_mov_b32 s44, %[lo]\n\t"
        "s_sub_u32 s47, %[n], 3\n\t"
        SISSUE SISSUE SISSUE
        "LL_loop%=:\n\t"
        "s_waitcnt vmcnt(8)\n\t"
        "s_barrier\n\t"
        SISSUE
        "s_sub_u32 s47, s47, 1\n\t"
        "s_cmp_lg_u32 s47, 0\n\t"
        "s_cbranch_scc1 LL_loop%=\n\t"
        ".if %c[wo]\n\t"
        "s_waitcnt vmcnt(8)\n\ts_barrier\n\t" WISSUE
        "s_waitcnt vmcnt(8)\n\ts_barrier\n\t" WISSUE
        "s_waitcnt vmcnt(8)\n\ts_barrier\n\t" WISSUE
        "s_barrier\n\t" WISSUE
        ".else\n\t"
        "s_waitcnt vmcnt(8)\n\ts_barrier\n\t"
        "s_waitcnt vmcnt(4)\n\ts_barrier\n\t"
        "s_waitcnt vmcnt(0)\n\ts_barrier\n\t"
        "s_barrier\n\t"
        ".endif\n\t"
        : [ow] "+s"(G.ow), [od] "+s"(G.od)
        : [wst] "s"(G.wst), [rw] "s"(G.rw), [rd] "s"(G.rd), [lo] "s"(lw * 2048), [de] "s"(G.de), [n] "s"(n), [g0] "v"(G.a), [g1] "v"(G.b), [d0] "v"(G.c),
          [wo] "i"(WO), [ro] "s"(ro), [lo2] "s"(lw * 4096), [o0] "v"(o0), [o1] "v"(o0 + 7168), [o2] "v"(o0 + 14336), [o3] "v"(o0 + 21504)
        : "memory", "scc", "s44", "s45", "s46", "s47");
}
#define SRD3(B, SLOT, K, TD)                                                                         \
    "ds_read_b64_tr_b16 v[" #B ":" #B "+1], %[t] offset:" #SLOT "*16384+" #K "*2048\n\t"              \
    "ds_read_b64_tr_b16 v[" #B "+2:" #B "+3], %[t] offset:" #SLOT "*16384+" #K "*2048+" #TD "\n\t"    \
    "ds_read_b128 v[" #B "+4:" #B "+7], %[dl] offset:" #SLOT "*16384+" #K "*1024\n\t"
#define SSTAGE(SLOT, MM, TD, N0, N1, N2, N3, P0, P1, P2, P3)                          \
    "s_waitcnt lgkmcnt(0)\n\t"                                                        \
    "s_barrier\n\t"                                                                   \
    MM(P0) SRD3(N0, SLOT, 0, TD) SRD3(N1, SLOT, 1, TD) BG1                            \
    MM(P1) SRD3(N2, SLOT, 2, TD) SRD3(N3, SLOT, 3, TD) BG2                            \
    MM(P2) BG3 MM(P3)
#define SST_E(SLOT, MM, TD) SSTAGE(SLOT, MM, TD, 32, 40, 48, 56, 96, 104, 112, 120)
#define SST_O(SLOT, MM, TD) SSTAGE(SLOT, MM, TD, 96, 104, 112, 120, 32, 40, 48, 56)
#define SMM_NONE(P) ""
#define SMM_WA(P) MF("v[64:79]", "v[" #P ":" #P "+3]", "v[" #P "+4:" #P "+7]", "v[64:79]")
#define SMM_WB(P) MF("v[80:95]", "v[" #P "+4:" #P "+7]", "v[" #P ":" #P "+3]", "v[80:95]")
#define SGEMM_ASM(ACC, MM, TD, NLOOP, TAIL)                                                                       \
    asm volatile(                                                                                                 \
        "s_mov_b32 s44, " #NLOOP "\n\t"                                                                           \
        "s_barrier\n\t"                                                                                           \
        SRD3(32, 0, 0, TD) SRD3(40, 0, 1, TD) SRD3(48, 0, 2, TD) SRD3(56, 0, 3, TD)                               \
        "LS_loop%=:\n\t"                                                                                          \
        SST_O(1, MM, TD) SST_E(2, MM, TD) SST_O(3, MM, TD) SST_E(0, MM, TD)                                       \
        "s_sub_u32 s44, s44, 1\n\t"                                                                               \
        "s_cmp_lg_u32 s44, 0\n\t"                                                                                 \
        "s_cbranch_scc1 LS_loop%=\n\t"                                                                            \
        TAIL                                                                                                      \
        "s_waitcnt lgkmcnt(0)\n\t"                                                                                \
        MM(96) BG3 MM(104) BG3 MM(112) BG3 MM(120)                                                                \
        "s_nop 15\n\t"                                                                                            \
        : ACC(acc)                                                                                                \
        : [t] "v"(G.a), [dl] "v"(G.b)                                                                             \
        : "memory", "scc", "s44",                                                                                 \
          "v32", "v33", "v34", "v35", "v36", "v37", "v38", "v39", "v40", "v41", "v42", "v43", "v44", "v45", "v46", "v47", \
          "v48", "v49", "v50", "v51", "v52", "v53", "v54", "v55", "v56", "v57", "v58", "v59", "v60", "v61", "v62", "v63", \
          "v96", "v97", "v98", "v99", "v100", "v101", "v102", "v103", "v104", "v105", "v106", "v107", "v108", "v109", "v110", "v111", \
          "v112", "v113", "v114", "v115", "v116", "v117", "v118", "v119", "v120", "v121", "v122", "v123", "v124", "v125", "v126", "v127")

template <int MODE>
DI SG sgemm_setup(const char* wbase, unsigned wbytes, int wrow, const char* dbase, int dblk, int rot, int w) {
    const int lane = lane_id(), row = lane >> 3, lw = w & 3;
    const int q = (lane & 15) >> 2, h2 = lane >> 5;
    SG G;
    G.rw = srd(wbase, wbytes); G.rd = srd(dbase, 2 * dblk);
    G.wst = 64 * wrow; G.de = (MODE == 0 ? 31 : 21) * 4096; G.ow = rot * G.wst; G.od = rot * 4096;
    if (w >= 4) {
        G.a = (16 * lw + row) * wrow + (((lane & 7) ^ (((row >> 1) & 1) << 2)) << 4);
        G.b = G.a + 8 * wrow - 1024;
        G.c = (lw >> 1) * dblk + (lw & 1) * 2048 + lane * 16;
    } else {
        G.a = ((MODE == 0 ? 8 : 4) * h2 + q) * 128 + (((lw & 1) ^ ((q >> 1) & 1)) << 6) + ((lane >> 4) & 1) * 32 + (lane & 3) * 8;
        G.b = 8192 + (lw >> 1) * 4096 + lane * 16;
        G.c = 0;
    }
    asm volatile("" : "+v"(G.a), "+v"(G.b), "+v"(G.c));
    return G;
}
#define ACC0 "+{v[64:79]}"
#define ACC1 "+{v[80:95]}"
template <int MODE>
DI void sgemm_run(f32x16& acc, SG& G) {
    if (MODE == 0) SGEMM_ASM(ACC0, SMM_WA, 512, 7, SST_O(1, SMM_WA, 512) SST_E(2, SMM_WA, 512) SST_O(3, SMM_WA, 512));
    else SGEMM_ASM(ACC1, SMM_WB, 1024, 5, SST_O(1, SMM_WB, 1024));
}
DI f32x4 sub4(const f32x16& a, int g) { return f32x4{a[4 * g], a[4 * g + 1], a[4 * g + 2], a[4 * g + 3]}; }

DI unsigned gelu2(float a, float b) {
    constexpr float c1 = -2.f * 0.7978845608028654f * 1.4426950408889634f, c2 = c1 * 0.044715f;
    const f32x2 u = {a, b};
    const f32x2 z = u * (u * u * c2 + c1);
    const f32x2 d = f32x2{__builtin_amdgcn_exp2f(z[0]), __builtin_amdgcn_exp2f(z[1])} + 1.f;
    const f32x2 o = u * f32x2{__builtin_amdgcn_rcpf(d[0]), __builtin_amdgcn_rcpf(d[1])};
    return cvtpk(o[0], o[1]);
}

#define WRD(R0, R1, R2, R3, STEP)                                                    \
    "ds_read_b64_tr_b16 " R0 ", %[t0] offset:" #STEP "*2048\n\t"                      \
    "ds_read_b64_tr_b16 " R1 ", %[t0] offset:" #STEP "*2048+1024\n\t"                 \
    "ds_read_b64_tr_b16 " R2 ", %[t1] offset:" #STEP "*2048\n\t"                      \
    "ds_read_b64_tr_b16 " R3 ", %[t1] offset:" #STEP "*2048+1024\n\t"
#define WRD0(S) WRD("v[32:33]", "v[34:35]", "v[36:37]", "v[38:39]", S)
#define WRD1(S) WRD("v[40:41]", "v[42:43]", "v[44:45]", "v[46:47]", S)
#define WRD2(S) WRD("v[48:49]", "v[50:51]", "v[52:53]", "v[54:55]", S)
#define WRD3(S) WRD("v[56:57]", "v[58:59]", "v[60:61]", "v[62:63]", S)
#define WMM(WAIT, W0, W1, O) "s_waitcnt " WAIT "\n\t" MF("v[96:111]", W0, O, "v[96:111]") MF("v[112:127]", W1, O, "v[112:127]")
#define OLD(K, R, V, OFF)                                             \
    "5:\n\t"                                                           \
    "s_bitcmp1_b32 s40, " #K "\n\t"                                    \
    "s_cbranch_scc1 6f\n\t"                                            \
    "global_load_dword v24, %[fo], %[f] sc1\n\t"                       \
    "s_sub_u32 s41, s41, 1\n\t"                                        \
    "s_cselect_b32 s42, -1, 0\n\t"                                     \
    "s_waitcnt vmcnt(0)\n\t"                                           \
    "v_cmp_eq_u32 vcc, %[tag], v24\n\t"                                \
    "s_or_b32 s40, vcc_lo, s42\n\t"                                    \
    "s_bitcmp1_b32 s40, " #K "\n\t"                                    \
    "s_cbranch_scc1 6f\n\t"                                            \
    "s_sleep 3\n\t"                                                    \
    "s_branch 5b\n\t"                                                  \
    "6:\n\t"                                                           \
    "global_load_dwordx4 " R ", " V ", %[o] offset:" #OFF " sc1\n\t"
DI void wo_gemm(f32x16& a0, f32x16& a1, f32x4& k0, f32x4& k1, float& k2, const char* osrc, const char* fsrc, unsigned tag, int w) {
    const int lane = lane_id();
    const int q = (lane & 15) >> 2, h2 = lane >> 5;
    const unsigned tb = (w >> 1) * 16384 + (4 * h2 + q) * 128 + ((lane >> 4) & 1) * 32 + (lane & 3) * 8;
    const unsigned t0 = tb + ((q >> 1) & 1) * 64, t1 = tb + (((q >> 1) & 1) ^ 1) * 64;
    const unsigned d0 = lane * 16, d1 = d0 + 4096, fo = (lane & 7) * 128;
    asm volatile(
        WRD0(0) WRD1(1) WRD2(2) WRD3(3)
        "s_mov_b32 s40, 0\n\t"
        "s_mov_b32 s41, 0x100000\n\t"
        "s_setprio 0\n\t"
        OLD(0, "v[64:67]", "%[d0]", 0) OLD(1, "v[68:71]", "%[d0]", 1024) OLD(2, "v[72:75]", "%[d0]", 2048) OLD(3, "v[76:79]", "%[d0]", 3072)
        OLD(4, "v[80:83]", "%[d1]", 0) OLD(5, "v[84:87]", "%[d1]", 1024) OLD(6, "v[88:91]", "%[d1]", 2048) OLD(7, "v[92:95]", "%[d1]", 3072)
        "s_setprio 3\n\t"
        "s_waitcnt vmcnt(7) lgkmcnt(0)\n\t"
        MF("v[96:111]", "v[32:35]", "v[64:67]", "0") MF("v[112:127]", "v[36:39]", "v[64:67]", "0")
        WRD0(4)
        WMM("vmcnt(6)", "v[40:43]", "v[44:47]", "v[68:71]") WRD1(5)
        WMM("vmcnt(5)", "v[48:51]", "v[52:55]", "v[72:75]") WRD2(6)
        WMM("vmcnt(4)", "v[56:59]", "v[60:63]", "v[76:79]") WRD3(7)
        WMM("vmcnt(3) lgkmcnt(12)", "v[32:35]", "v[36:39]", "v[80:83]")
        WMM("vmcnt(2) lgkmcnt(0)", "v[40:43]", "v[44:47]", "v[84:87]")
        WMM("vmcnt(1)", "v[48:51]", "v[52:55]", "v[88:91]")
        "s_barrier\n\t"
        WMM("vmcnt(0)", "v[56:59]", "v[60:63]", "v[92:95]")
        "s_nop 15\n\t"
        : "={v[96:111]}"(a0), "={v[112:127]}"(a1),
          "+{v[12:15]}"(k0), "+{v[16:19]}"(k1), "+{v20}"(k2)
        : [o] "s"(osrc), [f] "s"(fsrc), [tag] "s"(tag), [d0] "v"(d0), [d1] "v"(d1), [t0] "v"(t0), [t1] "v"(t1), [fo] "v"(fo)
        : "memory", "scc", "vcc", "s40", "s41", "s42", "v24",
          "v32", "v33", "v34", "v35", "v36", "v37", "v38", "v39", "v40", "v41", "v42", "v43", "v44", "v45", "v46", "v47",
          "v48", "v49", "v50", "v51", "v52", "v53", "v54", "v55", "v56", "v57", "v58", "v59", "v60", "v61", "v62", "v63",
          "v64", "v65", "v66", "v67", "v68", "v69", "v70", "v71", "v72", "v73", "v74", "v75", "v76", "v77", "v78", "v79",
          "v80", "v81", "v82", "v83", "v84", "v85", "v86", "v87", "v88", "v89", "v90", "v91", "v92", "v93", "v94", "v95");
}

DI void mixer_role(const char* x, const char* wqkv, const char* gw, const char* wo, const char* w1, const char* b1, const char* w2, const char* b2,
                   char* ws, char* out, char* lds, int bi, int w) {
    const int j = bi & 7, i = bi >> 3;
    __builtin_amdgcn_s_setprio(LNP);
    ST_DECL; ST(0);
    const unsigned ep = ld4(ws + WS_EPOCH);
    unsigned tag;
    {
        const int lane = lane_id(), h2 = lane >> 5, tid = 64 * w + lane;
        const u32x4 xr = *(GAS const u32x4*)(x + (size_t)(8 * bi + w) * 1024 + lane * 16);
        const u32x4 gr = *(GAS const u32x4*)(gw + lane * 16);
        tag = __builtin_amdgcn_readfirstlane(ep) * 0x9E3779B1u + 0x7F4A7C15u;
        float v[8], gg[8];
#pragma unroll
        for (int k = 0; k < 4; ++k) { v[2 * k] = bflo(xr[k]); v[2 * k + 1] = bfhi(xr[k]); gg[2 * k] = bflo(gr[k]); gg[2 * k + 1] = bfhi(gr[k]); }
        float s1 = 0.f, s3 = 0.f;
#pragma unroll
        for (int k = 0; k < 8; ++k) { s1 += v[k]; s3 += v[k] * gg[k]; }
        const float mu = wave_sum(s1) * (1.f / 512.f);
        s3 = wave_sum(s3);
        float s2 = 0.f;
#pragma unroll
        for (int k = 0; k < 8; ++k) { v[k] -= mu; s2 += v[k] * v[k]; }
        const float rs = __builtin_amdgcn_rsqf(wave_sum(s2) * (1.f / 512.f) + 1e-5f);
        u32x4 hv;
#pragma unroll
        for (int k = 0; k < 4; ++k) hv[k] = cvtpk(v[2 * k] * rs, v[2 * k + 1] * rs);
        *(LAS u32x4*)((LAS char*)lds + w * 1024 + lane * 16) = hv;
        const float g = __builtin_amdgcn_rcpf(1.f + __builtin_amdgcn_exp2f(-1.4426950408889634f * s3));
        st4(ws + WS_G + (8 * bi + w) * 4, uni(g));
        wgbar();
        LAS const unsigned short* lb = (LAS const unsigned short*)lds + tid;
#pragma unroll
        for (int k = 0; k < 4; ++k) hv[k] = (unsigned)lb[(2 * k) * 512] | ((unsigned)lb[(2 * k + 1) * 512] << 16);
        st16(ws + WS_H + (size_t)(2 * w * 128 + (bi >> 1)) * 1024 + (bi & 1) * 512, h2 * 131072 + (lane & 31) * 16, hv);
        drain();
        wgbar();
        if (w == 0) st4(ws + WS_FL + bi * 128, tag);
    }
    __builtin_amdgcn_s_setprio(3);
    const int wb = w & 1, db = (w >> 1) & 1, hf = w >> 2;
    const int L = (j >> 1) * 22 + i, ft = L >> 2, dt1 = 4 * (j & 1) + (L & 3);
    const int st = 8 * (j >> 1) + (i & 7), dt = 4 * (j & 1) + (i >> 3);
    SG G1, G2;
    unsigned ex, l16, og, wof;
    {
        const int lane = lane_id();
        og = (64 * st + 32 * wb + (lane & 31)) * 4;
        G1 = sgemm_setup<0>(w1 + ft * 128, 2048 * 2752 - ft * 128, 2752, ws + WS_H + (size_t)2 * dt1 * 131072, 131072, ROT1, w);
        G2 = sgemm_setup<1>(w2 + st * 128, 1376 * 4096 - st * 128, 4096, ws + WS_U + (size_t)2 * dt * 86 * 1024, 86 * 1024, ROT2, w);
        l16 = lane * 16;
        ex = 65536 + (w & 3) * 2048 + l16;
        wof = (32 * (w & 3) + (lane >> 3)) * 1024 + (((lane & 7) ^ (((lane >> 4) & 1) << 2)) << 4);
        asm volatile("" : "+v"(ex), "+v"(l16), "+v"(og), "+v"(wof));
    }
    {
        const int row = 16 + 8 * (j & 1) + (i & 7), col = 8 + 8 * (i >> 4) + 2 * (j >> 1) + ((i >> 3) & 1);
        if (w == 0) {
            *(LAS unsigned*)QCNT = 0u;
            wait_flags<2>(ws + WS_FA + bi * 128, 0, tag);
        }
        wgbar();
        if (PFA == 2 && i >= 22) warm(w1, 1376, ((i - 22) * 8 + j) * 8 + w, l16 * 4);
        if (w >= 4) qkv2_loader(x, wqkv, row, col, w - 4);
        else qkv2_body(ws, row, col, bi, w, tag);
    }
    ST(6);
    if (PFA && i >= 22) {
        const int wv = ((i - 22) * 8 + j) * 8 + w;
        if (PFA == 1) warm(w1, 1376, wv, l16 * 4);
        for (int k = 0; k < PFD; ++k) __builtin_amdgcn_s_sleep(64);
        warm(w2, 1376, wv, l16 * 4);
        warm(wo, 128, wv, l16 * 4);
    }
    if (i < 22) {
        if (w >= 4) wait_flags<8>(ws + WS_FL + (w - 4) * 8192, l16 * 8, tag);
        if (w == 4) wait_qkv(ws, bi, tag);
        if (BSLEEP) __builtin_amdgcn_s_sleep(BSLEEP);
        wgbar();
        ST(7);
        f32x4 v0, v1;
        if (w >= 4) {
            sgemm_load<0>(G1, 32, w - 4, G1.rw, 0);
            v0 = *(LAS const f32x4*)(uintptr_t)ex; v1 = *(LAS const f32x4*)(uintptr_t)(ex + 1024);
        } else {
            f32x16 acc1;
            const char* bsrc = b1 + (64 * ft + (ft < 21 ? 32 * wb : 0)) * 2 + (l16 >> 9) * 8;
#pragma unroll
            for (int k = 0; k < 4; ++k) {
                const u32x2 bv = *(GAS const u32x2*)(bsrc + 16 * k);
                acc1[4 * k] = bflo(bv[0]); acc1[4 * k + 1] = bfhi(bv[0]); acc1[4 * k + 2] = bflo(bv[1]); acc1[4 * k + 3] = bfhi(bv[1]);
            }
            sgemm_run<0>(acc1, G1);
            *(LAS f32x4*)(uintptr_t)ex = sub4(acc1, 2); *(LAS f32x4*)(uintptr_t)(ex + 1024) = sub4(acc1, 3);
            wgbar();
            v0 = sub4(acc1, 0); v1 = sub4(acc1, 1);
        }
        ST(1);
        const u32x4 o = {gelu2(v0[0], v0[1]), gelu2(v0[2], v0[3]), gelu2(v1[0], v1[1]), gelu2(v1[2], v1[3])};
        if (ft < 21 || wb == 0) st16(ws + WS_U + (size_t)((2 * dt1 + db) * 86 + 4 * ft + 2 * wb + hf) * 1024, l16, o);
        drain();
        wgbar();
        if (w == 0) st4(ws + WS_FU + (dt1 * 32 + ft) * 128, tag);
    }
    ST(2);
    if (bi == 255 && w == 1) {
        wait_flags256(ws + WS_FQ, l16 * 8, tag);
        wait_flags256(ws + WS_FL, l16 * 8, tag);
        st4(ws + WS_EPOCH, tag);
    }
    if (w == 0) wait_flags<8>(ws + WS_FU + dt * 4096, (l16 < 22 * 16 ? l16 : 0) * 8, tag);
    wgbar();
    ST(3);
    f32x4 bp0, bp1;
    float g;
    {
        asm volatile("global_load_dword %0, %1, %2 sc1" : "=v"(g) : "v"(og), "s"(ws + WS_G) : "memory");
        if (w >= 4) {
            sgemm_load<1>(G2, 22, w - 4, srd(wo + dt * 128, 524288 - dt * 128), wof);
            bp0 = *(LAS const f32x4*)(uintptr_t)ex; bp1 = *(LAS const f32x4*)(uintptr_t)(ex + 1024);
        } else {
            const float bb = bflo(*(GAS const unsigned short*)(b2 + (og >> 1)));
            f32x16 acc2 = {bb, bb, bb, bb, bb, bb, bb, bb, bb, bb, bb, bb, bb, bb, bb, bb};
            sgemm_run<1>(acc2, G2);
            *(LAS f32x4*)(uintptr_t)ex = sub4(acc2, 2); *(LAS f32x4*)(uintptr_t)(ex + 1024) = sub4(acc2, 3);
            wgbar();
            bp0 = sub4(acc2, 0); bp1 = sub4(acc2, 1);
            drain();
        }
        asm volatile("" : "+v"(g));
        ST(4);
        bp0 *= 1.f - g;
        bp1 *= 1.f - g;
        asm volatile("" : "+v"(bp0), "+v"(bp1));
    }
    drain();
    wgbar();
    ST(5);
    f32x16 a0, a1;
    wo_gemm(a0, a1, bp0, bp1, g, ws + WS_O + (size_t)((2 * st + wb) * 32 + 8 * (w >> 1)) * 1024,
            ws + WS_FO + ((2 * st + wb) * 32 + 8 * (w >> 1)) * 128, tag, w);
    {
        const int lane = lane_id(), kq = 2 * db + hf;
        LAS char* ex = (LAS char*)lds + w * 8192 + lane * 16;
#pragma unroll
        for (int k = 0; k < 4; ++k) { *(LAS f32x4*)(ex + k * 1024) = sub4(a0, k); *(LAS f32x4*)(ex + 4096 + k * 1024) = sub4(a1, k); }
        wgbar();
        LAS const char* exr = (LAS const char*)lds + wb * 8192 + kq * 2048 + lane * 16;
        f32x4 s0 = *(LAS const f32x4*)exr, s1 = *(LAS const f32x4*)(exr + 1024);
#pragma unroll
        for (int k = 1; k < 4; ++k) { s0 += *(LAS const f32x4*)(exr + k * 16384); s1 += *(LAS const f32x4*)(exr + k * 16384 + 1024); }
        s0 = s0 * g + bp0;
        s1 = s1 * g + bp1;
        const auto r0 = __builtin_amdgcn_permlane32_swap(cvtpk(s0[0], s0[1]), cvtpk(s1[0], s1[1]), false, false);
        const auto r1 = __builtin_amdgcn_permlane32_swap(cvtpk(s0[2], s0[3]), cvtpk(s1[2], s1[3]), false, false);
        *(GAS u32x4*)(out + (size_t)(64 * st + 32 * wb) * 1024 + (64 * dt + 16 * kq) * 2 + (lane & 31) * 1024 + (lane >> 5) * 16) = u32x4{r0[0], r1[0], r0[1], r1[1]};
    }
    ST_OUT(0);
}

constexpr int MK_LDS = 81920;
__global__ __launch_bounds__(512, 4) void mk(const char* x, const char* wqkv, const char* wo, const char* w1, const char* b1,
                                             const char* w2, const char* b2, const char* gw, char* ws, char* out) {
    extern __shared__ __attribute__((aligned(16))) char lds[];
    const int w = __builtin_amdgcn_readfirstlane(threadIdx.x >> 6);
    if (blockIdx.x < 256) attn_role(x, wqkv, ws, lds, blockIdx.x, w);
    else mixer_role(x, wqkv, gw, wo, w1, b1, w2, b2, ws, out, lds, blockIdx.x - 256, w);
}

extern "C" void kernel_launch(void* const* d_in, const int* in_sizes, int n_in, void* d_out, int out_size, void* d_ws, size_t ws_size,
                              hipStream_t stream) {
    (void)hipFuncSetAttribute((const void*)mk, hipFuncAttributeMaxDynamicSharedMemorySize, MK_LDS);
    mk<<<512, 512, MK_LDS, stream>>>((const char*)d_in[0], (const char*)d_in[1], (const char*)d_in[2], (const char*)d_in[4],
                                     (const char*)d_in[5], (const char*)d_in[6], (const char*)d_in[7], (const char*)d_in[8],
                                     (char*)d_ws, (char*)d_out);
}
```

```cpp
#include <hip/hip_runtime.h>
#include <hip/hip_bf16.h>
#include <stdint.h>

typedef __attribute__((ext_vector_type(8))) short bf16x8;
typedef __attribute__((ext_vector_type(4))) short s16x4;
typedef __attribute__((ext_vector_type(16))) float f32x16;
typedef __attribute__((ext_vector_type(4))) float f32x4;
typedef __attribute__((ext_vector_type(2))) float f32x2;
typedef __attribute__((ext_vector_type(4))) unsigned u32x4;
typedef __attribute__((ext_vector_type(2))) unsigned u32x2;
#define LAS __attribute__((address_space(3)))
#define GAS __attribute__((address_space(1)))
#define DI __device__ __forceinline__
#define MFMA32(a, b, c) __builtin_amdgcn_mfma_f32_32x32x16_bf16((a), (b), (c), 0, 0, 0)

#define LSLEEP 32
#define ROT1 (((dt1 + ft) & 1) * 16)
#define ROT2 (((st + dt) & 1) * 11)
#define GATEQ 1
#define BSLEEP 0
#define LNP 0
#define BGATE 1
#define FAEARLY 1
#define QROT 4
#define QROTM 4
constexpr int S_ = 2048, D_ = 512, F_ = 1376;
constexpr size_t WS_EPOCH = 0;
constexpr size_t WS_G = 4096;
constexpr size_t WS_FL = 98304;
constexpr size_t WS_FQ = 458752;
constexpr size_t WS_FU = 65536;
constexpr size_t WS_FA = 393216;
constexpr size_t WS_FO = 131072;
constexpr size_t WS_Q = 1u << 20;
constexpr size_t WS_K = WS_Q + (2u << 20);
constexpr size_t WS_V = WS_K + (2u << 20);
constexpr size_t WS_H = WS_V + (2u << 20);
constexpr size_t WS_U = WS_H + (2u << 20);
constexpr size_t WS_O = WS_U + (2u << 20);

struct Stamps { unsigned long long t0, t1; };
#define ST_DECL Stamps st_; st_.t0 = __builtin_amdgcn_s_memrealtime(); st_.t1 = st_.t0
#define ST_ARG , Stamps& st_
#define ST_PASS , st_
#define ST(i) do { if ((i) == PROBE_I) { asm volatile("" ::: "memory"); st_.t1 = __builtin_amdgcn_s_memrealtime(); asm volatile("s_waitcnt lgkmcnt(0)" ::: "memory"); } } while (0)
#define ST_OUT(k) do { if ((blockIdx.x >= 256) == PROBE_B) { while (__builtin_amdgcn_s_memrealtime() - st_.t1 < 4000ull) __builtin_amdgcn_s_sleep(1); } } while (0)
#define PROBE_B 0
#define PROBE_I 9
DI unsigned cvtpk(float lo, float hi) {
    typedef float f32x2_t __attribute__((ext_vector_type(2)));
    typedef __bf16 bf16x2_t __attribute__((ext_vector_type(2)));
    return __builtin_bit_cast(unsigned, __builtin_convertvector(f32x2_t{lo, hi}, bf16x2_t));
}
DI float bflo(unsigned u) { return __uint_as_float(u << 16); }
DI float bfhi(unsigned u) { return __uint_as_float(u & 0xffff0000u); }
DI s16x4 trread(LAS const char* p) {
    typedef short v4i16_t __attribute__((ext_vector_type(4)));
    return __builtin_bit_cast(s16x4, __builtin_amdgcn_ds_read_tr16_b64_v4i16((LAS v4i16_t*)p));
}
DI void glds(unsigned lds, unsigned voff, const void* sbase) {
    asm volatile("s_mov_b32 m0, %0\n\ts_nop 0\n\tglobal_load_lds_dwordx4 %1, %2" ::"s"(lds), "v"(voff), "s"(sbase) : "memory");
}
template <int CTRL> DI float dpp(float v) {
    return __int_as_float(__builtin_amdgcn_update_dpp(0, __float_as_int(v), CTRL, 0xf, 0xf, true));
}
DI float rl(float v, int l) { return __int_as_float(__builtin_amdgcn_readlane(__float_as_int(v), l)); }
DI float wave_sum(float v) {
    v += dpp<0xB1>(v); v += dpp<0x4E>(v); v += dpp<0x141>(v); v += dpp<0x140>(v);
    return (rl(v, 0) + rl(v, 16)) + (rl(v, 32) + rl(v, 48));
}
DI float wave_max(float v) {
    v = fmaxf(v, dpp<0xB1>(v)); v = fmaxf(v, dpp<0x4E>(v)); v = fmaxf(v, dpp<0x141>(v)); v = fmaxf(v, dpp<0x140>(v));
    return fmaxf(fmaxf(rl(v, 0), rl(v, 16)), fmaxf(rl(v, 32), rl(v, 48)));
}
DI float swap32_add(float v) {
    auto rr = __builtin_amdgcn_permlane32_swap(__float_as_uint(v), __float_as_uint(v), false, false);
    return __uint_as_float(rr[0]) + __uint_as_float(rr[1]);
}
DI float swap32_max(float v) {
    auto rr = __builtin_amdgcn_permlane32_swap(__float_as_uint(v), __float_as_uint(v), false, false);
    return fmaxf(__uint_as_float(rr[0]), __uint_as_float(rr[1]));
}

DI int lane_id() {
    int l;
    asm volatile("v_mbcnt_lo_u32_b32 %0, -1, 0\n\tv_mbcnt_hi_u32_b32 %0, -1, %0" : "=v"(l));
    __builtin_assume(l >= 0 && l < 64);
    return l;
}
DI void st16(const char* sbase, unsigned voff, u32x4 v) {
    asm volatile("global_store_dwordx4 %0, %1, %2 sc1\n\ts_nop 1" ::"v"(voff), "v"(v), "s"(sbase) : "memory");
}
DI void st4(const char* p, unsigned v) {
    unsigned t0, t1;
    asm volatile("s_nop 1\n\tv_mov_b32 %0, %2\n\tv_mov_b32 %1, 0\n\ts_mov_b64 exec, 1\n\tglobal_store_dword %1, %0, %3 sc1\n\ts_mov_b64 exec, -1"
                 : "=&v"(t0), "=&v"(t1) : "s"(v), "s"(p) : "memory");
}
DI unsigned uni(float v) { return __builtin_amdgcn_readfirstlane(__float_as_uint(v)); }
DI unsigned ld4(const char* p) { return __hip_atomic_load((GAS const unsigned*)p, __ATOMIC_RELAXED, __HIP_MEMORY_SCOPE_AGENT); }
DI void drain() { asm volatile("s_waitcnt vmcnt(0)" ::: "memory"); }
DI void wgbar() { asm volatile("s_waitcnt lgkmcnt(0)\n\ts_barrier" ::: "memory"); }
template <int SLEEP>
DI void wait_flags(const char* sbase, unsigned voff, unsigned tag) {
#pragma clang loop unroll(disable)
    for (unsigned spin = 0; spin < (1u << 20); ++spin) {
        unsigned f;
        asm volatile("global_load_dword %0, %1, %2 sc1\n\ts_waitcnt vmcnt(0)" : "=v"(f) : "v"(voff), "s"(sbase) : "memory");
        if (__all(f == tag)) break;
        __builtin_amdgcn_s_sleep(SLEEP);
    }
}

DI void wait_flags256(const char* sbase, unsigned voff, unsigned tag) {
#pragma clang loop unroll(disable)
    for (unsigned spin = 0; spin < (1u << 20); ++spin) {
        unsigned f0, f1, f2, f3;
        asm volatile("global_load_dword %0, %4, %5 sc1\n\tglobal_load_dword %1, %4, %6 sc1\n\tglobal_load_dword %2, %4, %7 sc1\n\t"
                     "global_load_dword %3, %4, %8 sc1\n\ts_waitcnt vmcnt(0)"
                     : "=&v"(f0), "=&v"(f1), "=&v"(f2), "=&v"(f3)
                     : "v"(voff), "s"(sbase), "s"(sbase + 8192), "s"(sbase + 16384), "s"(sbase + 24576) : "memory");
        if (__all(f0 == tag && f1 == tag && f2 == tag && f3 == tag)) break;
        __builtin_amdgcn_s_sleep(8);
    }
}

constexpr int QCNT = 73728;
#define MF(D, A, B, C) "v_mfma_f32_32x32x16_bf16 " D ", " A ", " B ", " C "\n\t"
DI u32x4 srd(const char* base, unsigned bytes) {
    return u32x4{(unsigned)(uintptr_t)base, (unsigned)((uintptr_t)base >> 32) & 0xffffu, bytes, 0x00020000u};
}
DI void qkv_store(char* ws, const f32x16& acc, int type, int head, int tile, int lane) {
    const float sc = (type == 0) ? 0.25f * 1.4426950408889634f : 1.f;
    const char* dst = ws + WS_Q + (size_t)type * (2u << 20) + (size_t)(head * 64 + tile) * 1024;
    const unsigned vo = type < 2 ? lane * 16 : ((lane >> 4) & 1) * 65536 + (lane >> 5) * 256 + (lane & 15) * 16;
#pragma unroll
    for (int hf = 0; hf < 2; ++hf) {
        u32x4 o;
#pragma unroll
        for (int i = 0; i < 4; ++i) o[i] = cvtpk(acc[8 * hf + 2 * i] * sc, acc[8 * hf + 2 * i + 1] * sc);
        st16(dst + hf * (type < 2 ? 65536 : 512), vo, o);
    }
}
DI void publish4(const char* flag, unsigned tag, int lane) {
    drain();
    unsigned arr = 0;
    if (lane == 0) arr = __hip_atomic_fetch_add((LAS unsigned*)QCNT, 1u, __ATOMIC_RELAXED, __HIP_MEMORY_SCOPE_WORKGROUP);
    if (__builtin_amdgcn_readfirstlane(arr) == 3) st4(flag, tag);
}
DI void qkv_loader(const char* x, const char* wqkv, int row, int col, int lw, const char* hint) {
    const int lane = lane_id(), xr = 16 * lw + (lane >> 3), wr = 16 * lw + (lane >> 4);
    const unsigned x0 = xr * 1024 + (((lane & 7) ^ ((xr >> 1) & 7)) << 4), x1 = (xr + 8) * 1024 + (((lane & 7) ^ (((xr + 8) >> 1) & 7)) << 4) - 1024;
    const unsigned w0 = wr * 3072 + (((lane & 15) ^ ((wr & 3) << 2)) << 4);
    const u32x4 rx = srd(x + (size_t)row * 65536, 65536), rw = srd(wqkv + col * 256, 512 * 3072 - col * 256);
    const int rot = (row + col) * QROTM & QROT;
    auto issue = [&](int s, int slot) {
        const int kc = (s + rot) & 7;
        asm volatile("s_mov_b32 m0, %[m]\n\ts_nop 0\n\t"
                     "buffer_load_dwordx4 %[x0], %[rx], %[ox] offen lds\n\t"
                     "buffer_load_dwordx4 %[x1], %[rx], %[ox] offen offset:1024 lds\n\t"
                     "s_mov_b32 m0, %[m2]\n\ts_nop 0\n\t"
                     "buffer_load_dwordx4 %[w0], %[rw], %[ow] offen lds\n\t"
                     "buffer_load_dwordx4 %[w1], %[rw], %[ow] offen offset:1024 lds\n\t"
                     "buffer_load_dwordx4 %[w2], %[rw], %[ow] offen offset:2048 lds\n\t"
                     "buffer_load_dwordx4 %[w3], %[rw], %[ow] offen offset:3072 lds"
                     :: [m] "s"(slot * 24576 + lw * 2048), [m2] "s"(slot * 24576 + 8192 + lw * 4096), [ow] "s"(kc * 196608), [ox] "s"(kc * 128),
                        [rw] "s"(rw), [rx] "s"(rx), [x0] "v"(x0), [x1] "v"(x1),
                        [w0] "v"(w0), [w1] "v"(w0 + 11264), [w2] "v"(w0 + 22528), [w3] "v"(w0 + 33792)
                     : "memory");
    };
    issue(0, 0);
    __builtin_amdgcn_s_setprio(2);
    issue(1, 1);
    __builtin_amdgcn_s_setprio(1);
    issue(2, 2);
    __builtin_amdgcn_s_setprio(3);
    asm volatile("s_waitcnt vmcnt(12)\n\ts_barrier" ::: "memory");
    int slot = 0;
#pragma clang loop unroll(disable)
    for (int s = 3; s < 8; ++s) {
        asm volatile("s_waitcnt vmcnt(6)\n\ts_barrier" ::: "memory");
        issue(s, slot);
        slot = slot == 2 ? 0 : slot + 1;
    }
    if (lw == 1) {
        st4(hint, __builtin_amdgcn_readfirstlane(*(LAS const unsigned*)(QCNT + 4)));
        asm volatile("s_waitcnt vmcnt(7)\n\ts_barrier\n\ts_waitcnt vmcnt(1)\n\ts_barrier" ::: "memory");
    } else
    asm volatile("s_waitcnt vmcnt(6)\n\ts_barrier\n\ts_waitcnt vmcnt(0)\n\ts_barrier" ::: "memory");
}
template <bool T>
DI void qkv_body(char* ws, char* lds, int row, int col, int a, int w, unsigned tag ST_ARG) {
    const int lane = lane_id(), r = lane & 31, h2 = lane >> 5, q = (lane & 15) >> 2;
    f32x16 acc[2] = {};
    const int xa0 = (32 * (w & 1) + r) * 128 + ((h2 ^ ((r >> 1) & 7)) << 4);
    const int wa0 = 8192 + (8 * h2 + q) * 256 + (((2 * (w >> 1)) ^ q) << 6) + ((lane >> 4) & 1) * 32 + (lane & 3) * 8;
    bf16x8 xf[4], wf[2][2];
    auto rdw = [&](LAS const char* sl, int st, int i) {
        LAS const char* pw = sl + (wa0 ^ (i << 6)) + st * 4096;
        const s16x4 lo = trread(pw), hi = trread(pw + 1024);
        wf[st & 1][i] = __builtin_shufflevector(lo, hi, 0, 1, 2, 3, 4, 5, 6, 7);
    };
    auto mm1 = [&](int st, int i) {
        acc[i] = T ? MFMA32(wf[st & 1][i], xf[st], acc[i]) : MFMA32(xf[st], wf[st & 1][i], acc[i]);
        __builtin_amdgcn_sched_barrier(0);
    };
    auto step = [&](int st, LAS const char* sl, bool bar) {
        const int sn = (st + 2) & 3;
        mm1(st, 0);
        if (bar) wgbar();
        xf[sn] = *(LAS const bf16x8*)(sl + (xa0 ^ (sn << 5))); rdw(sl, sn, 0); __builtin_amdgcn_sched_barrier(0);
        mm1(st, 1);
        rdw(sl, sn, 1); __builtin_amdgcn_sched_barrier(0);
    };
    if (w == 0) *(LAS u32x2*)QCNT = u32x2{0u, tag};
    wgbar();
    LAS const char* sl = (LAS const char*)lds;
#pragma unroll
    for (int st = 0; st < 2; ++st) { xf[st] = *(LAS const bf16x8*)(sl + (xa0 ^ (st << 5))); rdw(sl, st, 0); rdw(sl, st, 1); }
    __builtin_amdgcn_sched_barrier(0);
#pragma clang loop unroll(disable)
    for (int s = 0; s < 7; ++s) {
        step(0, sl, false); step(1, sl, false);
        sl = (sl == (LAS const char*)lds + 49152) ? (LAS const char*)lds : sl + 24576;
        step(2, sl, true); step(3, sl, false);
    }
    step(0, sl, false); step(1, sl, false);
    mm1(2, 0); mm1(2, 1); mm1(3, 0); mm1(3, 1);
    ST(4);
#pragma unroll
    for (int i = 0; i < 2; ++i) qkv_store(ws, acc[i], col >> 2, 8 * (col & 3) + 4 * (w >> 1) + 2 * i, 2 * row + (w & 1), lane);
    publish4(ws + WS_FQ + a * 128, tag, lane);
    ST(7);
}
DI void qkv2_loader(const char* x, const char* wqkv, int rowt, int col, int lw) {
    const int lane = lane_id(), row = 16 * lw + (lane >> 3);
    const unsigned w0 = row * 3072 + (((lane & 7) ^ (((row >> 1) & 1) << 2)) << 4), w1 = w0 + 8 * 3072 - 1024;
    const unsigned x0 = row * 1024 + (((lane & 7) ^ ((row >> 1) & 7)) << 4), x1 = (row + 8) * 1024 + (((lane & 7) ^ (((row + 8) >> 1) & 7)) << 4) - 1024;
    const u32x4 rx = srd(x + (size_t)rowt * 65536, 65536), rw = srd(wqkv + col * 128, 512 * 3072 - col * 128);
    auto issue = [&](int s) {
        asm volatile("s_mov_b32 m0, %[m]\n\ts_nop 0\n\t"
                     "buffer_load_dwordx4 %[w0], %[rw], %[ow] offen lds\n\t"
                     "buffer_load_dwordx4 %[w1], %[rw], %[ow] offen offset:1024 lds\n\t"
                     "s_add_u32 m0, %[m], 8192\n\ts_nop 0\n\t"
                     "buffer_load_dwordx4 %[x0], %[rx], %[ox] offen lds\n\t"
                     "buffer_load_dwordx4 %[x1], %[rx], %[ox] offen offset:1024 lds"
                     :: [m] "s"((s & 3) * 16384 + lw * 2048), [ow] "s"(s * 196608), [ox] "s"(s * 128), [rw] "s"(rw), [rx] "s"(rx),
                        [w0] "v"(w0), [w1] "v"(w1), [x0] "v"(x0), [x1] "v"(x1)
                     : "memory", "scc");
    };
    issue(0); issue(1); issue(2);
#pragma clang loop unroll(disable)
    for (int s = 3; s < 8; ++s) {
        asm volatile("s_waitcnt vmcnt(8)\n\ts_barrier" ::: "memory");
        issue(s);
    }
    asm volatile("s_waitcnt vmcnt(8)\n\ts_barrier\n\ts_waitcnt vmcnt(4)\n\ts_barrier\n\ts_waitcnt vmcnt(0)\n\ts_barrier" ::: "memory");
}
#define QRD3(B, SLOT, K)                                                                             \
    "ds_read_b64_tr_b16 v[" #B ":" #B "+1], %[t] offset:" #SLOT "*16384+" #K "*2048\n\t"              \
    "ds_read_b64_tr_b16 v[" #B "+2:" #B "+3], %[t] offset:" #SLOT "*16384+" #K "*2048+512\n\t"        \
    "ds_read_b128 v[" #B "+4:" #B "+7], %[x" #K "] offset:" #SLOT "*16384\n\t"
#define QSTAGE(SLOT, MM, N0, N1, N2, N3, P0, P1, P2, P3)                              \
    "s_waitcnt lgkmcnt(0)\n\t"                                                        \
    "s_barrier\n\t"                                                                   \
    MM(P0) QRD3(N0, SLOT, 0) MM(P1) QRD3(N1, SLOT, 1) MM(P2) QRD3(N2, SLOT, 2) MM(P3) QRD3(N3, SLOT, 3)
#define QST_E(SLOT, MM) QSTAGE(SLOT, MM, 32, 40, 48, 56, 96, 104, 112, 120)
#define QST_O(SLOT, MM) QSTAGE(SLOT, MM, 96, 104, 112, 120, 32, 40, 48, 56)
#define QMT(P) MF("v[16:31]", "v[" #P ":" #P "+3]", "v[" #P "+4:" #P "+7]", "v[16:31]")
#define QMV(P) MF("v[16:31]", "v[" #P "+4:" #P "+7]", "v[" #P ":" #P "+3]", "v[16:31]")
#define QTILE_ASM(MM)                                                                                             \
    asm volatile(                                                                                                 \
        "s_barrier\n\t"                                                                                           \
        QRD3(32, 0, 0) QRD3(40, 0, 1) QRD3(48, 0, 2) QRD3(56, 0, 3)                                               \
        QST_O(1, MM) QST_E(2, MM)                                                                                 \
        "s_mov_b32 s44, 0\n\t"                                                                                    \
        "LQ_loop%=:\n\t"                                                                                          \
        QST_O(3, MM)                                                                                              \
        "s_cmp_lg_u32 s44, 0\n\t"                                                                                 \
        "s_cbranch_scc1 LQ_end%=\n\t"                                                                             \
        QST_E(0, MM) QST_O(1, MM) QST_E(2, MM)                                                                    \
        "s_mov_b32 s44, 1\n\t"                                                                                    \
        "s_branch LQ_loop%=\n\t"                                                                                  \
        "LQ_end%=:\n\t"                                                                                           \
        "s_waitcnt lgkmcnt(0)\n\t"                                                                                \
        MM(96) MM(104) MM(112) MM(120)                                                                            \
        "s_nop 15\n\t"                                                                                            \
        : "+{v[16:31]}"(acc)                                                                                      \
        : [t] "v"(t), [x0] "v"(xa), [x1] "v"(xa ^ 32), [x2] "v"(xa ^ 64), [x3] "v"(xa ^ 96)                       \
        : "memory", "scc", "s44",                                                                                 \
          "v32", "v33", "v34", "v35", "v36", "v37", "v38", "v39", "v40", "v41", "v42", "v43", "v44", "v45", "v46", "v47", \
          "v48", "v49", "v50", "v51", "v52", "v53", "v54", "v55", "v56", "v57", "v58", "v59", "v60", "v61", "v62", "v63", \
          "v96", "v97", "v98", "v99", "v100", "v101", "v102", "v103", "v104", "v105", "v106", "v107", "v108", "v109", "v110", "v111", \
          "v112", "v113", "v114", "v115", "v116", "v117", "v118", "v119", "v120", "v121", "v122", "v123", "v124", "v125", "v126", "v127")
DI void qkv2_body(char* ws, int row, int col, int bi, int w, unsigned tag) {
    const int lane = lane_id(), r = lane & 31, h2 = lane >> 5, q = (lane & 15) >> 2;
    const unsigned t = (8 * h2 + q) * 128 + (((w & 1) ^ ((q >> 1) & 1)) << 6) + ((lane >> 4) & 1) * 32 + (lane & 3) * 8;
    const unsigned xa = 8192 + (32 * (w >> 1) + r) * 128 + ((h2 ^ ((r >> 1) & 7)) << 4);
    f32x16 acc = {};
    if (col < 16) QTILE_ASM(QMT);
    else QTILE_ASM(QMV);
    qkv_store(ws, acc, col >> 3, 4 * (col & 7) + 2 * (w & 1), 2 * row + (w >> 1), lane);
    publish4(ws + WS_FQ + 32768 + bi * 128, tag, lane);
}

#define EXP(a) "v_exp_f32 v" #a ", v" #a "\n\t"
#define CVT(d, a, b) "v_cvt_pk_bf16_f32 v" #d ", v" #a ", v" #b "\n\t"
#define ASTEP(c0, c1, c2, c3, c4, c5, c6, c7, c8, c9, c10, c11, c12, c13, c14, c15, M1, M2, CL, M3)                  \
    EXP(c0) EXP(c1) M1 EXP(c2) EXP(c3) EXP(c4) EXP(c5) M2 CL                                                          \
    EXP(c6) EXP(c7) CVT(96, c0, c1) CVT(97, c2, c3) CVT(98, c4, c5) CVT(99, c6, c7) EXP(c8) EXP(c9) M3                \
    EXP(c10) EXP(c11) EXP(c12) EXP(c13) EXP(c14) EXP(c15)                                                             \
    CVT(100, c8, c9) CVT(101, c10, c11) CVT(102, c12, c13) CVT(103, c14, c15)
#define R_OA "v[64:79]"
#define R_OB "v[80:95]"
#define R_SA "v[32:47]"
#define R_SB "v[48:63]"
#define R_NM "v[16:31]"
#define ATILE(KC, KN, V0C, V1C, V0N, V1N, KOFF, VOFF0, VOFF1, PRE)                                                    \
    ASTEP(32, 33, 34, 35, 36, 37, 38, 39, 40, 41, 42, 43, 44, 45, 46, 47,                                             \
          MF(R_OB, V1N, "v[100:103]", R_OB), MF(R_SB, KC, "v[8:11]", R_NM),                                           \
          "s_waitcnt lgkmcnt(0)\n\t" PRE                                                                              \
          "ds_read_b128 " KC ", v12 offset:" #KOFF "\n\t"                                                             \
          "ds_read_b128 " V0N ", v13 offset:" #VOFF0 "\n\t"                                                           \
          "ds_read_b128 " V1N ", v13 offset:" #VOFF1 "\n\t",                                                          \
          MF(R_OA, V0C, "v[96:99]", R_OA))                                                                            \
    ASTEP(48, 49, 50, 51, 52, 53, 54, 55, 56, 57, 58, 59, 60, 61, 62, 63,                                             \
          MF(R_OA, V1C, "v[100:103]", R_OA), MF(R_SA, KN, "v[4:7]", R_NM), "", MF(R_OB, V0C, "v[96:99]", R_OB))
#define ATILE_E(KOFF, VOFF0, VOFF1, PRE) ATILE("v[104:107]", "v[108:111]", "v[112:115]", "v[116:119]", "v[120:123]", "v[124:127]", KOFF, VOFF0, VOFF1, PRE)
#define ATILE_O(KOFF, VOFF0, VOFF1, PRE) ATILE("v[108:111]", "v[104:107]", "v[120:123]", "v[124:127]", "v[112:115]", "v[116:119]", KOFF, VOFF0, VOFF1, PRE)
#define MOV4(a, b, c, d, s) "v_mov_b32 v" #a ", " s "\n\tv_mov_b32 v" #b ", " s "\n\tv_mov_b32 v" #c ", " s "\n\tv_mov_b32 v" #d ", " s "\n\t"
#define AISSUE                                                   \
    "s_add_u32 m0, s44, %[lw]\n\t"                               \
    "s_add_u32 s44, s44, 0x4000\n\t"                             \
    "buffer_load_dwordx4 v14, %[rk], s43 offen sc1 lds\n\t"      \
    "s_add_u32 m0, m0, 0x2000\n\t"                               \
    "s_and_b32 s44, s44, 0xffff\n\t"                             \
    "buffer_load_dwordx4 v14, %[rv], s43 offen sc1 lds\n\t"      \
    "s_add_u32 s43, s43, 0x2000\n\t"
#define PR2 "s_setprio 2\n\t"
#define PR3 ""
#define PR0 "s_setprio 1\n\t"
#define PR1 "s_setprio 0\n\t"
constexpr int A_ONES = 65536, A_VOTE = 69632 + 512, A_LSUM = 71680;
struct AttnRegs { f32x16 oa, ob, nm; u32x4 p1, v1; unsigned ka, va, go, fo; };
DI unsigned qkv_dep(int a, bool early) {
    const int lane = lane_id(), r = lane & 15, v = (lane >> 4) & 1, R = 4 * (a >> 5) + (lane & 3);
    const int kv = (r >> 3) + 8 * ((early ? 8 + 8 * v : 16 * v + 8 * (a & 1)) + (r & 7));
    const int qq = ((R >> 3) & 1) + 8 * ((R >> 4) * 24 + (R & 7));
    return ((a & 6) + (lane < 32 || !early ? kv : qq)) * 128;
}
DI void attn_prep(AttnRegs& R, float negm, int a, int w) {
    const int lane = lane_id(), r = lane & 31, kh = w >> 2;
    R.ka = 4096 * kh + lane * 16;
    R.va = r < 16 ? 8192 + 4096 * kh + (lane >> 5) * 256 + r * 16 : A_ONES + (r == 16 ? 0 : r == 20 ? 64 : (r > 20 && r < 28) ? 80 : 16);
    R.go = w * 1024 + lane * 16;
    R.fo = qkv_dep(a, false);
#pragma unroll
    for (int i = 0; i < 16; ++i) { R.oa[i] = 0.f; R.ob[i] = 0.f; R.nm[i] = negm; }
    R.p1 = u32x4{0u, 0u, 0u, 0u};
    R.v1 = u32x4{0u, 0u, 0u, 0u};
    asm volatile("" : "+{v[64:79]}"(R.oa), "+{v[80:95]}"(R.ob), "+{v[16:31]}"(R.nm), "+{v[100:103]}"(R.p1), "+{v[124:127]}"(R.v1),
                      "+{v12}"(R.ka), "+{v13}"(R.va), "+{v14}"(R.go), "+{v2}"(R.fo));
}
DI void attn_pass(AttnRegs& R, const char* qa, const char* qb, const char* ksrc, const char* vsrc, const char* go_flag, unsigned tag, int w) {
    const u32x4 rk = srd(ksrc, 65536), rv = srd(vsrc, 65536);
    asm volatile(
        "v_and_b32 v15, 0x3ff, v14\n\t"
        "s_mov_b32 s43, 0\n\t"
        "s_mov_b32 s44, 0\n\t"
        AISSUE
        "global_load_dwordx4 v[4:7], v15, %[qa] sc1\n\t"
        "global_load_dwordx4 v[8:11], v15, %[qb] sc1\n\t"
        AISSUE AISSUE
        "s_mov_b32 s40, 0xffff\n\t"
        "s_mov_b32 s41, 0xffff\n\t"
        "s_mov_b32 s42, 8\n\t"
        "s_waitcnt vmcnt(4)\n\t"
        "s_barrier\n\t"
        "ds_read_b128 v[104:107], v12\n\t"
        "ds_read_b128 v[108:111], v12 offset:1024\n\t"
        "ds_read_b128 v[112:115], v13\n\t"
        "ds_read_b128 v[116:119], v13 offset:512\n\t"
        PR2
        "s_waitcnt lgkmcnt(3)\n\t"
        MF(R_SA, "v[104:107]", "v[4:7]", R_NM)
        "s_nop 15\n\t"
        "LA_loop%=:\n\t"
        ATILE_E(2048, 1024, 1536, PR0)
        ATILE_O(3072, 2048, 2560, PR1)
        ATILE_E(0, 3072, 3584,
                "s_waitcnt vmcnt(2)\n\t"
                "s_cmp_eq_u32 s42, 7\n\t"
                "s_cbranch_scc0 LA_f%=\n\t"
                "s_mov_b32 s45, 0x100000\n\t"
                "LA_p%=:\n\t"
                "v_cmp_ne_u32 vcc, %[tag], v3\n\t"
                "s_sub_u32 s45, s45, 1\n\t"
                "s_cbranch_scc1 LA_f%=\n\t"
                "s_cbranch_vccz LA_f%=\n\t"
                "global_load_dword v3, v2, %[ff] sc1\n\t"
                "s_waitcnt vmcnt(0)\n\t"
                "s_branch LA_p%=\n\t"
                "LA_f%=:\n\t"
                "s_barrier\n\t"
                "s_setprio 3\n\t"
                "v_add_u32 v12, 0x4000, v12\n\t"
                "s_cmp_eq_u32 s42, 8\n\t"
                "s_cbranch_scc0 LA_h%=\n\t"
                "global_load_dword v3, v2, %[ff] sc1\n\t"
                "LA_h%=:\n\t"
                AISSUE
                "v_and_b32 v12, 0xffff, v12\n\t"
                PR2)
        ATILE_O(1024, 0, 512,
                PR3
                "s_mov_b64 exec, s[40:41]\n\t"
                "v_add_u32 v13, 0x4000, v13\n\t"
                "s_sub_u32 s42, s42, 1\n\t"
                "v_and_b32 v13, 0xffff, v13\n\t"
                "s_mov_b64 exec, -1\n\t"
                "s_cmp_lg_u32 s42, 0\n\t")
        "s_cbranch_scc1 LA_loop%=\n\t"
        "s_waitcnt lgkmcnt(0)\n\t"
        "s_setprio 3\n\t"
        MF(R_OB, "v[124:127]", "v[100:103]", R_OB)
        "s_nop 15\n\t"
        : "+{v[64:79]}"(R.oa), "+{v[80:95]}"(R.ob), "+{v[100:103]}"(R.p1), "+{v[124:127]}"(R.v1), "+{v12}"(R.ka), "+{v13}"(R.va)
        : "{v[16:31]}"(R.nm), "{v14}"(R.go), [qa] "s"(qa), [qb] "s"(qb), [rk] "s"(rk), [rv] "s"(rv), [lw] "s"(w * 1024), [tag] "s"(tag), [ff] "s"(go_flag), "{v2}"(R.fo)
        : "memory", "scc", "vcc", "s40", "s41", "s42", "s43", "s44", "s45", "v3", "v4", "v5", "v6", "v7", "v8", "v9", "v10", "v11", "v15",
          "v32", "v33", "v34", "v35", "v36", "v37", "v38", "v39", "v40", "v41", "v42", "v43", "v44", "v45", "v46", "v47",
          "v48", "v49", "v50", "v51", "v52", "v53", "v54", "v55", "v56", "v57", "v58", "v59", "v60", "v61", "v62", "v63",
          "v96", "v97", "v98", "v99", "v104", "v105", "v106", "v107", "v108", "v109", "v110", "v111",
          "v112", "v113", "v114", "v115", "v116", "v117", "v118", "v119", "v120", "v121", "v122", "v123");
}

DI void wait_qkv(const char* ws, int a, unsigned tag) { wait_flags<4>(ws + WS_FQ, qkv_dep(a, true), tag); }
DI void attn_role(const char* x, const char* wqkv, char* ws, char* lds, int a, int w) {
    const int kh = w >> 2;
    const int head = 4 * (a & 7) + ((a >> 3) & 3), qt = 8 * (a >> 5) + 2 * (w & 3);
    ST_DECL; ST(0);
    __builtin_amdgcn_s_setprio(3);
    unsigned tag;
    const int ti = a >> 3, trow = 8 * (a & 1) + (ti & 7) + (ti >= 24 ? 16 : 0), tcol = (ti >= 24 ? 0 : 4 * (ti >> 3)) + ((a & 7) >> 1);
    if (w >= 4) {
        qkv_loader(x, wqkv, trow, tcol, w - 4, ws + WS_FA + a * 128);
        tag = __builtin_amdgcn_readfirstlane(*(LAS const unsigned*)(QCNT + 4));
        if (!FAEARLY && w == 5) st4(ws + WS_FA + a * 128, tag);
        if (w == 4) wait_qkv(ws, a, tag);
    } else {
        tag = __builtin_amdgcn_readfirstlane(ld4(ws + WS_EPOCH)) * 0x9E3779B1u + 0x7F4A7C15u;
        if (tcol < 8) qkv_body<true>(ws, lds, trow, tcol, a, w, tag ST_PASS);
        else qkv_body<false>(ws, lds, trow, tcol, a, w, tag ST_PASS);
    }
    AttnRegs R;
    attn_prep(R, 0.f, a, w);
    ST(5);
    wgbar();
    ST(6);
    {
        const int o = w * 512 + lane_id() * 8;
        const unsigned one = ((o & 0x1b0) == 0) ? 0x3f803f80u : 0u;
        *(LAS u32x2*)((LAS char*)lds + A_ONES + o) = u32x2{one, one};
        asm volatile("s_waitcnt lgkmcnt(0)" ::: "memory");
    }
    const char* ksrc = ws + WS_K + (size_t)head * 65536;
    const char* qa = ws + WS_Q + (size_t)(head * 64 + qt + kh) * 1024;
    const char* qb = ws + WS_Q + (size_t)(head * 64 + qt + 1 - kh) * 1024;
#pragma clang loop unroll(disable)
    for (int att = 0;; ++att) {
        ST(1);
        attn_pass(R, qa, qb, ksrc, ksrc + (WS_V - WS_K), ws + WS_FQ + 32768, tag, w);
        ST(2);
        const int lane = lane_id();
        const f32x16 oa = R.oa, ob = R.ob;
        LAS char* ex = (LAS char*)lds + w * 2048 + lane * 16;
        LAS const char* exr = (LAS const char*)lds + (w ^ 4) * 2048 + lane * 16;
        *(LAS f32x4*)ex = f32x4{ob[0], ob[1], ob[2], ob[3]};
        *(LAS f32x4*)(ex + 1024) = f32x4{ob[4], ob[5], ob[6], ob[7]};
        *(LAS float*)((LAS char*)lds + A_LSUM + w * 256 + lane * 4) = ob[8];
        float big = fmaxf(oa[8], ob[8]) * 2.7e8f;
#pragma unroll
        for (int i = 0; i < 8; ++i) big = fmaxf(fmaxf(big, __builtin_fabsf(oa[i])), __builtin_fabsf(ob[i]));
        const bool bad = !__all(fminf(oa[8], ob[8]) > 8.1e-28f && big < 1.7e38f);
        if (lane == 0) ((LAS unsigned*)((LAS char*)lds + A_VOTE))[w] = bad;
        wgbar();
        const f32x4 t0 = *(LAS const f32x4*)exr, t1 = *(LAS const f32x4*)(exr + 1024);
        const float t2 = *(LAS const float*)((LAS char*)lds + A_LSUM + (w ^ 4) * 256 + lane * 4);
        const u32x4 v0 = *(LAS const u32x4*)((LAS char*)lds + A_VOTE), v1 = *(LAS const u32x4*)((LAS char*)lds + A_VOTE + 16);
        const float rinv = __builtin_amdgcn_rcpf(oa[8] + t2);
        u32x4 ov;
        ov[0] = cvtpk((oa[0] + t0[0]) * rinv, (oa[1] + t0[1]) * rinv);
        ov[1] = cvtpk((oa[2] + t0[2]) * rinv, (oa[3] + t0[3]) * rinv);
        ov[2] = cvtpk((oa[4] + t1[0]) * rinv, (oa[5] + t1[1]) * rinv);
        ov[3] = cvtpk((oa[6] + t1[2]) * rinv, (oa[7] + t1[3]) * rinv);
        const bool redo = att == 0 && __builtin_amdgcn_readfirstlane(v0[0] | v0[1] | v0[2] | v0[3] | v1[0] | v1[1] | v1[2] | v1[3]);
        if (att == 0 ? !redo : att == 1 + kh) {
            st16(ws + WS_O + (size_t)((qt + kh) * 32 + head) * 1024, lane * 16, ov);
            drain();
            st4(ws + WS_FO + ((qt + kh) * 32 + head) * 128, tag);
        }
        asm volatile("" : "+s"(att));
        if (att == 2 || (att == 0 && !redo)) break;
        drain();
        wgbar();
        qa = qb = ws + WS_Q + (size_t)(head * 64 + qt + att) * 1024;
        bf16x8 qf;
        asm volatile("global_load_dwordx4 %0, %1, %2 sc1\n\ts_waitcnt vmcnt(0)" : "=v"(qf) : "v"(lane * 16), "s"(qa) : "memory");
        float mx = -INFINITY;
#pragma clang loop unroll(disable)
        for (int t = 0; t < 64; ++t) {
            bf16x8 kf;
            asm volatile("global_load_dwordx4 %0, %1, %2 sc1\n\ts_waitcnt vmcnt(0)" : "=v"(kf) : "v"(lane * 16), "s"(ksrc + t * 1024) : "memory");
            const f32x16 s = MFMA32(kf, qf, f32x16{});
#pragma unroll
            for (int i = 0; i < 16; ++i) mx = fmaxf(mx, s[i]);
        }
        attn_prep(R, -swap32_max(mx), a, w);
    }
    ST(3); ST_OUT(0);
}

#define BG1 "s_sleep 1\n\t"
#define BG2 "s_sleep 1\n\t"
#define BG3 "s_sleep 1\n\t"
#define SISSUE                                                             \
    "s_mov_b32 m0, s44\n\t"                                                 \
    "s_add_u32 s45, %[ow], %[wst]\n\t"                                      \
    "buffer_load_dwordx4 %[g0], %[rw], %[ow] offen lds\n\t"                 \
    "buffer_load_dwordx4 %[g1], %[rw], %[ow] offen offset:1024 lds\n\t"     \
    "s_add_u32 m0, s44, 8192\n\t"                                           \
    "s_add_u32 s46, %[od], 4096\n\t"                                        \
    "buffer_load_dwordx4 %[d0], %[rd], %[od] offen sc1 lds\n\t"             \
    "buffer_load_dwordx4 %[d0], %[rd], %[od] offen offset:1024 sc1 lds\n\t" \
    "s_add_u32 s44, s44, 0x4000\n\t"                                        \
    "s_and_b32 s44, s44, 0xffff\n\t"                                        \
    "s_cmp_eq_u32 %[od], %[de]\n\t"                                         \
    "s_cselect_b32 %[ow], 0, s45\n\t"                                       \
    "s_cselect_b32 %[od], 0, s46\n\t"
struct SG { u32x4 rw, rd; unsigned wst, de, ow, od, a, b, c; };
#define WISSUE                                                              \
    "s_and_b32 s45, s44, 0xc000\n\t"                                        \
    "s_add_u32 m0, s45, %[lo2]\n\t"                                         \
    "s_lshl_b32 s45, s45, 3\n\t"                                            \
    "buffer_load_dwordx4 %[o0], %[ro], s45 offen lds\n\t"                   \
    "buffer_load_dwordx4 %[o1], %[ro], s45 offen offset:1024 lds\n\t"       \
    "buffer_load_dwordx4 %[o2], %[ro], s45 offen offset:2048 lds\n\t"       \
    "buffer_load_dwordx4 %[o3], %[ro], s45 offen offset:3072 lds\n\t"       \
    "s_add_u32 s44, s44, 0x4000\n\t"                                        \
    "s_and_b32 s44, s44, 0xffff\n\t"
template <int WO>
DI void sgemm_load(SG& G, int n, int lw, u32x4 ro, unsigned o0) {
    asm volatile(
        "s_mov_b32 s44, %[lo]\n\t"
        "s_sub_u32 s47, %[n], 3\n\t"
        SISSUE SISSUE SISSUE
        "LL_loop%=:\n\t"
        "s_waitcnt vmcnt(8)\n\t"
        "s_barrier\n\t"
        SISSUE
        "s_sub_u32 s47, s47, 1\n\t"
        "s_cmp_lg_u32 s47, 0\n\t"
        "s_cbranch_scc1 LL_loop%=\n\t"
        ".if %c[wo]\n\t"
        "s_waitcnt vmcnt(8)\n\ts_barrier\n\t" WISSUE
        "s_waitcnt vmcnt(8)\n\ts_barrier\n\t" WISSUE
        "s_waitcnt vmcnt(8)\n\ts_barrier\n\t" WISSUE
        "s_barrier\n\t" WISSUE
        ".else\n\t"
        "s_waitcnt vmcnt(8)\n\ts_barrier\n\t"
        "s_waitcnt vmcnt(4)\n\ts_barrier\n\t"
        "s_waitcnt vmcnt(0)\n\ts_barrier\n\t"
        "s_barrier\n\t"
        ".endif\n\t"
        : [ow] "+s"(G.ow), [od] "+s"(G.od)
        : [wst] "s"(G.wst), [rw] "s"(G.rw), [rd] "s"(G.rd), [lo] "s"(lw * 2048), [de] "s"(G.de), [n] "s"(n), [g0] "v"(G.a), [g1] "v"(G.b), [d0] "v"(G.c),
          [wo] "i"(WO), [ro] "s"(ro), [lo2] "s"(lw * 4096), [o0] "v"(o0), [o1] "v"(o0 + 7168), [o2] "v"(o0 + 14336), [o3] "v"(o0 + 21504)
        : "memory", "scc", "s44", "s45", "s46", "s47");
}
#define SRD3(B, SLOT, K, TD)                                                                         \
    "ds_read_b64_tr_b16 v[" #B ":" #B "+1], %[t] offset:" #SLOT "*16384+" #K "*2048\n\t"              \
    "ds_read_b64_tr_b16 v[" #B "+2:" #B "+3], %[t] offset:" #SLOT "*16384+" #K "*2048+" #TD "\n\t"    \
    "ds_read_b128 v[" #B "+4:" #B "+7], %[dl] offset:" #SLOT "*16384+" #K "*1024\n\t"
#define SSTAGE(SLOT, MM, TD, N0, N1, N2, N3, P0, P1, P2, P3)                          \
    "s_waitcnt lgkmcnt(0)\n\t"                                                        \
    "s_barrier\n\t"                                                                   \
    MM(P0) SRD3(N0, SLOT, 0, TD) SRD3(N1, SLOT, 1, TD) BG1                            \
    MM(P1) SRD3(N2, SLOT, 2, TD) SRD3(N3, SLOT, 3, TD) BG2                            \
    MM(P2) BG3 MM(P3)
#define SST_E(SLOT, MM, TD) SSTAGE(SLOT, MM, TD, 32, 40, 48, 56, 96, 104, 112, 120)
#define SST_O(SLOT, MM, TD) SSTAGE(SLOT, MM, TD, 96, 104, 112, 120, 32, 40, 48, 56)
#define SMM_NONE(P) ""
#define SMM_WA(P) MF("v[64:79]", "v[" #P ":" #P "+3]", "v[" #P "+4:" #P "+7]", "v[64:79]")
#define SMM_WB(P) MF("v[80:95]", "v[" #P "+4:" #P "+7]", "v[" #P ":" #P "+3]", "v[80:95]")
#define SGEMM_ASM(ACC, MM, TD, NLOOP, TAIL)                                                                       \
    asm volatile(                                                                                                 \
        "s_mov_b32 s44, " #NLOOP "\n\t"                                                                           \
        "s_barrier\n\t"                                                                                           \
        SRD3(32, 0, 0, TD) SRD3(40, 0, 1, TD) SRD3(48, 0, 2, TD) SRD3(56, 0, 3, TD)                               \
        "LS_loop%=:\n\t"                                                                                          \
        SST_O(1, MM, TD) SST_E(2, MM, TD) SST_O(3, MM, TD) SST_E(0, MM, TD)                                       \
        "s_sub_u32 s44, s44, 1\n\t"                                                                               \
        "s_cmp_lg_u32 s44, 0\n\t"                                                                                 \
        "s_cbranch_scc1 LS_loop%=\n\t"                                                                            \
        TAIL                                                                                                      \
        "s_waitcnt lgkmcnt(0)\n\t"                                                                                \
        MM(96) BG3 MM(104) BG3 MM(112) BG3 MM(120)                                                                \
        "s_nop 15\n\t"                                                                                            \
        : ACC(acc)                                                                                                \
        : [t] "v"(G.a), [dl] "v"(G.b)                                                                             \
        : "memory", "scc", "s44",                                                                                 \
          "v32", "v33", "v34", "v35", "v36", "v37", "v38", "v39", "v40", "v41", "v42", "v43", "v44", "v45", "v46", "v47", \
          "v48", "v49", "v50", "v51", "v52", "v53", "v54", "v55", "v56", "v57", "v58", "v59", "v60", "v61", "v62", "v63", \
          "v96", "v97", "v98", "v99", "v100", "v101", "v102", "v103", "v104", "v105", "v106", "v107", "v108", "v109", "v110", "v111", \
          "v112", "v113", "v114", "v115", "v116", "v117", "v118", "v119", "v120", "v121", "v122", "v123", "v124", "v125", "v126", "v127")

template <int MODE>
DI SG sgemm_setup(const char* wbase, unsigned wbytes, int wrow, const char* dbase, int dblk, int rot, int w) {
    const int lane = lane_id(), row = lane >> 3, lw = w & 3;
    const int q = (lane & 15) >> 2, h2 = lane >> 5;
    SG G;
    G.rw = srd(wbase, wbytes); G.rd = srd(dbase, 2 * dblk);
    G.wst = 64 * wrow; G.de = (MODE == 0 ? 31 : 21) * 4096; G.ow = rot * G.wst; G.od = rot * 4096;
    if (w >= 4) {
        G.a = (16 * lw + row) * wrow + (((lane & 7) ^ (((row >> 1) & 1) << 2)) << 4);
        G.b = G.a + 8 * wrow - 1024;
        G.c = (lw >> 1) * dblk + (lw & 1) * 2048 + lane * 16;
    } else {
        G.a = ((MODE == 0 ? 8 : 4) * h2 + q) * 128 + (((lw & 1) ^ ((q >> 1) & 1)) << 6) + ((lane >> 4) & 1) * 32 + (lane & 3) * 8;
        G.b = 8192 + (lw >> 1) * 4096 + lane * 16;
        G.c = 0;
    }
    asm volatile("" : "+v"(G.a), "+v"(G.b), "+v"(G.c));
    return G;
}
#define ACC0 "+{v[64:79]}"
#define ACC1 "+{v[80:95]}"
template <int MODE>
DI void sgemm_run(f32x16& acc, SG& G) {
    if (MODE == 0) SGEMM_ASM(ACC0, SMM_WA, 512, 7, SST_O(1, SMM_WA, 512) SST_E(2, SMM_WA, 512) SST_O(3, SMM_WA, 512));
    else SGEMM_ASM(ACC1, SMM_WB, 1024, 5, SST_O(1, SMM_WB, 1024));
}
DI f32x4 sub4(const f32x16& a, int g) { return f32x4{a[4 * g], a[4 * g + 1], a[4 * g + 2], a[4 * g + 3]}; }

DI unsigned gelu2(float a, float b) {
    constexpr float c1 = -2.f * 0.7978845608028654f * 1.4426950408889634f, c2 = c1 * 0.044715f;
    const f32x2 u = {a, b};
    const f32x2 z = u * (u * u * c2 + c1);
    const f32x2 d = f32x2{__builtin_amdgcn_exp2f(z[0]), __builtin_amdgcn_exp2f(z[1])} + 1.f;
    const f32x2 o = u * f32x2{__builtin_amdgcn_rcpf(d[0]), __builtin_amdgcn_rcpf(d[1])};
    return cvtpk(o[0], o[1]);
}

#define WRD(R0, R1, R2, R3, STEP)                                                    \
    "ds_read_b64_tr_b16 " R0 ", %[t0] offset:" #STEP "*2048\n\t"                      \
    "ds_read_b64_tr_b16 " R1 ", %[t0] offset:" #STEP "*2048+1024\n\t"                 \
    "ds_read_b64_tr_b16 " R2 ", %[t1] offset:" #STEP "*2048\n\t"                      \
    "ds_read_b64_tr_b16 " R3 ", %[t1] offset:" #STEP "*2048+1024\n\t"
#define WRD0(S) WRD("v[32:33]", "v[34:35]", "v[36:37]", "v[38:39]", S)
#define WRD1(S) WRD("v[40:41]", "v[42:43]", "v[44:45]", "v[46:47]", S)
#define WRD2(S) WRD("v[48:49]", "v[50:51]", "v[52:53]", "v[54:55]", S)
#define WRD3(S) WRD("v[56:57]", "v[58:59]", "v[60:61]", "v[62:63]", S)
#define WMM(WAIT, W0, W1, O) "s_waitcnt " WAIT "\n\t" MF("v[96:111]", W0, O, "v[96:111]") MF("v[112:127]", W1, O, "v[112:127]")
#define OLD(K, R, V, OFF)                                             \
    "5:\n\t"                                                           \
    "s_bitcmp1_b32 s40, " #K "\n\t"                                    \
    "s_cbranch_scc1 6f\n\t"                                            \
    "global_load_dword v24, %[fo], %[f] sc1\n\t"                       \
    "s_sub_u32 s41, s41, 1\n\t"                                        \
    "s_cselect_b32 s42, -1, 0\n\t"                                     \
    "s_waitcnt vmcnt(0)\n\t"                                           \
    "v_cmp_eq_u32 vcc, %[tag], v24\n\t"                                \
    "s_or_b32 s40, vcc_lo, s42\n\t"                                    \
    "s_bitcmp1_b32 s40, " #K "\n\t"                                    \
    "s_cbranch_scc1 6f\n\t"                                            \
    "s_sleep 3\n\t"                                                    \
    "s_branch 5b\n\t"                                                  \
    "6:\n\t"                                                           \
    "global_load_dwordx4 " R ", " V ", %[o] offset:" #OFF " sc1\n\t"
DI void wo_gemm(f32x16& a0, f32x16& a1, f32x4& k0, f32x4& k1, float& k2, const char* osrc, const char* fsrc, unsigned tag, int w) {
    const int lane = lane_id();
    const int q = (lane & 15) >> 2, h2 = lane >> 5;
    const unsigned tb = (w >> 1) * 16384 + (4 * h2 + q) * 128 + ((lane >> 4) & 1) * 32 + (lane & 3) * 8;
    const unsigned t0 = tb + ((q >> 1) & 1) * 64, t1 = tb + (((q >> 1) & 1) ^ 1) * 64;
    const unsigned d0 = lane * 16, d1 = d0 + 4096, fo = (lane & 7) * 128;
    asm volatile(
        WRD0(0) WRD1(1) WRD2(2) WRD3(3)
        "s_mov_b32 s40, 0\n\t"
        "s_mov_b32 s41, 0x100000\n\t"
        "s_setprio 0\n\t"
        OLD(0, "v[64:67]", "%[d0]", 0) OLD(1, "v[68:71]", "%[d0]", 1024) OLD(2, "v[72:75]", "%[d0]", 2048) OLD(3, "v[76:79]", "%[d0]", 3072)
        OLD(4, "v[80:83]", "%[d1]", 0) OLD(5, "v[84:87]", "%[d1]", 1024) OLD(6, "v[88:91]", "%[d1]", 2048) OLD(7, "v[92:95]", "%[d1]", 3072)
        "s_setprio 3\n\t"
        "s_waitcnt vmcnt(7) lgkmcnt(0)\n\t"
        MF("v[96:111]", "v[32:35]", "v[64:67]", "0") MF("v[112:127]", "v[36:39]", "v[64:67]", "0")
        WRD0(4)
        WMM("vmcnt(6)", "v[40:43]", "v[44:47]", "v[68:71]") WRD1(5)
        WMM("vmcnt(5)", "v[48:51]", "v[52:55]", "v[72:75]") WRD2(6)
        WMM("vmcnt(4)", "v[56:59]", "v[60:63]", "v[76:79]") WRD3(7)
        WMM("vmcnt(3) lgkmcnt(12)", "v[32:35]", "v[36:39]", "v[80:83]")
        WMM("vmcnt(2) lgkmcnt(0)", "v[40:43]", "v[44:47]", "v[84:87]")
        WMM("vmcnt(1)", "v[48:51]", "v[52:55]", "v[88:91]")
        "s_barrier\n\t"
        WMM("vmcnt(0)", "v[56:59]", "v[60:63]", "v[92:95]")
        "s_nop 15\n\t"
        : "={v[96:111]}"(a0), "={v[112:127]}"(a1),
          "+{v[12:15]}"(k0), "+{v[16:19]}"(k1), "+{v20}"(k2)
        : [o] "s"(osrc), [f] "s"(fsrc), [tag] "s"(tag), [d0] "v"(d0), [d1] "v"(d1), [t0] "v"(t0), [t1] "v"(t1), [fo] "v"(fo)
        : "memory", "scc", "vcc", "s40", "s41", "s42", "v24",
          "v32", "v33", "v34", "v35", "v36", "v37", "v38", "v39", "v40", "v41", "v42", "v43", "v44", "v45", "v46", "v47",
          "v48", "v49", "v50", "v51", "v52", "v53", "v54", "v55", "v56", "v57", "v58", "v59", "v60", "v61", "v62", "v63",
          "v64", "v65", "v66", "v67", "v68", "v69", "v70", "v71", "v72", "v73", "v74", "v75", "v76", "v77", "v78", "v79",
          "v80", "v81", "v82", "v83", "v84", "v85", "v86", "v87", "v88", "v89", "v90", "v91", "v92", "v93", "v94", "v95");
}

DI void mixer_role(const char* x, const char* wqkv, const char* gw, const char* wo, const char* w1, const char* b1, const char* w2, const char* b2,
                   char* ws, char* out, char* lds, int bi, int w) {
    const int j = bi & 7, i = bi >> 3;
    __builtin_amdgcn_s_setprio(LNP);
    ST_DECL; ST(0);
    const unsigned ep = ld4(ws + WS_EPOCH);
    unsigned tag;
    {
        const int lane = lane_id(), h2 = lane >> 5, tid = 64 * w + lane;
        const u32x4 xr = *(GAS const u32x4*)(x + (size_t)(8 * bi + w) * 1024 + lane * 16);
        const u32x4 gr = *(GAS const u32x4*)(gw + lane * 16);
        tag = __builtin_amdgcn_readfirstlane(ep) * 0x9E3779B1u + 0x7F4A7C15u;
        float v[8], gg[8];
#pragma unroll
        for (int k = 0; k < 4; ++k) { v[2 * k] = bflo(xr[k]); v[2 * k + 1] = bfhi(xr[k]); gg[2 * k] = bflo(gr[k]); gg[2 * k + 1] = bfhi(gr[k]); }
        float s1 = 0.f, s3 = 0.f;
#pragma unroll
        for (int k = 0; k < 8; ++k) { s1 += v[k]; s3 += v[k] * gg[k]; }
        const float mu = wave_sum(s1) * (1.f / 512.f);
        s3 = wave_sum(s3);
        float s2 = 0.f;
#pragma unroll
        for (int k = 0; k < 8; ++k) { v[k] -= mu; s2 += v[k] * v[k]; }
        const float rs = __builtin_amdgcn_rsqf(wave_sum(s2) * (1.f / 512.f) + 1e-5f);
        u32x4 hv;
#pragma unroll
        for (int k = 0; k < 4; ++k) hv[k] = cvtpk(v[2 * k] * rs, v[2 * k + 1] * rs);
        *(LAS u32x4*)((LAS char*)lds + w * 1024 + lane * 16) = hv;
        const float g = __builtin_amdgcn_rcpf(1.f + __builtin_amdgcn_exp2f(-1.4426950408889634f * s3));
        st4(ws + WS_G + (8 * bi + w) * 4, uni(g));
        wgbar();
        LAS const unsigned short* lb = (LAS const unsigned short*)lds + tid;
#pragma unroll
        for (int k = 0; k < 4; ++k) hv[k] = (unsigned)lb[(2 * k) * 512] | ((unsigned)lb[(2 * k + 1) * 512] << 16);
        st16(ws + WS_H + (size_t)(2 * w * 128 + (bi >> 1)) * 1024 + (bi & 1) * 512, h2 * 131072 + (lane & 31) * 16, hv);
        drain();
        wgbar();
        if (w == 0) st4(ws + WS_FL + bi * 128, tag);
    }
    __builtin_amdgcn_s_setprio(3);
    const int wb = w & 1, db = (w >> 1) & 1, hf = w >> 2;
    const int L = (j >> 1) * 22 + i, ft = L >> 2, dt1 = 4 * (j & 1) + (L & 3);
    const int st = 8 * (j >> 1) + (i & 7), dt = 4 * (j & 1) + (i >> 3);
    SG G1, G2;
    unsigned ex, l16, og, wof;
    {
        const int lane = lane_id();
        og = (64 * st + 32 * wb + (lane & 31)) * 4;
        G1 = sgemm_setup<0>(w1 + ft * 128, 2048 * 2752 - ft * 128, 2752, ws + WS_H + (size_t)2 * dt1 * 131072, 131072, ROT1, w);
        G2 = sgemm_setup<1>(w2 + st * 128, 1376 * 4096 - st * 128, 4096, ws + WS_U + (size_t)2 * dt * 86 * 1024, 86 * 1024, ROT2, w);
        l16 = lane * 16;
        ex = 65536 + (w & 3) * 2048 + l16;
        wof = (32 * (w & 3) + (lane >> 3)) * 1024 + (((lane & 7) ^ (((lane >> 4) & 1) << 2)) << 4);
        asm volatile("" : "+v"(ex), "+v"(l16), "+v"(og), "+v"(wof));
    }
    {
        const int row = 16 + 8 * (j & 1) + (i & 7), col = 8 + 8 * (i >> 4) + 2 * (j >> 1) + ((i >> 3) & 1);
        if (w == 0) {
            *(LAS unsigned*)QCNT = 0u;
            wait_flags<2>(ws + WS_FA + bi * 128, 0, tag);
        }
        wgbar();
        if (w >= 4) qkv2_loader(x, wqkv, row, col, w - 4);
        else qkv2_body(ws, row, col, bi, w, tag);
    }
    ST(6);
    if (i < 22) {
        if (w >= 4) wait_flags<8>(ws + WS_FL + (w - 4) * 8192, l16 * 8, tag);
        if (w == 4) wait_qkv(ws, bi, tag);
        if (BSLEEP) __builtin_amdgcn_s_sleep(BSLEEP);
        wgbar();
        ST(7);
        f32x4 v0, v1;
        if (w >= 4) {
            sgemm_load<0>(G1, 32, w - 4, G1.rw, 0);
            v0 = *(LAS const f32x4*)(uintptr_t)ex; v1 = *(LAS const f32x4*)(uintptr_t)(ex + 1024);
        } else {
            f32x16 acc1;
            const char* bsrc = b1 + (64 * ft + (ft < 21 ? 32 * wb : 0)) * 2 + (l16 >> 9) * 8;
#pragma unroll
            for (int k = 0; k < 4; ++k) {
                const u32x2 bv = *(GAS const u32x2*)(bsrc + 16 * k);
                acc1[4 * k] = bflo(bv[0]); acc1[4 * k + 1] = bfhi(bv[0]); acc1[4 * k + 2] = bflo(bv[1]); acc1[4 * k + 3] = bfhi(bv[1]);
            }
            sgemm_run<0>(acc1, G1);
            *(LAS f32x4*)(uintptr_t)ex = sub4(acc1, 2); *(LAS f32x4*)(uintptr_t)(ex + 1024) = sub4(acc1, 3);
            wgbar();
            v0 = sub4(acc1, 0); v1 = sub4(acc1, 1);
        }
        ST(1);
        const u32x4 o = {gelu2(v0[0], v0[1]), gelu2(v0[2], v0[3]), gelu2(v1[0], v1[1]), gelu2(v1[2], v1[3])};
        if (ft < 21 || wb == 0) st16(ws + WS_U + (size_t)((2 * dt1 + db) * 86 + 4 * ft + 2 * wb + hf) * 1024, l16, o);
        drain();
        wgbar();
        if (w == 0) st4(ws + WS_FU + (dt1 * 32 + ft) * 128, tag);
    }
    ST(2);
    if (bi == 255 && w == 1) {
        wait_flags256(ws + WS_FQ, l16 * 8, tag);
        wait_flags256(ws + WS_FL, l16 * 8, tag);
        st4(ws + WS_EPOCH, tag);
    }
    if (w == 0) wait_flags<8>(ws + WS_FU + dt * 4096, (l16 < 22 * 16 ? l16 : 0) * 8, tag);
    wgbar();
    ST(3);
    f32x4 bp0, bp1;
    float g;
    {
        asm volatile("global_load_dword %0, %1, %2 sc1" : "=v"(g) : "v"(og), "s"(ws + WS_G) : "memory");
        if (w >= 4) {
            sgemm_load<1>(G2, 22, w - 4, srd(wo + dt * 128, 524288 - dt * 128), wof);
            bp0 = *(LAS const f32x4*)(uintptr_t)ex; bp1 = *(LAS const f32x4*)(uintptr_t)(ex + 1024);
        } else {
            const float bb = bflo(*(GAS const unsigned short*)(b2 + (og >> 1)));
            f32x16 acc2 = {bb, bb, bb, bb, bb, bb, bb, bb, bb, bb, bb, bb, bb, bb, bb, bb};
            sgemm_run<1>(acc2, G2);
            *(LAS f32x4*)(uintptr_t)ex = sub4(acc2, 2); *(LAS f32x4*)(uintptr_t)(ex + 1024) = sub4(acc2, 3);
            wgbar();
            bp0 = sub4(acc2, 0); bp1 = sub4(acc2, 1);
            drain();
        }
        asm volatile("" : "+v"(g));
        ST(4);
        bp0 *= 1.f - g;
        bp1 *= 1.f - g;
        asm volatile("" : "+v"(bp0), "+v"(bp1));
    }
    drain();
    wgbar();
    ST(5);
    f32x16 a0, a1;
    wo_gemm(a0, a1, bp0, bp1, g, ws + WS_O + (size_t)((2 * st + wb) * 32 + 8 * (w >> 1)) * 1024,
            ws + WS_FO + ((2 * st + wb) * 32 + 8 * (w >> 1)) * 128, tag, w);
    {
        const int lane = lane_id(), kq = 2 * db + hf;
        LAS char* ex = (LAS char*)lds + w * 8192 + lane * 16;
#pragma unroll
        for (int k = 0; k < 4; ++k) { *(LAS f32x4*)(ex + k * 1024) = sub4(a0, k); *(LAS f32x4*)(ex + 4096 + k * 1024) = sub4(a1, k); }
        wgbar();
        LAS const char* exr = (LAS const char*)lds + wb * 8192 + kq * 2048 + lane * 16;
        f32x4 s0 = *(LAS const f32x4*)exr, s1 = *(LAS const f32x4*)(exr + 1024);
#pragma unroll
        for (int k = 1; k < 4; ++k) { s0 += *(LAS const f32x4*)(exr + k * 16384); s1 += *(LAS const f32x4*)(exr + k * 16384 + 1024); }
        s0 = s0 * g + bp0;
        s1 = s1 * g + bp1;
        const auto r0 = __builtin_amdgcn_permlane32_swap(cvtpk(s0[0], s0[1]), cvtpk(s1[0], s1[1]), false, false);
        const auto r1 = __builtin_amdgcn_permlane32_swap(cvtpk(s0[2], s0[3]), cvtpk(s1[2], s1[3]), false, false);
        *(GAS u32x4*)(out + (size_t)(64 * st + 32 * wb) * 1024 + (64 * dt + 16 * kq) * 2 + (lane & 31) * 1024 + (lane >> 5) * 16) = u32x4{r0[0], r1[0], r0[1], r1[1]};
    }
    ST_OUT(0);
}

constexpr int MK_LDS = 81920;
__global__ __launch_bounds__(512, 4) void mk(const char* x, const char* wqkv, const char* wo, const char* w1, const char* b1,
                                             const char* w2, const char* b2, const char* gw, char* ws, char* out) {
    extern __shared__ __attribute__((aligned(16))) char lds[];
    const int w = __builtin_amdgcn_readfirstlane(threadIdx.x >> 6);
    if (blockIdx.x < 256) attn_role(x, wqkv, ws, lds, blockIdx.x, w);
    else mixer_role(x, wqkv, gw, wo, w1, b1, w2, b2, ws, out, lds, blockIdx.x - 256, w);
}

extern "C" void kernel_launch(void* const* d_in, const int* in_sizes, int n_in, void* d_out, int out_size, void* d_ws, size_t ws_size,
                              hipStream_t stream) {
    (void)hipFuncSetAttribute((const void*)mk, hipFuncAttributeMaxDynamicSharedMemorySize, MK_LDS);
    mk<<<512, 512, MK_LDS, stream>>>((const char*)d_in[0], (const char*)d_in[1], (const char*)d_in[2], (const char*)d_in[4],
                                     (const char*)d_in[5], (const char*)d_in[6], (const char*)d_in[7], (const char*)d_in[8],
                                     (char*)d_ws, (char*)d_out);
}
```

```cpp
#include <hip/hip_runtime.h>
#include <hip/hip_bf16.h>
#include <stdint.h>

typedef __attribute__((ext_vector_type(8))) short bf16x8;
typedef __attribute__((ext_vector_type(4))) short s16x4;
typedef __attribute__((ext_vector_type(16))) float f32x16;
typedef __attribute__((ext_vector_type(4))) float f32x4;
typedef __attribute__((ext_vector_type(2))) float f32x2;
typedef __attribute__((ext_vector_type(4))) unsigned u32x4;
typedef __attribute__((ext_vector_type(2))) unsigned u32x2;
#define LAS __attribute__((address_space(3)))
#define GAS __attribute__((address_space(1)))
#define DI __device__ __forceinline__
#define MFMA32(a, b, c) __builtin_amdgcn_mfma_f32_32x32x16_bf16((a), (b), (c), 0, 0, 0)

#define LSLEEP 32
#define ROT1 (((dt1 + ft) & 3) * 8)
#define ROT2 (((st + dt) & 3) * 5)
#define GATEQ 1
#define BSLEEP 0
#define LNP 0
#define BGATE 1
#define FAEARLY 1
#define QROT 4
#define QROTM 4
constexpr int S_ = 2048, D_ = 512, F_ = 1376;
constexpr size_t WS_EPOCH = 0;
constexpr size_t WS_G = 4096;
constexpr size_t WS_FL = 98304;
constexpr size_t WS_FQ = 458752;
constexpr size_t WS_FU = 65536;
constexpr size_t WS_FA = 393216;
constexpr size_t WS_FO = 131072;
constexpr size_t WS_Q = 1u << 20;
constexpr size_t WS_K = WS_Q + (2u << 20);
constexpr size_t WS_V = WS_K + (2u << 20);
constexpr size_t WS_H = WS_V + (2u << 20);
constexpr size_t WS_U = WS_H + (2u << 20);
constexpr size_t WS_O = WS_U + (2u << 20);

#define ST_DECL
#define ST_ARG
#define ST_PASS
#define ST(i)
#define ST_OUT(k)
DI unsigned cvtpk(float lo, float hi) {
    typedef float f32x2_t __attribute__((ext_vector_type(2)));
    typedef __bf16 bf16x2_t __attribute__((ext_vector_type(2)));
    return __builtin_bit_cast(unsigned, __builtin_convertvector(f32x2_t{lo, hi}, bf16x2_t));
}
DI float bflo(unsigned u) { return __uint_as_float(u << 16); }
DI float bfhi(unsigned u) { return __uint_as_float(u & 0xffff0000u); }
DI s16x4 trread(LAS const char* p) {
    typedef short v4i16_t __attribute__((ext_vector_type(4)));
    return __builtin_bit_cast(s16x4, __builtin_amdgcn_ds_read_tr16_b64_v4i16((LAS v4i16_t*)p));
}
DI void glds(unsigned lds, unsigned voff, const void* sbase) {
    asm volatile("s_mov_b32 m0, %0\n\ts_nop 0\n\tglobal_load_lds_dwordx4 %1, %2" ::"s"(lds), "v"(voff), "s"(sbase) : "memory");
}
template <int CTRL> DI float dpp(float v) {
    return __int_as_float(__builtin_amdgcn_update_dpp(0, __float_as_int(v), CTRL, 0xf, 0xf, true));
}
DI float rl(float v, int l) { return __int_as_float(__builtin_amdgcn_readlane(__float_as_int(v), l)); }
DI float wave_sum(float v) {
    v += dpp<0xB1>(v); v += dpp<0x4E>(v); v += dpp<0x141>(v); v += dpp<0x140>(v);
    return (rl(v, 0) + rl(v, 16)) + (rl(v, 32) + rl(v, 48));
}
DI float wave_max(float v) {
    v = fmaxf(v, dpp<0xB1>(v)); v = fmaxf(v, dpp<0x4E>(v)); v = fmaxf(v, dpp<0x141>(v)); v = fmaxf(v, dpp<0x140>(v));
    return fmaxf(fmaxf(rl(v, 0), rl(v, 16)), fmaxf(rl(v, 32), rl(v, 48)));
}
DI float swap32_add(float v) {
    auto rr = __builtin_amdgcn_permlane32_swap(__float_as_uint(v), __float_as_uint(v), false, false);
    return __uint_as_float(rr[0]) + __uint_as_float(rr[1]);
}
DI float swap32_max(float v) {
    auto rr = __builtin_amdgcn_permlane32_swap(__float_as_uint(v), __float_as_uint(v), false, false);
    return fmaxf(__uint_as_float(rr[0]), __uint_as_float(rr[1]));
}

DI int lane_id() {
    int l;
    asm volatile("v_mbcnt_lo_u32_b32 %0, -1, 0\n\tv_mbcnt_hi_u32_b32 %0, -1, %0" : "=v"(l));
    __builtin_assume(l >= 0 && l < 64);
    return l;
}
DI void st16(const char* sbase, unsigned voff, u32x4 v) {
    asm volatile("global_store_dwordx4 %0, %1, %2 sc1\n\ts_nop 1" ::"v"(voff), "v"(v), "s"(sbase) : "memory");
}
DI void st4(const char* p, unsigned v) {
    unsigned t0, t1;
    asm volatile("s_nop 1\n\tv_mov_b32 %0, %2\n\tv_mov_b32 %1, 0\n\ts_mov_b64 exec, 1\n\tglobal_store_dword %1, %0, %3 sc1\n\ts_mov_b64 exec, -1"
                 : "=&v"(t0), "=&v"(t1) : "s"(v), "s"(p) : "memory");
}
DI unsigned uni(float v) { return __builtin_amdgcn_readfirstlane(__float_as_uint(v)); }
DI unsigned ld4(const char* p) { return __hip_atomic_load((GAS const unsigned*)p, __ATOMIC_RELAXED, __HIP_MEMORY_SCOPE_AGENT); }
DI void drain() { asm volatile("s_waitcnt vmcnt(0)" ::: "memory"); }
DI void wgbar() { asm volatile("s_waitcnt lgkmcnt(0)\n\ts_barrier" ::: "memory"); }
template <int SLEEP>
DI void wait_flags(const char* sbase, unsigned voff, unsigned tag) {
#pragma clang loop unroll(disable)
    for (unsigned spin = 0; spin < (1u << 20); ++spin) {
        unsigned f;
        asm volatile("global_load_dword %0, %1, %2 sc1\n\ts_waitcnt vmcnt(0)" : "=v"(f) : "v"(voff), "s"(sbase) : "memory");
        if (__all(f == tag)) break;
        __builtin_amdgcn_s_sleep(SLEEP);
    }
}

DI void wait_flags256(const char* sbase, unsigned voff, unsigned tag) {
#pragma clang loop unroll(disable)
    for (unsigned spin = 0; spin < (1u << 20); ++spin) {
        unsigned f0, f1, f2, f3;
        asm volatile("global_load_dword %0, %4, %5 sc1\n\tglobal_load_dword %1, %4, %6 sc1\n\tglobal_load_dword %2, %4, %7 sc1\n\t"
                     "global_load_dword %3, %4, %8 sc1\n\ts_waitcnt vmcnt(0)"
                     : "=&v"(f0), "=&v"(f1), "=&v"(f2), "=&v"(f3)
                     : "v"(voff), "s"(sbase), "s"(sbase + 8192), "s"(sbase + 16384), "s"(sbase + 24576) : "memory");
        if (__all(f0 == tag && f1 == tag && f2 == tag && f3 == tag)) break;
        __builtin_amdgcn_s_sleep(8);
    }
}

constexpr int QCNT = 73728;
#define MF(D, A, B, C) "v_mfma_f32_32x32x16_bf16 " D ", " A ", " B ", " C "\n\t"
DI u32x4 srd(const char* base, unsigned bytes) {
    return u32x4{(unsigned)(uintptr_t)base, (unsigned)((uintptr_t)base >> 32) & 0xffffu, bytes, 0x00020000u};
}
DI void qkv_store(char* ws, const f32x16& acc, int type, int head, int tile, int lane) {
    const float sc = (type == 0) ? 0.25f * 1.4426950408889634f : 1.f;
    const char* dst = ws + WS_Q + (size_t)type * (2u << 20) + (size_t)(head * 64 + tile) * 1024;
    const unsigned vo = type < 2 ? lane * 16 : ((lane >> 4) & 1) * 65536 + (lane >> 5) * 256 + (lane & 15) * 16;
#pragma unroll
    for (int hf = 0; hf < 2; ++hf) {
        u32x4 o;
#pragma unroll
        for (int i = 0; i < 4; ++i) o[i] = cvtpk(acc[8 * hf + 2 * i] * sc, acc[8 * hf + 2 * i + 1] * sc);
        st16(dst + hf * (type < 2 ? 65536 : 512), vo, o);
    }
}
DI void publish4(const char* flag, unsigned tag, int lane) {
    drain();
    unsigned arr = 0;
    if (lane == 0) arr = __hip_atomic_fetch_add((LAS unsigned*)QCNT, 1u, __ATOMIC_RELAXED, __HIP_MEMORY_SCOPE_WORKGROUP);
    if (__builtin_amdgcn_readfirstlane(arr) == 3) st4(flag, tag);
}
DI void qkv_loader(const char* x, const char* wqkv, int row, int col, int lw, const char* hint) {
    const int lane = lane_id(), xr = 16 * lw + (lane >> 3), wr = 16 * lw + (lane >> 4);
    const unsigned x0 = xr * 1024 + (((lane & 7) ^ ((xr >> 1) & 7)) << 4), x1 = (xr + 8) * 1024 + (((lane & 7) ^ (((xr + 8) >> 1) & 7)) << 4) - 1024;
    const unsigned w0 = wr * 3072 + (((lane & 15) ^ ((wr & 3) << 2)) << 4);
    const u32x4 rx = srd(x + (size_t)row * 65536, 65536), rw = srd(wqkv + col * 256, 512 * 3072 - col * 256);
    const int rot = (row + col) * QROTM & QROT;
    auto issue = [&](int s, int slot) {
        const int kc = (s + rot) & 7;
        asm volatile("s_mov_b32 m0, %[m]\n\ts_nop 0\n\t"
                     "buffer_load_dwordx4 %[x0], %[rx], %[ox] offen lds\n\t"
                     "buffer_load_dwordx4 %[x1], %[rx], %[ox] offen offset:1024 lds\n\t"
                     "s_mov_b32 m0, %[m2]\n\ts_nop 0\n\t"
                     "buffer_load_dwordx4 %[w0], %[rw], %[ow] offen lds\n\t"
                     "buffer_load_dwordx4 %[w1], %[rw], %[ow] offen offset:1024 lds\n\t"
                     "buffer_load_dwordx4 %[w2], %[rw], %[ow] offen offset:2048 lds\n\t"
                     "buffer_load_dwordx4 %[w3], %[rw], %[ow] offen offset:3072 lds"
                     :: [m] "s"(slot * 24576 + lw * 2048), [m2] "s"(slot * 24576 + 8192 + lw * 4096), [ow] "s"(kc * 196608), [ox] "s"(kc * 128),
                        [rw] "s"(rw), [rx] "s"(rx), [x0] "v"(x0), [x1] "v"(x1),
                        [w0] "v"(w0), [w1] "v"(w0 + 11264), [w2] "v"(w0 + 22528), [w3] "v"(w0 + 33792)
                     : "memory");
    };
    issue(0, 0);
    __builtin_amdgcn_s_setprio(2);
    issue(1, 1);
    __builtin_amdgcn_s_setprio(1);
    issue(2, 2);
    __builtin_amdgcn_s_setprio(3);
    asm volatile("s_waitcnt vmcnt(12)\n\ts_barrier" ::: "memory");
    int slot = 0;
#pragma clang loop unroll(disable)
    for (int s = 3; s < 8; ++s) {
        asm volatile("s_waitcnt vmcnt(6)\n\ts_barrier" ::: "memory");
        issue(s, slot);
        slot = slot == 2 ? 0 : slot + 1;
    }
    if (lw == 1) {
        st4(hint, __builtin_amdgcn_readfirstlane(*(LAS const unsigned*)(QCNT + 4)));
        asm volatile("s_waitcnt vmcnt(7)\n\ts_barrier\n\ts_waitcnt vmcnt(1)\n\ts_barrier" ::: "memory");
    } else
    asm volatile("s_waitcnt vmcnt(6)\n\ts_barrier\n\ts_waitcnt vmcnt(0)\n\ts_barrier" ::: "memory");
}
template <bool T>
DI void qkv_body(char* ws, char* lds, int row, int col, int a, int w, unsigned tag ST_ARG) {
    const int lane = lane_id(), r = lane & 31, h2 = lane >> 5, q = (lane & 15) >> 2;
    f32x16 acc[2] = {};
    const int xa0 = (32 * (w & 1) + r) * 128 + ((h2 ^ ((r >> 1) & 7)) << 4);
    const int wa0 = 8192 + (8 * h2 + q) * 256 + (((2 * (w >> 1)) ^ q) << 6) + ((lane >> 4) & 1) * 32 + (lane & 3) * 8;
    bf16x8 xf[4], wf[2][2];
    auto rdw = [&](LAS const char* sl, int st, int i) {
        LAS const char* pw = sl + (wa0 ^ (i << 6)) + st * 4096;
        const s16x4 lo = trread(pw), hi = trread(pw + 1024);
        wf[st & 1][i] = __builtin_shufflevector(lo, hi, 0, 1, 2, 3, 4, 5, 6, 7);
    };
    auto mm1 = [&](int st, int i) {
        acc[i] = T ? MFMA32(wf[st & 1][i], xf[st], acc[i]) : MFMA32(xf[st], wf[st & 1][i], acc[i]);
        __builtin_amdgcn_sched_barrier(0);
    };
    auto step = [&](int st, LAS const char* sl, bool bar) {
        const int sn = (st + 2) & 3;
        mm1(st, 0);
        if (bar) wgbar();
        xf[sn] = *(LAS const bf16x8*)(sl + (xa0 ^ (sn << 5))); rdw(sl, sn, 0); __builtin_amdgcn_sched_barrier(0);
        mm1(st, 1);
        rdw(sl, sn, 1); __builtin_amdgcn_sched_barrier(0);
    };
    if (w == 0) *(LAS u32x2*)QCNT = u32x2{0u, tag};
    wgbar();
    LAS const char* sl = (LAS const char*)lds;
#pragma unroll
    for (int st = 0; st < 2; ++st) { xf[st] = *(LAS const bf16x8*)(sl + (xa0 ^ (st << 5))); rdw(sl, st, 0); rdw(sl, st, 1); }
    __builtin_amdgcn_sched_barrier(0);
#pragma clang loop unroll(disable)
    for (int s = 0; s < 7; ++s) {
        step(0, sl, false); step(1, sl, false);
        sl = (sl == (LAS const char*)lds + 49152) ? (LAS const char*)lds : sl + 24576;
        step(2, sl, true); step(3, sl, false);
    }
    step(0, sl, false); step(1, sl, false);
    mm1(2, 0); mm1(2, 1); mm1(3, 0); mm1(3, 1);
    ST(4);
#pragma unroll
    for (int i = 0; i < 2; ++i) qkv_store(ws, acc[i], col >> 2, 8 * (col & 3) + 4 * (w >> 1) + 2 * i, 2 * row + (w & 1), lane);
    publish4(ws + WS_FQ + a * 128, tag, lane);
    ST(7);
}
DI void qkv2_loader(const char* x, const char* wqkv, int rowt, int col, int lw) {
    const int lane = lane_id(), row = 16 * lw + (lane >> 3);
    const unsigned w0 = row * 3072 + (((lane & 7) ^ (((row >> 1) & 1) << 2)) << 4), w1 = w0 + 8 * 3072 - 1024;
    const unsigned x0 = row * 1024 + (((lane & 7) ^ ((row >> 1) & 7)) << 4), x1 = (row + 8) * 1024 + (((lane & 7) ^ (((row + 8) >> 1) & 7)) << 4) - 1024;
    const u32x4 rx = srd(x + (size_t)rowt * 65536, 65536), rw = srd(wqkv + col * 128, 512 * 3072 - col * 128);
    auto issue = [&](int s) {
        asm volatile("s_mov_b32 m0, %[m]\n\ts_nop 0\n\t"
                     "buffer_load_dwordx4 %[w0], %[rw], %[ow] offen lds\n\t"
                     "buffer_load_dwordx4 %[w1], %[rw], %[ow] offen offset:1024 lds\n\t"
                     "s_add_u32 m0, %[m], 8192\n\ts_nop 0\n\t"
                     "buffer_load_dwordx4 %[x0], %[rx], %[ox] offen lds\n\t"
                     "buffer_load_dwordx4 %[x1], %[rx], %[ox] offen offset:1024 lds"
                     :: [m] "s"((s & 3) * 16384 + lw * 2048), [ow] "s"(s * 196608), [ox] "s"(s * 128), [rw] "s"(rw), [rx] "s"(rx),
                        [w0] "v"(w0), [w1] "v"(w1), [x0] "v"(x0), [x1] "v"(x1)
                     : "memory", "scc");
    };
    issue(0); issue(1); issue(2);
#pragma clang loop unroll(disable)
    for (int s = 3; s < 8; ++s) {
        asm volatile("s_waitcnt vmcnt(8)\n\ts_barrier" ::: "memory");
        issue(s);
    }
    asm volatile("s_waitcnt vmcnt(8)\n\ts_barrier\n\ts_waitcnt vmcnt(4)\n\ts_barrier\n\ts_waitcnt vmcnt(0)\n\ts_barrier" ::: "memory");
}
#define QRD3(B, SLOT, K)                                                                             \
    "ds_read_b64_tr_b16 v[" #B ":" #B "+1], %[t] offset:" #SLOT "*16384+" #K "*2048\n\t"              \
    "ds_read_b64_tr_b16 v[" #B "+2:" #B "+3], %[t] offset:" #SLOT "*16384+" #K "*2048+512\n\t"        \
    "ds_read_b128 v[" #B "+4:" #B "+7], %[x" #K "] offset:" #SLOT "*16384\n\t"
#define QSTAGE(SLOT, MM, N0, N1, N2, N3, P0, P1, P2, P3)                              \
    "s_waitcnt lgkmcnt(0)\n\t"                                                        \
    "s_barrier\n\t"                                                                   \
    MM(P0) QRD3(N0, SLOT, 0) MM(P1) QRD3(N1, SLOT, 1) MM(P2) QRD3(N2, SLOT, 2) MM(P3) QRD3(N3, SLOT, 3)
#define QST_E(SLOT, MM) QSTAGE(SLOT, MM, 32, 40, 48, 56, 96, 104, 112, 120)
#define QST_O(SLOT, MM) QSTAGE(SLOT, MM, 96, 104, 112, 120, 32, 40, 48, 56)
#define QMT(P) MF("v[16:31]", "v[" #P ":" #P "+3]", "v[" #P "+4:" #P "+7]", "v[16:31]")
#define QMV(P) MF("v[16:31]", "v[" #P "+4:" #P "+7]", "v[" #P ":" #P "+3]", "v[16:31]")
#define QTILE_ASM(MM)                                                                                             \
    asm volatile(                                                                                                 \
        "s_barrier\n\t"                                                                                           \
        QRD3(32, 0, 0) QRD3(40, 0, 1) QRD3(48, 0, 2) QRD3(56, 0, 3)                                               \
        QST_O(1, MM) QST_E(2, MM)                                                                                 \
        "s_mov_b32 s44, 0\n\t"                                                                                    \
        "LQ_loop%=:\n\t"                                                                                          \
        QST_O(3, MM)                                                                                              \
        "s_cmp_lg_u32 s44, 0\n\t"                                                                                 \
        "s_cbranch_scc1 LQ_end%=\n\t"                                                                             \
        QST_E(0, MM) QST_O(1, MM) QST_E(2, MM)                                                                    \
        "s_mov_b32 s44, 1\n\t"                                                                                    \
        "s_branch LQ_loop%=\n\t"                                                                                  \
        "LQ_end%=:\n\t"                                                                                           \
        "s_waitcnt lgkmcnt(0)\n\t"                                                                                \
        MM(96) MM(104) MM(112) MM(120)                                                                            \
        "s_nop 15\n\t"                                                                                            \
        : "+{v[16:31]}"(acc)                                                                                      \
        : [t] "v"(t), [x0] "v"(xa), [x1] "v"(xa ^ 32), [x2] "v"(xa ^ 64), [x3] "v"(xa ^ 96)                       \
        : "memory", "scc", "s44",                                                                                 \
          "v32", "v33", "v34", "v35", "v36", "v37", "v38", "v39", "v40", "v41", "v42", "v43", "v44", "v45", "v46", "v47", \
          "v48", "v49", "v50", "v51", "v52", "v53", "v54", "v55", "v56", "v57", "v58", "v59", "v60", "v61", "v62", "v63", \
          "v96", "v97", "v98", "v99", "v100", "v101", "v102", "v103", "v104", "v105", "v106", "v107", "v108", "v109", "v110", "v111", \
          "v112", "v113", "v114", "v115", "v116", "v117", "v118", "v119", "v120", "v121", "v122", "v123", "v124", "v125", "v126", "v127")
DI void qkv2_body(char* ws, int row, int col, int bi, int w, unsigned tag) {
    const int lane = lane_id(), r = lane & 31, h2 = lane >> 5, q = (lane & 15) >> 2;
    const unsigned t = (8 * h2 + q) * 128 + (((w & 1) ^ ((q >> 1) & 1)) << 6) + ((lane >> 4) & 1) * 32 + (lane & 3) * 8;
    const unsigned xa = 8192 + (32 * (w >> 1) + r) * 128 + ((h2 ^ ((r >> 1) & 7)) << 4);
    f32x16 acc = {};
    if (col < 16) QTILE_ASM(QMT);
    else QTILE_ASM(QMV);
    qkv_store(ws, acc, col >> 3, 4 * (col & 7) + 2 * (w & 1), 2 * row + (w >> 1), lane);
    publish4(ws + WS_FQ + 32768 + bi * 128, tag, lane);
}

#define EXP(a) "v_exp_f32 v" #a ", v" #a "\n\t"
#define CVT(d, a, b) "v_cvt_pk_bf16_f32 v" #d ", v" #a ", v" #b "\n\t"
#define ASTEP(c0, c1, c2, c3, c4, c5, c6, c7, c8, c9, c10, c11, c12, c13, c14, c15, M1, M2, CL, M3)                  \
    EXP(c0) EXP(c1) M1 EXP(c2) EXP(c3) EXP(c4) EXP(c5) M2 CL                                                          \
    EXP(c6) EXP(c7) CVT(96, c0, c1) CVT(97, c2, c3) CVT(98, c4, c5) CVT(99, c6, c7) EXP(c8) EXP(c9) M3                \
    EXP(c10) EXP(c11) EXP(c12) EXP(c13) EXP(c14) EXP(c15)                                                             \
    CVT(100, c8, c9) CVT(101, c10, c11) CVT(102, c12, c13) CVT(103, c14, c15)
#define R_OA "v[64:79]"
#define R_OB "v[80:95]"
#define R_SA "v[32:47]"
#define R_SB "v[48:63]"
#define R_NM "v[16:31]"
#define ATILE(KC, KN, V0C, V1C, V0N, V1N, KOFF, VOFF0, VOFF1, PRE)                                                    \
    ASTEP(32, 33, 34, 35, 36, 37, 38, 39, 40, 41, 42, 43, 44, 45, 46, 47,                                             \
          MF(R_OB, V1N, "v[100:103]", R_OB), MF(R_SB, KC, "v[8:11]", R_NM),                                           \
          "s_waitcnt lgkmcnt(0)\n\t" PRE                                                                              \
          "ds_read_b128 " KC ", v12 offset:" #KOFF "\n\t"                                                             \
          "ds_read_b128 " V0N ", v13 offset:" #VOFF0 "\n\t"                                                           \
          "ds_read_b128 " V1N ", v13 offset:" #VOFF1 "\n\t",                                                          \
          MF(R_OA, V0C, "v[96:99]", R_OA))                                                                            \
    ASTEP(48, 49, 50, 51, 52, 53, 54, 55, 56, 57, 58, 59, 60, 61, 62, 63,                                             \
          MF(R_OA, V1C, "v[100:103]", R_OA), MF(R_SA, KN, "v[4:7]", R_NM), "", MF(R_OB, V0C, "v[96:99]", R_OB))
#define ATILE_E(KOFF, VOFF0, VOFF1, PRE) ATILE("v[104:107]", "v[108:111]", "v[112:115]", "v[116:119]", "v[120:123]", "v[124:127]", KOFF, VOFF0, VOFF1, PRE)
#define ATILE_O(KOFF, VOFF0, VOFF1, PRE) ATILE("v[108:111]", "v[104:107]", "v[120:123]", "v[124:127]", "v[112:115]", "v[116:119]", KOFF, VOFF0, VOFF1, PRE)
#define MOV4(a, b, c, d, s) "v_mov_b32 v" #a ", " s "\n\tv_mov_b32 v" #b ", " s "\n\tv_mov_b32 v" #c ", " s "\n\tv_mov_b32 v" #d ", " s "\n\t"
#define AISSUE                                                   \
    "s_add_u32 m0, s44, %[lw]\n\t"                               \
    "s_add_u32 s44, s44, 0x4000\n\t"                             \
    "buffer_load_dwordx4 v14, %[rk], s43 offen sc1 lds\n\t"      \
    "s_add_u32 m0, m0, 0x2000\n\t"                               \
    "s_and_b32 s44, s44, 0xffff\n\t"                             \
    "buffer_load_dwordx4 v14, %[rv], s43 offen sc1 lds\n\t"      \
    "s_add_u32 s43, s43, 0x2000\n\t"
#define PR2 "s_setprio 2\n\t"
#define PR3 ""
#define PR0 "s_setprio 1\n\t"
#define PR1 "s_setprio 0\n\t"
constexpr int A_ONES = 65536, A_VOTE = 69632 + 512, A_LSUM = 71680;
struct AttnRegs { f32x16 oa, ob, nm; u32x4 p1, v1; unsigned ka, va, go, fo; };
DI unsigned qkv_dep(int a, bool early) {
    const int lane = lane_id(), r = lane & 15, v = (lane >> 4) & 1, R = 4 * (a >> 5) + (lane & 3);
    const int kv = (r >> 3) + 8 * ((early ? 8 + 8 * v : 16 * v + 8 * (a & 1)) + (r & 7));
    const int qq = ((R >> 3) & 1) + 8 * ((R >> 4) * 24 + (R & 7));
    return ((a & 6) + (lane < 32 || !early ? kv : qq)) * 128;
}
DI void attn_prep(AttnRegs& R, float negm, int a, int w) {
    const int lane = lane_id(), r = lane & 31, kh = w >> 2;
    R.ka = 4096 * kh + lane * 16;
    R.va = r < 16 ? 8192 + 4096 * kh + (lane >> 5) * 256 + r * 16 : A_ONES + (r == 16 ? 0 : r == 20 ? 64 : (r > 20 && r < 28) ? 80 : 16);
    R.go = w * 1024 + lane * 16;
    R.fo = qkv_dep(a, false);
#pragma unroll
    for (int i = 0; i < 16; ++i) { R.oa[i] = 0.f; R.ob[i] = 0.f; R.nm[i] = negm; }
    R.p1 = u32x4{0u, 0u, 0u, 0u};
    R.v1 = u32x4{0u, 0u, 0u, 0u};
    asm volatile("" : "+{v[64:79]}"(R.oa), "+{v[80:95]}"(R.ob), "+{v[16:31]}"(R.nm), "+{v[100:103]}"(R.p1), "+{v[124:127]}"(R.v1),
                      "+{v12}"(R.ka), "+{v13}"(R.va), "+{v14}"(R.go), "+{v2}"(R.fo));
}
DI void attn_pass(AttnRegs& R, const char* qa, const char* qb, const char* ksrc, const char* vsrc, const char* go_flag, unsigned tag, int w) {
    const u32x4 rk = srd(ksrc, 65536), rv = srd(vsrc, 65536);
    asm volatile(
        "v_and_b32 v15, 0x3ff, v14\n\t"
        "s_mov_b32 s43, 0\n\t"
        "s_mov_b32 s44, 0\n\t"
        AISSUE
        "global_load_dwordx4 v[4:7], v15, %[qa] sc1\n\t"
        "global_load_dwordx4 v[8:11], v15, %[qb] sc1\n\t"
        AISSUE AISSUE
        "s_mov_b32 s40, 0xffff\n\t"
        "s_mov_b32 s41, 0xffff\n\t"
        "s_mov_b32 s42, 8\n\t"
        "s_waitcnt vmcnt(4)\n\t"
        "s_barrier\n\t"
        "ds_read_b128 v[104:107], v12\n\t"
        "ds_read_b128 v[108:111], v12 offset:1024\n\t"
        "ds_read_b128 v[112:115], v13\n\t"
        "ds_read_b128 v[116:119], v13 offset:512\n\t"
        PR2
        "s_waitcnt lgkmcnt(3)\n\t"
        MF(R_SA, "v[104:107]", "v[4:7]", R_NM)
        "s_nop 15\n\t"
        "LA_loop%=:\n\t"
        ATILE_E(2048, 1024, 1536, PR0)
        ATILE_O(3072, 2048, 2560, PR1)
        ATILE_E(0, 3072, 3584,
                "s_waitcnt vmcnt(2)\n\t"
                "s_cmp_eq_u32 s42, 7\n\t"
                "s_cbranch_scc0 LA_f%=\n\t"
                "s_mov_b32 s45, 0x100000\n\t"
                "LA_p%=:\n\t"
                "v_cmp_ne_u32 vcc, %[tag], v3\n\t"
                "s_sub_u32 s45, s45, 1\n\t"
                "s_cbranch_scc1 LA_f%=\n\t"
                "s_cbranch_vccz LA_f%=\n\t"
                "global_load_dword v3, v2, %[ff] sc1\n\t"
                "s_waitcnt vmcnt(0)\n\t"
                "s_branch LA_p%=\n\t"
                "LA_f%=:\n\t"
                "s_barrier\n\t"
                "s_setprio 3\n\t"
                "v_add_u32 v12, 0x4000, v12\n\t"
                "s_cmp_eq_u32 s42, 8\n\t"
                "s_cbranch_scc0 LA_h%=\n\t"
                "global_load_dword v3, v2, %[ff] sc1\n\t"
                "LA_h%=:\n\t"
                AISSUE
                "v_and_b32 v12, 0xffff, v12\n\t"
                PR2)
        ATILE_O(1024, 0, 512,
                PR3
                "s_mov_b64 exec, s[40:41]\n\t"
                "v_add_u32 v13, 0x4000, v13\n\t"
                "s_sub_u32 s42, s42, 1\n\t"
                "v_and_b32 v13, 0xffff, v13\n\t"
                "s_mov_b64 exec, -1\n\t"
                "s_cmp_lg_u32 s42, 0\n\t")
        "s_cbranch_scc1 LA_loop%=\n\t"
        "s_waitcnt lgkmcnt(0)\n\t"
        "s_setprio 3\n\t"
        MF(R_OB, "v[124:127]", "v[100:103]", R_OB)
        "s_nop 15\n\t"
        : "+{v[64:79]}"(R.oa), "+{v[80:95]}"(R.ob), "+{v[100:103]}"(R.p1), "+{v[124:127]}"(R.v1), "+{v12}"(R.ka), "+{v13}"(R.va)
        : "{v[16:31]}"(R.nm), "{v14}"(R.go), [qa] "s"(qa), [qb] "s"(qb), [rk] "s"(rk), [rv] "s"(rv), [lw] "s"(w * 1024), [tag] "s"(tag), [ff] "s"(go_flag), "{v2}"(R.fo)
        : "memory", "scc", "vcc", "s40", "s41", "s42", "s43", "s44", "s45", "v3", "v4", "v5", "v6", "v7", "v8", "v9", "v10", "v11", "v15",
          "v32", "v33", "v34", "v35", "v36", "v37", "v38", "v39", "v40", "v41", "v42", "v43", "v44", "v45", "v46", "v47",
          "v48", "v49", "v50", "v51", "v52", "v53", "v54", "v55", "v56", "v57", "v58", "v59", "v60", "v61", "v62", "v63",
          "v96", "v97", "v98", "v99", "v104", "v105", "v106", "v107", "v108", "v109", "v110", "v111",
          "v112", "v113", "v114", "v115", "v116", "v117", "v118", "v119", "v120", "v121", "v122", "v123");
}

DI void wait_qkv(const char* ws, int a, unsigned tag) { wait_flags<4>(ws + WS_FQ, qkv_dep(a, true), tag); }
DI void attn_role(const char* x, const char* wqkv, char* ws, char* lds, int a, int w) {
    const int kh = w >> 2;
    const int head = 4 * (a & 7) + ((a >> 3) & 3), qt = 8 * (a >> 5) + 2 * (w & 3);
    ST_DECL; ST(0);
    __builtin_amdgcn_s_setprio(3);
    unsigned tag;
    const int ti = a >> 3, trow = 8 * (a & 1) + (ti & 7) + (ti >= 24 ? 16 : 0), tcol = (ti >= 24 ? 0 : 4 * (ti >> 3)) + ((a & 7) >> 1);
    if (w >= 4) {
        qkv_loader(x, wqkv, trow, tcol, w - 4, ws + WS_FA + a * 128);
        tag = __builtin_amdgcn_readfirstlane(*(LAS const unsigned*)(QCNT + 4));
        if (!FAEARLY && w == 5) st4(ws + WS_FA + a * 128, tag);
        if (w == 4) wait_qkv(ws, a, tag);
    } else {
        tag = __builtin_amdgcn_readfirstlane(ld4(ws + WS_EPOCH)) * 0x9E3779B1u + 0x7F4A7C15u;
        if (tcol < 8) qkv_body<true>(ws, lds, trow, tcol, a, w, tag ST_PASS);
        else qkv_body<false>(ws, lds, trow, tcol, a, w, tag ST_PASS);
    }
    AttnRegs R;
    attn_prep(R, 0.f, a, w);
    ST(5);
    wgbar();
    ST(6);
    {
        const int o = w * 512 + lane_id() * 8;
        const unsigned one = ((o & 0x1b0) == 0) ? 0x3f803f80u : 0u;
        *(LAS u32x2*)((LAS char*)lds + A_ONES + o) = u32x2{one, one};
        asm volatile("s_waitcnt lgkmcnt(0)" ::: "memory");
    }
    const char* ksrc = ws + WS_K + (size_t)head * 65536;
    const char* qa = ws + WS_Q + (size_t)(head * 64 + qt + kh) * 1024;
    const char* qb = ws + WS_Q + (size_t)(head * 64 + qt + 1 - kh) * 1024;
#pragma clang loop unroll(disable)
    for (int att = 0;; ++att) {
        ST(1);
        attn_pass(R, qa, qb, ksrc, ksrc + (WS_V - WS_K), ws + WS_FQ + 32768, tag, w);
        ST(2);
        const int lane = lane_id();
        const f32x16 oa = R.oa, ob = R.ob;
        LAS char* ex = (LAS char*)lds + w * 2048 + lane * 16;
        LAS const char* exr = (LAS const char*)lds + (w ^ 4) * 2048 + lane * 16;
        *(LAS f32x4*)ex = f32x4{ob[0], ob[1], ob[2], ob[3]};
        *(LAS f32x4*)(ex + 1024) = f32x4{ob[4], ob[5], ob[6], ob[7]};
        *(LAS float*)((LAS char*)lds + A_LSUM + w * 256 + lane * 4) = ob[8];
        float big = fmaxf(oa[8], ob[8]) * 2.7e8f;
#pragma unroll
        for (int i = 0; i < 8; ++i) big = fmaxf(fmaxf(big, __builtin_fabsf(oa[i])), __builtin_fabsf(ob[i]));
        const bool bad = !__all(fminf(oa[8], ob[8]) > 8.1e-28f && big < 1.7e38f);
        if (lane == 0) ((LAS unsigned*)((LAS char*)lds + A_VOTE))[w] = bad;
        wgbar();
        const f32x4 t0 = *(LAS const f32x4*)exr, t1 = *(LAS const f32x4*)(exr + 1024);
        const float t2 = *(LAS const float*)((LAS char*)lds + A_LSUM + (w ^ 4) * 256 + lane * 4);
        const u32x4 v0 = *(LAS const u32x4*)((LAS char*)lds + A_VOTE), v1 = *(LAS const u32x4*)((LAS char*)lds + A_VOTE + 16);
        const float rinv = __builtin_amdgcn_rcpf(oa[8] + t2);
        u32x4 ov;
        ov[0] = cvtpk((oa[0] + t0[0]) * rinv, (oa[1] + t0[1]) * rinv);
        ov[1] = cvtpk((oa[2] + t0[2]) * rinv, (oa[3] + t0[3]) * rinv);
        ov[2] = cvtpk((oa[4] + t1[0]) * rinv, (oa[5] + t1[1]) * rinv);
        ov[3] = cvtpk((oa[6] + t1[2]) * rinv, (oa[7] + t1[3]) * rinv);
        const bool redo = att == 0 && __builtin_amdgcn_readfirstlane(v0[0] | v0[1] | v0[2] | v0[3] | v1[0] | v1[1] | v1[2] | v1[3]);
        if (att == 0 ? !redo : att == 1 + kh) {
            st16(ws + WS_O + (size_t)((qt + kh) * 32 + head) * 1024, lane * 16, ov);
            drain();
            st4(ws + WS_FO + ((qt + kh) * 32 + head) * 128, tag);
        }
        asm volatile("" : "+s"(att));
        if (att == 2 || (att == 0 && !redo)) break;
        drain();
        wgbar();
        qa = qb = ws + WS_Q + (size_t)(head * 64 + qt + att) * 1024;
        bf16x8 qf;
        asm volatile("global_load_dwordx4 %0, %1, %2 sc1\n\ts_waitcnt vmcnt(0)" : "=v"(qf) : "v"(lane * 16), "s"(qa) : "memory");
        float mx = -INFINITY;
#pragma clang loop unroll(disable)
        for (int t = 0; t < 64; ++t) {
            bf16x8 kf;
            asm volatile("global_load_dwordx4 %0, %1, %2 sc1\n\ts_waitcnt vmcnt(0)" : "=v"(kf) : "v"(lane * 16), "s"(ksrc + t * 1024) : "memory");
            const f32x16 s = MFMA32(kf, qf, f32x16{});
#pragma unroll
            for (int i = 0; i < 16; ++i) mx = fmaxf(mx, s[i]);
        }
        attn_prep(R, -swap32_max(mx), a, w);
    }
    ST(3); ST_OUT(0);
}

#define BG1 ""
#define BG2 ""
#define BG3 "s_sleep 1\n\t"
#define SISSUE                                                             \
    "s_mov_b32 m0, s44\n\t"                                                 \
    "s_add_u32 s45, %[ow], %[wst]\n\t"                                      \
    "buffer_load_dwordx4 %[g0], %[rw], %[ow] offen lds\n\t"                 \
    "buffer_load_dwordx4 %[g1], %[rw], %[ow] offen offset:1024 lds\n\t"     \
    "s_add_u32 m0, s44, 8192\n\t"                                           \
    "s_add_u32 s46, %[od], 4096\n\t"                                        \
    "buffer_load_dwordx4 %[d0], %[rd], %[od] offen sc1 lds\n\t"             \
    "buffer_load_dwordx4 %[d0], %[rd], %[od] offen offset:1024 sc1 lds\n\t" \
    "s_add_u32 s44, s44, 0x4000\n\t"                                        \
    "s_and_b32 s44, s44, 0xffff\n\t"                                        \
    "s_cmp_eq_u32 %[od], %[de]\n\t"                                         \
    "s_cselect_b32 %[ow], 0, s45\n\t"                                       \
    "s_cselect_b32 %[od], 0, s46\n\t"
struct SG { u32x4 rw, rd; unsigned wst, de, ow, od, a, b, c; };
#define WISSUE                                                              \
    "s_and_b32 s45, s44, 0xc000\n\t"                                        \
    "s_add_u32 m0, s45, %[lo2]\n\t"                                         \
    "s_lshl_b32 s45, s45, 3\n\t"                                            \
    "buffer_load_dwordx4 %[o0], %[ro], s45 offen lds\n\t"                   \
    "buffer_load_dwordx4 %[o1], %[ro], s45 offen offset:1024 lds\n\t"       \
    "buffer_load_dwordx4 %[o2], %[ro], s45 offen offset:2048 lds\n\t"       \
    "buffer_load_dwordx4 %[o3], %[ro], s45 offen offset:3072 lds\n\t"       \
    "s_add_u32 s44, s44, 0x4000\n\t"                                        \
    "s_and_b32 s44, s44, 0xffff\n\t"
template <int WO>
DI void sgemm_load(SG& G, int n, int lw, u32x4 ro, unsigned o0) {
    asm volatile(
        "s_mov_b32 s44, %[lo]\n\t"
        "s_sub_u32 s47, %[n], 3\n\t"
        SISSUE SISSUE SISSUE
        "LL_loop%=:\n\t"
        "s_waitcnt vmcnt(8)\n\t"
        "s_barrier\n\t"
        SISSUE
        "s_sub_u32 s47, s47, 1\n\t"
        "s_cmp_lg_u32 s47, 0\n\t"
        "s_cbranch_scc1 LL_loop%=\n\t"
        ".if %c[wo]\n\t"
        "s_waitcnt vmcnt(8)\n\ts_barrier\n\t" WISSUE
        "s_waitcnt vmcnt(8)\n\ts_barrier\n\t" WISSUE
        "s_waitcnt vmcnt(8)\n\ts_barrier\n\t" WISSUE
        "s_barrier\n\t" WISSUE
        ".else\n\t"
        "s_waitcnt vmcnt(8)\n\ts_barrier\n\t"
        "s_waitcnt vmcnt(4)\n\ts_barrier\n\t"
        "s_waitcnt vmcnt(0)\n\ts_barrier\n\t"
        "s_barrier\n\t"
        ".endif\n\t"
        : [ow] "+s"(G.ow), [od] "+s"(G.od)
        : [wst] "s"(G.wst), [rw] "s"(G.rw), [rd] "s"(G.rd), [lo] "s"(lw * 2048), [de] "s"(G.de), [n] "s"(n), [g0] "v"(G.a), [g1] "v"(G.b), [d0] "v"(G.c),
          [wo] "i"(WO), [ro] "s"(ro), [lo2] "s"(lw * 4096), [o0] "v"(o0), [o1] "v"(o0 + 7168), [o2] "v"(o0 + 14336), [o3] "v"(o0 + 21504)
        : "memory", "scc", "s44", "s45", "s46", "s47");
}
#define SRD3(B, SLOT, K, TD)                                                                         \
    "ds_read_b64_tr_b16 v[" #B ":" #B "+1], %[t] offset:" #SLOT "*16384+" #K "*2048\n\t"              \
    "ds_read_b64_tr_b16 v[" #B "+2:" #B "+3], %[t] offset:" #SLOT "*16384+" #K "*2048+" #TD "\n\t"    \
    "ds_read_b128 v[" #B "+4:" #B "+7], %[dl] offset:" #SLOT "*16384+" #K "*1024\n\t"
#define SSTAGE(SLOT, MM, TD, N0, N1, N2, N3, P0, P1, P2, P3)                          \
    "s_waitcnt lgkmcnt(0)\n\t"                                                        \
    "s_barrier\n\t"                                                                   \
    MM(P0) SRD3(N0, SLOT, 0, TD) SRD3(N1, SLOT, 1, TD) BG1                            \
    MM(P1) SRD3(N2, SLOT, 2, TD) SRD3(N3, SLOT, 3, TD) BG2                            \
    MM(P2) BG3 MM(P3)
#define SST_E(SLOT, MM, TD) SSTAGE(SLOT, MM, TD, 32, 40, 48, 56, 96, 104, 112, 120)
#define SST_O(SLOT, MM, TD) SSTAGE(SLOT, MM, TD, 96, 104, 112, 120, 32, 40, 48, 56)
#define SMM_NONE(P) ""
#define SMM_WA(P) MF("v[64:79]", "v[" #P ":" #P "+3]", "v[" #P "+4:" #P "+7]", "v[64:79]")
#define SMM_WB(P) MF("v[80:95]", "v[" #P "+4:" #P "+7]", "v[" #P ":" #P "+3]", "v[80:95]")
#define SGEMM_ASM(ACC, MM, TD, NLOOP, TAIL)                                                                       \
    asm volatile(                                                                                                 \
        "s_mov_b32 s44, " #NLOOP "\n\t"                                                                           \
        "s_barrier\n\t"                                                                                           \
        SRD3(32, 0, 0, TD) SRD3(40, 0, 1, TD) SRD3(48, 0, 2, TD) SRD3(56, 0, 3, TD)                               \
        "LS_loop%=:\n\t"                                                                                          \
        SST_O(1, MM, TD) SST_E(2, MM, TD) SST_O(3, MM, TD) SST_E(0, MM, TD)                                       \
        "s_sub_u32 s44, s44, 1\n\t"                                                                               \
        "s_cmp_lg_u32 s44, 0\n\t"                                                                                 \
        "s_cbranch_scc1 LS_loop%=\n\t"                                                                            \
        TAIL                                                                                                      \
        "s_waitcnt lgkmcnt(0)\n\t"                                                                                \
        MM(96) BG3 MM(104) BG3 MM(112) BG3 MM(120)                                                                \
        "s_nop 15\n\t"                                                                                            \
        : ACC(acc)                                                                                                \
        : [t] "v"(G.a), [dl] "v"(G.b)                                                                             \
        : "memory", "scc", "s44",                                                                                 \
          "v32", "v33", "v34", "v35", "v36", "v37", "v38", "v39", "v40", "v41", "v42", "v43", "v44", "v45", "v46", "v47", \
          "v48", "v49", "v50", "v51", "v52", "v53", "v54", "v55", "v56", "v57", "v58", "v59", "v60", "v61", "v62", "v63", \
          "v96", "v97", "v98", "v99", "v100", "v101", "v102", "v103", "v104", "v105", "v106", "v107", "v108", "v109", "v110", "v111", \
          "v112", "v113", "v114", "v115", "v116", "v117", "v118", "v119", "v120", "v121", "v122", "v123", "v124", "v125", "v126", "v127")

template <int MODE>
DI SG sgemm_setup(const char* wbase, unsigned wbytes, int wrow, const char* dbase, int dblk, int rot, int w) {
    const int lane = lane_id(), row = lane >> 3, lw = w & 3;
    const int q = (lane & 15) >> 2, h2 = lane >> 5;
    SG G;
    G.rw = srd(wbase, wbytes); G.rd = srd(dbase, 2 * dblk);
    G.wst = 64 * wrow; G.de = (MODE == 0 ? 31 : 21) * 4096; G.ow = rot * G.wst; G.od = rot * 4096;
    if (w >= 4) {
        G.a = (16 * lw + row) * wrow + (((lane & 7) ^ (((row >> 1) & 1) << 2)) << 4);
        G.b = G.a + 8 * wrow - 1024;
        G.c = (lw >> 1) * dblk + (lw & 1) * 2048 + lane * 16;
    } else {
        G.a = ((MODE == 0 ? 8 : 4) * h2 + q) * 128 + (((lw & 1) ^ ((q >> 1) & 1)) << 6) + ((lane >> 4) & 1) * 32 + (lane & 3) * 8;
        G.b = 8192 + (lw >> 1) * 4096 + lane * 16;
        G.c = 0;
    }
    asm volatile("" : "+v"(G.a), "+v"(G.b), "+v"(G.c));
    return G;
}
#define ACC0 "+{v[64:79]}"
#define ACC1 "+{v[80:95]}"
template <int MODE>
DI void sgemm_run(f32x16& acc, SG& G) {
    if (MODE == 0) SGEMM_ASM(ACC0, SMM_WA, 512, 7, SST_O(1, SMM_WA, 512) SST_E(2, SMM_WA, 512) SST_O(3, SMM_WA, 512));
    else SGEMM_ASM(ACC1, SMM_WB, 1024, 5, SST_O(1, SMM_WB, 1024));
}
DI f32x4 sub4(const f32x16& a, int g) { return f32x4{a[4 * g], a[4 * g + 1], a[4 * g + 2], a[4 * g + 3]}; }

DI unsigned gelu2(float a, float b) {
    constexpr float c1 = -2.f * 0.7978845608028654f * 1.4426950408889634f, c2 = c1 * 0.044715f;
    const f32x2 u = {a, b};
    const f32x2 z = u * (u * u * c2 + c1);
    const f32x2 d = f32x2{__builtin_amdgcn_exp2f(z[0]), __builtin_amdgcn_exp2f(z[1])} + 1.f;
    const f32x2 o = u * f32x2{__builtin_amdgcn_rcpf(d[0]), __builtin_amdgcn_rcpf(d[1])};
    return cvtpk(o[0], o[1]);
}

#define WRD(R0, R1, R2, R3, STEP)                                                    \
    "ds_read_b64_tr_b16 " R0 ", %[t0] offset:" #STEP "*2048\n\t"                      \
    "ds_read_b64_tr_b16 " R1 ", %[t0] offset:" #STEP "*2048+1024\n\t"                 \
    "ds_read_b64_tr_b16 " R2 ", %[t1] offset:" #STEP "*2048\n\t"                      \
    "ds_read_b64_tr_b16 " R3 ", %[t1] offset:" #STEP "*2048+1024\n\t"
#define WRD0(S) WRD("v[32:33]", "v[34:35]", "v[36:37]", "v[38:39]", S)
#define WRD1(S) WRD("v[40:41]", "v[42:43]", "v[44:45]", "v[46:47]", S)
#define WRD2(S) WRD("v[48:49]", "v[50:51]", "v[52:53]", "v[54:55]", S)
#define WRD3(S) WRD("v[56:57]", "v[58:59]", "v[60:61]", "v[62:63]", S)
#define WMM(WAIT, W0, W1, O) "s_waitcnt " WAIT "\n\t" MF("v[96:111]", W0, O, "v[96:111]") MF("v[112:127]", W1, O, "v[112:127]")
#define OLD(K, R, V, OFF)                                             \
    "5:\n\t"                                                           \
    "s_bitcmp1_b32 s40, " #K "\n\t"                                    \
    "s_cbranch_scc1 6f\n\t"                                            \
    "global_load_dword v24, %[fo], %[f] sc1\n\t"                       \
    "s_sub_u32 s41, s41, 1\n\t"                                        \
    "s_cselect_b32 s42, -1, 0\n\t"                                     \
    "s_waitcnt vmcnt(0)\n\t"                                           \
    "v_cmp_eq_u32 vcc, %[tag], v24\n\t"                                \
    "s_or_b32 s40, vcc_lo, s42\n\t"                                    \
    "s_bitcmp1_b32 s40, " #K "\n\t"                                    \
    "s_cbranch_scc1 6f\n\t"                                            \
    "s_sleep 3\n\t"                                                    \
    "s_branch 5b\n\t"                                                  \
    "6:\n\t"                                                           \
    "global_load_dwordx4 " R ", " V ", %[o] offset:" #OFF " sc1\n\t"
DI void wo_gemm(f32x16& a0, f32x16& a1, f32x4& k0, f32x4& k1, float& k2, const char* osrc, const char* fsrc, unsigned tag, int w) {
    const int lane = lane_id();
    const int q = (lane & 15) >> 2, h2 = lane >> 5;
    const unsigned tb = (w >> 1) * 16384 + (4 * h2 + q) * 128 + ((lane >> 4) & 1) * 32 + (lane & 3) * 8;
    const unsigned t0 = tb + ((q >> 1) & 1) * 64, t1 = tb + (((q >> 1) & 1) ^ 1) * 64;
    const unsigned d0 = lane * 16, d1 = d0 + 4096, fo = (lane & 7) * 128;
    asm volatile(
        WRD0(0) WRD1(1) WRD2(2) WRD3(3)
        "s_mov_b32 s40, 0\n\t"
        "s_mov_b32 s41, 0x100000\n\t"
        "s_setprio 0\n\t"
        OLD(0, "v[64:67]", "%[d0]", 0) OLD(1, "v[68:71]", "%[d0]", 1024) OLD(2, "v[72:75]", "%[d0]", 2048) OLD(3, "v[76:79]", "%[d0]", 3072)
        OLD(4, "v[80:83]", "%[d1]", 0) OLD(5, "v[84:87]", "%[d1]", 1024) OLD(6, "v[88:91]", "%[d1]", 2048) OLD(7, "v[92:95]", "%[d1]", 3072)
        "s_setprio 3\n\t"
        "s_waitcnt vmcnt(7) lgkmcnt(0)\n\t"
        MF("v[96:111]", "v[32:35]", "v[64:67]", "0") MF("v[112:127]", "v[36:39]", "v[64:67]", "0")
        WRD0(4)
        WMM("vmcnt(6)", "v[40:43]", "v[44:47]", "v[68:71]") WRD1(5)
        WMM("vmcnt(5)", "v[48:51]", "v[52:55]", "v[72:75]") WRD2(6)
        WMM("vmcnt(4)", "v[56:59]", "v[60:63]", "v[76:79]") WRD3(7)
        WMM("vmcnt(3) lgkmcnt(12)", "v[32:35]", "v[36:39]", "v[80:83]")
        WMM("vmcnt(2) lgkmcnt(0)", "v[40:43]", "v[44:47]", "v[84:87]")
        WMM("vmcnt(1)", "v[48:51]", "v[52:55]", "v[88:91]")
        "s_barrier\n\t"
        WMM("vmcnt(0)", "v[56:59]", "v[60:63]", "v[92:95]")
        "s_nop 15\n\t"
        : "={v[96:111]}"(a0), "={v[112:127]}"(a1),
          "+{v[12:15]}"(k0), "+{v[16:19]}"(k1), "+{v20}"(k2)
        : [o] "s"(osrc), [f] "s"(fsrc), [tag] "s"(tag), [d0] "v"(d0), [d1] "v"(d1), [t0] "v"(t0), [t1] "v"(t1), [fo] "v"(fo)
        : "memory", "scc", "vcc", "s40", "s41", "s42", "v24",
          "v32", "v33", "v34", "v35", "v36", "v37", "v38", "v39", "v40", "v41", "v42", "v43", "v44", "v45", "v46", "v47",
          "v48", "v49", "v50", "v51", "v52", "v53", "v54", "v55", "v56", "v57", "v58", "v59", "v60", "v61", "v62", "v63",
          "v64", "v65", "v66", "v67", "v68", "v69", "v70", "v71", "v72", "v73", "v74", "v75", "v76", "v77", "v78", "v79",
          "v80", "v81", "v82", "v83", "v84", "v85", "v86", "v87", "v88", "v89", "v90", "v91", "v92", "v93", "v94", "v95");
}

DI void mixer_role(const char* x, const char* wqkv, const char* gw, const char* wo, const char* w1, const char* b1, const char* w2, const char* b2,
                   char* ws, char* out, char* lds, int bi, int w) {
    const int j = bi & 7, i = bi >> 3;
    __builtin_amdgcn_s_setprio(LNP);
    ST_DECL; ST(0);
    const unsigned ep = ld4(ws + WS_EPOCH);
    unsigned tag;
    {
        const int lane = lane_id(), h2 = lane >> 5, tid = 64 * w + lane;
        const u32x4 xr = *(GAS const u32x4*)(x + (size_t)(8 * bi + w) * 1024 + lane * 16);
        const u32x4 gr = *(GAS const u32x4*)(gw + lane * 16);
        tag = __builtin_amdgcn_readfirstlane(ep) * 0x9E3779B1u + 0x7F4A7C15u;
        float v[8], gg[8];
#pragma unroll
        for (int k = 0; k < 4; ++k) { v[2 * k] = bflo(xr[k]); v[2 * k + 1] = bfhi(xr[k]); gg[2 * k] = bflo(gr[k]); gg[2 * k + 1] = bfhi(gr[k]); }
        float s1 = 0.f, s3 = 0.f;
#pragma unroll
        for (int k = 0; k < 8; ++k) { s1 += v[k]; s3 += v[k] * gg[k]; }
        const float mu = wave_sum(s1) * (1.f / 512.f);
        s3 = wave_sum(s3);
        float s2 = 0.f;
#pragma unroll
        for (int k = 0; k < 8; ++k) { v[k] -= mu; s2 += v[k] * v[k]; }
        const float rs = __builtin_amdgcn_rsqf(wave_sum(s2) * (1.f / 512.f) + 1e-5f);
        u32x4 hv;
#pragma unroll
        for (int k = 0; k < 4; ++k) hv[k] = cvtpk(v[2 * k] * rs, v[2 * k + 1] * rs);
        *(LAS u32x4*)((LAS char*)lds + w * 1024 + lane * 16) = hv;
        const float g = __builtin_amdgcn_rcpf(1.f + __builtin_amdgcn_exp2f(-1.4426950408889634f * s3));
        st4(ws + WS_G + (8 * bi + w) * 4, uni(g));
        wgbar();
        LAS const unsigned short* lb = (LAS const unsigned short*)lds + tid;
#pragma unroll
        for (int k = 0; k < 4; ++k) hv[k] = (unsigned)lb[(2 * k) * 512] | ((unsigned)lb[(2 * k + 1) * 512] << 16);
        st16(ws + WS_H + (size_t)(2 * w * 128 + (bi >> 1)) * 1024 + (bi & 1) * 512, h2 * 131072 + (lane & 31) * 16, hv);
        drain();
        wgbar();
        if (w == 0) st4(ws + WS_FL + bi * 128, tag);
    }
    __builtin_amdgcn_s_setprio(3);
    const int wb = w & 1, db = (w >> 1) & 1, hf = w >> 2;
    const int L = (j >> 1) * 22 + i, ft = L >> 2, dt1 = 4 * (j & 1) + (L & 3);
    const int st = 8 * (j >> 1) + (i & 7), dt = 4 * (j & 1) + (i >> 3);
    SG G1, G2;
    unsigned ex, l16, og, wof;
    {
        const int lane = lane_id();
        og = (64 * st + 32 * wb + (lane & 31)) * 4;
        G1 = sgemm_setup<0>(w1 + ft * 128, 2048 * 2752 - ft * 128, 2752, ws + WS_H + (size_t)2 * dt1 * 131072, 131072, ROT1, w);
        G2 = sgemm_setup<1>(w2 + st * 128, 1376 * 4096 - st * 128, 4096, ws + WS_U + (size_t)2 * dt * 86 * 1024, 86 * 1024, ROT2, w);
        l16 = lane * 16;
        ex = 65536 + (w & 3) * 2048 + l16;
        wof = (32 * (w & 3) + (lane >> 3)) * 1024 + (((lane & 7) ^ (((lane >> 4) & 1) << 2)) << 4);
        asm volatile("" : "+v"(ex), "+v"(l16), "+v"(og), "+v"(wof));
    }
    {
        const int row = 16 + 8 * (j & 1) + (i & 7), col = 8 + 8 * (i >> 4) + 2 * (j >> 1) + ((i >> 3) & 1);
        if (w == 0) {
            *(LAS unsigned*)QCNT = 0u;
            wait_flags<2>(ws + WS_FA + bi * 128, 0, tag);
        }
        wgbar();
        if (w >= 4) qkv2_loader(x, wqkv, row, col, w - 4);
        else qkv2_body(ws, row, col, bi, w, tag);
    }
    ST(6);
    if (i < 22) {
        if (w >= 4) wait_flags<8>(ws + WS_FL + (w - 4) * 8192, l16 * 8, tag);
        if (w == 4) wait_qkv(ws, bi, tag);
        if (BSLEEP) __builtin_amdgcn_s_sleep(BSLEEP);
        wgbar();
        ST(7);
        f32x4 v0, v1;
        if (w >= 4) {
            sgemm_load<0>(G1, 32, w - 4, G1.rw, 0);
            v0 = *(LAS const f32x4*)(uintptr_t)ex; v1 = *(LAS const f32x4*)(uintptr_t)(ex + 1024);
        } else {
            f32x16 acc1;
            const char* bsrc = b1 + (64 * ft + (ft < 21 ? 32 * wb : 0)) * 2 + (l16 >> 9) * 8;
#pragma unroll
            for (int k = 0; k < 4; ++k) {
                const u32x2 bv = *(GAS const u32x2*)(bsrc + 16 * k);
                acc1[4 * k] = bflo(bv[0]); acc1[4 * k + 1] = bfhi(bv[0]); acc1[4 * k + 2] = bflo(bv[1]); acc1[4 * k + 3] = bfhi(bv[1]);
            }
            sgemm_run<0>(acc1, G1);
            *(LAS f32x4*)(uintptr_t)ex = sub4(acc1, 2); *(LAS f32x4*)(uintptr_t)(ex + 1024) = sub4(acc1, 3);
            wgbar();
            v0 = sub4(acc1, 0); v1 = sub4(acc1, 1);
        }
        ST(1);
        const u32x4 o = {gelu2(v0[0], v0[1]), gelu2(v0[2], v0[3]), gelu2(v1[0], v1[1]), gelu2(v1[2], v1[3])};
        if (ft < 21 || wb == 0) st16(ws + WS_U + (size_t)((2 * dt1 + db) * 86 + 4 * ft + 2 * wb + hf) * 1024, l16, o);
        drain();
        wgbar();
        if (w == 0) st4(ws + WS_FU + (dt1 * 32 + ft) * 128, tag);
    }
    ST(2);
    if (bi == 255 && w == 1) {
        wait_flags256(ws + WS_FQ, l16 * 8, tag);
        wait_flags256(ws + WS_FL, l16 * 8, tag);
        st4(ws + WS_EPOCH, tag);
    }
    if (w == 0) wait_flags<8>(ws + WS_FU + dt * 4096, (l16 < 22 * 16 ? l16 : 0) * 8, tag);
    wgbar();
    ST(3);
    f32x4 bp0, bp1;
    float g;
    {
        asm volatile("global_load_dword %0, %1, %2 sc1" : "=v"(g) : "v"(og), "s"(ws + WS_G) : "memory");
        if (w >= 4) {
            sgemm_load<1>(G2, 22, w - 4, srd(wo + dt * 128, 524288 - dt * 128), wof);
            bp0 = *(LAS const f32x4*)(uintptr_t)ex; bp1 = *(LAS const f32x4*)(uintptr_t)(ex + 1024);
        } else {
            const float bb = bflo(*(GAS const unsigned short*)(b2 + (og >> 1)));
            f32x16 acc2 = {bb, bb, bb, bb, bb, bb, bb, bb, bb, bb, bb, bb, bb, bb, bb, bb};
            sgemm_run<1>(acc2, G2);
            *(LAS f32x4*)(uintptr_t)ex = sub4(acc2, 2); *(LAS f32x4*)(uintptr_t)(ex + 1024) = sub4(acc2, 3);
            wgbar();
            bp0 = sub4(acc2, 0); bp1 = sub4(acc2, 1);
            drain();
        }
        asm volatile("" : "+v"(g));
        ST(4);
        bp0 *= 1.f - g;
        bp1 *= 1.f - g;
        asm volatile("" : "+v"(bp0), "+v"(bp1));
    }
    drain();
    wgbar();
    ST(5);
    f32x16 a0, a1;
    wo_gemm(a0, a1, bp0, bp1, g, ws + WS_O + (size_t)((2 * st + wb) * 32 + 8 * (w >> 1)) * 1024,
            ws + WS_FO + ((2 * st + wb) * 32 + 8 * (w >> 1)) * 128, tag, w);
    {
        const int lane = lane_id(), kq = 2 * db + hf;
        LAS char* ex = (LAS char*)lds + w * 8192 + lane * 16;
#pragma unroll
        for (int k = 0; k < 4; ++k) { *(LAS f32x4*)(ex + k * 1024) = sub4(a0, k); *(LAS f32x4*)(ex + 4096 + k * 1024) = sub4(a1, k); }
        wgbar();
        LAS const char* exr = (LAS const char*)lds + wb * 8192 + kq * 2048 + lane * 16;
        f32x4 s0 = *(LAS const f32x4*)exr, s1 = *(LAS const f32x4*)(exr + 1024);
#pragma unroll
        for (int k = 1; k < 4; ++k) { s0 += *(LAS const f32x4*)(exr + k * 16384); s1 += *(LAS const f32x4*)(exr + k * 16384 + 1024); }
        s0 = s0 * g + bp0;
        s1 = s1 * g + bp1;
        const auto r0 = __builtin_amdgcn_permlane32_swap(cvtpk(s0[0], s0[1]), cvtpk(s1[0], s1[1]), false, false);
        const auto r1 = __builtin_amdgcn_permlane32_swap(cvtpk(s0[2], s0[3]), cvtpk(s1[2], s1[3]), false, false);
        *(GAS u32x4*)(out + (size_t)(64 * st + 32 * wb) * 1024 + (64 * dt + 16 * kq) * 2 + (lane & 31) * 1024 + (lane >> 5) * 16) = u32x4{r0[0], r1[0], r0[1], r1[1]};
    }
    ST_OUT(0);
}

constexpr int MK_LDS = 81920;
__global__ __launch_bounds__(512, 4) void mk(const char* x, const char* wqkv, const char* wo, const char* w1, const char* b1,
                                             const char* w2, const char* b2, const char* gw, char* ws, char* out) {
    extern __shared__ __attribute__((aligned(16))) char lds[];
    const int w = __builtin_amdgcn_readfirstlane(threadIdx.x >> 6);
    if (blockIdx.x < 256) attn_role(x, wqkv, ws, lds, blockIdx.x, w);
    else mixer_role(x, wqkv, gw, wo, w1, b1, w2, b2, ws, out, lds, blockIdx.x - 256, w);
}

extern "C" void kernel_launch(void* const* d_in, const int* in_sizes, int n_in, void* d_out, int out_size, void* d_ws, size_t ws_size,
                              hipStream_t stream) {
    (void)hipFuncSetAttribute((const void*)mk, hipFuncAttributeMaxDynamicSharedMemorySize, MK_LDS);
    mk<<<512, 512, MK_LDS, stream>>>((const char*)d_in[0], (const char*)d_in[1], (const char*)d_in[2], (const char*)d_in[4],
                                     (const char*)d_in[5], (const char*)d_in[6], (const char*)d_in[7], (const char*)d_in[8],
                                     (char*)d_ws, (char*)d_out);
}
```

```cpp
#include <hip/hip_runtime.h>
#include <hip/hip_bf16.h>
#include <stdint.h>

typedef __attribute__((ext_vector_type(8))) short bf16x8;
typedef __attribute__((ext_vector_type(4))) short s16x4;
typedef __attribute__((ext_vector_type(16))) float f32x16;
typedef __attribute__((ext_vector_type(4))) float f32x4;
typedef __attribute__((ext_vector_type(2))) float f32x2;
typedef __attribute__((ext_vector_type(4))) unsigned u32x4;
typedef __attribute__((ext_vector_type(2))) unsigned u32x2;
#define LAS __attribute__((address_space(3)))
#define GAS __attribute__((address_space(1)))
#define DI __device__ __forceinline__
#define MFMA32(a, b, c) __builtin_amdgcn_mfma_f32_32x32x16_bf16((a), (b), (c), 0, 0, 0)

#define LSLEEP 32
#define ROT1 ((4 * dt1 + 10 * ft) & 31)
#define ROT2 (((st + dt) & 1) * 11)
#define GATEQ 1
#define BSLEEP 0
#define LNP 0
#define BGATE 1
#define FAEARLY 1
#define PFA 3
#define PFD 2
#define QROT 4
#define QROTM 4
constexpr int S_ = 2048, D_ = 512, F_ = 1376;
constexpr size_t WS_EPOCH = 0;
constexpr size_t WS_G = 4096;
constexpr size_t WS_FL = 98304;
constexpr size_t WS_FQ = 458752;
constexpr size_t WS_FU = 65536;
constexpr size_t WS_FA = 393216;
constexpr size_t WS_FO = 131072;
constexpr size_t WS_Q = 1u << 20;
constexpr size_t WS_K = WS_Q + (2u << 20);
constexpr size_t WS_V = WS_K + (2u << 20);
constexpr size_t WS_H = WS_V + (2u << 20);
constexpr size_t WS_U = WS_H + (2u << 20);
constexpr size_t WS_O = WS_U + (2u << 20);

#define ST_DECL
#define ST_ARG
#define ST_PASS
#define ST(i)
#define ST_OUT(k)
DI unsigned cvtpk(float lo, float hi) {
    typedef float f32x2_t __attribute__((ext_vector_type(2)));
    typedef __bf16 bf16x2_t __attribute__((ext_vector_type(2)));
    return __builtin_bit_cast(unsigned, __builtin_convertvector(f32x2_t{lo, hi}, bf16x2_t));
}
DI float bflo(unsigned u) { return __uint_as_float(u << 16); }
DI float bfhi(unsigned u) { return __uint_as_float(u & 0xffff0000u); }
DI s16x4 trread(LAS const char* p) {
    typedef short v4i16_t __attribute__((ext_vector_type(4)));
    return __builtin_bit_cast(s16x4, __builtin_amdgcn_ds_read_tr16_b64_v4i16((LAS v4i16_t*)p));
}
DI void glds(unsigned lds, unsigned voff, const void* sbase) {
    asm volatile("s_mov_b32 m0, %0\n\ts_nop 0\n\tglobal_load_lds_dwordx4 %1, %2" ::"s"(lds), "v"(voff), "s"(sbase) : "memory");
}
template <int CTRL> DI float dpp(float v) {
    return __int_as_float(__builtin_amdgcn_update_dpp(0, __float_as_int(v), CTRL, 0xf, 0xf, true));
}
DI float rl(float v, int l) { return __int_as_float(__builtin_amdgcn_readlane(__float_as_int(v), l)); }
DI float wave_sum(float v) {
    v += dpp<0xB1>(v); v += dpp<0x4E>(v); v += dpp<0x141>(v); v += dpp<0x140>(v);
    return (rl(v, 0) + rl(v, 16)) + (rl(v, 32) + rl(v, 48));
}
DI float wave_max(float v) {
    v = fmaxf(v, dpp<0xB1>(v)); v = fmaxf(v, dpp<0x4E>(v)); v = fmaxf(v, dpp<0x141>(v)); v = fmaxf(v, dpp<0x140>(v));
    return fmaxf(fmaxf(rl(v, 0), rl(v, 16)), fmaxf(rl(v, 32), rl(v, 48)));
}
DI float swap32_add(float v) {
    auto rr = __builtin_amdgcn_permlane32_swap(__float_as_uint(v), __float_as_uint(v), false, false);
    return __uint_as_float(rr[0]) + __uint_as_float(rr[1]);
}
DI float swap32_max(float v) {
    auto rr = __builtin_amdgcn_permlane32_swap(__float_as_uint(v), __float_as_uint(v), false, false);
    return fmaxf(__uint_as_float(rr[0]), __uint_as_float(rr[1]));
}

DI int lane_id() {
    int l;
    asm volatile("v_mbcnt_lo_u32_b32 %0, -1, 0\n\tv_mbcnt_hi_u32_b32 %0, -1, %0" : "=v"(l));
    __builtin_assume(l >= 0 && l < 64);
    return l;
}
DI void st16(const char* sbase, unsigned voff, u32x4 v) {
    asm volatile("global_store_dwordx4 %0, %1, %2 sc1\n\ts_nop 1" ::"v"(voff), "v"(v), "s"(sbase) : "memory");
}
DI void st4(const char* p, unsigned v) {
    unsigned t0, t1;
    asm volatile("s_nop 1\n\tv_mov_b32 %0, %2\n\tv_mov_b32 %1, 0\n\ts_mov_b64 exec, 1\n\tglobal_store_dword %1, %0, %3 sc1\n\ts_mov_b64 exec, -1"
                 : "=&v"(t0), "=&v"(t1) : "s"(v), "s"(p) : "memory");
}
DI unsigned uni(float v) { return __builtin_amdgcn_readfirstlane(__float_as_uint(v)); }
DI unsigned ld4(const char* p) { return __hip_atomic_load((GAS const unsigned*)p, __ATOMIC_RELAXED, __HIP_MEMORY_SCOPE_AGENT); }
DI void warm(const char* base, int nchunks, int first, unsigned l64) {
#pragma clang loop unroll(disable)
    for (int n = first; n < nchunks; n += 640) {
        const char* p = base + (size_t)((n & 1) * (nchunks >> 1) + (n >> 1)) * 4096;
        asm volatile("s_mov_b32 m0, 0x13e00\n\ts_nop 0\n\tglobal_load_lds_dword %0, %1" ::"v"(l64), "s"(p) : "memory");
    }
}
DI void drain() { asm volatile("s_waitcnt vmcnt(0)" ::: "memory"); }
DI void wgbar() { asm volatile("s_waitcnt lgkmcnt(0)\n\ts_barrier" ::: "memory"); }
template <int SLEEP>
DI void wait_flags(const char* sbase, unsigned voff, unsigned tag) {
#pragma clang loop unroll(disable)
    for (unsigned spin = 0; spin < (1u << 20); ++spin) {
        unsigned f;
        asm volatile("global_load_dword %0, %1, %2 sc1\n\ts_waitcnt vmcnt(0)" : "=v"(f) : "v"(voff), "s"(sbase) : "memory");
        if (__all(f == tag)) break;
        __builtin_amdgcn_s_sleep(SLEEP);
    }
}

DI void wait_flags256(const char* sbase, unsigned voff, unsigned tag) {
#pragma clang loop unroll(disable)
    for (unsigned spin = 0; spin < (1u << 20); ++spin) {
        unsigned f0, f1, f2, f3;
        asm volatile("global_load_dword %0, %4, %5 sc1\n\tglobal_load_dword %1, %4, %6 sc1\n\tglobal_load_dword %2, %4, %7 sc1\n\t"
                     "global_load_dword %3, %4, %8 sc1\n\ts_waitcnt vmcnt(0)"
                     : "=&v"(f0), "=&v"(f1), "=&v"(f2), "=&v"(f3)
                     : "v"(voff), "s"(sbase), "s"(sbase + 8192), "s"(sbase + 16384), "s"(sbase + 24576) : "memory");
        if (__all(f0 == tag && f1 == tag && f2 == tag && f3 == tag)) break;
        __builtin_amdgcn_s_sleep(8);
    }
}

constexpr int QCNT = 73728;
#define MF(D, A, B, C) "v_mfma_f32_32x32x16_bf16 " D ", " A ", " B ", " C "\n\t"
DI u32x4 srd(const char* base, unsigned bytes) {
    return u32x4{(unsigned)(uintptr_t)base, (unsigned)((uintptr_t)base >> 32) & 0xffffu, bytes, 0x00020000u};
}
DI void qkv_store(char* ws, const f32x16& acc, int type, int head, int tile, int lane) {
    const float sc = (type == 0) ? 0.25f * 1.4426950408889634f : 1.f;
    const char* dst = ws + WS_Q + (size_t)type * (2u << 20) + (size_t)(head * 64 + tile) * 1024;
    const unsigned vo = type < 2 ? lane * 16 : ((lane >> 4) & 1) * 65536 + (lane >> 5) * 256 + (lane & 15) * 16;
#pragma unroll
    for (int hf = 0; hf < 2; ++hf) {
        u32x4 o;
#pragma unroll
        for (int i = 0; i < 4; ++i) o[i] = cvtpk(acc[8 * hf + 2 * i] * sc, acc[8 * hf + 2 * i + 1] * sc);
        st16(dst + hf * (type < 2 ? 65536 : 512), vo, o);
    }
}
DI void publish4(const char* flag, unsigned tag, int lane) {
    drain();
    unsigned arr = 0;
    if (lane == 0) arr = __hip_atomic_fetch_add((LAS unsigned*)QCNT, 1u, __ATOMIC_RELAXED, __HIP_MEMORY_SCOPE_WORKGROUP);
    if (__builtin_amdgcn_readfirstlane(arr) == 3) st4(flag, tag);
}
DI void qkv_loader(const char* x, const char* wqkv, int row, int col, int lw, const char* hint) {
    const int lane = lane_id(), xr = 16 * lw + (lane >> 3), wr = 16 * lw + (lane >> 4);
    const unsigned x0 = xr * 1024 + (((lane & 7) ^ ((xr >> 1) & 7)) << 4), x1 = (xr + 8) * 1024 + (((lane & 7) ^ (((xr + 8) >> 1) & 7)) << 4) - 1024;
    const unsigned w0 = wr * 3072 + (((lane & 15) ^ ((wr & 3) << 2)) << 4);
    const u32x4 rx = srd(x + (size_t)row * 65536, 65536), rw = srd(wqkv + col * 256, 512 * 3072 - col * 256);
    const int rot = (row + col) * QROTM & QROT;
    auto issue = [&](int s, int slot) {
        const int kc = (s + rot) & 7;
        asm volatile("s_mov_b32 m0, %[m]\n\ts_nop 0\n\t"
                     "buffer_load_dwordx4 %[x0], %[rx], %[ox] offen lds\n\t"
                     "buffer_load_dwordx4 %[x1], %[rx], %[ox] offen offset:1024 lds\n\t"
                     "s_mov_b32 m0, %[m2]\n\ts_nop 0\n\t"
                     "buffer_load_dwordx4 %[w0], %[rw], %[ow] offen lds\n\t"
                     "buffer_load_dwordx4 %[w1], %[rw], %[ow] offen offset:1024 lds\n\t"
                     "buffer_load_dwordx4 %[w2], %[rw], %[ow] offen offset:2048 lds\n\t"
                     "buffer_load_dwordx4 %[w3], %[rw], %[ow] offen offset:3072 lds"
                     :: [m] "s"(slot * 24576 + lw * 2048), [m2] "s"(slot * 24576 + 8192 + lw * 4096), [ow] "s"(kc * 196608), [ox] "s"(kc * 128),
                        [rw] "s"(rw), [rx] "s"(rx), [x0] "v"(x0), [x1] "v"(x1),
                        [w0] "v"(w0), [w1] "v"(w0 + 11264), [w2] "v"(w0 + 22528), [w3] "v"(w0 + 33792)
                     : "memory");
    };
    issue(0, 0);
    __builtin_amdgcn_s_setprio(2);
    issue(1, 1);
    __builtin_amdgcn_s_setprio(1);
    issue(2, 2);
    __builtin_amdgcn_s_setprio(3);
    asm volatile("s_waitcnt vmcnt(12)\n\ts_barrier" ::: "memory");
    int slot = 0;
#pragma clang loop unroll(disable)
    for (int s = 3; s < 8; ++s) {
        asm volatile("s_waitcnt vmcnt(6)\n\ts_barrier" ::: "memory");
        issue(s, slot);
        slot = slot == 2 ? 0 : slot + 1;
    }
    if (lw == 1) {
        st4(hint, __builtin_amdgcn_readfirstlane(*(LAS const unsigned*)(QCNT + 4)));
        asm volatile("s_waitcnt vmcnt(7)\n\ts_barrier\n\ts_waitcnt vmcnt(1)\n\ts_barrier" ::: "memory");
    } else
    asm volatile("s_waitcnt vmcnt(6)\n\ts_barrier\n\ts_waitcnt vmcnt(0)\n\ts_barrier" ::: "memory");
}
template <bool T>
DI void qkv_body(char* ws, char* lds, int row, int col, int a, int w, unsigned tag ST_ARG) {
    const int lane = lane_id(), r = lane & 31, h2 = lane >> 5, q = (lane & 15) >> 2;
    f32x16 acc[2] = {};
    const int xa0 = (32 * (w & 1) + r) * 128 + ((h2 ^ ((r >> 1) & 7)) << 4);
    const int wa0 = 8192 + (8 * h2 + q) * 256 + (((2 * (w >> 1)) ^ q) << 6) + ((lane >> 4) & 1) * 32 + (lane & 3) * 8;
    bf16x8 xf[4], wf[2][2];
    auto rdw = [&](LAS const char* sl, int st, int i) {
        LAS const char* pw = sl + (wa0 ^ (i << 6)) + st * 4096;
        const s16x4 lo = trread(pw), hi = trread(pw + 1024);
        wf[st & 1][i] = __builtin_shufflevector(lo, hi, 0, 1, 2, 3, 4, 5, 6, 7);
    };
    auto mm1 = [&](int st, int i) {
        acc[i] = T ? MFMA32(wf[st & 1][i], xf[st], acc[i]) : MFMA32(xf[st], wf[st & 1][i], acc[i]);
        __builtin_amdgcn_sched_barrier(0);
    };
    auto step = [&](int st, LAS const char* sl, bool bar) {
        const int sn = (st + 2) & 3;
        mm1(st, 0);
        if (bar) wgbar();
        xf[sn] = *(LAS const bf16x8*)(sl + (xa0 ^ (sn << 5))); rdw(sl, sn, 0); __builtin_amdgcn_sched_barrier(0);
        mm1(st, 1);
        rdw(sl, sn, 1); __builtin_amdgcn_sched_barrier(0);
    };
    if (w == 0) *(LAS u32x2*)QCNT = u32x2{0u, tag};
    wgbar();
    LAS const char* sl = (LAS const char*)lds;
#pragma unroll
    for (int st = 0; st < 2; ++st) { xf[st] = *(LAS const bf16x8*)(sl + (xa0 ^ (st << 5))); rdw(sl, st, 0); rdw(sl, st, 1); }
    __builtin_amdgcn_sched_barrier(0);
#pragma clang loop unroll(disable)
    for (int s = 0; s < 7; ++s) {
        step(0, sl, false); step(1, sl, false);
        sl = (sl == (LAS const char*)lds + 49152) ? (LAS const char*)lds : sl + 24576;
        step(2, sl, true); step(3, sl, false);
    }
    step(0, sl, false); step(1, sl, false);
    mm1(2, 0); mm1(2, 1); mm1(3, 0); mm1(3, 1);
    ST(4);
#pragma unroll
    for (int i = 0; i < 2; ++i) qkv_store(ws, acc[i], col >> 2, 8 * (col & 3) + 4 * (w >> 1) + 2 * i, 2 * row + (w & 1), lane);
    publish4(ws + WS_FQ + a * 128, tag, lane);
    ST(7);
}
DI void qkv2_loader(const char* x, const char* wqkv, int rowt, int col, int lw) {
    const int lane = lane_id(), row = 16 * lw + (lane >> 3);
    const unsigned w0 = row * 3072 + (((lane & 7) ^ (((row >> 1) & 1) << 2)) << 4), w1 = w0 + 8 * 3072 - 1024;
    const unsigned x0 = row * 1024 + (((lane & 7) ^ ((row >> 1) & 7)) << 4), x1 = (row + 8) * 1024 + (((lane & 7) ^ (((row + 8) >> 1) & 7)) << 4) - 1024;
    const u32x4 rx = srd(x + (size_t)rowt * 65536, 65536), rw = srd(wqkv + col * 128, 512 * 3072 - col * 128);
    auto issue = [&](int s) {
        asm volatile("s_mov_b32 m0, %[m]\n\ts_nop 0\n\t"
                     "buffer_load_dwordx4 %[w0], %[rw], %[ow] offen lds\n\t"
                     "buffer_load_dwordx4 %[w1], %[rw], %[ow] offen offset:1024 lds\n\t"
                     "s_add_u32 m0, %[m], 8192\n\ts_nop 0\n\t"
                     "buffer_load_dwordx4 %[x0], %[rx], %[ox] offen lds\n\t"
                     "buffer_load_dwordx4 %[x1], %[rx], %[ox] offen offset:1024 lds"
                     :: [m] "s"((s & 3) * 16384 + lw * 2048), [ow] "s"(s * 196608), [ox] "s"(s * 128), [rw] "s"(rw), [rx] "s"(rx),
                        [w0] "v"(w0), [w1] "v"(w1), [x0] "v"(x0), [x1] "v"(x1)
                     : "memory", "scc");
    };
    issue(0); issue(1); issue(2);
#pragma clang loop unroll(disable)
    for (int s = 3; s < 8; ++s) {
        asm volatile("s_waitcnt vmcnt(8)\n\ts_barrier" ::: "memory");
        issue(s);
    }
    asm volatile("s_waitcnt vmcnt(8)\n\ts_barrier\n\ts_waitcnt vmcnt(4)\n\ts_barrier\n\ts_waitcnt vmcnt(0)\n\ts_barrier" ::: "memory");
}
#define QRD3(B, SLOT, K)                                                                             \
    "ds_read_b64_tr_b16 v[" #B ":" #B "+1], %[t] offset:" #SLOT "*16384+" #K "*2048\n\t"              \
    "ds_read_b64_tr_b16 v[" #B "+2:" #B "+3], %[t] offset:" #SLOT "*16384+" #K "*2048+512\n\t"        \
    "ds_read_b128 v[" #B "+4:" #B "+7], %[x" #K "] offset:" #SLOT "*16384\n\t"
#define QSTAGE(SLOT, MM, N0, N1, N2, N3, P0, P1, P2, P3)                              \
    "s_waitcnt lgkmcnt(0)\n\t"                                                        \
    "s_barrier\n\t"                                                                   \
    MM(P0) QRD3(N0, SLOT, 0) MM(P1) QRD3(N1, SLOT, 1) MM(P2) QRD3(N2, SLOT, 2) MM(P3) QRD3(N3, SLOT, 3)
#define QST_E(SLOT, MM) QSTAGE(SLOT, MM, 32, 40, 48, 56, 96, 104, 112, 120)
#define QST_O(SLOT, MM) QSTAGE(SLOT, MM, 96, 104, 112, 120, 32, 40, 48, 56)
#define QMT(P) MF("v[16:31]", "v[" #P ":" #P "+3]", "v[" #P "+4:" #P "+7]", "v[16:31]")
#define QMV(P) MF("v[16:31]", "v[" #P "+4:" #P "+7]", "v[" #P ":" #P "+3]", "v[16:31]")
#define QTILE_ASM(MM)                                                                                             \
    asm volatile(                                                                                                 \
        "s_barrier\n\t"                                                                                           \
        QRD3(32, 0, 0) QRD3(40, 0, 1) QRD3(48, 0, 2) QRD3(56, 0, 3)                                               \
        QST_O(1, MM) QST_E(2, MM)                                                                                 \
        "s_mov_b32 s44, 0\n\t"                                                                                    \
        "LQ_loop%=:\n\t"                                                                                          \
        QST_O(3, MM)                                                                                              \
        "s_cmp_lg_u32 s44, 0\n\t"                                                                                 \
        "s_cbranch_scc1 LQ_end%=\n\t"                                                                             \
        QST_E(0, MM) QST_O(1, MM) QST_E(2, MM)                                                                    \
        "s_mov_b32 s44, 1\n\t"                                                                                    \
        "s_branch LQ_loop%=\n\t"                                                                                  \
        "LQ_end%=:\n\t"                                                                                           \
        "s_waitcnt lgkmcnt(0)\n\t"                                                                                \
        MM(96) MM(104) MM(112) MM(120)                                                                            \
        "s_nop 15\n\t"                                                                                            \
        : "+{v[16:31]}"(acc)                                                                                      \
        : [t] "v"(t), [x0] "v"(xa), [x1] "v"(xa ^ 32), [x2] "v"(xa ^ 64), [x3] "v"(xa ^ 96)                       \
        : "memory", "scc", "s44",                                                                                 \
          "v32", "v33", "v34", "v35", "v36", "v37", "v38", "v39", "v40", "v41", "v42", "v43", "v44", "v45", "v46", "v47", \
          "v48", "v49", "v50", "v51", "v52", "v53", "v54", "v55", "v56", "v57", "v58", "v59", "v60", "v61", "v62", "v63", \
          "v96", "v97", "v98", "v99", "v100", "v101", "v102", "v103", "v104", "v105", "v106", "v107", "v108", "v109", "v110", "v111", \
          "v112", "v113", "v114", "v115", "v116", "v117", "v118", "v119", "v120", "v121", "v122", "v123", "v124", "v125", "v126", "v127")
DI void qkv2_body(char* ws, int row, int col, int bi, int w, unsigned tag) {
    const int lane = lane_id(), r = lane & 31, h2 = lane >> 5, q = (lane & 15) >> 2;
    const unsigned t = (8 * h2 + q) * 128 + (((w & 1) ^ ((q >> 1) & 1)) << 6) + ((lane >> 4) & 1) * 32 + (lane & 3) * 8;
    const unsigned xa = 8192 + (32 * (w >> 1) + r) * 128 + ((h2 ^ ((r >> 1) & 7)) << 4);
    f32x16 acc = {};
    if (col < 16) QTILE_ASM(QMT);
    else QTILE_ASM(QMV);
    qkv_store(ws, acc, col >> 3, 4 * (col & 7) + 2 * (w & 1), 2 * row + (w >> 1), lane);
    publish4(ws + WS_FQ + 32768 + bi * 128, tag, lane);
}

#define EXP(a) "v_exp_f32 v" #a ", v" #a "\n\t"
#define CVT(d, a, b) "v_cvt_pk_bf16_f32 v" #d ", v" #a ", v" #b "\n\t"
#define ASTEP(c0, c1, c2, c3, c4, c5, c6, c7, c8, c9, c10, c11, c12, c13, c14, c15, M1, M2, CL, M3)                  \
    EXP(c0) EXP(c1) M1 EXP(c2) EXP(c3) EXP(c4) EXP(c5) M2 CL                                                          \
    EXP(c6) EXP(c7) CVT(96, c0, c1) CVT(97, c2, c3) CVT(98, c4, c5) CVT(99, c6, c7) EXP(c8) EXP(c9) M3                \
    EXP(c10) EXP(c11) EXP(c12) EXP(c13) EXP(c14) EXP(c15)                                                             \
    CVT(100, c8, c9) CVT(101, c10, c11) CVT(102, c12, c13) CVT(103, c14, c15)
#define R_OA "v[64:79]"
#define R_OB "v[80:95]"
#define R_SA "v[32:47]"
#define R_SB "v[48:63]"
#define R_NM "v[16:31]"
#define ATILE(KC, KN, V0C, V1C, V0N, V1N, KOFF, VOFF0, VOFF1, PRE)                                                    \
    ASTEP(32, 33, 34, 35, 36, 37, 38, 39, 40, 41, 42, 43, 44, 45, 46, 47,                                             \
          MF(R_OB, V1N, "v[100:103]", R_OB), MF(R_SB, KC, "v[8:11]", R_NM),                                           \
          "s_waitcnt lgkmcnt(0)\n\t" PRE                                                                              \
          "ds_read_b128 " KC ", v12 offset:" #KOFF "\n\t"                                                             \
          "ds_read_b128 " V0N ", v13 offset:" #VOFF0 "\n\t"                                                           \
          "ds_read_b128 " V1N ", v13 offset:" #VOFF1 "\n\t",                                                          \
          MF(R_OA, V0C, "v[96:99]", R_OA))                                                                            \
    ASTEP(48, 49, 50, 51, 52, 53, 54, 55, 56, 57, 58, 59, 60, 61, 62, 63,                                             \
          MF(R_OA, V1C, "v[100:103]", R_OA), MF(R_SA, KN, "v[4:7]", R_NM), "", MF(R_OB, V0C, "v[96:99]", R_OB))
#define ATILE_E(KOFF, VOFF0, VOFF1, PRE) ATILE("v[104:107]", "v[108:111]", "v[112:115]", "v[116:119]", "v[120:123]", "v[124:127]", KOFF, VOFF0, VOFF1, PRE)
#define ATILE_O(KOFF, VOFF0, VOFF1, PRE) ATILE("v[108:111]", "v[104:107]", "v[120:123]", "v[124:127]", "v[112:115]", "v[116:119]", KOFF, VOFF0, VOFF1, PRE)
#define MOV4(a, b, c, d, s) "v_mov_b32 v" #a ", " s "\n\tv_mov_b32 v" #b ", " s "\n\tv_mov_b32 v" #c ", " s "\n\tv_mov_b32 v" #d ", " s "\n\t"
#define AISSUE                                                   \
    "s_add_u32 m0, s44, %[lw]\n\t"                               \
    "s_add_u32 s44, s44, 0x4000\n\t"                             \
    "buffer_load_dwordx4 v14, %[rk], s43 offen sc1 lds\n\t"      \
    "s_add_u32 m0, m0, 0x2000\n\t"                               \
    "s_and_b32 s44, s44, 0xffff\n\t"                             \
    "buffer_load_dwordx4 v14, %[rv], s43 offen sc1 lds\n\t"      \
    "s_add_u32 s43, s43, 0x2000\n\t"
#define PR2 "s_setprio 2\n\t"
#define PR3 ""
#define PR0 "s_setprio 1\n\t"
#define PR1 "s_setprio 0\n\t"
constexpr int A_ONES = 65536, A_VOTE = 69632 + 512, A_LSUM = 71680;
struct AttnRegs { f32x16 oa, ob, nm; u32x4 p1, v1; unsigned ka, va, go, fo; };
DI unsigned qkv_dep(int a, bool early) {
    const int lane = lane_id(), r = lane & 15, v = (lane >> 4) & 1, R = 4 * (a >> 5) + (lane & 3);
    const int kv = (r >> 3) + 8 * ((early ? 8 + 8 * v : 16 * v + 8 * (a & 1)) + (r & 7));
    const int qq = ((R >> 3) & 1) + 8 * ((R >> 4) * 24 + (R & 7));
    return ((a & 6) + (lane < 32 || !early ? kv : qq)) * 128;
}
DI void attn_prep(AttnRegs& R, float negm, int a, int w) {
    const int lane = lane_id(), r = lane & 31, kh = w >> 2;
    R.ka = 4096 * kh + lane * 16;
    R.va = r < 16 ? 8192 + 4096 * kh + (lane >> 5) * 256 + r * 16 : A_ONES + (r == 16 ? 0 : r == 20 ? 64 : (r > 20 && r < 28) ? 80 : 16);
    R.go = w * 1024 + lane * 16;
    R.fo = qkv_dep(a, false);
#pragma unroll
    for (int i = 0; i < 16; ++i) { R.oa[i] = 0.f; R.ob[i] = 0.f; R.nm[i] = negm; }
    R.p1 = u32x4{0u, 0u, 0u, 0u};
    R.v1 = u32x4{0u, 0u, 0u, 0u};
    asm volatile("" : "+{v[64:79]}"(R.oa), "+{v[80:95]}"(R.ob), "+{v[16:31]}"(R.nm), "+{v[100:103]}"(R.p1), "+{v[124:127]}"(R.v1),
                      "+{v12}"(R.ka), "+{v13}"(R.va), "+{v14}"(R.go), "+{v2}"(R.fo));
}
DI void attn_pass(AttnRegs& R, const char* qa, const char* qb, const char* ksrc, const char* vsrc, const char* go_flag, unsigned tag, int w) {
    const u32x4 rk = srd(ksrc, 65536), rv = srd(vsrc, 65536);
    asm volatile(
        "v_and_b32 v15, 0x3ff, v14\n\t"
        "s_mov_b32 s43, 0\n\t"
        "s_mov_b32 s44, 0\n\t"
        AISSUE
        "global_load_dwordx4 v[4:7], v15, %[qa] sc1\n\t"
        "global_load_dwordx4 v[8:11], v15, %[qb] sc1\n\t"
        AISSUE AISSUE
        "s_mov_b32 s40, 0xffff\n\t"
        "s_mov_b32 s41, 0xffff\n\t"
        "s_mov_b32 s42, 8\n\t"
        "s_waitcnt vmcnt(4)\n\t"
        "s_barrier\n\t"
        "ds_read_b128 v[104:107], v12\n\t"
        "ds_read_b128 v[108:111], v12 offset:1024\n\t"
        "ds_read_b128 v[112:115], v13\n\t"
        "ds_read_b128 v[116:119], v13 offset:512\n\t"
        PR2
        "s_waitcnt lgkmcnt(3)\n\t"
        MF(R_SA, "v[104:107]", "v[4:7]", R_NM)
        "s_nop 15\n\t"
        "LA_loop%=:\n\t"
        ATILE_E(2048, 1024, 1536, PR0)
        ATILE_O(3072, 2048, 2560, PR1)
        ATILE_E(0, 3072, 3584,
                "s_waitcnt vmcnt(2)\n\t"
                "s_cmp_eq_u32 s42, 7\n\t"
                "s_cbranch_scc0 LA_f%=\n\t"
                "s_mov_b32 s45, 0x100000\n\t"
                "LA_p%=:\n\t"
                "v_cmp_ne_u32 vcc, %[tag], v3\n\t"
                "s_sub_u32 s45, s45, 1\n\t"
                "s_cbranch_scc1 LA_f%=\n\t"
                "s_cbranch_vccz LA_f%=\n\t"
                "global_load_dword v3, v2, %[ff] sc1\n\t"
                "s_waitcnt vmcnt(0)\n\t"
                "s_branch LA_p%=\n\t"
                "LA_f%=:\n\t"
                "s_barrier\n\t"
                "s_setprio 3\n\t"
                "v_add_u32 v12, 0x4000, v12\n\t"
                "s_cmp_eq_u32 s42, 8\n\t"
                "s_cbranch_scc0 LA_h%=\n\t"
                "global_load_dword v3, v2, %[ff] sc1\n\t"
                "LA_h%=:\n\t"
                AISSUE
                "v_and_b32 v12, 0xffff, v12\n\t"
                PR2)
        ATILE_O(1024, 0, 512,
                PR3
                "s_mov_b64 exec, s[40:41]\n\t"
                "v_add_u32 v13, 0x4000, v13\n\t"
                "s_sub_u32 s42, s42, 1\n\t"
                "v_and_b32 v13, 0xffff, v13\n\t"
                "s_mov_b64 exec, -1\n\t"
                "s_cmp_lg_u32 s42, 0\n\t")
        "s_cbranch_scc1 LA_loop%=\n\t"
        "s_waitcnt lgkmcnt(0)\n\t"
        "s_setprio 3\n\t"
        MF(R_OB, "v[124:127]", "v[100:103]", R_OB)
        "s_nop 15\n\t"
        : "+{v[64:79]}"(R.oa), "+{v[80:95]}"(R.ob), "+{v[100:103]}"(R.p1), "+{v[124:127]}"(R.v1), "+{v12}"(R.ka), "+{v13}"(R.va)
        : "{v[16:31]}"(R.nm), "{v14}"(R.go), [qa] "s"(qa), [qb] "s"(qb), [rk] "s"(rk), [rv] "s"(rv), [lw] "s"(w * 1024), [tag] "s"(tag), [ff] "s"(go_flag), "{v2}"(R.fo)
        : "memory", "scc", "vcc", "s40", "s41", "s42", "s43", "s44", "s45", "v3", "v4", "v5", "v6", "v7", "v8", "v9", "v10", "v11", "v15",
          "v32", "v33", "v34", "v35", "v36", "v37", "v38", "v39", "v40", "v41", "v42", "v43", "v44", "v45", "v46", "v47",
          "v48", "v49", "v50", "v51", "v52", "v53", "v54", "v55", "v56", "v57", "v58", "v59", "v60", "v61", "v62", "v63",
          "v96", "v97", "v98", "v99", "v104", "v105", "v106", "v107", "v108", "v109", "v110", "v111",
          "v112", "v113", "v114", "v115", "v116", "v117", "v118", "v119", "v120", "v121", "v122", "v123");
}

DI void wait_qkv(const char* ws, int a, unsigned tag) { wait_flags<4>(ws + WS_FQ, qkv_dep(a, true), tag); }
DI void attn_role(const char* x, const char* wqkv, char* ws, char* lds, int a, int w) {
    const int kh = w >> 2;
    const int head = 4 * (a & 7) + ((a >> 3) & 3), qt = 8 * (a >> 5) + 2 * (w & 3);
    ST_DECL; ST(0);
    __builtin_amdgcn_s_setprio(3);
    unsigned tag;
    const int ti = a >> 3, trow = 8 * (a & 1) + (ti & 7) + (ti >= 24 ? 16 : 0), tcol = (ti >= 24 ? 0 : 4 * (ti >> 3)) + ((a & 7) >> 1);
    if (w >= 4) {
        qkv_loader(x, wqkv, trow, tcol, w - 4, ws + WS_FA + a * 128);
        tag = __builtin_amdgcn_readfirstlane(*(LAS const unsigned*)(QCNT + 4));
        if (!FAEARLY && w == 5) st4(ws + WS_FA + a * 128, tag);
        if (w == 4) wait_qkv(ws, a, tag);
    } else {
        tag = __builtin_amdgcn_readfirstlane(ld4(ws + WS_EPOCH)) * 0x9E3779B1u + 0x7F4A7C15u;
        if (tcol < 8) qkv_body<true>(ws, lds, trow, tcol, a, w, tag ST_PASS);
        else qkv_body<false>(ws, lds, trow, tcol, a, w, tag ST_PASS);
    }
    AttnRegs R;
    attn_prep(R, 0.f, a, w);
    ST(5);
    wgbar();
    ST(6);
    {
        const int o = w * 512 + lane_id() * 8;
        const unsigned one = ((o & 0x1b0) == 0) ? 0x3f803f80u : 0u;
        *(LAS u32x2*)((LAS char*)lds + A_ONES + o) = u32x2{one, one};
        asm volatile("s_waitcnt lgkmcnt(0)" ::: "memory");
    }
    const char* ksrc = ws + WS_K + (size_t)head * 65536;
    const char* qa = ws + WS_Q + (size_t)(head * 64 + qt + kh) * 1024;
    const char* qb = ws + WS_Q + (size_t)(head * 64 + qt + 1 - kh) * 1024;
#pragma clang loop unroll(disable)
    for (int att = 0;; ++att) {
        ST(1);
        attn_pass(R, qa, qb, ksrc, ksrc + (WS_V - WS_K), ws + WS_FQ + 32768, tag, w);
        ST(2);
        const int lane = lane_id();
        const f32x16 oa = R.oa, ob = R.ob;
        LAS char* ex = (LAS char*)lds + w * 2048 + lane * 16;
        LAS const char* exr = (LAS const char*)lds + (w ^ 4) * 2048 + lane * 16;
        *(LAS f32x4*)ex = f32x4{ob[0], ob[1], ob[2], ob[3]};
        *(LAS f32x4*)(ex + 1024) = f32x4{ob[4], ob[5], ob[6], ob[7]};
        *(LAS float*)((LAS char*)lds + A_LSUM + w * 256 + lane * 4) = ob[8];
        float big = fmaxf(oa[8], ob[8]) * 2.7e8f;
#pragma unroll
        for (int i = 0; i < 8; ++i) big = fmaxf(fmaxf(big, __builtin_fabsf(oa[i])), __builtin_fabsf(ob[i]));
        const bool bad = !__all(fminf(oa[8], ob[8]) > 8.1e-28f && big < 1.7e38f);
        if (lane == 0) ((LAS unsigned*)((LAS char*)lds + A_VOTE))[w] = bad;
        wgbar();
        const f32x4 t0 = *(LAS const f32x4*)exr, t1 = *(LAS const f32x4*)(exr + 1024);
        const float t2 = *(LAS const float*)((LAS char*)lds + A_LSUM + (w ^ 4) * 256 + lane * 4);
        const u32x4 v0 = *(LAS const u32x4*)((LAS char*)lds + A_VOTE), v1 = *(LAS const u32x4*)((LAS char*)lds + A_VOTE + 16);
        const float rinv = __builtin_amdgcn_rcpf(oa[8] + t2);
        u32x4 ov;
        ov[0] = cvtpk((oa[0] + t0[0]) * rinv, (oa[1] + t0[1]) * rinv);
        ov[1] = cvtpk((oa[2] + t0[2]) * rinv, (oa[3] + t0[3]) * rinv);
        ov[2] = cvtpk((oa[4] + t1[0]) * rinv, (oa[5] + t1[1]) * rinv);
        ov[3] = cvtpk((oa[6] + t1[2]) * rinv, (oa[7] + t1[3]) * rinv);
        const bool redo = att == 0 && __builtin_amdgcn_readfirstlane(v0[0] | v0[1] | v0[2] | v0[3] | v1[0] | v1[1] | v1[2] | v1[3]);
        if (att == 0 ? !redo : att == 1 + kh) {
            st16(ws + WS_O + (size_t)((qt + kh) * 32 + head) * 1024, lane * 16, ov);
            drain();
            st4(ws + WS_FO + ((qt + kh) * 32 + head) * 128, tag);
        }
        asm volatile("" : "+s"(att));
        if (att == 2 || (att == 0 && !redo)) break;
        drain();
        wgbar();
        qa = qb = ws + WS_Q + (size_t)(head * 64 + qt + att) * 1024;
        bf16x8 qf;
        asm volatile("global_load_dwordx4 %0, %1, %2 sc1\n\ts_waitcnt vmcnt(0)" : "=v"(qf) : "v"(lane * 16), "s"(qa) : "memory");
        float mx = -INFINITY;
#pragma clang loop unroll(disable)
        for (int t = 0; t < 64; ++t) {
            bf16x8 kf;
            asm volatile("global_load_dwordx4 %0, %1, %2 sc1\n\ts_waitcnt vmcnt(0)" : "=v"(kf) : "v"(lane * 16), "s"(ksrc + t * 1024) : "memory");
            const f32x16 s = MFMA32(kf, qf, f32x16{});
#pragma unroll
            for (int i = 0; i < 16; ++i) mx = fmaxf(mx, s[i]);
        }
        attn_prep(R, -swap32_max(mx), a, w);
    }
    ST(3); ST_OUT(0);
}

#define BG1 "s_sleep 1\n\t"
#define BG2 "s_sleep 1\n\t"
#define BG3 "s_sleep 1\n\t"
#define SISSUE                                                             \
    "s_mov_b32 m0, s44\n\t"                                                 \
    "s_add_u32 s45, %[ow], %[wst]\n\t"                                      \
    "buffer_load_dwordx4 %[g0], %[rw], %[ow] offen lds\n\t"                 \
    "buffer_load_dwordx4 %[g1], %[rw], %[ow] offen offset:1024 lds\n\t"     \
    "s_add_u32 m0, s44, 8192\n\t"                                           \
    "s_add_u32 s46, %[od], 4096\n\t"                                        \
    "buffer_load_dwordx4 %[d0], %[rd], %[od] offen sc1 lds\n\t"             \
    "buffer_load_dwordx4 %[d0], %[rd], %[od] offen offset:1024 sc1 lds\n\t" \
    "s_add_u32 s44, s44, 0x4000\n\t"                                        \
    "s_and_b32 s44, s44, 0xffff\n\t"                                        \
    "s_cmp_eq_u32 %[od], %[de]\n\t"                                         \
    "s_cselect_b32 %[ow], 0, s45\n\t"                                       \
    "s_cselect_b32 %[od], 0, s46\n\t"
struct SG { u32x4 rw, rd; unsigned wst, de, ow, od, a, b, c; };
#define WISSUE                                                              \
    "s_and_b32 s45, s44, 0xc000\n\t"                                        \
    "s_add_u32 m0, s45, %[lo2]\n\t"                                         \
    "s_lshl_b32 s45, s45, 3\n\t"                                            \
    "buffer_load_dwordx4 %[o0], %[ro], s45 offen lds\n\t"                   \
    "buffer_load_dwordx4 %[o1], %[ro], s45 offen offset:1024 lds\n\t"       \
    "buffer_load_dwordx4 %[o2], %[ro], s45 offen offset:2048 lds\n\t"       \
    "buffer_load_dwordx4 %[o3], %[ro], s45 offen offset:3072 lds\n\t"       \
    "s_add_u32 s44, s44, 0x4000\n\t"                                        \
    "s_and_b32 s44, s44, 0xffff\n\t"
template <int WO>
DI void sgemm_load(SG& G, int n, int lw, u32x4 ro, unsigned o0) {
    asm volatile(
        "s_mov_b32 s44, %[lo]\n\t"
        "s_sub_u32 s47, %[n], 3\n\t"
        SISSUE SISSUE SISSUE
        "LL_loop%=:\n\t"
        "s_waitcnt vmcnt(8)\n\t"
        "s_barrier\n\t"
        SISSUE
        "s_sub_u32 s47, s47, 1\n\t"
        "s_cmp_lg_u32 s47, 0\n\t"
        "s_cbranch_scc1 LL_loop%=\n\t"
        ".if %c[wo]\n\t"
        "s_waitcnt vmcnt(8)\n\ts_barrier\n\t" WISSUE
        "s_waitcnt vmcnt(8)\n\ts_barrier\n\t" WISSUE
        "s_waitcnt vmcnt(8)\n\ts_barrier\n\t" WISSUE
        "s_barrier\n\t" WISSUE
        ".else\n\t"
        "s_waitcnt vmcnt(8)\n\ts_barrier\n\t"
        "s_waitcnt vmcnt(4)\n\ts_barrier\n\t"
        "s_waitcnt vmcnt(0)\n\ts_barrier\n\t"
        "s_barrier\n\t"
        ".endif\n\t"
        : [ow] "+s"(G.ow), [od] "+s"(G.od)
        : [wst] "s"(G.wst), [rw] "s"(G.rw), [rd] "s"(G.rd), [lo] "s"(lw * 2048), [de] "s"(G.de), [n] "s"(n), [g0] "v"(G.a), [g1] "v"(G.b), [d0] "v"(G.c),
          [wo] "i"(WO), [ro] "s"(ro), [lo2] "s"(lw * 4096), [o0] "v"(o0), [o1] "v"(o0 + 7168), [o2] "v"(o0 + 14336), [o3] "v"(o0 + 21504)
        : "memory", "scc", "s44", "s45", "s46", "s47");
}
#define SRD3(B, SLOT, K, TD)                                                                         \
    "ds_read_b64_tr_b16 v[" #B ":" #B "+1], %[t] offset:" #SLOT "*16384+" #K "*2048\n\t"              \
    "ds_read_b64_tr_b16 v[" #B "+2:" #B "+3], %[t] offset:" #SLOT "*16384+" #K "*2048+" #TD "\n\t"    \
    "ds_read_b128 v[" #B "+4:" #B "+7], %[dl] offset:" #SLOT "*16384+" #K "*1024\n\t"
#define SSTAGE(SLOT, MM, TD, N0, N1, N2, N3, P0, P1, P2, P3)                          \
    "s_waitcnt lgkmcnt(0)\n\t"                                                        \
    "s_barrier\n\t"                                                                   \
    MM(P0) SRD3(N0, SLOT, 0, TD) SRD3(N1, SLOT, 1, TD) BG1                            \
    MM(P1) SRD3(N2, SLOT, 2, TD) SRD3(N3, SLOT, 3, TD) BG2                            \
    MM(P2) BG3 MM(P3)
#define SST_E(SLOT, MM, TD) SSTAGE(SLOT, MM, TD, 32, 40, 48, 56, 96, 104, 112, 120)
#define SST_O(SLOT, MM, TD) SSTAGE(SLOT, MM, TD, 96, 104, 112, 120, 32, 40, 48, 56)
#define SMM_NONE(P) ""
#define SMM_WA(P) MF("v[64:79]", "v[" #P ":" #P "+3]", "v[" #P "+4:" #P "+7]", "v[64:79]")
#define SMM_WB(P) MF("v[80:95]", "v[" #P "+4:" #P "+7]", "v[" #P ":" #P "+3]", "v[80:95]")
#define SGEMM_ASM(ACC, MM, TD, NLOOP, TAIL)                                                                       \
    asm volatile(                                                                                                 \
        "s_mov_b32 s44, " #NLOOP "\n\t"                                                                           \
        "s_barrier\n\t"                                                                                           \
        SRD3(32, 0, 0, TD) SRD3(40, 0, 1, TD) SRD3(48, 0, 2, TD) SRD3(56, 0, 3, TD)                               \
        "LS_loop%=:\n\t"                                                                                          \
        SST_O(1, MM, TD) SST_E(2, MM, TD) SST_O(3, MM, TD) SST_E(0, MM, TD)                                       \
        "s_sub_u32 s44, s44, 1\n\t"                                                                               \
        "s_cmp_lg_u32 s44, 0\n\t"                                                                                 \
        "s_cbranch_scc1 LS_loop%=\n\t"                                                                            \
        TAIL                                                                                                      \
        "s_waitcnt lgkmcnt(0)\n\t"                                                                                \
        MM(96) BG3 MM(104) BG3 MM(112) BG3 MM(120)                                                                \
        "s_nop 15\n\t"                                                                                            \
        : ACC(acc)                                                                                                \
        : [t] "v"(G.a), [dl] "v"(G.b)                                                                             \
        : "memory", "scc", "s44",                                                                                 \
          "v32", "v33", "v34", "v35", "v36", "v37", "v38", "v39", "v40", "v41", "v42", "v43", "v44", "v45", "v46", "v47", \
          "v48", "v49", "v50", "v51", "v52", "v53", "v54", "v55", "v56", "v57", "v58", "v59", "v60", "v61", "v62", "v63", \
          "v96", "v97", "v98", "v99", "v100", "v101", "v102", "v103", "v104", "v105", "v106", "v107", "v108", "v109", "v110", "v111", \
          "v112", "v113", "v114", "v115", "v116", "v117", "v118", "v119", "v120", "v121", "v122", "v123", "v124", "v125", "v126", "v127")

template <int MODE>
DI SG sgemm_setup(const char* wbase, unsigned wbytes, int wrow, const char* dbase, int dblk, int rot, int w) {
    const int lane = lane_id(), row = lane >> 3, lw = w & 3;
    const int q = (lane & 15) >> 2, h2 = lane >> 5;
    SG G;
    G.rw = srd(wbase, wbytes); G.rd = srd(dbase, 2 * dblk);
    G.wst = 64 * wrow; G.de = (MODE == 0 ? 31 : 21) * 4096; G.ow = rot * G.wst; G.od = rot * 4096;
    if (w >= 4) {
        G.a = (16 * lw + row) * wrow + (((lane & 7) ^ (((row >> 1) & 1) << 2)) << 4);
        G.b = G.a + 8 * wrow - 1024;
        G.c = (lw >> 1) * dblk + (lw & 1) * 2048 + lane * 16;
    } else {
        G.a = ((MODE == 0 ? 8 : 4) * h2 + q) * 128 + (((lw & 1) ^ ((q >> 1) & 1)) << 6) + ((lane >> 4) & 1) * 32 + (lane & 3) * 8;
        G.b = 8192 + (lw >> 1) * 4096 + lane * 16;
        G.c = 0;
    }
    asm volatile("" : "+v"(G.a), "+v"(G.b), "+v"(G.c));
    return G;
}
#define ACC0 "+{v[64:79]}"
#define ACC1 "+{v[80:95]}"
template <int MODE>
DI void sgemm_run(f32x16& acc, SG& G) {
    if (MODE == 0) SGEMM_ASM(ACC0, SMM_WA, 512, 7, SST_O(1, SMM_WA, 512) SST_E(2, SMM_WA, 512) SST_O(3, SMM_WA, 512));
    else SGEMM_ASM(ACC1, SMM_WB, 1024, 5, SST_O(1, SMM_WB, 1024));
}
DI f32x4 sub4(const f32x16& a, int g) { return f32x4{a[4 * g], a[4 * g + 1], a[4 * g + 2], a[4 * g + 3]}; }

DI unsigned gelu2(float a, float b) {
    constexpr float c1 = -2.f * 0.7978845608028654f * 1.4426950408889634f, c2 = c1 * 0.044715f;
    const f32x2 u = {a, b};
    const f32x2 z = u * (u * u * c2 + c1);
    const f32x2 d = f32x2{__builtin_amdgcn_exp2f(z[0]), __builtin_amdgcn_exp2f(z[1])} + 1.f;
    const f32x2 o = u * f32x2{__builtin_amdgcn_rcpf(d[0]), __builtin_amdgcn_rcpf(d[1])};
    return cvtpk(o[0], o[1]);
}

#define WRD(R0, R1, R2, R3, STEP)                                                    \
    "ds_read_b64_tr_b16 " R0 ", %[t0] offset:" #STEP "*2048\n\t"                      \
    "ds_read_b64_tr_b16 " R1 ", %[t0] offset:" #STEP "*2048+1024\n\t"                 \
    "ds_read_b64_tr_b16 " R2 ", %[t1] offset:" #STEP "*2048\n\t"                      \
    "ds_read_b64_tr_b16 " R3 ", %[t1] offset:" #STEP "*2048+1024\n\t"
#define WRD0(S) WRD("v[32:33]", "v[34:35]", "v[36:37]", "v[38:39]", S)
#define WRD1(S) WRD("v[40:41]", "v[42:43]", "v[44:45]", "v[46:47]", S)
#define WRD2(S) WRD("v[48:49]", "v[50:51]", "v[52:53]", "v[54:55]", S)
#define WRD3(S) WRD("v[56:57]", "v[58:59]", "v[60:61]", "v[62:63]", S)
#define WMM(WAIT, W0, W1, O) "s_waitcnt " WAIT "\n\t" MF("v[96:111]", W0, O, "v[96:111]") MF("v[112:127]", W1, O, "v[112:127]")
#define OLD(K, R, V, OFF)                                             \
    "5:\n\t"                                                           \
    "s_bitcmp1_b32 s40, " #K "\n\t"                                    \
    "s_cbranch_scc1 6f\n\t"                                            \
    "global_load_dword v24, %[fo], %[f] sc1\n\t"                       \
    "s_sub_u32 s41, s41, 1\n\t"                                        \
    "s_cselect_b32 s42, -1, 0\n\t"                                     \
    "s_waitcnt vmcnt(0)\n\t"                                           \
    "v_cmp_eq_u32 vcc, %[tag], v24\n\t"                                \
    "s_or_b32 s40, vcc_lo, s42\n\t"                                    \
    "s_bitcmp1_b32 s40, " #K "\n\t"                                    \
    "s_cbranch_scc1 6f\n\t"                                            \
    "s_sleep 3\n\t"                                                    \
    "s_branch 5b\n\t"                                                  \
    "6:\n\t"                                                           \
    "global_load_dwordx4 " R ", " V ", %[o] offset:" #OFF " sc1\n\t"
DI void wo_gemm(f32x16& a0, f32x16& a1, f32x4& k0, f32x4& k1, float& k2, const char* osrc, const char* fsrc, unsigned tag, int w) {
    const int lane = lane_id();
    const int q = (lane & 15) >> 2, h2 = lane >> 5;
    const unsigned tb = (w >> 1) * 16384 + (4 * h2 + q) * 128 + ((lane >> 4) & 1) * 32 + (lane & 3) * 8;
    const unsigned t0 = tb + ((q >> 1) & 1) * 64, t1 = tb + (((q >> 1) & 1) ^ 1) * 64;
    const unsigned d0 = lane * 16, d1 = d0 + 4096, fo = (lane & 7) * 128;
    asm volatile(
        WRD0(0) WRD1(1) WRD2(2) WRD3(3)
        "s_mov_b32 s40, 0\n\t"
        "s_mov_b32 s41, 0x100000\n\t"
        "s_setprio 0\n\t"
        OLD(0, "v[64:67]", "%[d0]", 0) OLD(1, "v[68:71]", "%[d0]", 1024) OLD(2, "v[72:75]", "%[d0]", 2048) OLD(3, "v[76:79]", "%[d0]", 3072)
        OLD(4, "v[80:83]", "%[d1]", 0) OLD(5, "v[84:87]", "%[d1]", 1024) OLD(6, "v[88:91]", "%[d1]", 2048) OLD(7, "v[92:95]", "%[d1]", 3072)
        "s_setprio 3\n\t"
        "s_waitcnt vmcnt(7) lgkmcnt(0)\n\t"
        MF("v[96:111]", "v[32:35]", "v[64:67]", "0") MF("v[112:127]", "v[36:39]", "v[64:67]", "0")
        WRD0(4)
        WMM("vmcnt(6)", "v[40:43]", "v[44:47]", "v[68:71]") WRD1(5)
        WMM("vmcnt(5)", "v[48:51]", "v[52:55]", "v[72:75]") WRD2(6)
        WMM("vmcnt(4)", "v[56:59]", "v[60:63]", "v[76:79]") WRD3(7)
        WMM("vmcnt(3) lgkmcnt(12)", "v[32:35]", "v[36:39]", "v[80:83]")
        WMM("vmcnt(2) lgkmcnt(0)", "v[40:43]", "v[44:47]", "v[84:87]")
        WMM("vmcnt(1)", "v[48:51]", "v[52:55]", "v[88:91]")
        "s_barrier\n\t"
        WMM("vmcnt(0)", "v[56:59]", "v[60:63]", "v[92:95]")
        "s_nop 15\n\t"
        : "={v[96:111]}"(a0), "={v[112:127]}"(a1),
          "+{v[12:15]}"(k0), "+{v[16:19]}"(k1), "+{v20}"(k2)
        : [o] "s"(osrc), [f] "s"(fsrc), [tag] "s"(tag), [d0] "v"(d0), [d1] "v"(d1), [t0] "v"(t0), [t1] "v"(t1), [fo] "v"(fo)
        : "memory", "scc", "vcc", "s40", "s41", "s42", "v24",
          "v32", "v33", "v34", "v35", "v36", "v37", "v38", "v39", "v40", "v41", "v42", "v43", "v44", "v45", "v46", "v47",
          "v48", "v49", "v50", "v51", "v52", "v53", "v54", "v55", "v56", "v57", "v58", "v59", "v60", "v61", "v62", "v63",
          "v64", "v65", "v66", "v67", "v68", "v69", "v70", "v71", "v72", "v73", "v74", "v75", "v76", "v77", "v78", "v79",
          "v80", "v81", "v82", "v83", "v84", "v85", "v86", "v87", "v88", "v89", "v90", "v91", "v92", "v93", "v94", "v95");
}

DI void mixer_role(const char* x, const char* wqkv, const char* gw, const char* wo, const char* w1, const char* b1, const char* w2, const char* b2,
                   char* ws, char* out, char* lds, int bi, int w) {
    const int j = bi & 7, i = bi >> 3;
    __builtin_amdgcn_s_setprio(LNP);
    ST_DECL; ST(0);
    const unsigned ep = ld4(ws + WS_EPOCH);
    unsigned tag;
    {
        const int lane = lane_id(), h2 = lane >> 5, tid = 64 * w + lane;
        const u32x4 xr = *(GAS const u32x4*)(x + (size_t)(8 * bi + w) * 1024 + lane * 16);
        const u32x4 gr = *(GAS const u32x4*)(gw + lane * 16);
        tag = __builtin_amdgcn_readfirstlane(ep) * 0x9E3779B1u + 0x7F4A7C15u;
        float v[8], gg[8];
#pragma unroll
        for (int k = 0; k < 4; ++k) { v[2 * k] = bflo(xr[k]); v[2 * k + 1] = bfhi(xr[k]); gg[2 * k] = bflo(gr[k]); gg[2 * k + 1] = bfhi(gr[k]); }
        float s1 = 0.f, s3 = 0.f;
#pragma unroll
        for (int k = 0; k < 8; ++k) { s1 += v[k]; s3 += v[k] * gg[k]; }
        const float mu = wave_sum(s1) * (1.f / 512.f);
        s3 = wave_sum(s3);
        float s2 = 0.f;
#pragma unroll
        for (int k = 0; k < 8; ++k) { v[k] -= mu; s2 += v[k] * v[k]; }
        const float rs = __builtin_amdgcn_rsqf(wave_sum(s2) * (1.f / 512.f) + 1e-5f);
        u32x4 hv;
#pragma unroll
        for (int k = 0; k < 4; ++k) hv[k] = cvtpk(v[2 * k] * rs, v[2 * k + 1] * rs);
        *(LAS u32x4*)((LAS char*)lds + w * 1024 + lane * 16) = hv;
        const float g = __builtin_amdgcn_rcpf(1.f + __builtin_amdgcn_exp2f(-1.4426950408889634f * s3));
        st4(ws + WS_G + (8 * bi + w) * 4, uni(g));
        wgbar();
        LAS const unsigned short* lb = (LAS const unsigned short*)lds + tid;
#pragma unroll
        for (int k = 0; k < 4; ++k) hv[k] = (unsigned)lb[(2 * k) * 512] | ((unsigned)lb[(2 * k + 1) * 512] << 16);
        st16(ws + WS_H + (size_t)(2 * w * 128 + (bi >> 1)) * 1024 + (bi & 1) * 512, h2 * 131072 + (lane & 31) * 16, hv);
        drain();
        wgbar();
        if (w == 0) st4(ws + WS_FL + bi * 128, tag);
    }
    __builtin_amdgcn_s_setprio(3);
    const int wb = w & 1, db = (w >> 1) & 1, hf = w >> 2;
    const int L = (j >> 1) * 22 + i, ft = L >> 2, dt1 = 4 * (j & 1) + (L & 3);
    const int st = 8 * (j >> 1) + (i & 7), dt = 4 * (j & 1) + (i >> 3);
    SG G1, G2;
    unsigned ex, l16, og, wof;
    {
        const int lane = lane_id();
        og = (64 * st + 32 * wb + (lane & 31)) * 4;
        G1 = sgemm_setup<0>(w1 + ft * 128, 2048 * 2752 - ft * 128, 2752, ws + WS_H + (size_t)2 * dt1 * 131072, 131072, ROT1, w);
        G2 = sgemm_setup<1>(w2 + st * 128, 1376 * 4096 - st * 128, 4096, ws + WS_U + (size_t)2 * dt * 86 * 1024, 86 * 1024, ROT2, w);
        l16 = lane * 16;
        ex = 65536 + (w & 3) * 2048 + l16;
        wof = (32 * (w & 3) + (lane >> 3)) * 1024 + (((lane & 7) ^ (((lane >> 4) & 1) << 2)) << 4);
        asm volatile("" : "+v"(ex), "+v"(l16), "+v"(og), "+v"(wof));
    }
    {
        const int row = 16 + 8 * (j & 1) + (i & 7), col = 8 + 8 * (i >> 4) + 2 * (j >> 1) + ((i >> 3) & 1);
        if (w == 0) {
            *(LAS unsigned*)QCNT = 0u;
            wait_flags<2>(ws + WS_FA + bi * 128, 0, tag);
        }
        wgbar();
        if (PFA == 2 && i >= 22) warm(w1, 1376, ((i - 22) * 8 + j) * 8 + w, l16 * 4);
        if (w >= 4) qkv2_loader(x, wqkv, row, col, w - 4);
        else qkv2_body(ws, row, col, bi, w, tag);
    }
    ST(6);
    if (PFA && i >= 22) {
        const int wv = ((i - 22) * 8 + j) * 8 + w;
        if (PFA == 1) warm(w1, 1376, wv, l16 * 4);
        for (int k = 0; k < PFD; ++k) __builtin_amdgcn_s_sleep(64);
        warm(w2, 1376, wv, l16 * 4);
        warm(wo, 128, wv, l16 * 4);
    }
    if (i < 22) {
        if (w >= 4) wait_flags<8>(ws + WS_FL + (w - 4) * 8192, l16 * 8, tag);
        if (w == 4) wait_qkv(ws, bi, tag);
        if (BSLEEP) __builtin_amdgcn_s_sleep(BSLEEP);
        wgbar();
        ST(7);
        f32x4 v0, v1;
        if (w >= 4) {
            sgemm_load<0>(G1, 32, w - 4, G1.rw, 0);
            v0 = *(LAS const f32x4*)(uintptr_t)ex; v1 = *(LAS const f32x4*)(uintptr_t)(ex + 1024);
        } else {
            f32x16 acc1;
            const char* bsrc = b1 + (64 * ft + (ft < 21 ? 32 * wb : 0)) * 2 + (l16 >> 9) * 8;
#pragma unroll
            for (int k = 0; k < 4; ++k) {
                const u32x2 bv = *(GAS const u32x2*)(bsrc + 16 * k);
                acc1[4 * k] = bflo(bv[0]); acc1[4 * k + 1] = bfhi(bv[0]); acc1[4 * k + 2] = bflo(bv[1]); acc1[4 * k + 3] = bfhi(bv[1]);
            }
            sgemm_run<0>(acc1, G1);
            *(LAS f32x4*)(uintptr_t)ex = sub4(acc1, 2); *(LAS f32x4*)(uintptr_t)(ex + 1024) = sub4(acc1, 3);
            wgbar();
            v0 = sub4(acc1, 0); v1 = sub4(acc1, 1);
        }
        ST(1);
        const u32x4 o = {gelu2(v0[0], v0[1]), gelu2(v0[2], v0[3]), gelu2(v1[0], v1[1]), gelu2(v1[2], v1[3])};
        if (ft < 21 || wb == 0) st16(ws + WS_U + (size_t)((2 * dt1 + db) * 86 + 4 * ft + 2 * wb + hf) * 1024, l16, o);
        drain();
        wgbar();
        if (w == 0) st4(ws + WS_FU + (dt1 * 32 + ft) * 128, tag);
    }
    ST(2);
    if (bi == 255 && w == 1) {
        wait_flags256(ws + WS_FQ, l16 * 8, tag);
        wait_flags256(ws + WS_FL, l16 * 8, tag);
        st4(ws + WS_EPOCH, tag);
    }
    if (w == 0) wait_flags<8>(ws + WS_FU + dt * 4096, (l16 < 22 * 16 ? l16 : 0) * 8, tag);
    wgbar();
    ST(3);
    f32x4 bp0, bp1;
    float g;
    {
        asm volatile("global_load_dword %0, %1, %2 sc1" : "=v"(g) : "v"(og), "s"(ws + WS_G) : "memory");
        if (w >= 4) {
            sgemm_load<1>(G2, 22, w - 4, srd(wo + dt * 128, 524288 - dt * 128), wof);
            bp0 = *(LAS const f32x4*)(uintptr_t)ex; bp1 = *(LAS const f32x4*)(uintptr_t)(ex + 1024);
        } else {
            const float bb = bflo(*(GAS const unsigned short*)(b2 + (og >> 1)));
            f32x16 acc2 = {bb, bb, bb, bb, bb, bb, bb, bb, bb, bb, bb, bb, bb, bb, bb, bb};
            sgemm_run<1>(acc2, G2);
            *(LAS f32x4*)(uintptr_t)ex = sub4(acc2, 2); *(LAS f32x4*)(uintptr_t)(ex + 1024) = sub4(acc2, 3);
            wgbar();
            bp0 = sub4(acc2, 0); bp1 = sub4(acc2, 1);
            drain();
        }
        asm volatile("" : "+v"(g));
        ST(4);
        bp0 *= 1.f - g;
        bp1 *= 1.f - g;
        asm volatile("" : "+v"(bp0), "+v"(bp1));
    }
    drain();
    wgbar();
    ST(5);
    f32x16 a0, a1;
    wo_gemm(a0, a1, bp0, bp1, g, ws + WS_O + (size_t)((2 * st + wb) * 32 + 8 * (w >> 1)) * 1024,
            ws + WS_FO + ((2 * st + wb) * 32 + 8 * (w >> 1)) * 128, tag, w);
    {
        const int lane = lane_id(), kq = 2 * db + hf;
        LAS char* ex = (LAS char*)lds + w * 8192 + lane * 16;
#pragma unroll
        for (int k = 0; k < 4; ++k) { *(LAS f32x4*)(ex + k * 1024) = sub4(a0, k); *(LAS f32x4*)(ex + 4096 + k * 1024) = sub4(a1, k); }
        wgbar();
        LAS const char* exr = (LAS const char*)lds + wb * 8192 + kq * 2048 + lane * 16;
        f32x4 s0 = *(LAS const f32x4*)exr, s1 = *(LAS const f32x4*)(exr + 1024);
#pragma unroll
        for (int k = 1; k < 4; ++k) { s0 += *(LAS const f32x4*)(exr + k * 16384); s1 += *(LAS const f32x4*)(exr + k * 16384 + 1024); }
        s0 = s0 * g + bp0;
        s1 = s1 * g + bp1;
        const auto r0 = __builtin_amdgcn_permlane32_swap(cvtpk(s0[0], s0[1]), cvtpk(s1[0], s1[1]), false, false);
        const auto r1 = __builtin_amdgcn_permlane32_swap(cvtpk(s0[2], s0[3]), cvtpk(s1[2], s1[3]), false, false);
        *(GAS u32x4*)(out + (size_t)(64 * st + 32 * wb) * 1024 + (64 * dt + 16 * kq) * 2 + (lane & 31) * 1024 + (lane >> 5) * 16) = u32x4{r0[0], r1[0], r0[1], r1[1]};
    }
    ST_OUT(0);
}

constexpr int MK_LDS = 81920;
__global__ __launch_bounds__(512, 4) void mk(const char* x, const char* wqkv, const char* wo, const char* w1, const char* b1,
                                             const char* w2, const char* b2, const char* gw, char* ws, char* out) {
    extern __shared__ __attribute__((aligned(16))) char lds[];
    const int w = __builtin_amdgcn_readfirstlane(threadIdx.x >> 6);
    if (blockIdx.x < 256) attn_role(x, wqkv, ws, lds, blockIdx.x, w);
    else mixer_role(x, wqkv, gw, wo, w1, b1, w2, b2, ws, out, lds, blockIdx.x - 256, w);
}

extern "C" void kernel_launch(void* const* d_in, const int* in_sizes, int n_in, void* d_out, int out_size, void* d_ws, size_t ws_size,
                              hipStream_t stream) {
    (void)hipFuncSetAttribute((const void*)mk, hipFuncAttributeMaxDynamicSharedMemorySize, MK_LDS);
    mk<<<512, 512, MK_LDS, stream>>>((const char*)d_in[0], (const char*)d_in[1], (const char*)d_in[2], (const char*)d_in[4],
                                     (const char*)d_in[5], (const char*)d_in[6], (const char*)d_in[7], (const char*)d_in[8],
                                     (char*)d_ws, (char*)d_out);
}
```

```cpp
#include <hip/hip_runtime.h>
#include <hip/hip_bf16.h>
#include <stdint.h>

typedef __attribute__((ext_vector_type(8))) short bf16x8;
typedef __attribute__((ext_vector_type(4))) short s16x4;
typedef __attribute__((ext_vector_type(16))) float f32x16;
typedef __attribute__((ext_vector_type(4))) float f32x4;
typedef __attribute__((ext_vector_type(2))) float f32x2;
typedef __attribute__((ext_vector_type(4))) unsigned u32x4;
typedef __attribute__((ext_vector_type(2))) unsigned u32x2;
typedef unsigned long long u64;
#define LAS __attribute__((address_space(3)))
#define GAS __attribute__((address_space(1)))
#define DI __device__ __forceinline__
#define MFMA32(a, b, c) __builtin_amdgcn_mfma_f32_32x32x16_bf16((a), (b), (c), 0, 0, 0)

#define ROT1 ((4 * dt1 + 10 * ft) & 31)
#define ROT2 (((st + dt) & 1) * 11)
#define LNP 0
#define PFA 3
#define PFD 2
#define PSL 2
#define STAG 1
#define QROT 6
#define QROTM 2
constexpr int S_ = 2048, D_ = 512, F_ = 1376;
constexpr size_t WS_EPOCH = 0;
constexpr size_t WS_G = 4096;
constexpr size_t WS_FL = 98304;
constexpr size_t WS_FQ = 458752;
constexpr size_t WS_FU = 65536;
constexpr size_t WS_FA = 393216;
constexpr size_t WS_FO = 131072;
constexpr size_t WS_Q = 1u << 20;
constexpr size_t WS_K = WS_Q + (2u << 20);
constexpr size_t WS_V = WS_K + (2u << 20);
constexpr size_t WS_H = WS_V + (2u << 20);
constexpr size_t WS_U = WS_H + (2u << 20);
constexpr size_t WS_O = WS_U + (2u << 20);

#define ST_DECL
#define ST_ARG
#define ST_PASS
#define ST(i)
#define ST_OUT(k)
DI unsigned cvtpk(float lo, float hi) {
    typedef float f32x2_t __attribute__((ext_vector_type(2)));
    typedef __bf16 bf16x2_t __attribute__((ext_vector_type(2)));
    return __builtin_bit_cast(unsigned, __builtin_convertvector(f32x2_t{lo, hi}, bf16x2_t));
}
DI float bflo(unsigned u) { return __uint_as_float(u << 16); }
DI float bfhi(unsigned u) { return __uint_as_float(u & 0xffff0000u); }
DI s16x4 trread(LAS const char* p) {
    typedef short v4i16_t __attribute__((ext_vector_type(4)));
    return __builtin_bit_cast(s16x4, __builtin_amdgcn_ds_read_tr16_b64_v4i16((LAS v4i16_t*)p));
}
template <int CTRL> DI float dpp(float v) {
    return __int_as_float(__builtin_amdgcn_update_dpp(0, __float_as_int(v), CTRL, 0xf, 0xf, true));
}
DI float rl(float v, int l) { return __int_as_float(__builtin_amdgcn_readlane(__float_as_int(v), l)); }
DI float wave_sum(float v) {
    v += dpp<0xB1>(v); v += dpp<0x4E>(v); v += dpp<0x141>(v); v += dpp<0x140>(v);
    return (rl(v, 0) + rl(v, 16)) + (rl(v, 32) + rl(v, 48));
}
DI float swap32_max(float v) {
    auto rr = __builtin_amdgcn_permlane32_swap(__float_as_uint(v), __float_as_uint(v), false, false);
    return fmaxf(__uint_as_float(rr[0]), __uint_as_float(rr[1]));
}

DI int lane_id() {
    int l;
    asm volatile("v_mbcnt_lo_u32_b32 %0, -1, 0\n\tv_mbcnt_hi_u32_b32 %0, -1, %0" : "=v"(l));
    __builtin_assume(l >= 0 && l < 64);
    return l;
}
DI void st16(const char* sbase, unsigned voff, u32x4 v) {
    asm volatile("global_store_dwordx4 %0, %1, %2 sc1\n\ts_nop 1" ::"v"(voff), "v"(v), "s"(sbase) : "memory");
}
DI void st4(const char* p, unsigned v) {
    unsigned t0, t1;
    asm volatile("s_nop 1\n\tv_mov_b32 %0, %2\n\tv_mov_b32 %1, 0\n\ts_mov_b64 exec, 1\n\tglobal_store_dword %1, %0, %3 sc1\n\ts_mov_b64 exec, -1"
                 : "=&v"(t0), "=&v"(t1) : "s"(v), "s"(p) : "memory");
}
DI void st_flag(const char* p, u64 v) {
    u64 t0;
    unsigned t1;
    asm volatile("s_nop 1\n\tv_mov_b64 %0, %2\n\tv_mov_b32 %1, 0\n\ts_mov_b64 exec, 1\n\tglobal_store_dwordx2 %1, %0, %3 sc1\n\ts_mov_b64 exec, -1"
                 : "=&v"(t0), "=&v"(t1) : "s"(v), "s"(p) : "memory");
}
DI u64 uni64(u64 v) {
    return (u64)(unsigned)__builtin_amdgcn_readfirstlane((unsigned)v) | ((u64)(unsigned)__builtin_amdgcn_readfirstlane((unsigned)(v >> 32)) << 32);
}
DI u64 ld_epoch(const char* ws) { return __hip_atomic_load((GAS const u64*)(ws + WS_EPOCH), __ATOMIC_RELAXED, __HIP_MEMORY_SCOPE_AGENT); }
DI u64 next_tag(u64 e) { return uni64(e) * 6364136223846793005ull + 1442695040888963407ull; }
DI unsigned uni(float v) { return __builtin_amdgcn_readfirstlane(__float_as_uint(v)); }
DI void warm(const char* base, int nchunks, int first, unsigned l64) {
#pragma clang loop unroll(disable)
    for (int n = first; n < nchunks; n += 640) {
        const char* p = base + (size_t)((n & 1) * (nchunks >> 1) + (n >> 1)) * 4096;
        asm volatile("s_mov_b32 m0, 0x13e00\n\ts_nop 0\n\tglobal_load_lds_dword %0, %1" ::"v"(l64), "s"(p) : "memory");
    }
}
DI void drain() { asm volatile("s_waitcnt vmcnt(0)" ::: "memory"); }
DI void wgbar() { asm volatile("s_waitcnt lgkmcnt(0)\n\ts_barrier" ::: "memory"); }
template <int SLEEP>
DI void wait_flags(const char* sbase, unsigned voff, u64 tag) {
#pragma clang loop unroll(disable)
    for (unsigned spin = 0; spin < (1u << 20); ++spin) {
        u64 f;
        asm volatile("global_load_dwordx2 %0, %1, %2 sc1\n\ts_waitcnt vmcnt(0)" : "=v"(f) : "v"(voff), "s"(sbase) : "memory");
        if (__all(f == tag)) break;
        __builtin_amdgcn_s_sleep(SLEEP);
    }
}

DI void wait_flags256(const char* sbase, unsigned voff, u64 tag) {
#pragma clang loop unroll(disable)
    for (unsigned spin = 0; spin < (1u << 20); ++spin) {
        u64 f0, f1, f2, f3;
        asm volatile("global_load_dwordx2 %0, %4, %5 sc1\n\tglobal_load_dwordx2 %1, %4, %6 sc1\n\tglobal_load_dwordx2 %2, %4, %7 sc1\n\t"
                     "global_load_dwordx2 %3, %4, %8 sc1\n\ts_waitcnt vmcnt(0)"
                     : "=&v"(f0), "=&v"(f1), "=&v"(f2), "=&v"(f3)
                     : "v"(voff), "s"(sbase), "s"(sbase + 8192), "s"(sbase + 16384), "s"(sbase + 24576) : "memory");
        if (__all(f0 == tag && f1 == tag && f2 == tag && f3 == tag)) break;
        __builtin_amdgcn_s_sleep(8);
    }
}

constexpr int QCNT = 73728;
#define MF(D, A, B, C) "v_mfma_f32_32x32x16_bf16 " D ", " A ", " B ", " C "\n\t"
DI u32x4 srd(const char* base, unsigned bytes) {
    return u32x4{(unsigned)(uintptr_t)base, (unsigned)((uintptr_t)base >> 32) & 0xffffu, bytes, 0x00020000u};
}
DI void qkv_store(char* ws, const f32x16& acc, int type, int head, int tile, int lane) {
    const float sc = (type == 0) ? 0.25f * 1.4426950408889634f : 1.f;
    const char* dst = ws + WS_Q + (size_t)type * (2u << 20) + (size_t)(head * 64 + tile) * 1024;
    const unsigned vo = type < 2 ? lane * 16 : ((lane >> 4) & 1) * 65536 + (lane >> 5) * 256 + (lane & 15) * 16;
#pragma unroll
    for (int hf = 0; hf < 2; ++hf) {
        u32x4 o;
#pragma unroll
        for (int i = 0; i < 4; ++i) o[i] = cvtpk(acc[8 * hf + 2 * i] * sc, acc[8 * hf + 2 * i + 1] * sc);
        st16(dst + hf * (type < 2 ? 65536 : 512), vo, o);
    }
}
DI void publish4(const char* flag, u64 tag, int lane) {
    drain();
    unsigned arr = 0;
    if (lane == 0) arr = __hip_atomic_fetch_add((LAS unsigned*)QCNT, 1u, __ATOMIC_RELAXED, __HIP_MEMORY_SCOPE_WORKGROUP);
    if (__builtin_amdgcn_readfirstlane(arr) == 3) st_flag(flag, tag);
}
DI void qkv_loader(const char* x, const char* wqkv, int row, int col, int lw, const char* hint) {
    const int lane = lane_id(), xr = 16 * lw + (lane >> 3), wr = 16 * lw + (lane >> 4);
    const unsigned x0 = xr * 1024 + (((lane & 7) ^ ((xr >> 1) & 7)) << 4), x1 = (xr + 8) * 1024 + (((lane & 7) ^ (((xr + 8) >> 1) & 7)) << 4) - 1024;
    const unsigned w0 = wr * 3072 + (((lane & 15) ^ ((wr & 3) << 2)) << 4);
    const u32x4 rx = srd(x + (size_t)row * 65536, 65536), rw = srd(wqkv + col * 256, 512 * 3072 - col * 256);
    const int rot = (row + col) * QROTM & QROT;
    auto issue = [&](int s, int slot) {
        const int kc = (s + rot) & 7;
        asm volatile("s_mov_b32 m0, %[m]\n\ts_nop 0\n\t"
                     "buffer_load_dwordx4 %[x0], %[rx], %[ox] offen lds\n\t"
                     "buffer_load_dwordx4 %[x1], %[rx], %[ox] offen offset:1024 lds\n\t"
                     "s_mov_b32 m0, %[m2]\n\ts_nop 0\n\t"
                     "buffer_load_dwordx4 %[w0], %[rw], %[ow] offen lds\n\t"
                     "buffer_load_dwordx4 %[w1], %[rw], %[ow] offen offset:1024 lds\n\t"
                     "buffer_load_dwordx4 %[w2], %[rw], %[ow] offen offset:2048 lds\n\t"
                     "buffer_load_dwordx4 %[w3], %[rw], %[ow] offen offset:3072 lds"
                     :: [m] "s"(slot * 24576 + lw * 2048), [m2] "s"(slot * 24576 + 8192 + lw * 4096), [ow] "s"(kc * 196608), [ox] "s"(kc * 128),
                        [rw] "s"(rw), [rx] "s"(rx), [x0] "v"(x0), [x1] "v"(x1),
                        [w0] "v"(w0), [w1] "v"(w0 + 11264), [w2] "v"(w0 + 22528), [w3] "v"(w0 + 33792)
                     : "memory");
    };
    issue(0, 0);
    __builtin_amdgcn_s_setprio(2);
    issue(1, 1);
    __builtin_amdgcn_s_setprio(1);
    issue(2, 2);
    __builtin_amdgcn_s_setprio(3);
    asm volatile("s_waitcnt vmcnt(12)\n\ts_barrier" ::: "memory");
    int slot = 0;
#pragma clang loop unroll(disable)
    for (int s = 3; s < 8; ++s) {
        asm volatile("s_waitcnt vmcnt(6)\n\ts_barrier" ::: "memory");
        issue(s, slot);
        slot = slot == 2 ? 0 : slot + 1;
    }
    if (lw == 1) {
        st_flag(hint, uni64(*(LAS const u64*)(QCNT + 8)));
        asm volatile("s_waitcnt vmcnt(7)\n\ts_barrier\n\ts_waitcnt vmcnt(1)\n\ts_barrier" ::: "memory");
    } else
    asm volatile("s_waitcnt vmcnt(6)\n\ts_barrier\n\ts_waitcnt vmcnt(0)\n\ts_barrier" ::: "memory");
}
template <bool T>
DI void qkv_body(char* ws, char* lds, int row, int col, int a, int w, u64 tag ST_ARG) {
    const int lane = lane_id(), r = lane & 31, h2 = lane >> 5, q = (lane & 15) >> 2;
    f32x16 acc[2] = {};
    const int xa0 = (32 * (w & 1) + r) * 128 + ((h2 ^ ((r >> 1) & 7)) << 4);
    const int wa0 = 8192 + (8 * h2 + q) * 256 + (((2 * (w >> 1)) ^ q) << 6) + ((lane >> 4) & 1) * 32 + (lane & 3) * 8;
    bf16x8 xf[4], wf[2][2];
    auto rdw = [&](LAS const char* sl, int st, int i) {
        LAS const char* pw = sl + (wa0 ^ (i << 6)) + st * 4096;
        const s16x4 lo = trread(pw), hi = trread(pw + 1024);
        wf[st & 1][i] = __builtin_shufflevector(lo, hi, 0, 1, 2, 3, 4, 5, 6, 7);
    };
    auto mm1 = [&](int st, int i) {
        acc[i] = T ? MFMA32(wf[st & 1][i], xf[st], acc[i]) : MFMA32(xf[st], wf[st & 1][i], acc[i]);
        __builtin_amdgcn_sched_barrier(0);
    };
    auto step = [&](int st, LAS const char* sl, bool bar) {
        const int sn = (st + 2) & 3;
        mm1(st, 0);
        if (bar) wgbar();
        xf[sn] = *(LAS const bf16x8*)(sl + (xa0 ^ (sn << 5))); rdw(sl, sn, 0); __builtin_amdgcn_sched_barrier(0);
        mm1(st, 1);
        rdw(sl, sn, 1); __builtin_amdgcn_sched_barrier(0);
    };
    if (w == 0) *(LAS u32x4*)QCNT = u32x4{0u, 0u, (unsigned)tag, (unsigned)(tag >> 32)};
    wgbar();
    LAS const char* sl = (LAS const char*)lds;
#pragma unroll
    for (int st = 0; st < 2; ++st) { xf[st] = *(LAS const bf16x8*)(sl + (xa0 ^ (st << 5))); rdw(sl, st, 0); rdw(sl, st, 1); }
    __builtin_amdgcn_sched_barrier(0);
#pragma clang loop unroll(disable)
    for (int s = 0; s < 7; ++s) {
        step(0, sl, false); step(1, sl, false);
        sl = (sl == (LAS const char*)lds + 49152) ? (LAS const char*)lds : sl + 24576;
        step(2, sl, true); step(3, sl, false);
    }
    step(0, sl, false); step(1, sl, false);
    mm1(2, 0); mm1(2, 1); mm1(3, 0); mm1(3, 1);
    ST(4);
#pragma unroll
    for (int i = 0; i < 2; ++i) qkv_store(ws, acc[i], col >> 2, 8 * (col & 3) + 4 * (w >> 1) + 2 * i, 2 * row + (w & 1), lane);
    publish4(ws + WS_FQ + a * 128, tag, lane);
    ST(7);
}
DI void qkv2_loader(const char* x, const char* wqkv, int rowt, int col, int lw) {
    const int lane = lane_id(), row = 16 * lw + (lane >> 3);
    const unsigned w0 = row * 3072 + (((lane & 7) ^ (((row >> 1) & 1) << 2)) << 4), w1 = w0 + 8 * 3072 - 1024;
    const unsigned x0 = row * 1024 + (((lane & 7) ^ ((row >> 1) & 7)) << 4), x1 = (row + 8) * 1024 + (((lane & 7) ^ (((row + 8) >> 1) & 7)) << 4) - 1024;
    const u32x4 rx = srd(x + (size_t)rowt * 65536, 65536), rw = srd(wqkv + col * 128, 512 * 3072 - col * 128);
    auto issue = [&](int s) {
        asm volatile("s_mov_b32 m0, %[m]\n\ts_nop 0\n\t"
                     "buffer_load_dwordx4 %[w0], %[rw], %[ow] offen lds\n\t"
                     "buffer_load_dwordx4 %[w1], %[rw], %[ow] offen offset:1024 lds\n\t"
                     "s_add_u32 m0, %[m], 8192\n\ts_nop 0\n\t"
                     "buffer_load_dwordx4 %[x0], %[rx], %[ox] offen lds\n\t"
                     "buffer_load_dwordx4 %[x1], %[rx], %[ox] offen offset:1024 lds"
                     :: [m] "s"((s & 3) * 16384 + lw * 2048), [ow] "s"(s * 196608), [ox] "s"(s * 128), [rw] "s"(rw), [rx] "s"(rx),
                        [w0] "v"(w0), [w1] "v"(w1), [x0] "v"(x0), [x1] "v"(x1)
                     : "memory", "scc");
    };
    issue(0); issue(1); issue(2);
#pragma clang loop unroll(disable)
    for (int s = 3; s < 8; ++s) {
        asm volatile("s_waitcnt vmcnt(8)\n\ts_barrier" ::: "memory");
        issue(s);
    }
    asm volatile("s_waitcnt vmcnt(8)\n\ts_barrier\n\ts_waitcnt vmcnt(4)\n\ts_barrier\n\ts_waitcnt vmcnt(0)\n\ts_barrier" ::: "memory");
}
#define QRD3(B, SLOT, K)                                                                             \
    "ds_read_b64_tr_b16 v[" #B ":" #B "+1], %[t] offset:" #SLOT "*16384+" #K "*2048\n\t"              \
    "ds_read_b64_tr_b16 v[" #B "+2:" #B "+3], %[t] offset:" #SLOT "*16384+" #K "*2048+512\n\t"        \
    "ds_read_b128 v[" #B "+4:" #B "+7], %[x" #K "] offset:" #SLOT "*16384\n\t"
#define QSTAGE(SLOT, MM, N0, N1, N2, N3, P0, P1, P2, P3)                              \
    "s_waitcnt lgkmcnt(0)\n\t"                                                        \
    "s_barrier\n\t"                                                                   \
    MM(P0) QRD3(N0, SLOT, 0) MM(P1) QRD3(N1, SLOT, 1) MM(P2) QRD3(N2, SLOT, 2) MM(P3) QRD3(N3, SLOT, 3)
#define QST_E(SLOT, MM) QSTAGE(SLOT, MM, 32, 40, 48, 56, 96, 104, 112, 120)
#define QST_O(SLOT, MM) QSTAGE(SLOT, MM, 96, 104, 112, 120, 32, 40, 48, 56)
#define QMT(P) MF("v[16:31]", "v[" #P ":" #P "+3]", "v[" #P "+4:" #P "+7]", "v[16:31]")
#define QMV(P) MF("v[16:31]", "v[" #P "+4:" #P "+7]", "v[" #P ":" #P "+3]", "v[16:31]")
#define QTILE_ASM(MM)                                                                                             \
    asm volatile(                                                                                                 \
        "s_barrier\n\t"                                                                                           \
        QRD3(32, 0, 0) QRD3(40, 0, 1) QRD3(48, 0, 2) QRD3(56, 0, 3)                                               \
        QST_O(1, MM) QST_E(2, MM)                                                                                 \
        "s_mov_b32 s44, 0\n\t"                                                                                    \
        "LQ_loop%=:\n\t"                                                                                          \
        QST_O(3, MM)                                                                                              \
        "s_cmp_lg_u32 s44, 0\n\t"                                                                                 \
        "s_cbranch_scc1 LQ_end%=\n\t"                                                                             \
        QST_E(0, MM) QST_O(1, MM) QST_E(2, MM)                                                                    \
        "s_mov_b32 s44, 1\n\t"                                                                                    \
        "s_branch LQ_loop%=\n\t"                                                                                  \
        "LQ_end%=:\n\t"                                                                                           \
        "s_waitcnt lgkmcnt(0)\n\t"                                                                                \
        MM(96) MM(104) MM(112) MM(120)                                                                            \
        "s_nop 15\n\t"                                                                                            \
        : "+{v[16:31]}"(acc)                                                                                      \
        : [t] "v"(t), [x0] "v"(xa), [x1] "v"(xa ^ 32), [x2] "v"(xa ^ 64), [x3] "v"(xa ^ 96)                       \
        : "memory", "scc", "s44",                                                                                 \
          "v32", "v33", "v34", "v35", "v36", "v37", "v38", "v39", "v40", "v41", "v42", "v43", "v44", "v45", "v46", "v47", \
          "v48", "v49", "v50", "v51", "v52", "v53", "v54", "v55", "v56", "v57", "v58", "v59", "v60", "v61", "v62", "v63", \
          "v96", "v97", "v98", "v99", "v100", "v101", "v102", "v103", "v104", "v105", "v106", "v107", "v108", "v109", "v110", "v111", \
          "v112", "v113", "v114", "v115", "v116", "v117", "v118", "v119", "v120", "v121", "v122", "v123", "v124", "v125", "v126", "v127")
DI void qkv2_body(char* ws, int row, int col, int bi, int w, u64 tag) {
    const int lane = lane_id(), r = lane & 31, h2 = lane >> 5, q = (lane & 15) >> 2;
    const unsigned t = (8 * h2 + q) * 128 + (((w & 1) ^ ((q >> 1) & 1)) << 6) + ((lane >> 4) & 1) * 32 + (lane & 3) * 8;
    const unsigned xa = 8192 + (32 * (w >> 1) + r) * 128 + ((h2 ^ ((r >> 1) & 7)) << 4);
    f32x16 acc = {};
    if (col < 16) QTILE_ASM(QMT);
    else QTILE_ASM(QMV);
    qkv_store(ws, acc, col >> 3, 4 * (col & 7) + 2 * (w & 1), 2 * row + (w >> 1), lane);
    publish4(ws + WS_FQ + 32768 + bi * 128, tag, lane);
}

#define EXP(a) "v_exp_f32 v" #a ", v" #a "\n\t"
#define CVT(d, a, b) "v_cvt_pk_bf16_f32 v" #d ", v" #a ", v" #b "\n\t"
#define ASTEP(c0, c1, c2, c3, c4, c5, c6, c7, c8, c9, c10, c11, c12, c13, c14, c15, M1, M2, CL, M3)                  \
    EXP(c0) EXP(c1) M1 EXP(c2) EXP(c3) EXP(c4) EXP(c5) M2 CL                                                          \
    EXP(c6) EXP(c7) CVT(96, c0, c1) CVT(97, c2, c3) CVT(98, c4, c5) CVT(99, c6, c7) EXP(c8) EXP(c9) M3                \
    EXP(c10) EXP(c11) EXP(c12) EXP(c13) EXP(c14) EXP(c15)                                                             \
    CVT(100, c8, c9) CVT(101, c10, c11) CVT(102, c12, c13) CVT(103, c14, c15)
#define R_OA "v[64:79]"
#define R_OB "v[80:95]"
#define R_SA "v[32:47]"
#define R_SB "v[48:63]"
#define ATILE(R_NM, KC, KN, V0C, V1C, V0N, V1N, KOFF, VA, VOFF0, VOFF1, PRE)                                                    \
    ASTEP(32, 33, 34, 35, 36, 37, 38, 39, 40, 41, 42, 43, 44, 45, 46, 47,                                             \
          MF(R_OB, V1N, "v[100:103]", R_OB), MF(R_SB, KC, "v[8:11]", R_NM),                                           \
          PRE                                                                                                         \
          "ds_read_b128 " KC ", v12 offset:" KOFF "\n\t"                                                              \
          "ds_read_b128 " V0N ", " VA " offset:" #VOFF0 "\n\t"                                                        \
          "ds_read_b128 " V1N ", " VA " offset:" #VOFF1 "\n\t",                                                          \
          MF(R_OA, V0C, "v[96:99]", R_OA))                                                                            \
    ASTEP(48, 49, 50, 51, 52, 53, 54, 55, 56, 57, 58, 59, 60, 61, 62, 63,                                             \
          MF(R_OA, V1C, "v[100:103]", R_OA), MF(R_SA, KN, "v[4:7]", R_NM), "", MF(R_OB, V0C, "v[96:99]", R_OB))
#define ATILE_E(NM, KOFF, VA, VOFF0, VOFF1, PRE) ATILE(NM, "v[104:107]", "v[108:111]", "v[112:115]", "v[116:119]", "v[120:123]", "v[124:127]", KOFF, VA, VOFF0, VOFF1, PRE)
#define ATILE_O(NM, KOFF, VA, VOFF0, VOFF1, PRE) ATILE(NM, "v[108:111]", "v[104:107]", "v[120:123]", "v[124:127]", "v[112:115]", "v[116:119]", KOFF, VA, VOFF0, VOFF1, PRE)
#define MOV4(a, b, c, d, s) "v_mov_b32 v" #a ", " s "\n\tv_mov_b32 v" #b ", " s "\n\tv_mov_b32 v" #c ", " s "\n\tv_mov_b32 v" #d ", " s "\n\t"
#define WL "s_waitcnt lgkmcnt(0)\n\t"
#define AISSUE                                                   \
    "s_add_u32 m0, s44, %[lw]\n\t"                               \
    "s_add_u32 s44, s44, 0x4000\n\t"                             \
    "buffer_load_dwordx4 v14, %[rk], s43 offen sc1 lds\n\t"      \
    "s_add_u32 m0, m0, 0x2000\n\t"                               \
    "s_and_b32 s44, s44, 0xffff\n\t"                             \
    "buffer_load_dwordx4 v14, %[rv], s43 offen sc1 lds\n\t"      \
    "s_add_u32 s43, s43, 0x2000\n\t"
#define PR2 "s_setprio 2\n\t"
#define PR3 ""
#define PR0 "s_setprio 1\n\t"
#define PR1 "s_setprio 0\n\t"
#define PRI "s_setprio 3\n\t"
constexpr int A_ONES = 65536, A_VOTE = 69632 + 512, A_LSUM = 71680;
struct AttnRegs { f32x16 oa, ob, nm; u32x4 p1, v1; unsigned ka, va, va1, va2, va3, go, fo; };
DI unsigned qkv_dep(int a, bool early) {
    const int lane = lane_id(), r = lane & 15, v = (lane >> 4) & 1, R = 4 * (a >> 5) + (lane & 3);
    const int kv = (r >> 3) + 8 * ((early ? 8 + 8 * v : 16 * v + 8 * (a & 1)) + (r & 7));
    const int qq = ((R >> 3) & 1) + 8 * ((R >> 4) * 24 + (R & 7));
    return ((a & 6) + (lane < 32 || !early ? kv : qq)) * 128;
}
DI void attn_prep(AttnRegs& R, int a, int w) {
    const int lane = lane_id(), r = lane & 31, kh = w >> 2;
    R.ka = 4096 * kh + lane * 16;
    R.va = r < 16 ? 8192 + 4096 * kh + (lane >> 5) * 256 + r * 16 : A_ONES + (r == 16 ? 0 : r == 20 ? 64 : (r > 20 && r < 28) ? 80 : 16);
    R.go = w * 1024 + lane * 16;
#pragma unroll
    for (int i = 0; i < 16; ++i) { R.oa[i] = 0.f; R.ob[i] = 0.f; }
    R.p1 = u32x4{0u, 0u, 0u, 0u};
    R.v1 = u32x4{0u, 0u, 0u, 0u};
    asm volatile("" : "+{v[64:79]}"(R.oa), "+{v[80:95]}"(R.ob), "+{v[100:103]}"(R.p1), "+{v[124:127]}"(R.v1),
                      "+{v12}"(R.ka), "+{v13}"(R.va), "+{v14}"(R.go));
}
#define ATTN_ASM(NM, NMIN) \
    asm volatile( \
        "v_and_b32 v15, 0x3ff, v14\n\t" \
        "s_mov_b32 s43, 0\n\t" \
        "s_mov_b32 s44, 0\n\t" \
        AISSUE \
        "global_load_dwordx4 v[4:7], v15, %[qa] sc1\n\t" \
        "global_load_dwordx4 v[8:11], v15, %[qb] sc1\n\t" \
        AISSUE AISSUE \
        "s_mov_b32 s40, 0xffff\n\t" \
        "s_mov_b32 s41, 0xffff\n\t" \
        "s_mov_b32 s42, 8\n\t" \
        "s_waitcnt vmcnt(4)\n\t" \
        "s_barrier\n\t" \
        "ds_read_b128 v[104:107], v12\n\t" \
        "ds_read_b128 v[108:111], v12 offset:1024\n\t" \
        "ds_read_b128 v[112:115], v13\n\t" \
        "ds_read_b128 v[116:119], v13 offset:512\n\t" \
        PR2 \
        "s_waitcnt lgkmcnt(3)\n\t" \
        MF(R_SA, "v[104:107]", "v[4:7]", NM) \
        "s_nop 15\n\t" \
        "LA_loop%=:\n\t" \
        ATILE_E(NM, "2048", "v13", 1024, 1536, WL PR0) \
        ATILE_O(NM, "3072", "v13", 2048, 2560, WL PR1) \
        ATILE_E(NM, "0", "v13", 3072, 3584, \
                "s_waitcnt vmcnt(2) lgkmcnt(0)\n\t" \
                "s_barrier\n\t" \
                "s_setprio 3\n\t" \
                "v_add_u32 v12, 0x4000, v12\n\t" \
                AISSUE \
                "v_and_b32 v12, 0xffff, v12\n\t" \
                PR2) \
        ATILE_O(NM, "1024", "v13", 0, 512, \
                WL PR3 \
                "s_mov_b64 exec, s[40:41]\n\t" \
                "v_add_u32 v13, 0x4000, v13\n\t" \
                "s_sub_u32 s42, s42, 1\n\t" \
                "v_and_b32 v13, 0xffff, v13\n\t" \
                "s_mov_b64 exec, -1\n\t" \
                "s_cmp_lg_u32 s42, 0\n\t") \
        "s_cbranch_scc1 LA_loop%=\n\t" \
        "s_waitcnt lgkmcnt(0)\n\t" \
        "s_setprio 3\n\t" \
        MF(R_OB, "v[124:127]", "v[100:103]", R_OB) \
        "s_nop 15\n\t" \
        : "+{v[64:79]}"(R.oa), "+{v[80:95]}"(R.ob), "+{v[100:103]}"(R.p1), "+{v[124:127]}"(R.v1), "+{v12}"(R.ka), "+{v13}"(R.va) \
        : NMIN "{v14}"(R.go), [qa] "s"(qa), [qb] "s"(qb), [rk] "s"(rk), [rv] "s"(rv), [lw] "s"(w * 1024) \
        : "memory", "scc", "vcc", "s40", "s41", "s42", "s43", "s44", "s45", "v0", "v1", "v4", "v5", "v6", "v7", "v8", "v9", "v10", "v11", "v15", \
          "v32", "v33", "v34", "v35", "v36", "v37", "v38", "v39", "v40", "v41", "v42", "v43", "v44", "v45", "v46", "v47", \
          "v48", "v49", "v50", "v51", "v52", "v53", "v54", "v55", "v56", "v57", "v58", "v59", "v60", "v61", "v62", "v63", \
          "v96", "v97", "v98", "v99", "v104", "v105", "v106", "v107", "v108", "v109", "v110", "v111", \
          "v112", "v113", "v114", "v115", "v116", "v117", "v118", "v119", "v120", "v121", "v122", "v123")
DI void attn_pass_shift(AttnRegs& R, const char* qa, const char* qb, const char* ksrc, const char* vsrc, int w) {
    const u32x4 rk = srd(ksrc, 65536), rv = srd(vsrc, 65536);
#define NM_IN "{v[16:31]}"(R.nm),
    ATTN_ASM("v[16:31]", NM_IN);
}
#define FISS(S)                                                  \
    "s_add_u32 m0, %[lw], " #S "*16384\n\t"                      \
    "s_add_u32 s43, s43, 0x2000\n\t"                             \
    "buffer_load_dwordx4 v14, %[rk], s43 offen sc1 lds\n\t"      \
    "s_add_u32 m0, %[lw], " #S "*16384+8192\n\t"                 \
    "s_nop 0\n\t"                                                \
    "buffer_load_dwordx4 v14, %[rv], s43 offen sc1 lds\n\t"
#define FI2(I) "s_cbranch_vccz 7f\n\t" PRI I "7:\n\t"
#define FI3(I) "s_cbranch_vccnz 7f\n\t" PRI I PR2 "7:\n\t"
#define FCHUNK(S, N, VC, VN, BND, I2, I3)                        \
    ATILE_E("0", #S "*16384+2048", VC, 1024, 1536, WL PR0)       \
    ATILE_O("0", #S "*16384+3072", VC, 2048, 2560, WL PR1)       \
    ATILE_E("0", #N "*16384", VC, 3072, 3584, BND I2 PR2)        \
    ATILE_O("0", #N "*16384+1024", VN, 0, 512, WL PR3 I3)
#define FLD "global_load_dwordx2 v[0:1], v2, %[ff] sc1\n\t"
#define VCCSET "s_mov_b32 vcc_lo, %[lo4]\n\ts_mov_b32 vcc_hi, 0\n\t"
#define FBAR(VM) "s_waitcnt vmcnt(" #VM ") lgkmcnt(0)\n\ts_barrier\n\t"
DI void attn_pass(AttnRegs& R, const char* qa, const char* qb, const char* ksrc, const char* vsrc, const char* late_flags, u64 tag, int w) {
    const u32x4 rk = srd(ksrc, 65536), rv = srd(vsrc, 65536);
    asm volatile(
        "v_and_b32 v15, 0x3ff, v14\n\t"
        "s_mov_b32 s43, -0x2000\n\t"
        FISS(0)
        "global_load_dwordx4 v[4:7], v15, %[qa] sc1\n\t"
        "global_load_dwordx4 v[8:11], v15, %[qb] sc1\n\t"
        FISS(1) FISS(2) VCCSET
        "s_waitcnt vmcnt(4)\n\t"
        "s_barrier\n\t"
        "ds_read_b128 v[104:107], v12\n\t"
        "ds_read_b128 v[108:111], v12 offset:1024\n\t"
        "ds_read_b128 v[112:115], v13\n\t"
        "ds_read_b128 v[116:119], v13 offset:512\n\t"
        PR2
        "s_waitcnt lgkmcnt(3)\n\t"
        MF(R_SA, "v[104:107]", "v[4:7]", "0")
        "s_nop 15\n\t"
        FCHUNK(0, 1, "v13", "v16", FBAR(2), FI2(FLD FISS(3)), FI3(FLD FISS(3)))
        FCHUNK(1, 2, "v16", "v17",
               "s_waitcnt vmcnt(2) lgkmcnt(0)\n\t"
               "s_mov_b32 s45, 0x100000\n\t"
               "LA_p%=:\n\t"
               "v_cmp_ne_u64 vcc, %[tag], v[0:1]\n\t"
               "s_sub_u32 s45, s45, 1\n\t"
               "s_cbranch_scc1 LA_f%=\n\t"
               "s_cbranch_vccz LA_f%=\n\t"
               "global_load_dwordx2 v[0:1], v2, %[ff] sc1\n\t"
               "s_waitcnt vmcnt(0)\n\t"
               "s_branch LA_p%=\n\t"
               "LA_f%=:\n\t"
               "s_barrier\n\t" VCCSET, FI2(FISS(0)), FI3(FISS(0)))
        FCHUNK(2, 3, "v17", "v18", FBAR(2), FI2(FISS(1)), FI3(FISS(1)))
        FCHUNK(3, 0, "v18", "v13", FBAR(2), FI2(FISS(2)), FI3(FISS(2)))
        FCHUNK(0, 1, "v13", "v16", FBAR(2), FI2(FISS(3)), FI3(FISS(3)))
        FCHUNK(1, 2, "v16", "v17", FBAR(2), "", "")
        FCHUNK(2, 3, "v17", "v18", FBAR(0), "", "")
        FCHUNK(3, 0, "v18", "v13", WL, "", "")
        "s_waitcnt lgkmcnt(0)\n\t"
        "s_setprio 3\n\t"
        MF(R_OB, "v[124:127]", "v[100:103]", R_OB)
        "s_nop 15\n\t"
        : "+{v[64:79]}"(R.oa), "+{v[80:95]}"(R.ob), "+{v[100:103]}"(R.p1), "+{v[124:127]}"(R.v1)
        : "{v12}"(R.ka), "{v13}"(R.va), "{v16}"(R.va1), "{v17}"(R.va2), "{v18}"(R.va3), "{v14}"(R.go), "{v2}"(R.fo),
          [qa] "s"(qa), [qb] "s"(qb), [rk] "s"(rk), [rv] "s"(rv), [lw] "s"(w * 1024), [tag] "s"(tag), [ff] "s"(late_flags), [lo4] "s"(1 - (w >> 2))
        : "memory", "scc", "vcc", "s43", "s45", "v0", "v1", "v4", "v5", "v6", "v7", "v8", "v9", "v10", "v11", "v15",
          "v32", "v33", "v34", "v35", "v36", "v37", "v38", "v39", "v40", "v41", "v42", "v43", "v44", "v45", "v46", "v47",
          "v48", "v49", "v50", "v51", "v52", "v53", "v54", "v55", "v56", "v57", "v58", "v59", "v60", "v61", "v62", "v63",
          "v96", "v97", "v98", "v99", "v104", "v105", "v106", "v107", "v108", "v109", "v110", "v111",
          "v112", "v113", "v114", "v115", "v116", "v117", "v118", "v119", "v120", "v121", "v122", "v123");
}

DI void wait_qkv(const char* ws, int a, u64 tag) { wait_flags<PSL>(ws + WS_FQ, qkv_dep(a, true), tag); }
DI void attn_role(const char* x, const char* wqkv, char* ws, char* lds, int a, int w) {
    const int kh = w >> 2;
    const int head = 4 * (a & 7) + ((a >> 3) & 3), qt = 8 * (a >> 5) + 2 * (w & 3);
    ST_DECL; ST(0);
    __builtin_amdgcn_s_setprio(3);
    u64 tag;
    const int ti = a >> 3, trow = 8 * (a & 1) + (ti & 7) + (ti >= 24 ? 16 : 0), tcol = (ti >= 24 ? 0 : 4 * (ti >> 3)) + ((a & 7) >> 1);
    if (w >= 4) {
        qkv_loader(x, wqkv, trow, tcol, w - 4, ws + WS_FA + a * 128);
        tag = uni64(*(LAS const u64*)(QCNT + 8));
        if (w == 4) wait_qkv(ws, a, tag);
    } else {
        tag = next_tag(ld_epoch(ws));
        if (tcol < 8) qkv_body<true>(ws, lds, trow, tcol, a, w, tag ST_PASS);
        else qkv_body<false>(ws, lds, trow, tcol, a, w, tag ST_PASS);
    }
    AttnRegs R;
    attn_prep(R, a, w);
    {
        const unsigned st = (lane_id() & 16) ? 0u : 16384u;
        R.va1 = R.va + st; R.va2 = R.va1 + st; R.va3 = R.va2 + st;
        R.fo = qkv_dep(a, false);
        asm volatile("" : "+{v16}"(R.va1), "+{v17}"(R.va2), "+{v18}"(R.va3), "+{v2}"(R.fo));
    }
    ST(5);
    wgbar();
    ST(6);
    {
        const int o = w * 512 + lane_id() * 8;
        const unsigned one = ((o & 0x1b0) == 0) ? 0x3f803f80u : 0u;
        *(LAS u32x2*)((LAS char*)lds + A_ONES + o) = u32x2{one, one};
        asm volatile("s_waitcnt lgkmcnt(0)" ::: "memory");
    }
    const char* ksrc = ws + WS_K + (size_t)head * 65536;
    const char* qsrc = ws + WS_Q + (size_t)(head * 64 + qt) * 1024;
    auto finish = [&](int att) {
        const int lane = lane_id();
        const f32x16 oa = R.oa, ob = R.ob;
        LAS char* ex = (LAS char*)lds + w * 2048 + lane * 16;
        LAS const char* exr = (LAS const char*)lds + (w ^ 4) * 2048 + lane * 16;
        *(LAS f32x4*)ex = f32x4{ob[0], ob[1], ob[2], ob[3]};
        *(LAS f32x4*)(ex + 1024) = f32x4{ob[4], ob[5], ob[6], ob[7]};
        *(LAS float*)((LAS char*)lds + A_LSUM + w * 256 + lane * 4) = ob[8];
        if (att == 0) {
            float big = fmaxf(oa[8], ob[8]) * 2.7e8f;
#pragma unroll
            for (int i = 0; i < 8; ++i) big = fmaxf(fmaxf(big, __builtin_fabsf(oa[i])), __builtin_fabsf(ob[i]));
            const bool bad = !__all(fminf(oa[8], ob[8]) > 8.1e-28f && big < 1.7e38f);
            if (lane == 0) ((LAS unsigned*)((LAS char*)lds + A_VOTE))[w] = bad;
        }
        wgbar();
        const f32x4 t0 = *(LAS const f32x4*)exr, t1 = *(LAS const f32x4*)(exr + 1024);
        const float t2 = *(LAS const float*)((LAS char*)lds + A_LSUM + (w ^ 4) * 256 + lane * 4);
        unsigned redo = 0;
        if (att == 0) {
            const u32x4 v0 = *(LAS const u32x4*)((LAS char*)lds + A_VOTE), v1 = *(LAS const u32x4*)((LAS char*)lds + A_VOTE + 16);
            redo = __builtin_amdgcn_readfirstlane(v0[0] | v0[1] | v0[2] | v0[3] | v1[0] | v1[1] | v1[2] | v1[3]);
        }
        const float rinv = __builtin_amdgcn_rcpf(oa[8] + t2);
        u32x4 ov;
        ov[0] = cvtpk((oa[0] + t0[0]) * rinv, (oa[1] + t0[1]) * rinv);
        ov[1] = cvtpk((oa[2] + t0[2]) * rinv, (oa[3] + t0[3]) * rinv);
        ov[2] = cvtpk((oa[4] + t1[0]) * rinv, (oa[5] + t1[1]) * rinv);
        ov[3] = cvtpk((oa[6] + t1[2]) * rinv, (oa[7] + t1[3]) * rinv);
        if (att == 0 ? !redo : att == 1 + kh) {
            st16(ws + WS_O + (size_t)((qt + kh) * 32 + head) * 1024, lane * 16, ov);
            drain();
            st_flag(ws + WS_FO + ((qt + kh) * 32 + head) * 128, tag);
        }
        asm volatile("" : "+s"(redo));
        return redo;
    };
    ST(1);
    attn_pass(R, qsrc + kh * 1024, qsrc + (1 - kh) * 1024, ksrc, ksrc + (WS_V - WS_K), ws + WS_FQ + 32768, tag, w);
    ST(2);
    if (finish(0)) {
#pragma clang loop unroll(disable)
        for (int att = 1; att < 3; ++att) {
            drain();
            wgbar();
            const int lane = lane_id();
            const char* qa = qsrc + (att - 1) * 1024;
            bf16x8 qf;
            asm volatile("global_load_dwordx4 %0, %1, %2 sc1\n\ts_waitcnt vmcnt(0)" : "=v"(qf) : "v"(lane * 16), "s"(qa) : "memory");
            float mx = -INFINITY;
#pragma clang loop unroll(disable)
            for (int t = 0; t < 64; ++t) {
                bf16x8 kf;
                asm volatile("global_load_dwordx4 %0, %1, %2 sc1\n\ts_waitcnt vmcnt(0)" : "=v"(kf) : "v"(lane * 16), "s"(ksrc + t * 1024) : "memory");
                const f32x16 s = MFMA32(kf, qf, f32x16{});
#pragma unroll
                for (int i = 0; i < 16; ++i) mx = fmaxf(mx, s[i]);
            }
            mx = -swap32_max(mx);
            attn_prep(R, a, w);
#pragma unroll
            for (int i = 0; i < 16; ++i) R.nm[i] = mx;
            attn_pass_shift(R, qa, qa, ksrc, ksrc + (WS_V - WS_K), w);
            finish(att);
        }
    }
    ST(3); ST_OUT(0);
}

#define BG1 ""
#define BG2 "s_sleep 1\n\t"
#define BG3 "s_sleep 1\n\t"
#define SISSUE                                                             \
    "s_mov_b32 m0, s44\n\t"                                                 \
    "s_add_u32 s45, %[ow], %[wst]\n\t"                                      \
    "buffer_load_dwordx4 %[g0], %[rw], %[ow] offen lds\n\t"                 \
    "buffer_load_dwordx4 %[g1], %[rw], %[ow] offen offset:1024 lds\n\t"     \
    "s_add_u32 m0, s44, 8192\n\t"                                           \
    "s_add_u32 s46, %[od], 4096\n\t"                                        \
    "buffer_load_dwordx4 %[d0], %[rd], %[od] offen sc1 lds\n\t"             \
    "buffer_load_dwordx4 %[d0], %[rd], %[od] offen offset:1024 sc1 lds\n\t" \
    "s_add_u32 s44, s44, 0x4000\n\t"                                        \
    "s_and_b32 s44, s44, 0xffff\n\t"                                        \
    "s_cmp_eq_u32 %[od], %[de]\n\t"                                         \
    "s_cselect_b32 %[ow], 0, s45\n\t"                                       \
    "s_cselect_b32 %[od], 0, s46\n\t"
#define SISW                                                               \
    "s_mov_b32 m0, s44\n\t"                                                 \
    "s_add_u32 s45, %[ow], %[wst]\n\t"                                      \
    "buffer_load_dwordx4 %[g0], %[rw], %[ow] offen lds\n\t"                 \
    "buffer_load_dwordx4 %[g1], %[rw], %[ow] offen offset:1024 lds\n\t"     \
    "s_add_u32 s44, s44, 0x4000\n\t"                                        \
    "s_mov_b32 %[ow], s45\n\t"
#define SISD                                                               \
    "s_add_u32 m0, s44, 8192\n\t"                                           \
    "s_add_u32 s44, s44, 0x4000\n\t"                                        \
    "buffer_load_dwordx4 %[d0], %[rd], %[od] offen sc1 lds\n\t"             \
    "buffer_load_dwordx4 %[d0], %[rd], %[od] offen offset:1024 sc1 lds\n\t" \
    "s_add_u32 %[od], %[od], 4096\n\t"
struct SG { u32x4 rw, rd; unsigned wst, de, ow, od, a, b, c; };
DI void sgemm_prew(SG& G, int lw) {
    asm volatile("s_mov_b32 s44, %[lo]\n\t" SISW SISW SISW
                 : [ow] "+s"(G.ow) : [wst] "s"(G.wst), [rw] "s"(G.rw), [lo] "s"(lw * 2048), [g0] "v"(G.a), [g1] "v"(G.b) : "memory", "scc", "s44", "s45");
}
#define WISSUE                                                              \
    "s_and_b32 s45, s44, 0xc000\n\t"                                        \
    "s_add_u32 m0, s45, %[lo2]\n\t"                                         \
    "s_lshl_b32 s45, s45, 3\n\t"                                            \
    "buffer_load_dwordx4 %[o0], %[ro], s45 offen lds\n\t"                   \
    "buffer_load_dwordx4 %[o1], %[ro], s45 offen offset:1024 lds\n\t"       \
    "buffer_load_dwordx4 %[o2], %[ro], s45 offen offset:2048 lds\n\t"       \
    "buffer_load_dwordx4 %[o3], %[ro], s45 offen offset:3072 lds\n\t"       \
    "s_add_u32 s44, s44, 0x4000\n\t"                                        \
    "s_and_b32 s44, s44, 0xffff\n\t"
template <int WO>
DI void sgemm_load(SG& G, int n, int lw, u32x4 ro, unsigned o0) {
    asm volatile(
        "s_mov_b32 s44, %[lo]\n\t"
        ".if %c[wo]\n\t"
        "s_sub_u32 s47, %[n], 5\n\t"
        SISD SISD SISD
        "s_waitcnt vmcnt(4)\n\ts_barrier\n\t" SISSUE
        "s_waitcnt vmcnt(6)\n\ts_barrier\n\t" SISSUE
        ".else\n\t"
        "s_sub_u32 s47, %[n], 3\n\t"
        SISSUE SISSUE SISSUE
        ".endif\n\t"
        "LL_loop%=:\n\t"
        "s_waitcnt vmcnt(8)\n\t"
        "s_barrier\n\t"
        SISSUE
        "s_sub_u32 s47, s47, 1\n\t"
        "s_cmp_lg_u32 s47, 0\n\t"
        "s_cbranch_scc1 LL_loop%=\n\t"
        ".if %c[wo]\n\t"
        "s_waitcnt vmcnt(8)\n\ts_barrier\n\t" WISSUE
        "s_waitcnt vmcnt(8)\n\ts_barrier\n\t" WISSUE
        "s_waitcnt vmcnt(8)\n\ts_barrier\n\t" WISSUE
        "s_barrier\n\t" WISSUE
        ".else\n\t"
        "s_waitcnt vmcnt(8)\n\ts_barrier\n\t"
        "s_waitcnt vmcnt(4)\n\ts_barrier\n\t"
        "s_waitcnt vmcnt(0)\n\ts_barrier\n\t"
        "s_barrier\n\t"
        ".endif\n\t"
        : [ow] "+s"(G.ow), [od] "+s"(G.od)
        : [wst] "s"(G.wst), [rw] "s"(G.rw), [rd] "s"(G.rd), [lo] "s"(lw * 2048), [de] "s"(G.de), [n] "s"(n), [g0] "v"(G.a), [g1] "v"(G.b), [d0] "v"(G.c),
          [wo] "i"(WO), [ro] "s"(ro), [lo2] "s"(lw * 4096), [o0] "v"(o0), [o1] "v"(o0 + 7168), [o2] "v"(o0 + 14336), [o3] "v"(o0 + 21504)
        : "memory", "scc", "s44", "s45", "s46", "s47");
}
#define SRD3(B, SLOT, K, TD)                                                                         \
    "ds_read_b64_tr_b16 v[" #B ":" #B "+1], %[t] offset:" #SLOT "*16384+" #K "*2048\n\t"              \
    "ds_read_b64_tr_b16 v[" #B "+2:" #B "+3], %[t] offset:" #SLOT "*16384+" #K "*2048+" #TD "\n\t"    \
    "ds_read_b128 v[" #B "+4:" #B "+7], %[dl] offset:" #SLOT "*16384+" #K "*1024\n\t"
#define SSTAGE(SLOT, MM, TD, N0, N1, N2, N3, P0, P1, P2, P3)                          \
    "s_waitcnt lgkmcnt(0)\n\t"                                                        \
    "s_barrier\n\t"                                                                   \
    MM(P0) SRD3(N0, SLOT, 0, TD) SRD3(N1, SLOT, 1, TD) BG1                            \
    MM(P1) SRD3(N2, SLOT, 2, TD) SRD3(N3, SLOT, 3, TD) BG2                            \
    MM(P2) BG3 MM(P3)
#define SST_E(SLOT, MM, TD) SSTAGE(SLOT, MM, TD, 32, 40, 48, 56, 96, 104, 112, 120)
#define SST_O(SLOT, MM, TD) SSTAGE(SLOT, MM, TD, 96, 104, 112, 120, 32, 40, 48, 56)
#define SMM_NONE(P) ""
#define SMM_WA(P) MF("v[64:79]", "v[" #P ":" #P "+3]", "v[" #P "+4:" #P "+7]", "v[64:79]")
#define SMM_WB(P) MF("v[80:95]", "v[" #P "+4:" #P "+7]", "v[" #P ":" #P "+3]", "v[80:95]")
#define SGEMM_ASM(ACC, MM, TD, NLOOP, TAIL)                                                                       \
    asm volatile(                                                                                                 \
        "s_mov_b32 s44, " #NLOOP "\n\t"                                                                           \
        "s_barrier\n\t"                                                                                           \
        SRD3(32, 0, 0, TD) SRD3(40, 0, 1, TD) SRD3(48, 0, 2, TD) SRD3(56, 0, 3, TD)                               \
        "LS_loop%=:\n\t"                                                                                          \
        SST_O(1, MM, TD) SST_E(2, MM, TD) SST_O(3, MM, TD) SST_E(0, MM, TD)                                       \
        "s_sub_u32 s44, s44, 1\n\t"                                                                               \
        "s_cmp_lg_u32 s44, 0\n\t"                                                                                 \
        "s_cbranch_scc1 LS_loop%=\n\t"                                                                            \
        TAIL                                                                                                      \
        "s_waitcnt lgkmcnt(0)\n\t"                                                                                \
        MM(96) BG3 MM(104) BG3 MM(112) BG3 MM(120)                                                                \
        "s_nop 15\n\t"                                                                                            \
        : ACC(acc)                                                                                                \
        : [t] "v"(G.a), [dl] "v"(G.b)                                                                             \
        : "memory", "scc", "s44",                                                                                 \
          "v32", "v33", "v34", "v35", "v36", "v37", "v38", "v39", "v40", "v41", "v42", "v43", "v44", "v45", "v46", "v47", \
          "v48", "v49", "v50", "v51", "v52", "v53", "v54", "v55", "v56", "v57", "v58", "v59", "v60", "v61", "v62", "v63", \
          "v96", "v97", "v98", "v99", "v100", "v101", "v102", "v103", "v104", "v105", "v106", "v107", "v108", "v109", "v110", "v111", \
          "v112", "v113", "v114", "v115", "v116", "v117", "v118", "v119", "v120", "v121", "v122", "v123", "v124", "v125", "v126", "v127")

template <int MODE>
DI SG sgemm_setup(const char* wbase, unsigned wbytes, int wrow, const char* dbase, int dblk, int rot, int w) {
    const int lane = lane_id(), row = lane >> 3, lw = w & 3;
    const int q = (lane & 15) >> 2, h2 = lane >> 5;
    SG G;
    G.rw = srd(wbase, wbytes); G.rd = srd(dbase, 2 * dblk);
    G.wst = 64 * wrow; G.de = (MODE == 0 ? 31 : 21) * 4096; G.ow = rot * G.wst; G.od = rot * 4096;
    if (w >= 4) {
        G.a = (16 * lw + row) * wrow + (((lane & 7) ^ (((row >> 1) & 1) << 2)) << 4);
        G.b = G.a + 8 * wrow - 1024;
        G.c = (lw >> 1) * dblk + (lw & 1) * 2048 + lane * 16;
    } else {
        G.a = ((MODE == 0 ? 8 : 4) * h2 + q) * 128 + (((lw & 1) ^ ((q >> 1) & 1)) << 6) + ((lane >> 4) & 1) * 32 + (lane & 3) * 8;
        G.b = 8192 + (lw >> 1) * 4096 + lane * 16;
        G.c = 0;
    }
    asm volatile("" : "+v"(G.a), "+v"(G.b), "+v"(G.c));
    return G;
}
#define ACC0 "+{v[64:79]}"
#define ACC1 "+{v[80:95]}"
template <int MODE>
DI void sgemm_run(f32x16& acc, SG& G) {
    if (MODE == 0) SGEMM_ASM(ACC0, SMM_WA, 512, 7, SST_O(1, SMM_WA, 512) SST_E(2, SMM_WA, 512) SST_O(3, SMM_WA, 512));
    else SGEMM_ASM(ACC1, SMM_WB, 1024, 5, SST_O(1, SMM_WB, 1024));
}
DI f32x4 sub4(const f32x16& a, int g) { return f32x4{a[4 * g], a[4 * g + 1], a[4 * g + 2], a[4 * g + 3]}; }

DI unsigned gelu2(float a, float b) {
    constexpr float c1 = -2.f * 0.7978845608028654f * 1.4426950408889634f, c2 = c1 * 0.044715f;
    const f32x2 u = {a, b};
    const f32x2 z = u * (u * u * c2 + c1);
    const f32x2 d = f32x2{__builtin_amdgcn_exp2f(z[0]), __builtin_amdgcn_exp2f(z[1])} + 1.f;
    const f32x2 o = u * f32x2{__builtin_amdgcn_rcpf(d[0]), __builtin_amdgcn_rcpf(d[1])};
    return cvtpk(o[0], o[1]);
}

#define WRD(R0, R1, R2, R3, STEP)                                                    \
    "ds_read_b64_tr_b16 " R0 ", %[t0] offset:" #STEP "*2048\n\t"                      \
    "ds_read_b64_tr_b16 " R1 ", %[t0] offset:" #STEP "*2048+1024\n\t"                 \
    "ds_read_b64_tr_b16 " R2 ", %[t1] offset:" #STEP "*2048\n\t"                      \
    "ds_read_b64_tr_b16 " R3 ", %[t1] offset:" #STEP "*2048+1024\n\t"
#define WRD0(S) WRD("v[32:33]", "v[34:35]", "v[36:37]", "v[38:39]", S)
#define WRD1(S) WRD("v[40:41]", "v[42:43]", "v[44:45]", "v[46:47]", S)
#define WRD2(S) WRD("v[48:49]", "v[50:51]", "v[52:53]", "v[54:55]", S)
#define WRD3(S) WRD("v[56:57]", "v[58:59]", "v[60:61]", "v[62:63]", S)
#define WMM(WAIT, W0, W1, O) "s_waitcnt " WAIT "\n\t" MF("v[96:111]", W0, O, "v[96:111]") MF("v[112:127]", W1, O, "v[112:127]")
#define OLD(K, R, V, OFF)                                             \
    "5:\n\t"                                                           \
    "s_bitcmp1_b32 s40, " #K "\n\t"                                    \
    "s_cbranch_scc1 6f\n\t"                                            \
    "global_load_dwordx2 v[24:25], %[fo], %[f] sc1\n\t"                       \
    "s_sub_u32 s41, s41, 1\n\t"                                        \
    "s_cselect_b32 s42, -1, 0\n\t"                                     \
    "s_waitcnt vmcnt(0)\n\t"                                           \
    "v_cmp_eq_u64 vcc, %[tag], v[24:25]\n\t"                                \
    "s_or_b32 s40, vcc_lo, s42\n\t"                                    \
    "s_bitcmp1_b32 s40, " #K "\n\t"                                    \
    "s_cbranch_scc1 6f\n\t"                                            \
    "s_sleep 1\n\t"                                                    \
    "s_branch 5b\n\t"                                                  \
    "6:\n\t"                                                           \
    "global_load_dwordx4 " R ", " V ", %[o] offset:" #OFF " sc1\n\t"
DI void wo_gemm(f32x16& a0, f32x16& a1, f32x4& k0, f32x4& k1, float& k2, const char* osrc, const char* fsrc, u64 tag, int w) {
    const int lane = lane_id();
    const int q = (lane & 15) >> 2, h2 = lane >> 5;
    const unsigned tb = (w >> 1) * 16384 + (4 * h2 + q) * 128 + ((lane >> 4) & 1) * 32 + (lane & 3) * 8;
    const unsigned t0 = tb + ((q >> 1) & 1) * 64, t1 = tb + (((q >> 1) & 1) ^ 1) * 64;
    const unsigned d0 = lane * 16, d1 = d0 + 4096, fo = (lane & 7) * 128;
    asm volatile(
        WRD0(0) WRD1(1) WRD2(2) WRD3(3)
        "s_mov_b32 s40, 0\n\t"
        "s_mov_b32 s41, 0x100000\n\t"
        "s_setprio 0\n\t"
        OLD(0, "v[64:67]", "%[d0]", 0) OLD(1, "v[68:71]", "%[d0]", 1024) OLD(2, "v[72:75]", "%[d0]", 2048) OLD(3, "v[76:79]", "%[d0]", 3072)
        OLD(4, "v[80:83]", "%[d1]", 0) OLD(5, "v[84:87]", "%[d1]", 1024) OLD(6, "v[88:91]", "%[d1]", 2048) OLD(7, "v[92:95]", "%[d1]", 3072)
        "s_setprio 3\n\t"
        "s_waitcnt vmcnt(7) lgkmcnt(0)\n\t"
        MF("v[96:111]", "v[32:35]", "v[64:67]", "0") MF("v[112:127]", "v[36:39]", "v[64:67]", "0")
        WRD0(4)
        WMM("vmcnt(6)", "v[40:43]", "v[44:47]", "v[68:71]") WRD1(5)
        WMM("vmcnt(5)", "v[48:51]", "v[52:55]", "v[72:75]") WRD2(6)
        WMM("vmcnt(4)", "v[56:59]", "v[60:63]", "v[76:79]") WRD3(7)
        WMM("vmcnt(3) lgkmcnt(12)", "v[32:35]", "v[36:39]", "v[80:83]")
        WMM("vmcnt(2) lgkmcnt(0)", "v[40:43]", "v[44:47]", "v[84:87]")
        WMM("vmcnt(1)", "v[48:51]", "v[52:55]", "v[88:91]")
        "s_barrier\n\t"
        WMM("vmcnt(0)", "v[56:59]", "v[60:63]", "v[92:95]")
        "s_nop 15\n\t"
        : "={v[96:111]}"(a0), "={v[112:127]}"(a1),
          "+{v[12:15]}"(k0), "+{v[16:19]}"(k1), "+{v20}"(k2)
        : [o] "s"(osrc), [f] "s"(fsrc), [tag] "s"(tag), [d0] "v"(d0), [d1] "v"(d1), [t0] "v"(t0), [t1] "v"(t1), [fo] "v"(fo)
        : "memory", "scc", "vcc", "s40", "s41", "s42", "v24", "v25",
          "v32", "v33", "v34", "v35", "v36", "v37", "v38", "v39", "v40", "v41", "v42", "v43", "v44", "v45", "v46", "v47",
          "v48", "v49", "v50", "v51", "v52", "v53", "v54", "v55", "v56", "v57", "v58", "v59", "v60", "v61", "v62", "v63",
          "v64", "v65", "v66", "v67", "v68", "v69", "v70", "v71", "v72", "v73", "v74", "v75", "v76", "v77", "v78", "v79",
          "v80", "v81", "v82", "v83", "v84", "v85", "v86", "v87", "v88", "v89", "v90", "v91", "v92", "v93", "v94", "v95");
}

DI void mixer_role(const char* x, const char* wqkv, const char* gw, const char* wo, const char* w1, const char* b1, const char* w2, const char* b2,
                   char* ws, char* out, char* lds, int bi, int w) {
    const int j = bi & 7, i = bi >> 3;
    __builtin_amdgcn_s_setprio(LNP);
    ST_DECL; ST(0);
    const u64 ep = ld_epoch(ws);
    u64 tag;
    {
        const int lane = lane_id(), h2 = lane >> 5, tid = 64 * w + lane;
        const u32x4 xr = *(GAS const u32x4*)(x + (size_t)(8 * bi + w) * 1024 + lane * 16);
        const u32x4 gr = *(GAS const u32x4*)(gw + lane * 16);
        tag = next_tag(ep);
        float v[8], gg[8];
#pragma unroll
        for (int k = 0; k < 4; ++k) { v[2 * k] = bflo(xr[k]); v[2 * k + 1] = bfhi(xr[k]); gg[2 * k] = bflo(gr[k]); gg[2 * k + 1] = bfhi(gr[k]); }
        float s1 = 0.f, s3 = 0.f;
#pragma unroll
        for (int k = 0; k < 8; ++k) { s1 += v[k]; s3 += v[k] * gg[k]; }
        const float mu = wave_sum(s1) * (1.f / 512.f);
        s3 = wave_sum(s3);
        float s2 = 0.f;
#pragma unroll
        for (int k = 0; k < 8; ++k) { v[k] -= mu; s2 += v[k] * v[k]; }
        const float rs = __builtin_amdgcn_rsqf(wave_sum(s2) * (1.f / 512.f) + 1e-5f);
        u32x4 hv;
#pragma unroll
        for (int k = 0; k < 4; ++k) hv[k] = cvtpk(v[2 * k] * rs, v[2 * k + 1] * rs);
        *(LAS u32x4*)((LAS char*)lds + w * 1024 + lane * 16) = hv;
        const float g = __builtin_amdgcn_rcpf(1.f + __builtin_amdgcn_exp2f(-1.4426950408889634f * s3));
        st4(ws + WS_G + (8 * bi + w) * 4, uni(g));
        wgbar();
        LAS const unsigned short* lb = (LAS const unsigned short*)lds + tid;
#pragma unroll
        for (int k = 0; k < 4; ++k) hv[k] = (unsigned)lb[(2 * k) * 512] | ((unsigned)lb[(2 * k + 1) * 512] << 16);
        st16(ws + WS_H + (size_t)(2 * w * 128 + (bi >> 1)) * 1024 + (bi & 1) * 512, h2 * 131072 + (lane & 31) * 16, hv);
        drain();
        wgbar();
        if (w == 0) st_flag(ws + WS_FL + bi * 128, tag);
    }
    __builtin_amdgcn_s_setprio(3);
    const int wb = w & 1, db = (w >> 1) & 1, hf = w >> 2;
    const int L = (j >> 1) * 22 + i, ft = L >> 2, dt1 = 4 * (j & 1) + (L & 3);
    const int st = 8 * (j >> 1) + (i & 7), dt = 4 * (j & 1) + (i >> 3);
    SG G1, G2;
    unsigned ex, l16, og, wof;
    {
        const int lane = lane_id();
        og = (64 * st + 32 * wb + (lane & 31)) * 4;
        G1 = sgemm_setup<0>(w1 + ft * 128, 2048 * 2752 - ft * 128, 2752, ws + WS_H + (size_t)2 * dt1 * 131072, 131072, ROT1, w);
        G2 = sgemm_setup<1>(w2 + st * 128, 1376 * 4096 - st * 128, 4096, ws + WS_U + (size_t)2 * dt * 86 * 1024, 86 * 1024, ROT2, w);
        l16 = lane * 16;
        ex = 65536 + (w & 3) * 2048 + l16;
        wof = (32 * (w & 3) + (lane >> 3)) * 1024 + (((lane & 7) ^ (((lane >> 4) & 1) << 2)) << 4);
        asm volatile("" : "+v"(ex), "+v"(l16), "+v"(og), "+v"(wof));
    }
    {
        const int row = 16 + 8 * (j & 1) + (i & 7), col = 8 + 8 * (i >> 4) + 2 * (j >> 1) + ((i >> 3) & 1);
        if (w == 0) {
            *(LAS unsigned*)QCNT = 0u;
            wait_flags<2>(ws + WS_FA + bi * 128, 0, tag);
        }
        wgbar();
        if (w >= 4) qkv2_loader(x, wqkv, row, col, w - 4);
        else qkv2_body(ws, row, col, bi, w, tag);
    }
    ST(6);
    if (PFA && i >= 22) {
        const int wv = ((i - 22) * 8 + j) * 8 + w;
        for (int k = 0; k < PFD; ++k) __builtin_amdgcn_s_sleep(64);
        warm(w2, 1376, wv, l16 * 4);
        warm(wo, 128, wv, l16 * 4);
    }
    if (i < 22) {
        if (w >= 4) wait_flags<8>(ws + WS_FL + (w - 4) * 8192, l16 * 8, tag);
        if (w == 4) wait_qkv(ws, bi, tag);
        wgbar();
        ST(7);
        f32x4 v0, v1;
        if (w >= 4) {
            sgemm_load<0>(G1, 32, w - 4, G1.rw, 0);
            v0 = *(LAS const f32x4*)(uintptr_t)ex; v1 = *(LAS const f32x4*)(uintptr_t)(ex + 1024);
        } else {
            f32x16 acc1;
            const char* bsrc = b1 + (64 * ft + (ft < 21 ? 32 * wb : 0)) * 2 + (l16 >> 9) * 8;
#pragma unroll
            for (int k = 0; k < 4; ++k) {
                const u32x2 bv = *(GAS const u32x2*)(bsrc + 16 * k);
                acc1[4 * k] = bflo(bv[0]); acc1[4 * k + 1] = bfhi(bv[0]); acc1[4 * k + 2] = bflo(bv[1]); acc1[4 * k + 3] = bfhi(bv[1]);
            }
            sgemm_run<0>(acc1, G1);
            *(LAS f32x4*)(uintptr_t)ex = sub4(acc1, 2); *(LAS f32x4*)(uintptr_t)(ex + 1024) = sub4(acc1, 3);
            wgbar();
            v0 = sub4(acc1, 0); v1 = sub4(acc1, 1);
        }
        ST(1);
        const u32x4 o = {gelu2(v0[0], v0[1]), gelu2(v0[2], v0[3]), gelu2(v1[0], v1[1]), gelu2(v1[2], v1[3])};
        if (ft < 21 || wb == 0) st16(ws + WS_U + (size_t)((2 * dt1 + db) * 86 + 4 * ft + 2 * wb + hf) * 1024, l16, o);
        drain();
        wgbar();
        if (w == 0) st_flag(ws + WS_FU + (dt1 * 32 + ft) * 128, tag);
    }
    ST(2);
    if (w >= 4) sgemm_prew(G2, w - 4);
    if (bi == 255 && w == 1) {
        wait_flags256(ws + WS_FQ, l16 * 8, tag);
        wait_flags256(ws + WS_FL, l16 * 8, tag);
        st_flag(ws + WS_EPOCH, tag);
    }
    if (w == 0) wait_flags<8>(ws + WS_FU + dt * 4096, (l16 < 22 * 16 ? l16 : 0) * 8, tag);
    wgbar();
    ST(3);
    f32x4 bp0, bp1;
    float g;
    {
        asm volatile("global_load_dword %0, %1, %2 sc1" : "=v"(g) : "v"(og), "s"(ws + WS_G) : "memory");
        if (w >= 4) {
            sgemm_load<1>(G2, 22, w - 4, srd(wo + dt * 128, 524288 - dt * 128), wof);
            bp0 = *(LAS const f32x4*)(uintptr_t)ex; bp1 = *(LAS const f32x4*)(uintptr_t)(ex + 1024);
        } else {
            const float bb = bflo(*(GAS const unsigned short*)(b2 + (og >> 1)));
            f32x16 acc2 = {bb, bb, bb, bb, bb, bb, bb, bb, bb, bb, bb, bb, bb, bb, bb, bb};
            sgemm_run<1>(acc2, G2);
            *(LAS f32x4*)(uintptr_t)ex = sub4(acc2, 2); *(LAS f32x4*)(uintptr_t)(ex + 1024) = sub4(acc2, 3);
            wgbar();
            bp0 = sub4(acc2, 0); bp1 = sub4(acc2, 1);
            drain();
        }
        asm volatile("" : "+v"(g));
        ST(4);
        bp0 *= 1.f - g;
        bp1 *= 1.f - g;
        asm volatile("" : "+v"(bp0), "+v"(bp1));
    }
    drain();
    wgbar();
    ST(5);
    f32x16 a0, a1;
    wo_gemm(a0, a1, bp0, bp1, g, ws + WS_O + (size_t)((2 * st + wb) * 32 + 8 * (w >> 1)) * 1024,
            ws + WS_FO + ((2 * st + wb) * 32 + 8 * (w >> 1)) * 128, tag, w);
    {
        const int lane = lane_id(), kq = 2 * db + hf;
        LAS char* ex = (LAS char*)lds + w * 8192 + lane * 16;
#pragma unroll
        for (int k = 0; k < 4; ++k) { *(LAS f32x4*)(ex + k * 1024) = sub4(a0, k); *(LAS f32x4*)(ex + 4096 + k * 1024) = sub4(a1, k); }
        wgbar();
        LAS const char* exr = (LAS const char*)lds + wb * 8192 + kq * 2048 + lane * 16;
        f32x4 s0 = *(LAS const f32x4*)exr, s1 = *(LAS const f32x4*)(exr + 1024);
#pragma unroll
        for (int k = 1; k < 4; ++k) { s0 += *(LAS const f32x4*)(exr + k * 16384); s1 += *(LAS const f32x4*)(exr + k * 16384 + 1024); }
        s0 = s0 * g + bp0;
        s1 = s1 * g + bp1;
        const auto r0 = __builtin_amdgcn_permlane32_swap(cvtpk(s0[0], s0[1]), cvtpk(s1[0], s1[1]), false, false);
        const auto r1 = __builtin_amdgcn_permlane32_swap(cvtpk(s0[2], s0[3]), cvtpk(s1[2], s1[3]), false, false);
        *(GAS u32x4*)(out + (size_t)(64 * st + 32 * wb) * 1024 + (64 * dt + 16 * kq) * 2 + (lane & 31) * 1024 + (lane >> 5) * 16) = u32x4{r0[0], r1[0], r0[1], r1[1]};
    }
    ST_OUT(0);
}

constexpr int MK_LDS = 81920;
__global__ __launch_bounds__(512, 4) void mk(const char* x, const char* wqkv, const char* wo, const char* w1, const char* b1,
                                             const char* w2, const char* b2, const char* gw, char* ws, char* out) {
    extern __shared__ __attribute__((aligned(16))) char lds[];
    const int w = __builtin_amdgcn_readfirstlane(threadIdx.x >> 6);
    if (blockIdx.x < 256) attn_role(x, wqkv, ws, lds, blockIdx.x, w);
    else mixer_role(x, wqkv, gw, wo, w1, b1, w2, b2, ws, out, lds, blockIdx.x - 256, w);
}

extern "C" void kernel_launch(void* const* d_in, const int* in_sizes, int n_in, void* d_out, int out_size, void* d_ws, size_t ws_size,
                              hipStream_t stream) {
    (void)hipFuncSetAttribute((const void*)mk, hipFuncAttributeMaxDynamicSharedMemorySize, MK_LDS);
    mk<<<512, 512, MK_LDS, stream>>>((const char*)d_in[0], (const char*)d_in[1], (const char*)d_in[2], (const char*)d_in[4],
                                     (const char*)d_in[5], (const char*)d_in[6], (const char*)d_in[7], (const char*)d_in[8],
                                     (char*)d_ws, (char*)d_out);
}
```

```cpp
#include <hip/hip_runtime.h>
#include <hip/hip_bf16.h>
#include <stdint.h>

typedef __attribute__((ext_vector_type(8))) short bf16x8;
typedef __attribute__((ext_vector_type(4))) short s16x4;
typedef __attribute__((ext_vector_type(16))) float f32x16;
typedef __attribute__((ext_vector_type(4))) float f32x4;
typedef __attribute__((ext_vector_type(2))) float f32x2;
typedef __attribute__((ext_vector_type(4))) unsigned u32x4;
typedef __attribute__((ext_vector_type(2))) unsigned u32x2;
typedef unsigned long long u64;
#define LAS __attribute__((address_space(3)))
#define GAS __attribute__((address_space(1)))
#define DI __device__ __forceinline__
#define MFMA32(a, b, c) __builtin_amdgcn_mfma_f32_32x32x16_bf16((a), (b), (c), 0, 0, 0)

#define ROT1 ((4 * dt1 + 10 * ft) & 31)
#define ROT2 (((st + dt) & 1) * 11)
#define LNP 0
#define PFA 3
#define PFD 2
#define PSL 2
#define STAG 1
#define QROT 6
#define QROTM 2
constexpr int S_ = 2048, D_ = 512, F_ = 1376;
constexpr size_t WS_EPOCH = 0;
constexpr size_t WS_G = 4096;
constexpr size_t WS_FL = 98304;
constexpr size_t WS_FQ = 458752;
constexpr size_t WS_FU = 65536;
constexpr size_t WS_FA = 393216;
constexpr size_t WS_FO = 131072;
constexpr size_t WS_Q = 1u << 20;
constexpr size_t WS_K = WS_Q + (2u << 20);
constexpr size_t WS_V = WS_K + (2u << 20);
constexpr size_t WS_H = WS_V + (2u << 20);
constexpr size_t WS_U = WS_H + (2u << 20);
constexpr size_t WS_O = WS_U + (2u << 20);

#define ST_DECL
#define ST_ARG
#define ST_PASS
#define ST(i)
#define ST_OUT(k)
DI unsigned cvtpk(float lo, float hi) {
    typedef float f32x2_t __attribute__((ext_vector_type(2)));
    typedef __bf16 bf16x2_t __attribute__((ext_vector_type(2)));
    return __builtin_bit_cast(unsigned, __builtin_convertvector(f32x2_t{lo, hi}, bf16x2_t));
}
DI float bflo(unsigned u) { return __uint_as_float(u << 16); }
DI float bfhi(unsigned u) { return __uint_as_float(u & 0xffff0000u); }
DI s16x4 trread(LAS const char* p) {
    typedef short v4i16_t __attribute__((ext_vector_type(4)));
    return __builtin_bit_cast(s16x4, __builtin_amdgcn_ds_read_tr16_b64_v4i16((LAS v4i16_t*)p));
}
template <int CTRL> DI float dpp(float v) {
    return __int_as_float(__builtin_amdgcn_update_dpp(0, __float_as_int(v), CTRL, 0xf, 0xf, true));
}
DI float rl(float v, int l) { return __int_as_float(__builtin_amdgcn_readlane(__float_as_int(v), l)); }
DI float wave_sum(float v) {
    v += dpp<0xB1>(v); v += dpp<0x4E>(v); v += dpp<0x141>(v); v += dpp<0x140>(v);
    return (rl(v, 0) + rl(v, 16)) + (rl(v, 32) + rl(v, 48));
}
DI float swap32_max(float v) {
    auto rr = __builtin_amdgcn_permlane32_swap(__float_as_uint(v), __float_as_uint(v), false, false);
    return fmaxf(__uint_as_float(rr[0]), __uint_as_float(rr[1]));
}

DI int lane_id() {
    int l;
    asm volatile("v_mbcnt_lo_u32_b32 %0, -1, 0\n\tv_mbcnt_hi_u32_b32 %0, -1, %0" : "=v"(l));
    __builtin_assume(l >= 0 && l < 64);
    return l;
}
DI void st16(const char* sbase, unsigned voff, u32x4 v) {
    asm volatile("global_store_dwordx4 %0, %1, %2 sc1\n\ts_nop 1" ::"v"(voff), "v"(v), "s"(sbase) : "memory");
}
DI void st4(const char* p, unsigned v) {
    unsigned t0, t1;
    asm volatile("s_nop 1\n\tv_mov_b32 %0, %2\n\tv_mov_b32 %1, 0\n\ts_mov_b64 exec, 1\n\tglobal_store_dword %1, %0, %3 sc1\n\ts_mov_b64 exec, -1"
                 : "=&v"(t0), "=&v"(t1) : "s"(v), "s"(p) : "memory");
}
DI void st_flag(const char* p, u64 v) {
    u64 t0;
    unsigned t1;
    asm volatile("s_nop 1\n\tv_mov_b64 %0, %2\n\tv_mov_b32 %1, 0\n\ts_mov_b64 exec, 1\n\tglobal_store_dwordx2 %1, %0, %3 sc1\n\ts_mov_b64 exec, -1"
                 : "=&v"(t0), "=&v"(t1) : "s"(v), "s"(p) : "memory");
}
DI u64 uni64(u64 v) {
    return (u64)(unsigned)__builtin_amdgcn_readfirstlane((unsigned)v) | ((u64)(unsigned)__builtin_amdgcn_readfirstlane((unsigned)(v >> 32)) << 32);
}
DI u64 ld_epoch(const char* ws) { return __hip_atomic_load((GAS const u64*)(ws + WS_EPOCH), __ATOMIC_RELAXED, __HIP_MEMORY_SCOPE_AGENT); }
DI u64 next_tag(u64 e) { return uni64(e) * 6364136223846793005ull + 1442695040888963407ull; }
DI unsigned uni(float v) { return __builtin_amdgcn_readfirstlane(__float_as_uint(v)); }
DI void warm(const char* base, int nchunks, int first, unsigned l64) {
#pragma clang loop unroll(disable)
    for (int n = first; n < nchunks; n += 640) {
        const char* p = base + (size_t)((n & 1) * (nchunks >> 1) + (n >> 1)) * 4096;
        asm volatile("s_mov_b32 m0, 0x13e00\n\ts_nop 0\n\tglobal_load_lds_dword %0, %1" ::"v"(l64), "s"(p) : "memory");
    }
}
DI void drain() { asm volatile("s_waitcnt vmcnt(0)" ::: "memory"); }
DI void wgbar() { asm volatile("s_waitcnt lgkmcnt(0)\n\ts_barrier" ::: "memory"); }
template <int SLEEP>
DI void wait_flags(const char* sbase, unsigned voff, u64 tag) {
#pragma clang loop unroll(disable)
    for (unsigned spin = 0; spin < (1u << 20); ++spin) {
        u64 f;
        asm volatile("global_load_dwordx2 %0, %1, %2 sc1\n\ts_waitcnt vmcnt(0)" : "=v"(f) : "v"(voff), "s"(sbase) : "memory");
        if (__all(f == tag)) break;
        __builtin_amdgcn_s_sleep(SLEEP);
    }
}

DI void wait_flags256(const char* sbase, unsigned voff, u64 tag) {
#pragma clang loop unroll(disable)
    for (unsigned spin = 0; spin < (1u << 20); ++spin) {
        u64 f0, f1, f2, f3;
        asm volatile("global_load_dwordx2 %0, %4, %5 sc1\n\tglobal_load_dwordx2 %1, %4, %6 sc1\n\tglobal_load_dwordx2 %2, %4, %7 sc1\n\t"
                     "global_load_dwordx2 %3, %4, %8 sc1\n\ts_waitcnt vmcnt(0)"
                     : "=&v"(f0), "=&v"(f1), "=&v"(f2), "=&v"(f3)
                     : "v"(voff), "s"(sbase), "s"(sbase + 8192), "s"(sbase + 16384), "s"(sbase + 24576) : "memory");
        if (__all(f0 == tag && f1 == tag && f2 == tag && f3 == tag)) break;
        __builtin_amdgcn_s_sleep(8);
    }
}

constexpr int QCNT = 73728;
#define MF(D, A, B, C) "v_mfma_f32_32x32x16_bf16 " D ", " A ", " B ", " C "\n\t"
DI u32x4 srd(const char* base, unsigned bytes) {
    return u32x4{(unsigned)(uintptr_t)base, (unsigned)((uintptr_t)base >> 32) & 0xffffu, bytes, 0x00020000u};
}
DI void qkv_store(char* ws, const f32x16& acc, int type, int head, int tile, int lane) {
    const float sc = (type == 0) ? 0.25f * 1.4426950408889634f : 1.f;
    const char* dst = ws + WS_Q + (size_t)type * (2u << 20) + (size_t)(head * 64 + tile) * 1024;
    const unsigned vo = type < 2 ? lane * 16 : ((lane >> 4) & 1) * 65536 + (lane >> 5) * 256 + (lane & 15) * 16;
#pragma unroll
    for (int hf = 0; hf < 2; ++hf) {
        u32x4 o;
#pragma unroll
        for (int i = 0; i < 4; ++i) o[i] = cvtpk(acc[8 * hf + 2 * i] * sc, acc[8 * hf + 2 * i + 1] * sc);
        st16(dst + hf * (type < 2 ? 65536 : 512), vo, o);
    }
}
DI void publish4(const char* flag, u64 tag, int lane) {
    drain();
    unsigned arr = 0;
    if (lane == 0) arr = __hip_atomic_fetch_add((LAS unsigned*)QCNT, 1u, __ATOMIC_RELAXED, __HIP_MEMORY_SCOPE_WORKGROUP);
    if (__builtin_amdgcn_readfirstlane(arr) == 3) st_flag(flag, tag);
}
DI void qkv_loader(const char* x, const char* wqkv, int row, int col, int lw, const char* hint) {
    const int lane = lane_id(), xr = 16 * lw + (lane >> 3), wr = 16 * lw + (lane >> 4);
    const unsigned x0 = xr * 1024 + (((lane & 7) ^ ((xr >> 1) & 7)) << 4), x1 = (xr + 8) * 1024 + (((lane & 7) ^ (((xr + 8) >> 1) & 7)) << 4) - 1024;
    const unsigned w0 = wr * 3072 + (((lane & 15) ^ ((wr & 3) << 2)) << 4);
    const u32x4 rx = srd(x + (size_t)row * 65536, 65536), rw = srd(wqkv + col * 256, 512 * 3072 - col * 256);
    const int rot = (row + col) * QROTM & QROT;
    auto issue = [&](int s, int slot) {
        const int kc = (s + rot) & 7;
        asm volatile("s_mov_b32 m0, %[m]\n\ts_nop 0\n\t"
                     "buffer_load_dwordx4 %[x0], %[rx], %[ox] offen lds\n\t"
                     "buffer_load_dwordx4 %[x1], %[rx], %[ox] offen offset:1024 lds\n\t"
                     "s_mov_b32 m0, %[m2]\n\ts_nop 0\n\t"
                     "buffer_load_dwordx4 %[w0], %[rw], %[ow] offen lds\n\t"
                     "buffer_load_dwordx4 %[w1], %[rw], %[ow] offen offset:1024 lds\n\t"
                     "buffer_load_dwordx4 %[w2], %[rw], %[ow] offen offset:2048 lds\n\t"
                     "buffer_load_dwordx4 %[w3], %[rw], %[ow] offen offset:3072 lds"
                     :: [m] "s"(slot * 24576 + lw * 2048), [m2] "s"(slot * 24576 + 8192 + lw * 4096), [ow] "s"(kc * 196608), [ox] "s"(kc * 128),
                        [rw] "s"(rw), [rx] "s"(rx), [x0] "v"(x0), [x1] "v"(x1),
                        [w0] "v"(w0), [w1] "v"(w0 + 11264), [w2] "v"(w0 + 22528), [w3] "v"(w0 + 33792)
                     : "memory");
    };
    issue(0, 0);
    __builtin_amdgcn_s_setprio(2);
    issue(1, 1);
    __builtin_amdgcn_s_setprio(1);
    issue(2, 2);
    __builtin_amdgcn_s_setprio(3);
    asm volatile("s_waitcnt vmcnt(12)\n\ts_barrier" ::: "memory");
    int slot = 0;
#pragma clang loop unroll(disable)
    for (int s = 3; s < 8; ++s) {
        asm volatile("s_waitcnt vmcnt(6)\n\ts_barrier" ::: "memory");
        issue(s, slot);
        slot = slot == 2 ? 0 : slot + 1;
    }
    if (lw == 1) {
        st_flag(hint, uni64(*(LAS const u64*)(QCNT + 8)));
        asm volatile("s_waitcnt vmcnt(7)\n\ts_barrier\n\ts_waitcnt vmcnt(1)\n\ts_barrier" ::: "memory");
    } else
    asm volatile("s_waitcnt vmcnt(6)\n\ts_barrier\n\ts_waitcnt vmcnt(0)\n\ts_barrier" ::: "memory");
}
template <bool T>
DI void qkv_body(char* ws, char* lds, int row, int col, int a, int w, u64 tag ST_ARG) {
    const int lane = lane_id(), r = lane & 31, h2 = lane >> 5, q = (lane & 15) >> 2;
    f32x16 acc[2] = {};
    const int xa0 = (32 * (w & 1) + r) * 128 + ((h2 ^ ((r >> 1) & 7)) << 4);
    const int wa0 = 8192 + (8 * h2 + q) * 256 + (((2 * (w >> 1)) ^ q) << 6) + ((lane >> 4) & 1) * 32 + (lane & 3) * 8;
    bf16x8 xf[4], wf[2][2];
    auto rdw = [&](LAS const char* sl, int st, int i) {
        LAS const char* pw = sl + (wa0 ^ (i << 6)) + st * 4096;
        const s16x4 lo = trread(pw), hi = trread(pw + 1024);
        wf[st & 1][i] = __builtin_shufflevector(lo, hi, 0, 1, 2, 3, 4, 5, 6, 7);
    };
    auto mm1 = [&](int st, int i) {
        acc[i] = T ? MFMA32(wf[st & 1][i], xf[st], acc[i]) : MFMA32(xf[st], wf[st & 1][i], acc[i]);
        __builtin_amdgcn_sched_barrier(0);
    };
    auto step = [&](int st, LAS const char* sl, bool bar) {
        const int sn = (st + 2) & 3;
        mm1(st, 0);
        if (bar) wgbar();
        xf[sn] = *(LAS const bf16x8*)(sl + (xa0 ^ (sn << 5))); rdw(sl, sn, 0); __builtin_amdgcn_sched_barrier(0);
        mm1(st, 1);
        rdw(sl, sn, 1); __builtin_amdgcn_sched_barrier(0);
    };
    if (w == 0) *(LAS u32x4*)QCNT = u32x4{0u, 0u, (unsigned)tag, (unsigned)(tag >> 32)};
    wgbar();
    LAS const char* sl = (LAS const char*)lds;
#pragma unroll
    for (int st = 0; st < 2; ++st) { xf[st] = *(LAS const bf16x8*)(sl + (xa0 ^ (st << 5))); rdw(sl, st, 0); rdw(sl, st, 1); }
    __builtin_amdgcn_sched_barrier(0);
#pragma clang loop unroll(disable)
    for (int s = 0; s < 7; ++s) {
        step(0, sl, false); step(1, sl, false);
        sl = (sl == (LAS const char*)lds + 49152) ? (LAS const char*)lds : sl + 24576;
        step(2, sl, true); step(3, sl, false);
    }
    step(0, sl, false); step(1, sl, false);
    mm1(2, 0); mm1(2, 1); mm1(3, 0); mm1(3, 1);
    ST(4);
#pragma unroll
    for (int i = 0; i < 2; ++i) qkv_store(ws, acc[i], col >> 2, 8 * (col & 3) + 4 * (w >> 1) + 2 * i, 2 * row + (w & 1), lane);
    publish4(ws + WS_FQ + a * 128, tag, lane);
    ST(7);
}
DI void qkv2_loader(const char* x, const char* wqkv, int rowt, int col, int lw) {
    const int lane = lane_id(), row = 16 * lw + (lane >> 3);
    const unsigned w0 = row * 3072 + (((lane & 7) ^ (((row >> 1) & 1) << 2)) << 4), w1 = w0 + 8 * 3072 - 1024;
    const unsigned x0 = row * 1024 + (((lane & 7) ^ ((row >> 1) & 7)) << 4), x1 = (row + 8) * 1024 + (((lane & 7) ^ (((row + 8) >> 1) & 7)) << 4) - 1024;
    const u32x4 rx = srd(x + (size_t)rowt * 65536, 65536), rw = srd(wqkv + col * 128, 512 * 3072 - col * 128);
    auto issue = [&](int s) {
        asm volatile("s_mov_b32 m0, %[m]\n\ts_nop 0\n\t"
                     "buffer_load_dwordx4 %[w0], %[rw], %[ow] offen lds\n\t"
                     "buffer_load_dwordx4 %[w1], %[rw], %[ow] offen offset:1024 lds\n\t"
                     "s_add_u32 m0, %[m], 8192\n\ts_nop 0\n\t"
                     "buffer_load_dwordx4 %[x0], %[rx], %[ox] offen lds\n\t"
                     "buffer_load_dwordx4 %[x1], %[rx], %[ox] offen offset:1024 lds"
                     :: [m] "s"((s & 3) * 16384 + lw * 2048), [ow] "s"(s * 196608), [ox] "s"(s * 128), [rw] "s"(rw), [rx] "s"(rx),
                        [w0] "v"(w0), [w1] "v"(w1), [x0] "v"(x0), [x1] "v"(x1)
                     : "memory", "scc");
    };
    issue(0); issue(1); issue(2);
#pragma clang loop unroll(disable)
    for (int s = 3; s < 8; ++s) {
        asm volatile("s_waitcnt vmcnt(8)\n\ts_barrier" ::: "memory");
        issue(s);
    }
    asm volatile("s_waitcnt vmcnt(8)\n\ts_barrier\n\ts_waitcnt vmcnt(4)\n\ts_barrier\n\ts_waitcnt vmcnt(0)\n\ts_barrier" ::: "memory");
}
#define QRD3(B, SLOT, K)                                                                             \
    "ds_read_b64_tr_b16 v[" #B ":" #B "+1], %[t] offset:" #SLOT "*16384+" #K "*2048\n\t"              \
    "ds_read_b64_tr_b16 v[" #B "+2:" #B "+3], %[t] offset:" #SLOT "*16384+" #K "*2048+512\n\t"        \
    "ds_read_b128 v[" #B "+4:" #B "+7], %[x" #K "] offset:" #SLOT "*16384\n\t"
#define QSTAGE(SLOT, MM, N0, N1, N2, N3, P0, P1, P2, P3)                              \
    "s_waitcnt lgkmcnt(0)\n\t"                                                        \
    "s_barrier\n\t"                                                                   \
    MM(P0) QRD3(N0, SLOT, 0) MM(P1) QRD3(N1, SLOT, 1) MM(P2) QRD3(N2, SLOT, 2) MM(P3) QRD3(N3, SLOT, 3)
#define QST_E(SLOT, MM) QSTAGE(SLOT, MM, 32, 40, 48, 56, 96, 104, 112, 120)
#define QST_O(SLOT, MM) QSTAGE(SLOT, MM, 96, 104, 112, 120, 32, 40, 48, 56)
#define QMT(P) MF("v[16:31]", "v[" #P ":" #P "+3]", "v[" #P "+4:" #P "+7]", "v[16:31]")
#define QMV(P) MF("v[16:31]", "v[" #P "+4:" #P "+7]", "v[" #P ":" #P "+3]", "v[16:31]")
#define QTILE_ASM(MM)                                                                                             \
    asm volatile(                                                                                                 \
        "s_barrier\n\t"                                                                                           \
        QRD3(32, 0, 0) QRD3(40, 0, 1) QRD3(48, 0, 2) QRD3(56, 0, 3)                                               \
        QST_O(1, MM) QST_E(2, MM)                                                                                 \
        "s_mov_b32 s44, 0\n\t"                                                                                    \
        "LQ_loop%=:\n\t"                                                                                          \
        QST_O(3, MM)                                                                                              \
        "s_cmp_lg_u32 s44, 0\n\t"                                                                                 \
        "s_cbranch_scc1 LQ_end%=\n\t"                                                                             \
        QST_E(0, MM) QST_O(1, MM) QST_E(2, MM)                                                                    \
        "s_mov_b32 s44, 1\n\t"                                                                                    \
        "s_branch LQ_loop%=\n\t"                                                                                  \
        "LQ_end%=:\n\t"                                                                                           \
        "s_waitcnt lgkmcnt(0)\n\t"                                                                                \
        MM(96) MM(104) MM(112) MM(120)                                                                            \
        "s_nop 15\n\t"                                                                                            \
        : "+{v[16:31]}"(acc)                                                                                      \
        : [t] "v"(t), [x0] "v"(xa), [x1] "v"(xa ^ 32), [x2] "v"(xa ^ 64), [x3] "v"(xa ^ 96)                       \
        : "memory", "scc", "s44",                                                                                 \
          "v32", "v33", "v34", "v35", "v36", "v37", "v38", "v39", "v40", "v41", "v42", "v43", "v44", "v45", "v46", "v47", \
          "v48", "v49", "v50", "v51", "v52", "v53", "v54", "v55", "v56", "v57", "v58", "v59", "v60", "v61", "v62", "v63", \
          "v96", "v97", "v98", "v99", "v100", "v101", "v102", "v103", "v104", "v105", "v106", "v107", "v108", "v109", "v110", "v111", \
          "v112", "v113", "v114", "v115", "v116", "v117", "v118", "v119", "v120", "v121", "v122", "v123", "v124", "v125", "v126", "v127")
DI void qkv2_body(char* ws, int row, int col, int bi, int w, u64 tag) {
    const int lane = lane_id(), r = lane & 31, h2 = lane >> 5, q = (lane & 15) >> 2;
    const unsigned t = (8 * h2 + q) * 128 + (((w & 1) ^ ((q >> 1) & 1)) << 6) + ((lane >> 4) & 1) * 32 + (lane & 3) * 8;
    const unsigned xa = 8192 + (32 * (w >> 1) + r) * 128 + ((h2 ^ ((r >> 1) & 7)) << 4);
    f32x16 acc = {};
    if (col < 16) QTILE_ASM(QMT);
    else QTILE_ASM(QMV);
    qkv_store(ws, acc, col >> 3, 4 * (col & 7) + 2 * (w & 1), 2 * row + (w >> 1), lane);
    publish4(ws + WS_FQ + 32768 + bi * 128, tag, lane);
}

#define EXP(a) "v_exp_f32 v" #a ", v" #a "\n\t"
#define CVT(d, a, b) "v_cvt_pk_bf16_f32 v" #d ", v" #a ", v" #b "\n\t"
#define ASTEP(c0, c1, c2, c3, c4, c5, c6, c7, c8, c9, c10, c11, c12, c13, c14, c15, M1, M2, CL, M3)                  \
    EXP(c0) EXP(c1) M1 EXP(c2) EXP(c3) EXP(c4) EXP(c5) M2 CL                                                          \
    EXP(c6) EXP(c7) CVT(96, c0, c1) CVT(97, c2, c3) CVT(98, c4, c5) CVT(99, c6, c7) EXP(c8) EXP(c9) M3                \
    EXP(c10) EXP(c11) EXP(c12) EXP(c13) EXP(c14) EXP(c15)                                                             \
    CVT(100, c8, c9) CVT(101, c10, c11) CVT(102, c12, c13) CVT(103, c14, c15)
#define R_OA "v[64:79]"
#define R_OB "v[80:95]"
#define R_SA "v[32:47]"
#define R_SB "v[48:63]"
#define ATILE(R_NM, KC, KN, V0C, V1C, V0N, V1N, KOFF, VA, VOFF0, VOFF1, PRE)                                                    \
    ASTEP(32, 33, 34, 35, 36, 37, 38, 39, 40, 41, 42, 43, 44, 45, 46, 47,                                             \
          MF(R_OB, V1N, "v[100:103]", R_OB), MF(R_SB, KC, "v[8:11]", R_NM),                                           \
          PRE                                                                                                         \
          "ds_read_b128 " KC ", v12 offset:" KOFF "\n\t"                                                              \
          "ds_read_b128 " V0N ", " VA " offset:" #VOFF0 "\n\t"                                                        \
          "ds_read_b128 " V1N ", " VA " offset:" #VOFF1 "\n\t",                                                          \
          MF(R_OA, V0C, "v[96:99]", R_OA))                                                                            \
    ASTEP(48, 49, 50, 51, 52, 53, 54, 55, 56, 57, 58, 59, 60, 61, 62, 63,                                             \
          MF(R_OA, V1C, "v[100:103]", R_OA), MF(R_SA, KN, "v[4:7]", R_NM), "", MF(R_OB, V0C, "v[96:99]", R_OB))
#define ATILE_E(NM, KOFF, VA, VOFF0, VOFF1, PRE) ATILE(NM, "v[104:107]", "v[108:111]", "v[112:115]", "v[116:119]", "v[120:123]", "v[124:127]", KOFF, VA, VOFF0, VOFF1, PRE)
#define ATILE_O(NM, KOFF, VA, VOFF0, VOFF1, PRE) ATILE(NM, "v[108:111]", "v[104:107]", "v[120:123]", "v[124:127]", "v[112:115]", "v[116:119]", KOFF, VA, VOFF0, VOFF1, PRE)
#define MOV4(a, b, c, d, s) "v_mov_b32 v" #a ", " s "\n\tv_mov_b32 v" #b ", " s "\n\tv_mov_b32 v" #c ", " s "\n\tv_mov_b32 v" #d ", " s "\n\t"
#define WL "s_waitcnt lgkmcnt(0)\n\t"
#define AISSUE                                                   \
    "s_add_u32 m0, s44, %[lw]\n\t"                               \
    "s_add_u32 s44, s44, 0x4000\n\t"                             \
    "buffer_load_dwordx4 v14, %[rk], s43 offen sc1 lds\n\t"      \
    "s_add_u32 m0, m0, 0x2000\n\t"                               \
    "s_and_b32 s44, s44, 0xffff\n\t"                             \
    "buffer_load_dwordx4 v14, %[rv], s43 offen sc1 lds\n\t"      \
    "s_add_u32 s43, s43, 0x2000\n\t"
#define PR2 "s_setprio 2\n\t"
#define PR3 ""
#define PR0 "s_setprio 1\n\t"
#define PR1 "s_setprio 0\n\t"
#define PRI "s_setprio 3\n\t"
constexpr int A_ONES = 65536, A_VOTE = 69632 + 512, A_LSUM = 71680;
struct AttnRegs { f32x16 oa, ob, nm; u32x4 p1, v1; unsigned ka, va, va1, va2, va3, go, fo; };
DI unsigned qkv_dep(int a, bool early) {
    const int lane = lane_id(), r = lane & 15, v = (lane >> 4) & 1, R = 4 * (a >> 5) + (lane & 3);
    const int kv = (r >> 3) + 8 * ((early ? 8 + 8 * v : 16 * v + 8 * (a & 1)) + (r & 7));
    const int qq = ((R >> 3) & 1) + 8 * ((R >> 4) * 24 + (R & 7));
    return ((a & 6) + (lane < 32 || !early ? kv : qq)) * 128;
}
DI void attn_prep(AttnRegs& R, int a, int w) {
    const int lane = lane_id(), r = lane & 31, kh = w >> 2;
    R.ka = 4096 * kh + lane * 16;
    R.va = r < 16 ? 8192 + 4096 * kh + (lane >> 5) * 256 + r * 16 : A_ONES + (r == 16 ? 0 : r == 20 ? 64 : (r > 20 && r < 28) ? 80 : 16);
    R.go = w * 1024 + lane * 16;
#pragma unroll
    for (int i = 0; i < 16; ++i) { R.oa[i] = 0.f; R.ob[i] = 0.f; }
    R.p1 = u32x4{0u, 0u, 0u, 0u};
    R.v1 = u32x4{0u, 0u, 0u, 0u};
    asm volatile("" : "+{v[64:79]}"(R.oa), "+{v[80:95]}"(R.ob), "+{v[100:103]}"(R.p1), "+{v[124:127]}"(R.v1),
                      "+{v12}"(R.ka), "+{v13}"(R.va), "+{v14}"(R.go));
}
#define ATTN_ASM(NM, NMIN) \
    asm volatile( \
        "v_and_b32 v15, 0x3ff, v14\n\t" \
        "s_mov_b32 s43, 0\n\t" \
        "s_mov_b32 s44, 0\n\t" \
        AISSUE \
        "global_load_dwordx4 v[4:7], v15, %[qa] sc1\n\t" \
        "global_load_dwordx4 v[8:11], v15, %[qb] sc1\n\t" \
        AISSUE AISSUE \
        "s_mov_b32 s40, 0xffff\n\t" \
        "s_mov_b32 s41, 0xffff\n\t" \
        "s_mov_b32 s42, 8\n\t" \
        "s_waitcnt vmcnt(4)\n\t" \
        "s_barrier\n\t" \
        "ds_read_b128 v[104:107], v12\n\t" \
        "ds_read_b128 v[108:111], v12 offset:1024\n\t" \
        "ds_read_b128 v[112:115], v13\n\t" \
        "ds_read_b128 v[116:119], v13 offset:512\n\t" \
        PR2 \
        "s_waitcnt lgkmcnt(3)\n\t" \
        MF(R_SA, "v[104:107]", "v[4:7]", NM) \
        "s_nop 15\n\t" \
        "LA_loop%=:\n\t" \
        ATILE_E(NM, "2048", "v13", 1024, 1536, WL PR0) \
        ATILE_O(NM, "3072", "v13", 2048, 2560, WL PR1) \
        ATILE_E(NM, "0", "v13", 3072, 3584, \
                "s_waitcnt vmcnt(2) lgkmcnt(0)\n\t" \
                "s_barrier\n\t" \
                "s_setprio 3\n\t" \
                "v_add_u32 v12, 0x4000, v12\n\t" \
                AISSUE \
                "v_and_b32 v12, 0xffff, v12\n\t" \
                PR2) \
        ATILE_O(NM, "1024", "v13", 0, 512, \
                WL PR3 \
                "s_mov_b64 exec, s[40:41]\n\t" \
                "v_add_u32 v13, 0x4000, v13\n\t" \
                "s_sub_u32 s42, s42, 1\n\t" \
                "v_and_b32 v13, 0xffff, v13\n\t" \
                "s_mov_b64 exec, -1\n\t" \
                "s_cmp_lg_u32 s42, 0\n\t") \
        "s_cbranch_scc1 LA_loop%=\n\t" \
        "s_waitcnt lgkmcnt(0)\n\t" \
        "s_setprio 3\n\t" \
        MF(R_OB, "v[124:127]", "v[100:103]", R_OB) \
        "s_nop 15\n\t" \
        : "+{v[64:79]}"(R.oa), "+{v[80:95]}"(R.ob), "+{v[100:103]}"(R.p1), "+{v[124:127]}"(R.v1), "+{v12}"(R.ka), "+{v13}"(R.va) \
        : NMIN "{v14}"(R.go), [qa] "s"(qa), [qb] "s"(qb), [rk] "s"(rk), [rv] "s"(rv), [lw] "s"(w * 1024) \
        : "memory", "scc", "vcc", "s40", "s41", "s42", "s43", "s44", "s45", "v0", "v1", "v4", "v5", "v6", "v7", "v8", "v9", "v10", "v11", "v15", \
          "v32", "v33", "v34", "v35", "v36", "v37", "v38", "v39", "v40", "v41", "v42", "v43", "v44", "v45", "v46", "v47", \
          "v48", "v49", "v50", "v51", "v52", "v53", "v54", "v55", "v56", "v57", "v58", "v59", "v60", "v61", "v62", "v63", \
          "v96", "v97", "v98", "v99", "v104", "v105", "v106", "v107", "v108", "v109", "v110", "v111", \
          "v112", "v113", "v114", "v115", "v116", "v117", "v118", "v119", "v120", "v121", "v122", "v123")
DI void attn_pass_shift(AttnRegs& R, const char* qa, const char* qb, const char* ksrc, const char* vsrc, int w) {
    const u32x4 rk = srd(ksrc, 65536), rv = srd(vsrc, 65536);
#define NM_IN "{v[16:31]}"(R.nm),
    ATTN_ASM("v[16:31]", NM_IN);
}
#define FISS(S)                                                  \
    "s_add_u32 m0, %[lw], " #S "*16384\n\t"                      \
    "s_add_u32 s43, s43, 0x2000\n\t"                             \
    "buffer_load_dwordx4 v14, %[rk], s43 offen sc1 lds\n\t"      \
    "s_add_u32 m0, %[lw], " #S "*16384+8192\n\t"                 \
    "s_nop 0\n\t"                                                \
    "buffer_load_dwordx4 v14, %[rv], s43 offen sc1 lds\n\t"
#define FI2(I) "s_cbranch_vccz 7f\n\t" PRI I "7:\n\t"
#define FI3(I) "s_cbranch_vccnz 7f\n\t" PRI I PR2 "7:\n\t"
#define LA2 ""
#define LB2 PR2
#define LB3 ""
#define FCHUNK(S, N, VC, VN, BND, I2, I3)                        \
    ATILE_E("0", #S "*16384+2048", VC, 1024, 1536, WL PR0)       \
    ATILE_O("0", #S "*16384+3072", VC, 2048, 2560, WL PR1)       \
    ATILE_E("0", #N "*16384", VC, 3072, 3584, BND LA2 I2 LB2)    \
    ATILE_O("0", #N "*16384+1024", VN, 0, 512, WL I3 LB3)
#define FLD "global_load_dwordx2 v[0:1], v2, %[ff] sc1\n\t"
#define VCCSET "s_mov_b32 vcc_lo, %[lo4]\n\ts_mov_b32 vcc_hi, 0\n\t"
#define FBAR(VM) "s_waitcnt vmcnt(" #VM ") lgkmcnt(0)\n\ts_barrier\n\t"
DI void attn_pass(AttnRegs& R, const char* qa, const char* qb, const char* ksrc, const char* vsrc, const char* late_flags, u64 tag, int w) {
    const u32x4 rk = srd(ksrc, 65536), rv = srd(vsrc, 65536);
    asm volatile(
        "v_and_b32 v15, 0x3ff, v14\n\t"
        "s_mov_b32 s43, -0x2000\n\t"
        FISS(0)
        "global_load_dwordx4 v[4:7], v15, %[qa] sc1\n\t"
        "global_load_dwordx4 v[8:11], v15, %[qb] sc1\n\t"
        FISS(1) FISS(2) VCCSET
        "s_waitcnt vmcnt(4)\n\t"
        "s_barrier\n\t"
        "ds_read_b128 v[104:107], v12\n\t"
        "ds_read_b128 v[108:111], v12 offset:1024\n\t"
        "ds_read_b128 v[112:115], v13\n\t"
        "ds_read_b128 v[116:119], v13 offset:512\n\t"
        PR2
        "s_waitcnt lgkmcnt(3)\n\t"
        MF(R_SA, "v[104:107]", "v[4:7]", "0")
        "s_nop 15\n\t"
        FCHUNK(0, 1, "v13", "v16", FBAR(2), FI2(FLD FISS(3)), FI3(FLD FISS(3)))
        FCHUNK(1, 2, "v16", "v17",
               "s_waitcnt vmcnt(2) lgkmcnt(0)\n\t"
               "s_mov_b32 s45, 0x100000\n\t"
               "LA_p%=:\n\t"
               "v_cmp_ne_u64 vcc, %[tag], v[0:1]\n\t"
               "s_sub_u32 s45, s45, 1\n\t"
               "s_cbranch_scc1 LA_f%=\n\t"
               "s_cbranch_vccz LA_f%=\n\t"
               "global_load_dwordx2 v[0:1], v2, %[ff] sc1\n\t"
               "s_waitcnt vmcnt(0)\n\t"
               "s_branch LA_p%=\n\t"
               "LA_f%=:\n\t"
               "s_barrier\n\t" VCCSET, FI2(FISS(0)), FI3(FISS(0)))
        FCHUNK(2, 3, "v17", "v18", FBAR(2), FI2(FISS(1)), FI3(FISS(1)))
        FCHUNK(3, 0, "v18", "v13", FBAR(2), FI2(FISS(2)), FI3(FISS(2)))
        FCHUNK(0, 1, "v13", "v16", FBAR(2), FI2(FISS(3)), FI3(FISS(3)))
        FCHUNK(1, 2, "v16", "v17", FBAR(2), "", "")
        FCHUNK(2, 3, "v17", "v18", FBAR(0), "", "")
        FCHUNK(3, 0, "v18", "v13", WL, "", "")
        "s_waitcnt lgkmcnt(0)\n\t"
        "s_setprio 3\n\t"
        MF(R_OB, "v[124:127]", "v[100:103]", R_OB)
        "s_nop 15\n\t"
        : "+{v[64:79]}"(R.oa), "+{v[80:95]}"(R.ob), "+{v[100:103]}"(R.p1), "+{v[124:127]}"(R.v1)
        : "{v12}"(R.ka), "{v13}"(R.va), "{v16}"(R.va1), "{v17}"(R.va2), "{v18}"(R.va3), "{v14}"(R.go), "{v2}"(R.fo),
          [qa] "s"(qa), [qb] "s"(qb), [rk] "s"(rk), [rv] "s"(rv), [lw] "s"(w * 1024), [tag] "s"(tag), [ff] "s"(late_flags), [lo4] "s"(1 - (w >> 2))
        : "memory", "scc", "vcc", "s43", "s45", "v0", "v1", "v4", "v5", "v6", "v7", "v8", "v9", "v10", "v11", "v15",
          "v32", "v33", "v34", "v35", "v36", "v37", "v38", "v39", "v40", "v41", "v42", "v43", "v44", "v45", "v46", "v47",
          "v48", "v49", "v50", "v51", "v52", "v53", "v54", "v55", "v56", "v57", "v58", "v59", "v60", "v61", "v62", "v63",
          "v96", "v97", "v98", "v99", "v104", "v105", "v106", "v107", "v108", "v109", "v110", "v111",
          "v112", "v113", "v114", "v115", "v116", "v117", "v118", "v119", "v120", "v121", "v122", "v123");
}

DI void wait_qkv(const char* ws, int a, u64 tag) { wait_flags<PSL>(ws + WS_FQ, qkv_dep(a, true), tag); }
DI void attn_role(const char* x, const char* wqkv, char* ws, char* lds, int a, int w) {
    const int kh = w >> 2;
    const int head = 4 * (a & 7) + ((a >> 3) & 3), qt = 8 * (a >> 5) + 2 * (w & 3);
    ST_DECL; ST(0);
    __builtin_amdgcn_s_setprio(3);
    u64 tag;
    const int ti = a >> 3, trow = 8 * (a & 1) + (ti & 7) + (ti >= 24 ? 16 : 0), tcol = (ti >= 24 ? 0 : 4 * (ti >> 3)) + ((a & 7) >> 1);
    if (w >= 4) {
        qkv_loader(x, wqkv, trow, tcol, w - 4, ws + WS_FA + a * 128);
        tag = uni64(*(LAS const u64*)(QCNT + 8));
        if (w == 4) wait_qkv(ws, a, tag);
    } else {
        tag = next_tag(ld_epoch(ws));
        if (tcol < 8) qkv_body<true>(ws, lds, trow, tcol, a, w, tag ST_PASS);
        else qkv_body<false>(ws, lds, trow, tcol, a, w, tag ST_PASS);
    }
    AttnRegs R;
    attn_prep(R, a, w);
    {
        const unsigned st = (lane_id() & 16) ? 0u : 16384u;
        R.va1 = R.va + st; R.va2 = R.va1 + st; R.va3 = R.va2 + st;
        R.fo = qkv_dep(a, false);
        asm volatile("" : "+{v16}"(R.va1), "+{v17}"(R.va2), "+{v18}"(R.va3), "+{v2}"(R.fo));
    }
    ST(5);
    wgbar();
    ST(6);
    {
        const int o = w * 512 + lane_id() * 8;
        const unsigned one = ((o & 0x1b0) == 0) ? 0x3f803f80u : 0u;
        *(LAS u32x2*)((LAS char*)lds + A_ONES + o) = u32x2{one, one};
        asm volatile("s_waitcnt lgkmcnt(0)" ::: "memory");
    }
    const char* ksrc = ws + WS_K + (size_t)head * 65536;
    const char* qsrc = ws + WS_Q + (size_t)(head * 64 + qt) * 1024;
    auto finish = [&](int att) {
        const int lane = lane_id();
        const f32x16 oa = R.oa, ob = R.ob;
        LAS char* ex = (LAS char*)lds + w * 2048 + lane * 16;
        LAS const char* exr = (LAS const char*)lds + (w ^ 4) * 2048 + lane * 16;
        *(LAS f32x4*)ex = f32x4{ob[0], ob[1], ob[2], ob[3]};
        *(LAS f32x4*)(ex + 1024) = f32x4{ob[4], ob[5], ob[6], ob[7]};
        *(LAS float*)((LAS char*)lds + A_LSUM + w * 256 + lane * 4) = ob[8];
        if (att == 0) {
            float big = fmaxf(oa[8], ob[8]) * 2.7e8f;
#pragma unroll
            for (int i = 0; i < 8; ++i) big = fmaxf(fmaxf(big, __builtin_fabsf(oa[i])), __builtin_fabsf(ob[i]));
            const bool bad = !__all(fminf(oa[8], ob[8]) > 8.1e-28f && big < 1.7e38f);
            if (lane == 0) ((LAS unsigned*)((LAS char*)lds + A_VOTE))[w] = bad;
        }
        wgbar();
        const f32x4 t0 = *(LAS const f32x4*)exr, t1 = *(LAS const f32x4*)(exr + 1024);
        const float t2 = *(LAS const float*)((LAS char*)lds + A_LSUM + (w ^ 4) * 256 + lane * 4);
        unsigned redo = 0;
        if (att == 0) {
            const u32x4 v0 = *(LAS const u32x4*)((LAS char*)lds + A_VOTE), v1 = *(LAS const u32x4*)((LAS char*)lds + A_VOTE + 16);
            redo = __builtin_amdgcn_readfirstlane(v0[0] | v0[1] | v0[2] | v0[3] | v1[0] | v1[1] | v1[2] | v1[3]);
        }
        const float rinv = __builtin_amdgcn_rcpf(oa[8] + t2);
        u32x4 ov;
        ov[0] = cvtpk((oa[0] + t0[0]) * rinv, (oa[1] + t0[1]) * rinv);
        ov[1] = cvtpk((oa[2] + t0[2]) * rinv, (oa[3] + t0[3]) * rinv);
        ov[2] = cvtpk((oa[4] + t1[0]) * rinv, (oa[5] + t1[1]) * rinv);
        ov[3] = cvtpk((oa[6] + t1[2]) * rinv, (oa[7] + t1[3]) * rinv);
        if (att == 0 ? !redo : att == 1 + kh) {
            st16(ws + WS_O + (size_t)((qt + kh) * 32 + head) * 1024, lane * 16, ov);
            drain();
            st_flag(ws + WS_FO + ((qt + kh) * 32 + head) * 128, tag);
        }
        asm volatile("" : "+s"(redo));
        return redo;
    };
    ST(1);
    attn_pass(R, qsrc + kh * 1024, qsrc + (1 - kh) * 1024, ksrc, ksrc + (WS_V - WS_K), ws + WS_FQ + 32768, tag, w);
    ST(2);
    if (finish(0)) {
#pragma clang loop unroll(disable)
        for (int att = 1; att < 3; ++att) {
            drain();
            wgbar();
            const int lane = lane_id();
            const char* qa = qsrc + (att - 1) * 1024;
            bf16x8 qf;
            asm volatile("global_load_dwordx4 %0, %1, %2 sc1\n\ts_waitcnt vmcnt(0)" : "=v"(qf) : "v"(lane * 16), "s"(qa) : "memory");
            float mx = -INFINITY;
#pragma clang loop unroll(disable)
            for (int t = 0; t < 64; ++t) {
                bf16x8 kf;
                asm volatile("global_load_dwordx4 %0, %1, %2 sc1\n\ts_waitcnt vmcnt(0)" : "=v"(kf) : "v"(lane * 16), "s"(ksrc + t * 1024) : "memory");
                const f32x16 s = MFMA32(kf, qf, f32x16{});
#pragma unroll
                for (int i = 0; i < 16; ++i) mx = fmaxf(mx, s[i]);
            }
            mx = -swap32_max(mx);
            attn_prep(R, a, w);
#pragma unroll
            for (int i = 0; i < 16; ++i) R.nm[i] = mx;
            attn_pass_shift(R, qa, qa, ksrc, ksrc + (WS_V - WS_K), w);
            finish(att);
        }
    }
    ST(3); ST_OUT(0);
}

#define BPR "s_setprio 0\n\t"
#define BG1 ""
#define BG2 "s_sleep 1\n\t"
#define BG3 "s_sleep 1\n\t"
#define SISSUE                                                             \
    "s_mov_b32 m0, s44\n\t"                                                 \
    "s_add_u32 s45, %[ow], %[wst]\n\t"                                      \
    "buffer_load_dwordx4 %[g0], %[rw], %[ow] offen lds\n\t"                 \
    "buffer_load_dwordx4 %[g1], %[rw], %[ow] offen offset:1024 lds\n\t"     \
    "s_add_u32 m0, s44, 8192\n\t"                                           \
    "s_add_u32 s46, %[od], 4096\n\t"                                        \
    "buffer_load_dwordx4 %[d0], %[rd], %[od] offen sc1 lds\n\t"             \
    "buffer_load_dwordx4 %[d0], %[rd], %[od] offen offset:1024 sc1 lds\n\t" \
    "s_add_u32 s44, s44, 0x4000\n\t"                                        \
    "s_and_b32 s44, s44, 0xffff\n\t"                                        \
    "s_cmp_eq_u32 %[od], %[de]\n\t"                                         \
    "s_cselect_b32 %[ow], 0, s45\n\t"                                       \
    "s_cselect_b32 %[od], 0, s46\n\t"
#define SISW                                                               \
    "s_mov_b32 m0, s44\n\t"                                                 \
    "s_add_u32 s45, %[ow], %[wst]\n\t"                                      \
    "buffer_load_dwordx4 %[g0], %[rw], %[ow] offen lds\n\t"                 \
    "buffer_load_dwordx4 %[g1], %[rw], %[ow] offen offset:1024 lds\n\t"     \
    "s_add_u32 s44, s44, 0x4000\n\t"                                        \
    "s_mov_b32 %[ow], s45\n\t"
#define SISD                                                               \
    "s_add_u32 m0, s44, 8192\n\t"                                           \
    "s_add_u32 s44, s44, 0x4000\n\t"                                        \
    "buffer_load_dwordx4 %[d0], %[rd], %[od] offen sc1 lds\n\t"             \
    "buffer_load_dwordx4 %[d0], %[rd], %[od] offen offset:1024 sc1 lds\n\t" \
    "s_add_u32 %[od], %[od], 4096\n\t"
struct SG { u32x4 rw, rd; unsigned wst, de, ow, od, a, b, c; };
DI void sgemm_prew(SG& G, int lw) {
    asm volatile("s_mov_b32 s44, %[lo]\n\t" SISW SISW SISW
                 : [ow] "+s"(G.ow) : [wst] "s"(G.wst), [rw] "s"(G.rw), [lo] "s"(lw * 2048), [g0] "v"(G.a), [g1] "v"(G.b) : "memory", "scc", "s44", "s45");
}
#define WISSUE                                                              \
    "s_and_b32 s45, s44, 0xc000\n\t"                                        \
    "s_add_u32 m0, s45, %[lo2]\n\t"                                         \
    "s_lshl_b32 s45, s45, 3\n\t"                                            \
    "buffer_load_dwordx4 %[o0], %[ro], s45 offen lds\n\t"                   \
    "buffer_load_dwordx4 %[o1], %[ro], s45 offen offset:1024 lds\n\t"       \
    "buffer_load_dwordx4 %[o2], %[ro], s45 offen offset:2048 lds\n\t"       \
    "buffer_load_dwordx4 %[o3], %[ro], s45 offen offset:3072 lds\n\t"       \
    "s_add_u32 s44, s44, 0x4000\n\t"                                        \
    "s_and_b32 s44, s44, 0xffff\n\t"
template <int WO>
DI void sgemm_load(SG& G, int n, int lw, u32x4 ro, unsigned o0) {
    asm volatile(
        "s_mov_b32 s44, %[lo]\n\t"
        ".if %c[wo]\n\t"
        "s_sub_u32 s47, %[n], 5\n\t"
        SISD SISD SISD
        "s_waitcnt vmcnt(4)\n\ts_barrier\n\t" SISSUE
        "s_waitcnt vmcnt(6)\n\ts_barrier\n\t" SISSUE
        ".else\n\t"
        "s_sub_u32 s47, %[n], 3\n\t"
        SISSUE SISSUE SISSUE
        ".endif\n\t"
        "LL_loop%=:\n\t"
        "s_waitcnt vmcnt(8)\n\t"
        "s_barrier\n\t"
        SISSUE
        "s_sub_u32 s47, s47, 1\n\t"
        "s_cmp_lg_u32 s47, 0\n\t"
        "s_cbranch_scc1 LL_loop%=\n\t"
        ".if %c[wo]\n\t"
        "s_waitcnt vmcnt(8)\n\ts_barrier\n\t" WISSUE
        "s_waitcnt vmcnt(8)\n\ts_barrier\n\t" WISSUE
        "s_waitcnt vmcnt(8)\n\ts_barrier\n\t" WISSUE
        "s_barrier\n\t" WISSUE
        ".else\n\t"
        "s_waitcnt vmcnt(8)\n\ts_barrier\n\t"
        "s_waitcnt vmcnt(4)\n\ts_barrier\n\t"
        "s_waitcnt vmcnt(0)\n\ts_barrier\n\t"
        "s_barrier\n\t"
        ".endif\n\t"
        : [ow] "+s"(G.ow), [od] "+s"(G.od)
        : [wst] "s"(G.wst), [rw] "s"(G.rw), [rd] "s"(G.rd), [lo] "s"(lw * 2048), [de] "s"(G.de), [n] "s"(n), [g0] "v"(G.a), [g1] "v"(G.b), [d0] "v"(G.c),
          [wo] "i"(WO), [ro] "s"(ro), [lo2] "s"(lw * 4096), [o0] "v"(o0), [o1] "v"(o0 + 7168), [o2] "v"(o0 + 14336), [o3] "v"(o0 + 21504)
        : "memory", "scc", "s44", "s45", "s46", "s47");
}
#define SRD3(B, SLOT, K, TD)                                                                         \
    "ds_read_b64_tr_b16 v[" #B ":" #B "+1], %[t] offset:" #SLOT "*16384+" #K "*2048\n\t"              \
    "ds_read_b64_tr_b16 v[" #B "+2:" #B "+3], %[t] offset:" #SLOT "*16384+" #K "*2048+" #TD "\n\t"    \
    "ds_read_b128 v[" #B "+4:" #B "+7], %[dl] offset:" #SLOT "*16384+" #K "*1024\n\t"
#define SSTAGE(SLOT, MM, TD, N0, N1, N2, N3, P0, P1, P2, P3)                          \
    "s_waitcnt lgkmcnt(0)\n\t"                                                        \
    "s_barrier\n\t"                                                                   \
    MM(P0) SRD3(N0, SLOT, 0, TD) SRD3(N1, SLOT, 1, TD) BG1                            \
    MM(P1) SRD3(N2, SLOT, 2, TD) SRD3(N3, SLOT, 3, TD) BG2                            \
    MM(P2) BG3 MM(P3)
#define SST_E(SLOT, MM, TD) SSTAGE(SLOT, MM, TD, 32, 40, 48, 56, 96, 104, 112, 120)
#define SST_O(SLOT, MM, TD) SSTAGE(SLOT, MM, TD, 96, 104, 112, 120, 32, 40, 48, 56)
#define SMM_NONE(P) ""
#define SMM_WA(P) MF("v[64:79]", "v[" #P ":" #P "+3]", "v[" #P "+4:" #P "+7]", "v[64:79]")
#define SMM_WB(P) MF("v[80:95]", "v[" #P "+4:" #P "+7]", "v[" #P ":" #P "+3]", "v[80:95]")
#define SGEMM_ASM(ACC, MM, TD, NLOOP, TAIL)                                                                       \
    asm volatile(                                                                                                 \
        "s_mov_b32 s44, " #NLOOP "\n\t" BPR                                                                       \
        "s_barrier\n\t"                                                                                           \
        SRD3(32, 0, 0, TD) SRD3(40, 0, 1, TD) SRD3(48, 0, 2, TD) SRD3(56, 0, 3, TD)                               \
        "LS_loop%=:\n\t"                                                                                          \
        SST_O(1, MM, TD) SST_E(2, MM, TD) SST_O(3, MM, TD) SST_E(0, MM, TD)                                       \
        "s_sub_u32 s44, s44, 1\n\t"                                                                               \
        "s_cmp_lg_u32 s44, 0\n\t"                                                                                 \
        "s_cbranch_scc1 LS_loop%=\n\t"                                                                            \
        TAIL                                                                                                      \
        "s_waitcnt lgkmcnt(0)\n\t"                                                                                \
        MM(96) BG3 MM(104) BG3 MM(112) BG3 MM(120)                                                                \
        "s_setprio 3\n\ts_nop 15\n\t"                                                                                            \
        : ACC(acc)                                                                                                \
        : [t] "v"(G.a), [dl] "v"(G.b)                                                                             \
        : "memory", "scc", "s44",                                                                                 \
          "v32", "v33", "v34", "v35", "v36", "v37", "v38", "v39", "v40", "v41", "v42", "v43", "v44", "v45", "v46", "v47", \
          "v48", "v49", "v50", "v51", "v52", "v53", "v54", "v55", "v56", "v57", "v58", "v59", "v60", "v61", "v62", "v63", \
          "v96", "v97", "v98", "v99", "v100", "v101", "v102", "v103", "v104", "v105", "v106", "v107", "v108", "v109", "v110", "v111", \
          "v112", "v113", "v114", "v115", "v116", "v117", "v118", "v119", "v120", "v121", "v122", "v123", "v124", "v125", "v126", "v127")

template <int MODE>
DI SG sgemm_setup(const char* wbase, unsigned wbytes, int wrow, const char* dbase, int dblk, int rot, int w) {
    const int lane = lane_id(), row = lane >> 3, lw = w & 3;
    const int q = (lane & 15) >> 2, h2 = lane >> 5;
    SG G;
    G.rw = srd(wbase, wbytes); G.rd = srd(dbase, 2 * dblk);
    G.wst = 64 * wrow; G.de = (MODE == 0 ? 31 : 21) * 4096; G.ow = rot * G.wst; G.od = rot * 4096;
    if (w >= 4) {
        G.a = (16 * lw + row) * wrow + (((lane & 7) ^ (((row >> 1) & 1) << 2)) << 4);
        G.b = G.a + 8 * wrow - 1024;
        G.c = (lw >> 1) * dblk + (lw & 1) * 2048 + lane * 16;
    } else {
        G.a = ((MODE == 0 ? 8 : 4) * h2 + q) * 128 + (((lw & 1) ^ ((q >> 1) & 1)) << 6) + ((lane >> 4) & 1) * 32 + (lane & 3) * 8;
        G.b = 8192 + (lw >> 1) * 4096 + lane * 16;
        G.c = 0;
    }
    asm volatile("" : "+v"(G.a), "+v"(G.b), "+v"(G.c));
    return G;
}
#define ACC0 "+{v[64:79]}"
#define ACC1 "+{v[80:95]}"
template <int MODE>
DI void sgemm_run(f32x16& acc, SG& G) {
    if (MODE == 0) SGEMM_ASM(ACC0, SMM_WA, 512, 7, SST_O(1, SMM_WA, 512) SST_E(2, SMM_WA, 512) SST_O(3, SMM_WA, 512));
    else SGEMM_ASM(ACC1, SMM_WB, 1024, 5, SST_O(1, SMM_WB, 1024));
}
DI f32x4 sub4(const f32x16& a, int g) { return f32x4{a[4 * g], a[4 * g + 1], a[4 * g + 2], a[4 * g + 3]}; }

DI unsigned gelu2(float a, float b) {
    constexpr float c1 = -2.f * 0.7978845608028654f * 1.4426950408889634f, c2 = c1 * 0.044715f;
    const f32x2 u = {a, b};
    const f32x2 z = u * (u * u * c2 + c1);
    const f32x2 d = f32x2{__builtin_amdgcn_exp2f(z[0]), __builtin_amdgcn_exp2f(z[1])} + 1.f;
    const f32x2 o = u * f32x2{__builtin_amdgcn_rcpf(d[0]), __builtin_amdgcn_rcpf(d[1])};
    return cvtpk(o[0], o[1]);
}

#define WRD(R0, R1, R2, R3, STEP)                                                    \
    "ds_read_b64_tr_b16 " R0 ", %[t0] offset:" #STEP "*2048\n\t"                      \
    "ds_read_b64_tr_b16 " R1 ", %[t0] offset:" #STEP "*2048+1024\n\t"                 \
    "ds_read_b64_tr_b16 " R2 ", %[t1] offset:" #STEP "*2048\n\t"                      \
    "ds_read_b64_tr_b16 " R3 ", %[t1] offset:" #STEP "*2048+1024\n\t"
#define WRD0(S) WRD("v[32:33]", "v[34:35]", "v[36:37]", "v[38:39]", S)
#define WRD1(S) WRD("v[40:41]", "v[42:43]", "v[44:45]", "v[46:47]", S)
#define WRD2(S) WRD("v[48:49]", "v[50:51]", "v[52:53]", "v[54:55]", S)
#define WRD3(S) WRD("v[56:57]", "v[58:59]", "v[60:61]", "v[62:63]", S)
#define WMM(WAIT, W0, W1, O) "s_waitcnt " WAIT "\n\t" MF("v[96:111]", W0, O, "v[96:111]") MF("v[112:127]", W1, O, "v[112:127]")
#define OLD(K, R, V, OFF)                                             \
    "5:\n\t"                                                           \
    "s_bitcmp1_b32 s40, " #K "\n\t"                                    \
    "s_cbranch_scc1 6f\n\t"                                            \
    "global_load_dwordx2 v[24:25], %[fo], %[f] sc1\n\t"                       \
    "s_sub_u32 s41, s41, 1\n\t"                                        \
    "s_cselect_b32 s42, -1, 0\n\t"                                     \
    "s_waitcnt vmcnt(0)\n\t"                                           \
    "v_cmp_eq_u64 vcc, %[tag], v[24:25]\n\t"                                \
    "s_or_b32 s40, vcc_lo, s42\n\t"                                    \
    "s_bitcmp1_b32 s40, " #K "\n\t"                                    \
    "s_cbranch_scc1 6f\n\t"                                            \
    "s_sleep 1\n\t"                                                    \
    "s_branch 5b\n\t"                                                  \
    "6:\n\t"                                                           \
    "global_load_dwordx4 " R ", " V ", %[o] offset:" #OFF " sc1\n\t"
DI void wo_gemm(f32x16& a0, f32x16& a1, f32x4& k0, f32x4& k1, float& k2, const char* osrc, const char* fsrc, u64 tag, int w) {
    const int lane = lane_id();
    const int q = (lane & 15) >> 2, h2 = lane >> 5;
    const unsigned tb = (w >> 1) * 16384 + (4 * h2 + q) * 128 + ((lane >> 4) & 1) * 32 + (lane & 3) * 8;
    const unsigned t0 = tb + ((q >> 1) & 1) * 64, t1 = tb + (((q >> 1) & 1) ^ 1) * 64;
    const unsigned d0 = lane * 16, d1 = d0 + 4096, fo = (lane & 7) * 128;
    asm volatile(
        WRD0(0) WRD1(1) WRD2(2) WRD3(3)
        "s_mov_b32 s40, 0\n\t"
        "s_mov_b32 s41, 0x100000\n\t"
        "s_setprio 0\n\t"
        OLD(0, "v[64:67]", "%[d0]", 0) OLD(1, "v[68:71]", "%[d0]", 1024) OLD(2, "v[72:75]", "%[d0]", 2048) OLD(3, "v[76:79]", "%[d0]", 3072)
        OLD(4, "v[80:83]", "%[d1]", 0) OLD(5, "v[84:87]", "%[d1]", 1024) OLD(6, "v[88:91]", "%[d1]", 2048) OLD(7, "v[92:95]", "%[d1]", 3072)
        "s_setprio 3\n\t"
        "s_waitcnt vmcnt(7) lgkmcnt(0)\n\t"
        MF("v[96:111]", "v[32:35]", "v[64:67]", "0") MF("v[112:127]", "v[36:39]", "v[64:67]", "0")
        WRD0(4)
        WMM("vmcnt(6)", "v[40:43]", "v[44:47]", "v[68:71]") WRD1(5)
        WMM("vmcnt(5)", "v[48:51]", "v[52:55]", "v[72:75]") WRD2(6)
        WMM("vmcnt(4)", "v[56:59]", "v[60:63]", "v[76:79]") WRD3(7)
        WMM("vmcnt(3) lgkmcnt(12)", "v[32:35]", "v[36:39]", "v[80:83]")
        WMM("vmcnt(2) lgkmcnt(0)", "v[40:43]", "v[44:47]", "v[84:87]")
        WMM("vmcnt(1)", "v[48:51]", "v[52:55]", "v[88:91]")
        "s_barrier\n\t"
        WMM("vmcnt(0)", "v[56:59]", "v[60:63]", "v[92:95]")
        "s_nop 15\n\t"
        : "={v[96:111]}"(a0), "={v[112:127]}"(a1),
          "+{v[12:15]}"(k0), "+{v[16:19]}"(k1), "+{v20}"(k2)
        : [o] "s"(osrc), [f] "s"(fsrc), [tag] "s"(tag), [d0] "v"(d0), [d1] "v"(d1), [t0] "v"(t0), [t1] "v"(t1), [fo] "v"(fo)
        : "memory", "scc", "vcc", "s40", "s41", "s42", "v24", "v25",
          "v32", "v33", "v34", "v35", "v36", "v37", "v38", "v39", "v40", "v41", "v42", "v43", "v44", "v45", "v46", "v47",
          "v48", "v49", "v50", "v51", "v52", "v53", "v54", "v55", "v56", "v57", "v58", "v59", "v60", "v61", "v62", "v63",
          "v64", "v65", "v66", "v67", "v68", "v69", "v70", "v71", "v72", "v73", "v74", "v75", "v76", "v77", "v78", "v79",
          "v80", "v81", "v82", "v83", "v84", "v85", "v86", "v87", "v88", "v89", "v90", "v91", "v92", "v93", "v94", "v95");
}

DI void mixer_role(const char* x, const char* wqkv, const char* gw, const char* wo, const char* w1, const char* b1, const char* w2, const char* b2,
                   char* ws, char* out, char* lds, int bi, int w) {
    const int j = bi & 7, i = bi >> 3;
    __builtin_amdgcn_s_setprio(LNP);
    ST_DECL; ST(0);
    const u64 ep = ld_epoch(ws);
    u64 tag;
    {
        const int lane = lane_id(), h2 = lane >> 5, tid = 64 * w + lane;
        const u32x4 xr = *(GAS const u32x4*)(x + (size_t)(8 * bi + w) * 1024 + lane * 16);
        const u32x4 gr = *(GAS const u32x4*)(gw + lane * 16);
        tag = next_tag(ep);
        float v[8], gg[8];
#pragma unroll
        for (int k = 0; k < 4; ++k) { v[2 * k] = bflo(xr[k]); v[2 * k + 1] = bfhi(xr[k]); gg[2 * k] = bflo(gr[k]); gg[2 * k + 1] = bfhi(gr[k]); }
        float s1 = 0.f, s3 = 0.f;
#pragma unroll
        for (int k = 0; k < 8; ++k) { s1 += v[k]; s3 += v[k] * gg[k]; }
        const float mu = wave_sum(s1) * (1.f / 512.f);
        s3 = wave_sum(s3);
        float s2 = 0.f;
#pragma unroll
        for (int k = 0; k < 8; ++k) { v[k] -= mu; s2 += v[k] * v[k]; }
        const float rs = __builtin_amdgcn_rsqf(wave_sum(s2) * (1.f / 512.f) + 1e-5f);
        u32x4 hv;
#pragma unroll
        for (int k = 0; k < 4; ++k) hv[k] = cvtpk(v[2 * k] * rs, v[2 * k + 1] * rs);
        *(LAS u32x4*)((LAS char*)lds + w * 1024 + lane * 16) = hv;
        const float g = __builtin_amdgcn_rcpf(1.f + __builtin_amdgcn_exp2f(-1.4426950408889634f * s3));
        st4(ws + WS_G + (8 * bi + w) * 4, uni(g));
        wgbar();
        LAS const unsigned short* lb = (LAS const unsigned short*)lds + tid;
#pragma unroll
        for (int k = 0; k < 4; ++k) hv[k] = (unsigned)lb[(2 * k) * 512] | ((unsigned)lb[(2 * k + 1) * 512] << 16);
        st16(ws + WS_H + (size_t)(2 * w * 128 + (bi >> 1)) * 1024 + (bi & 1) * 512, h2 * 131072 + (lane & 31) * 16, hv);
        drain();
        wgbar();
        if (w == 0) st_flag(ws + WS_FL + bi * 128, tag);
    }
    __builtin_amdgcn_s_setprio(3);
    const int wb = w & 1, db = (w >> 1) & 1, hf = w >> 2;
    const int L = (j >> 1) * 22 + i, ft = L >> 2, dt1 = 4 * (j & 1) + (L & 3);
    const int st = 8 * (j >> 1) + (i & 7), dt = 4 * (j & 1) + (i >> 3);
    SG G1, G2;
    unsigned ex, l16, og, wof;
    {
        const int lane = lane_id();
        og = (64 * st + 32 * wb + (lane & 31)) * 4;
        G1 = sgemm_setup<0>(w1 + ft * 128, 2048 * 2752 - ft * 128, 2752, ws + WS_H + (size_t)2 * dt1 * 131072, 131072, ROT1, w);
        G2 = sgemm_setup<1>(w2 + st * 128, 1376 * 4096 - st * 128, 4096, ws + WS_U + (size_t)2 * dt * 86 * 1024, 86 * 1024, ROT2, w);
        l16 = lane * 16;
        ex = 65536 + (w & 3) * 2048 + l16;
        wof = (32 * (w & 3) + (lane >> 3)) * 1024 + (((lane & 7) ^ (((lane >> 4) & 1) << 2)) << 4);
        asm volatile("" : "+v"(ex), "+v"(l16), "+v"(og), "+v"(wof));
    }
    {
        const int row = 16 + 8 * (j & 1) + (i & 7), col = 8 + 8 * (i >> 4) + 2 * (j >> 1) + ((i >> 3) & 1);
        if (w == 0) {
            *(LAS unsigned*)QCNT = 0u;
            wait_flags<2>(ws + WS_FA + bi * 128, 0, tag);
        }
        wgbar();
        if (w >= 4) qkv2_loader(x, wqkv, row, col, w - 4);
        else qkv2_body(ws, row, col, bi, w, tag);
    }
    ST(6);
    if (PFA && i >= 22) {
        const int wv = ((i - 22) * 8 + j) * 8 + w;
        for (int k = 0; k < PFD; ++k) __builtin_amdgcn_s_sleep(64);
        warm(w2, 1376, wv, l16 * 4);
        warm(wo, 128, wv, l16 * 4);
    }
    if (i < 22) {
        if (w >= 4) wait_flags<8>(ws + WS_FL + (w - 4) * 8192, l16 * 8, tag);
        if (w == 4) wait_qkv(ws, bi, tag);
        wgbar();
        ST(7);
        f32x4 v0, v1;
        if (w >= 4) {
            sgemm_load<0>(G1, 32, w - 4, G1.rw, 0);
            v0 = *(LAS const f32x4*)(uintptr_t)ex; v1 = *(LAS const f32x4*)(uintptr_t)(ex + 1024);
        } else {
            f32x16 acc1;
            const char* bsrc = b1 + (64 * ft + (ft < 21 ? 32 * wb : 0)) * 2 + (l16 >> 9) * 8;
#pragma unroll
            for (int k = 0; k < 4; ++k) {
                const u32x2 bv = *(GAS const u32x2*)(bsrc + 16 * k);
                acc1[4 * k] = bflo(bv[0]); acc1[4 * k + 1] = bfhi(bv[0]); acc1[4 * k + 2] = bflo(bv[1]); acc1[4 * k + 3] = bfhi(bv[1]);
            }
            sgemm_run<0>(acc1, G1);
            *(LAS f32x4*)(uintptr_t)ex = sub4(acc1, 2); *(LAS f32x4*)(uintptr_t)(ex + 1024) = sub4(acc1, 3);
            wgbar();
            v0 = sub4(acc1, 0); v1 = sub4(acc1, 1);
        }
        ST(1);
        const u32x4 o = {gelu2(v0[0], v0[1]), gelu2(v0[2], v0[3]), gelu2(v1[0], v1[1]), gelu2(v1[2], v1[3])};
        if (ft < 21 || wb == 0) st16(ws + WS_U + (size_t)((2 * dt1 + db) * 86 + 4 * ft + 2 * wb + hf) * 1024, l16, o);
        drain();
        wgbar();
        if (w == 0) st_flag(ws + WS_FU + (dt1 * 32 + ft) * 128, tag);
    }
    ST(2);
    if (w >= 4) sgemm_prew(G2, w - 4);
    if (bi == 255 && w == 1) {
        wait_flags256(ws + WS_FQ, l16 * 8, tag);
        wait_flags256(ws + WS_FL, l16 * 8, tag);
        st_flag(ws + WS_EPOCH, tag);
    }
    if (w == 0) wait_flags<8>(ws + WS_FU + dt * 4096, (l16 < 22 * 16 ? l16 : 0) * 8, tag);
    wgbar();
    ST(3);
    f32x4 bp0, bp1;
    float g;
    {
        asm volatile("global_load_dword %0, %1, %2 sc1" : "=v"(g) : "v"(og), "s"(ws + WS_G) : "memory");
        if (w >= 4) {
            sgemm_load<1>(G2, 22, w - 4, srd(wo + dt * 128, 524288 - dt * 128), wof);
            bp0 = *(LAS const f32x4*)(uintptr_t)ex; bp1 = *(LAS const f32x4*)(uintptr_t)(ex + 1024);
        } else {
            const float bb = bflo(*(GAS const unsigned short*)(b2 + (og >> 1)));
            f32x16 acc2 = {bb, bb, bb, bb, bb, bb, bb, bb, bb, bb, bb, bb, bb, bb, bb, bb};
            sgemm_run<1>(acc2, G2);
            *(LAS f32x4*)(uintptr_t)ex = sub4(acc2, 2); *(LAS f32x4*)(uintptr_t)(ex + 1024) = sub4(acc2, 3);
            wgbar();
            bp0 = sub4(acc2, 0); bp1 = sub4(acc2, 1);
            drain();
        }
        asm volatile("" : "+v"(g));
        ST(4);
        bp0 *= 1.f - g;
        bp1 *= 1.f - g;
        asm volatile("" : "+v"(bp0), "+v"(bp1));
    }
    drain();
    wgbar();
    ST(5);
    f32x16 a0, a1;
    wo_gemm(a0, a1, bp0, bp1, g, ws + WS_O + (size_t)((2 * st + wb) * 32 + 8 * (w >> 1)) * 1024,
            ws + WS_FO + ((2 * st + wb) * 32 + 8 * (w >> 1)) * 128, tag, w);
    {
        const int lane = lane_id(), kq = 2 * db + hf;
        LAS char* ex = (LAS char*)lds + w * 8192 + lane * 16;
#pragma unroll
        for (int k = 0; k < 4; ++k) { *(LAS f32x4*)(ex + k * 1024) = sub4(a0, k); *(LAS f32x4*)(ex + 4096 + k * 1024) = sub4(a1, k); }
        wgbar();
        LAS const char* exr = (LAS const char*)lds + wb * 8192 + kq * 2048 + lane * 16;
        f32x4 s0 = *(LAS const f32x4*)exr, s1 = *(LAS const f32x4*)(exr + 1024);
#pragma unroll
        for (int k = 1; k < 4; ++k) { s0 += *(LAS const f32x4*)(exr + k * 16384); s1 += *(LAS const f32x4*)(exr + k * 16384 + 1024); }
        s0 = s0 * g + bp0;
        s1 = s1 * g + bp1;
        const auto r0 = __builtin_amdgcn_permlane32_swap(cvtpk(s0[0], s0[1]), cvtpk(s1[0], s1[1]), false, false);
        const auto r1 = __builtin_amdgcn_permlane32_swap(cvtpk(s0[2], s0[3]), cvtpk(s1[2], s1[3]), false, false);
        *(GAS u32x4*)(out + (size_t)(64 * st + 32 * wb) * 1024 + (64 * dt + 16 * kq) * 2 + (lane & 31) * 1024 + (lane >> 5) * 16) = u32x4{r0[0], r1[0], r0[1], r1[1]};
    }
    ST_OUT(0);
}

constexpr int MK_LDS = 81920;
__global__ __launch_bounds__(512, 4) void mk(const char* x, const char* wqkv, const char* wo, const char* w1, const char* b1,
                                             const char* w2, const char* b2, const char* gw, char* ws, char* out) {
    extern __shared__ __attribute__((aligned(16))) char lds[];
    const int w = __builtin_amdgcn_readfirstlane(threadIdx.x >> 6);
    if (blockIdx.x < 256) attn_role(x, wqkv, ws, lds, blockIdx.x, w);
    else mixer_role(x, wqkv, gw, wo, w1, b1, w2, b2, ws, out, lds, blockIdx.x - 256, w);
}

extern "C" void kernel_launch(void* const* d_in, const int* in_sizes, int n_in, void* d_out, int out_size, void* d_ws, size_t ws_size,
                              hipStream_t stream) {
    (void)hipFuncSetAttribute((const void*)mk, hipFuncAttributeMaxDynamicSharedMemorySize, MK_LDS);
    mk<<<512, 512, MK_LDS, stream>>>((const char*)d_in[0], (const char*)d_in[1], (const char*)d_in[2], (const char*)d_in[4],
                                     (const char*)d_in[5], (const char*)d_in[6], (const char*)d_in[7], (const char*)d_in[8],
                                     (char*)d_ws, (char*)d_out);
}
```

```cpp
#include <hip/hip_runtime.h>
#include <hip/hip_bf16.h>
#include <stdint.h>

typedef __attribute__((ext_vector_type(8))) short bf16x8;
typedef __attribute__((ext_vector_type(4))) short s16x4;
typedef __attribute__((ext_vector_type(16))) float f32x16;
typedef __attribute__((ext_vector_type(4))) float f32x4;
typedef __attribute__((ext_vector_type(2))) float f32x2;
typedef __attribute__((ext_vector_type(4))) unsigned u32x4;
typedef __attribute__((ext_vector_type(2))) unsigned u32x2;
typedef unsigned long long u64;
#define LAS __attribute__((address_space(3)))
#define GAS __attribute__((address_space(1)))
#define DI __device__ __forceinline__
#define MFMA32(a, b, c) __builtin_amdgcn_mfma_f32_32x32x16_bf16((a), (b), (c), 0, 0, 0)

#define ROT1 ((4 * dt1 + 10 * ft) & 31)
#define ROT2 (((st + dt) & 1) * 11)
#define LNP 0
#define PFA 3
#define PFD 2
#define PSL 2
#define STAG 1
#define CODEPF 1
#define QROT 6
#define QROTM 2
constexpr int S_ = 2048, D_ = 512, F_ = 1376;
constexpr size_t WS_EPOCH = 0;
constexpr size_t WS_G = 4096;
constexpr size_t WS_FL = 98304;
constexpr size_t WS_FQ = 458752;
constexpr size_t WS_FU = 65536;
constexpr size_t WS_FA = 393216;
constexpr size_t WS_FO = 131072;
constexpr size_t WS_Q = 1u << 20;
constexpr size_t WS_K = WS_Q + (2u << 20);
constexpr size_t WS_V = WS_K + (2u << 20);
constexpr size_t WS_H = WS_V + (2u << 20);
constexpr size_t WS_U = WS_H + (2u << 20);
constexpr size_t WS_O = WS_U + (2u << 20);

#define ST_DECL
#define ST_ARG
#define ST_PASS
#define ST(i)
#define ST_OUT(k)
DI unsigned cvtpk(float lo, float hi) {
    typedef float f32x2_t __attribute__((ext_vector_type(2)));
    typedef __bf16 bf16x2_t __attribute__((ext_vector_type(2)));
    return __builtin_bit_cast(unsigned, __builtin_convertvector(f32x2_t{lo, hi}, bf16x2_t));
}
DI float bflo(unsigned u) { return __uint_as_float(u << 16); }
DI float bfhi(unsigned u) { return __uint_as_float(u & 0xffff0000u); }
DI s16x4 trread(LAS const char* p) {
    typedef short v4i16_t __attribute__((ext_vector_type(4)));
    return __builtin_bit_cast(s16x4, __builtin_amdgcn_ds_read_tr16_b64_v4i16((LAS v4i16_t*)p));
}
template <int CTRL> DI float dpp(float v) {
    return __int_as_float(__builtin_amdgcn_update_dpp(0, __float_as_int(v), CTRL, 0xf, 0xf, true));
}
DI float rl(float v, int l) { return __int_as_float(__builtin_amdgcn_readlane(__float_as_int(v), l)); }
DI float wave_sum(float v) {
    v += dpp<0xB1>(v); v += dpp<0x4E>(v); v += dpp<0x141>(v); v += dpp<0x140>(v);
    return (rl(v, 0) + rl(v, 16)) + (rl(v, 32) + rl(v, 48));
}
DI float swap32_max(float v) {
    auto rr = __builtin_amdgcn_permlane32_swap(__float_as_uint(v), __float_as_uint(v), false, false);
    return fmaxf(__uint_as_float(rr[0]), __uint_as_float(rr[1]));
}

DI int lane_id() {
    int l;
    asm volatile("v_mbcnt_lo_u32_b32 %0, -1, 0\n\tv_mbcnt_hi_u32_b32 %0, -1, %0" : "=v"(l));
    __builtin_assume(l >= 0 && l < 64);
    return l;
}
DI void st16(const char* sbase, unsigned voff, u32x4 v) {
    asm volatile("global_store_dwordx4 %0, %1, %2 sc1\n\ts_nop 1" ::"v"(voff), "v"(v), "s"(sbase) : "memory");
}
DI void st4(const char* p, unsigned v) {
    unsigned t0, t1;
    asm volatile("s_nop 1\n\tv_mov_b32 %0, %2\n\tv_mov_b32 %1, 0\n\ts_mov_b64 exec, 1\n\tglobal_store_dword %1, %0, %3 sc1\n\ts_mov_b64 exec, -1"
                 : "=&v"(t0), "=&v"(t1) : "s"(v), "s"(p) : "memory");
}
DI void st_flag(const char* p, u64 v) {
    u64 t0;
    unsigned t1;
    asm volatile("s_nop 1\n\tv_mov_b64 %0, %2\n\tv_mov_b32 %1, 0\n\ts_mov_b64 exec, 1\n\tglobal_store_dwordx2 %1, %0, %3 sc1\n\ts_mov_b64 exec, -1"
                 : "=&v"(t0), "=&v"(t1) : "s"(v), "s"(p) : "memory");
}
DI u64 uni64(u64 v) {
    return (u64)(unsigned)__builtin_amdgcn_readfirstlane((unsigned)v) | ((u64)(unsigned)__builtin_amdgcn_readfirstlane((unsigned)(v >> 32)) << 32);
}
DI u64 ld_epoch(const char* ws) { return __hip_atomic_load((GAS const u64*)(ws + WS_EPOCH), __ATOMIC_RELAXED, __HIP_MEMORY_SCOPE_AGENT); }
DI u64 next_tag(u64 e) { return uni64(e) * 6364136223846793005ull + 1442695040888963407ull; }
DI unsigned uni(float v) { return __builtin_amdgcn_readfirstlane(__float_as_uint(v)); }
DI void warm(const char* base, int nchunks, int first, unsigned l64) {
#pragma clang loop unroll(disable)
    for (int n = first; n < nchunks; n += 640) {
        const char* p = base + (size_t)((n & 1) * (nchunks >> 1) + (n >> 1)) * 4096;
        asm volatile("s_mov_b32 m0, 0x13e00\n\ts_nop 0\n\tglobal_load_lds_dword %0, %1" ::"v"(l64), "s"(p) : "memory");
    }
}
DI void drain() { asm volatile("s_waitcnt vmcnt(0)" ::: "memory"); }
DI void wgbar() { asm volatile("s_waitcnt lgkmcnt(0)\n\ts_barrier" ::: "memory"); }
template <int SLEEP>
DI void wait_flags(const char* sbase, unsigned voff, u64 tag) {
#pragma clang loop unroll(disable)
    for (unsigned spin = 0; spin < (1u << 20); ++spin) {
        u64 f;
        asm volatile("global_load_dwordx2 %0, %1, %2 sc1\n\ts_waitcnt vmcnt(0)" : "=v"(f) : "v"(voff), "s"(sbase) : "memory");
        if (__all(f == tag)) break;
        __builtin_amdgcn_s_sleep(SLEEP);
    }
}

DI void wait_flags256(const char* sbase, unsigned voff, u64 tag) {
#pragma clang loop unroll(disable)
    for (unsigned spin = 0; spin < (1u << 20); ++spin) {
        u64 f0, f1, f2, f3;
        asm volatile("global_load_dwordx2 %0, %4, %5 sc1\n\tglobal_load_dwordx2 %1, %4, %6 sc1\n\tglobal_load_dwordx2 %2, %4, %7 sc1\n\t"
                     "global_load_dwordx2 %3, %4, %8 sc1\n\ts_waitcnt vmcnt(0)"
                     : "=&v"(f0), "=&v"(f1), "=&v"(f2), "=&v"(f3)
                     : "v"(voff), "s"(sbase), "s"(sbase + 8192), "s"(sbase + 16384), "s"(sbase + 24576) : "memory");
        if (__all(f0 == tag && f1 == tag && f2 == tag && f3 == tag)) break;
        __builtin_amdgcn_s_sleep(8);
    }
}

constexpr int QCNT = 73728;
#define MF(D, A, B, C) "v_mfma_f32_32x32x16_bf16 " D ", " A ", " B ", " C "\n\t"
DI u32x4 srd(const char* base, unsigned bytes) {
    return u32x4{(unsigned)(uintptr_t)base, (unsigned)((uintptr_t)base >> 32) & 0xffffu, bytes, 0x00020000u};
}
DI void qkv_store(char* ws, const f32x16& acc, int type, int head, int tile, int lane) {
    const float sc = (type == 0) ? 0.25f * 1.4426950408889634f : 1.f;
    const char* dst = ws + WS_Q + (size_t)type * (2u << 20) + (size_t)(head * 64 + tile) * 1024;
    const unsigned vo = type < 2 ? lane * 16 : ((lane >> 4) & 1) * 65536 + (lane >> 5) * 256 + (lane & 15) * 16;
#pragma unroll
    for (int hf = 0; hf < 2; ++hf) {
        u32x4 o;
#pragma unroll
        for (int i = 0; i < 4; ++i) o[i] = cvtpk(acc[8 * hf + 2 * i] * sc, acc[8 * hf + 2 * i + 1] * sc);
        st16(dst + hf * (type < 2 ? 65536 : 512), vo, o);
    }
}
DI void publish4(const char* flag, u64 tag, int lane) {
    drain();
    unsigned arr = 0;
    if (lane == 0) arr = __hip_atomic_fetch_add((LAS unsigned*)QCNT, 1u, __ATOMIC_RELAXED, __HIP_MEMORY_SCOPE_WORKGROUP);
    if (__builtin_amdgcn_readfirstlane(arr) == 3) st_flag(flag, tag);
}
DI void qkv_loader(const char* x, const char* wqkv, int row, int col, int lw, const char* hint) {
    const int lane = lane_id(), xr = 16 * lw + (lane >> 3), wr = 16 * lw + (lane >> 4);
    const unsigned x0 = xr * 1024 + (((lane & 7) ^ ((xr >> 1) & 7)) << 4), x1 = (xr + 8) * 1024 + (((lane & 7) ^ (((xr + 8) >> 1) & 7)) << 4) - 1024;
    const unsigned w0 = wr * 3072 + (((lane & 15) ^ ((wr & 3) << 2)) << 4);
    const u32x4 rx = srd(x + (size_t)row * 65536, 65536), rw = srd(wqkv + col * 256, 512 * 3072 - col * 256);
    const int rot = (row + col) * QROTM & QROT;
    auto issue = [&](int s, int slot) {
        const int kc = (s + rot) & 7;
        asm volatile("s_mov_b32 m0, %[m]\n\ts_nop 0\n\t"
                     "buffer_load_dwordx4 %[x0], %[rx], %[ox] offen lds\n\t"
                     "buffer_load_dwordx4 %[x1], %[rx], %[ox] offen offset:1024 lds\n\t"
                     "s_mov_b32 m0, %[m2]\n\ts_nop 0\n\t"
                     "buffer_load_dwordx4 %[w0], %[rw], %[ow] offen lds\n\t"
                     "buffer_load_dwordx4 %[w1], %[rw], %[ow] offen offset:1024 lds\n\t"
                     "buffer_load_dwordx4 %[w2], %[rw], %[ow] offen offset:2048 lds\n\t"
                     "buffer_load_dwordx4 %[w3], %[rw], %[ow] offen offset:3072 lds"
                     :: [m] "s"(slot * 24576 + lw * 2048), [m2] "s"(slot * 24576 + 8192 + lw * 4096), [ow] "s"(kc * 196608), [ox] "s"(kc * 128),
                        [rw] "s"(rw), [rx] "s"(rx), [x0] "v"(x0), [x1] "v"(x1),
                        [w0] "v"(w0), [w1] "v"(w0 + 11264), [w2] "v"(w0 + 22528), [w3] "v"(w0 + 33792)
                     : "memory");
    };
    issue(0, 0);
    __builtin_amdgcn_s_setprio(2);
    issue(1, 1);
    __builtin_amdgcn_s_setprio(1);
    issue(2, 2);
    __builtin_amdgcn_s_setprio(3);
    asm volatile("s_waitcnt vmcnt(12)\n\ts_barrier" ::: "memory");
    int slot = 0;
#pragma clang loop unroll(disable)
    for (int s = 3; s < 8; ++s) {
        asm volatile("s_waitcnt vmcnt(6)\n\ts_barrier" ::: "memory");
        issue(s, slot);
        slot = slot == 2 ? 0 : slot + 1;
    }
    if (lw == 1) {
        st_flag(hint, uni64(*(LAS const u64*)(QCNT + 8)));
        asm volatile("s_waitcnt vmcnt(7)\n\ts_barrier\n\ts_waitcnt vmcnt(1)\n\ts_barrier" ::: "memory");
    } else
    asm volatile("s_waitcnt vmcnt(6)\n\ts_barrier\n\ts_waitcnt vmcnt(0)\n\ts_barrier" ::: "memory");
}
template <bool T>
DI void qkv_body(char* ws, char* lds, int row, int col, int a, int w, u64 tag ST_ARG) {
    const int lane = lane_id(), r = lane & 31, h2 = lane >> 5, q = (lane & 15) >> 2;
    f32x16 acc[2] = {};
    const int xa0 = (32 * (w & 1) + r) * 128 + ((h2 ^ ((r >> 1) & 7)) << 4);
    const int wa0 = 8192 + (8 * h2 + q) * 256 + (((2 * (w >> 1)) ^ q) << 6) + ((lane >> 4) & 1) * 32 + (lane & 3) * 8;
    bf16x8 xf[4], wf[2][2];
    auto rdw = [&](LAS const char* sl, int st, int i) {
        LAS const char* pw = sl + (wa0 ^ (i << 6)) + st * 4096;
        const s16x4 lo = trread(pw), hi = trread(pw + 1024);
        wf[st & 1][i] = __builtin_shufflevector(lo, hi, 0, 1, 2, 3, 4, 5, 6, 7);
    };
    auto mm1 = [&](int st, int i) {
        acc[i] = T ? MFMA32(wf[st & 1][i], xf[st], acc[i]) : MFMA32(xf[st], wf[st & 1][i], acc[i]);
        __builtin_amdgcn_sched_barrier(0);
    };
    auto step = [&](int st, LAS const char* sl, bool bar) {
        const int sn = (st + 2) & 3;
        mm1(st, 0);
        if (bar) wgbar();
        xf[sn] = *(LAS const bf16x8*)(sl + (xa0 ^ (sn << 5))); rdw(sl, sn, 0); __builtin_amdgcn_sched_barrier(0);
        mm1(st, 1);
        rdw(sl, sn, 1); __builtin_amdgcn_sched_barrier(0);
    };
    if (w == 0) *(LAS u32x4*)QCNT = u32x4{0u, 0u, (unsigned)tag, (unsigned)(tag >> 32)};
    wgbar();
    LAS const char* sl = (LAS const char*)lds;
#pragma unroll
    for (int st = 0; st < 2; ++st) { xf[st] = *(LAS const bf16x8*)(sl + (xa0 ^ (st << 5))); rdw(sl, st, 0); rdw(sl, st, 1); }
    __builtin_amdgcn_sched_barrier(0);
#pragma clang loop unroll(disable)
    for (int s = 0; s < 7; ++s) {
        step(0, sl, false); step(1, sl, false);
        sl = (sl == (LAS const char*)lds + 49152) ? (LAS const char*)lds : sl + 24576;
        step(2, sl, true); step(3, sl, false);
    }
    step(0, sl, false); step(1, sl, false);
    mm1(2, 0); mm1(2, 1); mm1(3, 0); mm1(3, 1);
    ST(4);
#pragma unroll
    for (int i = 0; i < 2; ++i) qkv_store(ws, acc[i], col >> 2, 8 * (col & 3) + 4 * (w >> 1) + 2 * i, 2 * row + (w & 1), lane);
    publish4(ws + WS_FQ + a * 128, tag, lane);
    ST(7);
}
DI void qkv2_loader(const char* x, const char* wqkv, int rowt, int col, int lw) {
    const int lane = lane_id(), row = 16 * lw + (lane >> 3);
    const unsigned w0 = row * 3072 + (((lane & 7) ^ (((row >> 1) & 1) << 2)) << 4), w1 = w0 + 8 * 3072 - 1024;
    const unsigned x0 = row * 1024 + (((lane & 7) ^ ((row >> 1) & 7)) << 4), x1 = (row + 8) * 1024 + (((lane & 7) ^ (((row + 8) >> 1) & 7)) << 4) - 1024;
    const u32x4 rx = srd(x + (size_t)rowt * 65536, 65536), rw = srd(wqkv + col * 128, 512 * 3072 - col * 128);
    auto issue = [&](int s) {
        asm volatile("s_mov_b32 m0, %[m]\n\ts_nop 0\n\t"
                     "buffer_load_dwordx4 %[w0], %[rw], %[ow] offen lds\n\t"
                     "buffer_load_dwordx4 %[w1], %[rw], %[ow] offen offset:1024 lds\n\t"
                     "s_add_u32 m0, %[m], 8192\n\ts_nop 0\n\t"
                     "buffer_load_dwordx4 %[x0], %[rx], %[ox] offen lds\n\t"
                     "buffer_load_dwordx4 %[x1], %[rx], %[ox] offen offset:1024 lds"
                     :: [m] "s"((s & 3) * 16384 + lw * 2048), [ow] "s"(s * 196608), [ox] "s"(s * 128), [rw] "s"(rw), [rx] "s"(rx),
                        [w0] "v"(w0), [w1] "v"(w1), [x0] "v"(x0), [x1] "v"(x1)
                     : "memory", "scc");
    };
    issue(0); issue(1); issue(2);
#pragma clang loop unroll(disable)
    for (int s = 3; s < 8; ++s) {
        asm volatile("s_waitcnt vmcnt(8)\n\ts_barrier" ::: "memory");
        issue(s);
    }
    asm volatile("s_waitcnt vmcnt(8)\n\ts_barrier\n\ts_waitcnt vmcnt(4)\n\ts_barrier\n\ts_waitcnt vmcnt(0)\n\ts_barrier" ::: "memory");
}
#define QRD3(B, SLOT, K)                                                                             \
    "ds_read_b64_tr_b16 v[" #B ":" #B "+1], %[t] offset:" #SLOT "*16384+" #K "*2048\n\t"              \
    "ds_read_b64_tr_b16 v[" #B "+2:" #B "+3], %[t] offset:" #SLOT "*16384+" #K "*2048+512\n\t"        \
    "ds_read_b128 v[" #B "+4:" #B "+7], %[x" #K "] offset:" #SLOT "*16384\n\t"
#define QSTAGE(SLOT, MM, N0, N1, N2, N3, P0, P1, P2, P3)                              \
    "s_waitcnt lgkmcnt(0)\n\t"                                                        \
    "s_barrier\n\t"                                                                   \
    MM(P0) QRD3(N0, SLOT, 0) MM(P1) QRD3(N1, SLOT, 1) MM(P2) QRD3(N2, SLOT, 2) MM(P3) QRD3(N3, SLOT, 3)
#define QST_E(SLOT, MM) QSTAGE(SLOT, MM, 32, 40, 48, 56, 96, 104, 112, 120)
#define QST_O(SLOT, MM) QSTAGE(SLOT, MM, 96, 104, 112, 120, 32, 40, 48, 56)
#define QMT(P) MF("v[16:31]", "v[" #P ":" #P "+3]", "v[" #P "+4:" #P "+7]", "v[16:31]")
#define QMV(P) MF("v[16:31]", "v[" #P "+4:" #P "+7]", "v[" #P ":" #P "+3]", "v[16:31]")
#define QTILE_ASM(MM)                                                                                             \
    asm volatile(                                                                                                 \
        "s_barrier\n\t"                                                                                           \
        QRD3(32, 0, 0) QRD3(40, 0, 1) QRD3(48, 0, 2) QRD3(56, 0, 3)                                               \
        QST_O(1, MM) QST_E(2, MM)                                                                                 \
        "s_mov_b32 s44, 0\n\t"                                                                                    \
        "LQ_loop%=:\n\t"                                                                                          \
        QST_O(3, MM)                                                                                              \
        "s_cmp_lg_u32 s44, 0\n\t"                                                                                 \
        "s_cbranch_scc1 LQ_end%=\n\t"                                                                             \
        QST_E(0, MM) QST_O(1, MM) QST_E(2, MM)                                                                    \
        "s_mov_b32 s44, 1\n\t"                                                                                    \
        "s_branch LQ_loop%=\n\t"                                                                                  \
        "LQ_end%=:\n\t"                                                                                           \
        "s_waitcnt lgkmcnt(0)\n\t"                                                                                \
        MM(96) MM(104) MM(112) MM(120)                                                                            \
        "s_nop 15\n\t"                                                                                            \
        : "+{v[16:31]}"(acc)                                                                                      \
        : [t] "v"(t), [x0] "v"(xa), [x1] "v"(xa ^ 32), [x2] "v"(xa ^ 64), [x3] "v"(xa ^ 96)                       \
        : "memory", "scc", "s44",                                                                                 \
          "v32", "v33", "v34", "v35", "v36", "v37", "v38", "v39", "v40", "v41", "v42", "v43", "v44", "v45", "v46", "v47", \
          "v48", "v49", "v50", "v51", "v52", "v53", "v54", "v55", "v56", "v57", "v58", "v59", "v60", "v61", "v62", "v63", \
          "v96", "v97", "v98", "v99", "v100", "v101", "v102", "v103", "v104", "v105", "v106", "v107", "v108", "v109", "v110", "v111", \
          "v112", "v113", "v114", "v115", "v116", "v117", "v118", "v119", "v120", "v121", "v122", "v123", "v124", "v125", "v126", "v127")
DI void qkv2_body(char* ws, int row, int col, int bi, int w, u64 tag) {
    const int lane = lane_id(), r = lane & 31, h2 = lane >> 5, q = (lane & 15) >> 2;
    const unsigned t = (8 * h2 + q) * 128 + (((w & 1) ^ ((q >> 1) & 1)) << 6) + ((lane >> 4) & 1) * 32 + (lane & 3) * 8;
    const unsigned xa = 8192 + (32 * (w >> 1) + r) * 128 + ((h2 ^ ((r >> 1) & 7)) << 4);
    f32x16 acc = {};
    if (col < 16) QTILE_ASM(QMT);
    else QTILE_ASM(QMV);
    qkv_store(ws, acc, col >> 3, 4 * (col & 7) + 2 * (w & 1), 2 * row + (w >> 1), lane);
    publish4(ws + WS_FQ + 32768 + bi * 128, tag, lane);
}

#define EXP(a) "v_exp_f32 v" #a ", v" #a "\n\t"
#define CVT(d, a, b) "v_cvt_pk_bf16_f32 v" #d ", v" #a ", v" #b "\n\t"
#define ASTEP(c0, c1, c2, c3, c4, c5, c6, c7, c8, c9, c10, c11, c12, c13, c14, c15, M1, M2, CL, M3)                  \
    EXP(c0) EXP(c1) M1 EXP(c2) EXP(c3) EXP(c4) EXP(c5) M2 CL                                                          \
    EXP(c6) EXP(c7) CVT(96, c0, c1) CVT(97, c2, c3) CVT(98, c4, c5) CVT(99, c6, c7) EXP(c8) EXP(c9) M3                \
    EXP(c10) EXP(c11) EXP(c12) EXP(c13) EXP(c14) EXP(c15)                                                             \
    CVT(100, c8, c9) CVT(101, c10, c11) CVT(102, c12, c13) CVT(103, c14, c15)
#define R_OA "v[64:79]"
#define R_OB "v[80:95]"
#define R_SA "v[32:47]"
#define R_SB "v[48:63]"
#define ATILE(R_NM, KC, KN, V0C, V1C, V0N, V1N, KOFF, VA, VOFF0, VOFF1, PRE)                                                    \
    ASTEP(32, 33, 34, 35, 36, 37, 38, 39, 40, 41, 42, 43, 44, 45, 46, 47,                                             \
          MF(R_OB, V1N, "v[100:103]", R_OB), MF(R_SB, KC, "v[8:11]", R_NM),                                           \
          PRE                                                                                                         \
          "ds_read_b128 " KC ", v12 offset:" KOFF "\n\t"                                                              \
          "ds_read_b128 " V0N ", " VA " offset:" #VOFF0 "\n\t"                                                        \
          "ds_read_b128 " V1N ", " VA " offset:" #VOFF1 "\n\t",                                                          \
          MF(R_OA, V0C, "v[96:99]", R_OA))                                                                            \
    ASTEP(48, 49, 50, 51, 52, 53, 54, 55, 56, 57, 58, 59, 60, 61, 62, 63,                                             \
          MF(R_OA, V1C, "v[100:103]", R_OA), MF(R_SA, KN, "v[4:7]", R_NM), "", MF(R_OB, V0C, "v[96:99]", R_OB))
#define ATILE_E(NM, KOFF, VA, VOFF0, VOFF1, PRE) ATILE(NM, "v[104:107]", "v[108:111]", "v[112:115]", "v[116:119]", "v[120:123]", "v[124:127]", KOFF, VA, VOFF0, VOFF1, PRE)
#define ATILE_O(NM, KOFF, VA, VOFF0, VOFF1, PRE) ATILE(NM, "v[108:111]", "v[104:107]", "v[120:123]", "v[124:127]", "v[112:115]", "v[116:119]", KOFF, VA, VOFF0, VOFF1, PRE)
#define MOV4(a, b, c, d, s) "v_mov_b32 v" #a ", " s "\n\tv_mov_b32 v" #b ", " s "\n\tv_mov_b32 v" #c ", " s "\n\tv_mov_b32 v" #d ", " s "\n\t"
#define WL "s_waitcnt lgkmcnt(0)\n\t"
#define AISSUE                                                   \
    "s_add_u32 m0, s44, %[lw]\n\t"                               \
    "s_add_u32 s44, s44, 0x4000\n\t"                             \
    "buffer_load_dwordx4 v14, %[rk], s43 offen sc1 lds\n\t"      \
    "s_add_u32 m0, m0, 0x2000\n\t"                               \
    "s_and_b32 s44, s44, 0xffff\n\t"                             \
    "buffer_load_dwordx4 v14, %[rv], s43 offen sc1 lds\n\t"      \
    "s_add_u32 s43, s43, 0x2000\n\t"
#define PR2 "s_setprio 2\n\t"
#define PR3 ""
#define PR0 "s_setprio 1\n\t"
#define PR1 "s_setprio 0\n\t"
#define PRI "s_setprio 3\n\t"
constexpr int A_ONES = 65536, A_VOTE = 69632 + 512, A_LSUM = 71680;
struct AttnRegs { f32x16 oa, ob, nm; u32x4 p1, v1; unsigned ka, va, va1, va2, va3, go, fo; };
DI unsigned qkv_dep(int a, bool early) {
    const int lane = lane_id(), r = lane & 15, v = (lane >> 4) & 1, R = 4 * (a >> 5) + (lane & 3);
    const int kv = (r >> 3) + 8 * ((early ? 8 + 8 * v : 16 * v + 8 * (a & 1)) + (r & 7));
    const int qq = ((R >> 3) & 1) + 8 * ((R >> 4) * 24 + (R & 7));
    return ((a & 6) + (lane < 32 || !early ? kv : qq)) * 128;
}
DI void attn_prep(AttnRegs& R, int a, int w) {
    const int lane = lane_id(), r = lane & 31, kh = w >> 2;
    R.ka = 4096 * kh + lane * 16;
    R.va = r < 16 ? 8192 + 4096 * kh + (lane >> 5) * 256 + r * 16 : A_ONES + (r == 16 ? 0 : r == 20 ? 64 : (r > 20 && r < 28) ? 80 : 16);
    R.go = w * 1024 + lane * 16;
#pragma unroll
    for (int i = 0; i < 16; ++i) { R.oa[i] = 0.f; R.ob[i] = 0.f; }
    R.p1 = u32x4{0u, 0u, 0u, 0u};
    R.v1 = u32x4{0u, 0u, 0u, 0u};
    asm volatile("" : "+{v[64:79]}"(R.oa), "+{v[80:95]}"(R.ob), "+{v[100:103]}"(R.p1), "+{v[124:127]}"(R.v1),
                      "+{v12}"(R.ka), "+{v13}"(R.va), "+{v14}"(R.go));
}
#define ATTN_ASM(NM, NMIN) \
    asm volatile( \
        "v_and_b32 v15, 0x3ff, v14\n\t" \
        "s_mov_b32 s43, 0\n\t" \
        "s_mov_b32 s44, 0\n\t" \
        AISSUE \
        "global_load_dwordx4 v[4:7], v15, %[qa] sc1\n\t" \
        "global_load_dwordx4 v[8:11], v15, %[qb] sc1\n\t" \
        AISSUE AISSUE \
        "s_mov_b32 s40, 0xffff\n\t" \
        "s_mov_b32 s41, 0xffff\n\t" \
        "s_mov_b32 s42, 8\n\t" \
        "s_waitcnt vmcnt(4)\n\t" \
        "s_barrier\n\t" \
        "ds_read_b128 v[104:107], v12\n\t" \
        "ds_read_b128 v[108:111], v12 offset:1024\n\t" \
        "ds_read_b128 v[112:115], v13\n\t" \
        "ds_read_b128 v[116:119], v13 offset:512\n\t" \
        PR2 \
        "s_waitcnt lgkmcnt(3)\n\t" \
        MF(R_SA, "v[104:107]", "v[4:7]", NM) \
        "s_nop 15\n\t" \
        "LA_loop%=:\n\t" \
        ATILE_E(NM, "2048", "v13", 1024, 1536, WL PR0) \
        ATILE_O(NM, "3072", "v13", 2048, 2560, WL PR1) \
        ATILE_E(NM, "0", "v13", 3072, 3584, \
                "s_waitcnt vmcnt(2) lgkmcnt(0)\n\t" \
                "s_barrier\n\t" \
                "s_setprio 3\n\t" \
                "v_add_u32 v12, 0x4000, v12\n\t" \
                AISSUE \
                "v_and_b32 v12, 0xffff, v12\n\t" \
                PR2) \
        ATILE_O(NM, "1024", "v13", 0, 512, \
                WL PR3 \
                "s_mov_b64 exec, s[40:41]\n\t" \
                "v_add_u32 v13, 0x4000, v13\n\t" \
                "s_sub_u32 s42, s42, 1\n\t" \
                "v_and_b32 v13, 0xffff, v13\n\t" \
                "s_mov_b64 exec, -1\n\t" \
                "s_cmp_lg_u32 s42, 0\n\t") \
        "s_cbranch_scc1 LA_loop%=\n\t" \
        "s_waitcnt lgkmcnt(0)\n\t" \
        "s_setprio 3\n\t" \
        MF(R_OB, "v[124:127]", "v[100:103]", R_OB) \
        "s_nop 15\n\t" \
        : "+{v[64:79]}"(R.oa), "+{v[80:95]}"(R.ob), "+{v[100:103]}"(R.p1), "+{v[124:127]}"(R.v1), "+{v12}"(R.ka), "+{v13}"(R.va) \
        : NMIN "{v14}"(R.go), [qa] "s"(qa), [qb] "s"(qb), [rk] "s"(rk), [rv] "s"(rv), [lw] "s"(w * 1024) \
        : "memory", "scc", "vcc", "s40", "s41", "s42", "s43", "s44", "s45", "v0", "v1", "v4", "v5", "v6", "v7", "v8", "v9", "v10", "v11", "v15", \
          "v32", "v33", "v34", "v35", "v36", "v37", "v38", "v39", "v40", "v41", "v42", "v43", "v44", "v45", "v46", "v47", \
          "v48", "v49", "v50", "v51", "v52", "v53", "v54", "v55", "v56", "v57", "v58", "v59", "v60", "v61", "v62", "v63", \
          "v96", "v97", "v98", "v99", "v104", "v105", "v106", "v107", "v108", "v109", "v110", "v111", \
          "v112", "v113", "v114", "v115", "v116", "v117", "v118", "v119", "v120", "v121", "v122", "v123")
DI void attn_pass_shift(AttnRegs& R, const char* qa, const char* qb, const char* ksrc, const char* vsrc, int w) {
    const u32x4 rk = srd(ksrc, 65536), rv = srd(vsrc, 65536);
#define NM_IN "{v[16:31]}"(R.nm),
    ATTN_ASM("v[16:31]", NM_IN);
}
#define FISS(S)                                                  \
    "s_add_u32 m0, %[lw], " #S "*16384\n\t"                      \
    "s_add_u32 s43, s43, 0x2000\n\t"                             \
    "buffer_load_dwordx4 v14, %[rk], s43 offen sc1 lds\n\t"      \
    "s_add_u32 m0, %[lw], " #S "*16384+8192\n\t"                 \
    "s_nop 0\n\t"                                                \
    "buffer_load_dwordx4 v14, %[rv], s43 offen sc1 lds\n\t"
#define FI2(I) "s_cbranch_vccz 7f\n\t" PRI I "7:\n\t"
#define FI3(I) "s_cbranch_vccnz 7f\n\t" PRI I PR2 "7:\n\t"
#define LA2 ""
#define LB2 PR2
#define LB3 ""
#define FCHUNK(S, N, VC, VN, BND, I2, I3)                        \
    ATILE_E("0", #S "*16384+2048", VC, 1024, 1536, WL PR0)       \
    ATILE_O("0", #S "*16384+3072", VC, 2048, 2560, WL PR1)       \
    ATILE_E("0", #N "*16384", VC, 3072, 3584, BND LA2 I2 LB2)    \
    ATILE_O("0", #N "*16384+1024", VN, 0, 512, WL I3 LB3)
#define FLD "global_load_dwordx2 v[0:1], v2, %[ff] sc1\n\t"
#define VCCSET "s_mov_b32 vcc_lo, %[lo4]\n\ts_mov_b32 vcc_hi, 0\n\t"
#define FBAR(VM) "s_waitcnt vmcnt(" #VM ") lgkmcnt(0)\n\ts_barrier\n\t"
DI void attn_pass(AttnRegs& R, const char* qa, const char* qb, const char* ksrc, const char* vsrc, const char* late_flags, u64 tag, int w) {
    const u32x4 rk = srd(ksrc, 65536), rv = srd(vsrc, 65536);
    asm volatile(
        "v_and_b32 v15, 0x3ff, v14\n\t"
        "s_mov_b32 s43, -0x2000\n\t"
        FISS(0)
        "global_load_dwordx4 v[4:7], v15, %[qa] sc1\n\t"
        "global_load_dwordx4 v[8:11], v15, %[qb] sc1\n\t"
        FISS(1) FISS(2) VCCSET
        "s_waitcnt vmcnt(4)\n\t"
        "s_barrier\n\t"
        "ds_read_b128 v[104:107], v12\n\t"
        "ds_read_b128 v[108:111], v12 offset:1024\n\t"
        "ds_read_b128 v[112:115], v13\n\t"
        "ds_read_b128 v[116:119], v13 offset:512\n\t"
        PR2
        "s_waitcnt lgkmcnt(3)\n\t"
        MF(R_SA, "v[104:107]", "v[4:7]", "0")
        "s_nop 15\n\t"
        FCHUNK(0, 1, "v13", "v16", FBAR(2), FI2(FLD FISS(3)), FI3(FLD FISS(3)))
        FCHUNK(1, 2, "v16", "v17",
               "s_waitcnt vmcnt(2) lgkmcnt(0)\n\t"
               "s_mov_b32 s45, 0x100000\n\t"
               "LA_p%=:\n\t"
               "v_cmp_ne_u64 vcc, %[tag], v[0:1]\n\t"
               "s_sub_u32 s45, s45, 1\n\t"
               "s_cbranch_scc1 LA_f%=\n\t"
               "s_cbranch_vccz LA_f%=\n\t"
               "global_load_dwordx2 v[0:1], v2, %[ff] sc1\n\t"
               "s_waitcnt vmcnt(0)\n\t"
               "s_branch LA_p%=\n\t"
               "LA_f%=:\n\t"
               "s_barrier\n\t" VCCSET, FI2(FISS(0)), FI3(FISS(0)))
        FCHUNK(2, 3, "v17", "v18", FBAR(2), FI2(FISS(1)), FI3(FISS(1)))
        FCHUNK(3, 0, "v18", "v13", FBAR(2), FI2(FISS(2)), FI3(FISS(2)))
        FCHUNK(0, 1, "v13", "v16", FBAR(2), FI2(FISS(3)), FI3(FISS(3)))
        FCHUNK(1, 2, "v16", "v17", FBAR(2), "", "")
        FCHUNK(2, 3, "v17", "v18", FBAR(0), "", "")
        FCHUNK(3, 0, "v18", "v13", WL, "", "")
        "s_waitcnt lgkmcnt(0)\n\t"
        "s_setprio 3\n\t"
        MF(R_OB, "v[124:127]", "v[100:103]", R_OB)
        "s_nop 15\n\t"
        "Lmk_hot_end:\n\t"
        : "+{v[64:79]}"(R.oa), "+{v[80:95]}"(R.ob), "+{v[100:103]}"(R.p1), "+{v[124:127]}"(R.v1)
        : "{v12}"(R.ka), "{v13}"(R.va), "{v16}"(R.va1), "{v17}"(R.va2), "{v18}"(R.va3), "{v14}"(R.go), "{v2}"(R.fo),
          [qa] "s"(qa), [qb] "s"(qb), [rk] "s"(rk), [rv] "s"(rv), [lw] "s"(w * 1024), [tag] "s"(tag), [ff] "s"(late_flags), [lo4] "s"(1 - (w >> 2))
        : "memory", "scc", "vcc", "s43", "s45", "v0", "v1", "v4", "v5", "v6", "v7", "v8", "v9", "v10", "v11", "v15",
          "v32", "v33", "v34", "v35", "v36", "v37", "v38", "v39", "v40", "v41", "v42", "v43", "v44", "v45", "v46", "v47",
          "v48", "v49", "v50", "v51", "v52", "v53", "v54", "v55", "v56", "v57", "v58", "v59", "v60", "v61", "v62", "v63",
          "v96", "v97", "v98", "v99", "v104", "v105", "v106", "v107", "v108", "v109", "v110", "v111",
          "v112", "v113", "v114", "v115", "v116", "v117", "v118", "v119", "v120", "v121", "v122", "v123");
}

DI void wait_qkv(const char* ws, int a, u64 tag) { wait_flags<PSL>(ws + WS_FQ, qkv_dep(a, true), tag); }
DI void attn_role(const char* x, const char* wqkv, char* ws, char* lds, int a, int w) {
    const int kh = w >> 2;
    const int head = 4 * (a & 7) + ((a >> 3) & 3), qt = 8 * (a >> 5) + 2 * (w & 3);
    ST_DECL; ST(0);
    __builtin_amdgcn_s_setprio(3);
    u64 tag;
    const int ti = a >> 3, trow = 8 * (a & 1) + (ti & 7) + (ti >= 24 ? 16 : 0), tcol = (ti >= 24 ? 0 : 4 * (ti >> 3)) + ((a & 7) >> 1);
    if (w >= 4) {
        qkv_loader(x, wqkv, trow, tcol, w - 4, ws + WS_FA + a * 128);
        tag = uni64(*(LAS const u64*)(QCNT + 8));
        if (w == 4) wait_qkv(ws, a, tag);
    } else {
        tag = next_tag(ld_epoch(ws));
        if (tcol < 8) qkv_body<true>(ws, lds, trow, tcol, a, w, tag ST_PASS);
        else qkv_body<false>(ws, lds, trow, tcol, a, w, tag ST_PASS);
    }
    AttnRegs R;
    attn_prep(R, a, w);
    {
        const unsigned st = (lane_id() & 16) ? 0u : 16384u;
        R.va1 = R.va + st; R.va2 = R.va1 + st; R.va3 = R.va2 + st;
        R.fo = qkv_dep(a, false);
        asm volatile("" : "+{v16}"(R.va1), "+{v17}"(R.va2), "+{v18}"(R.va3), "+{v2}"(R.fo));
    }
    ST(5);
    wgbar();
    ST(6);
    {
        const int o = w * 512 + lane_id() * 8;
        const unsigned one = ((o & 0x1b0) == 0) ? 0x3f803f80u : 0u;
        *(LAS u32x2*)((LAS char*)lds + A_ONES + o) = u32x2{one, one};
        asm volatile("s_waitcnt lgkmcnt(0)" ::: "memory");
    }
    const char* ksrc = ws + WS_K + (size_t)head * 65536;
    const char* qsrc = ws + WS_Q + (size_t)(head * 64 + qt) * 1024;
    auto finish = [&](int att) {
        const int lane = lane_id();
        const f32x16 oa = R.oa, ob = R.ob;
        LAS char* ex = (LAS char*)lds + w * 2048 + lane * 16;
        LAS const char* exr = (LAS const char*)lds + (w ^ 4) * 2048 + lane * 16;
        *(LAS f32x4*)ex = f32x4{ob[0], ob[1], ob[2], ob[3]};
        *(LAS f32x4*)(ex + 1024) = f32x4{ob[4], ob[5], ob[6], ob[7]};
        *(LAS float*)((LAS char*)lds + A_LSUM + w * 256 + lane * 4) = ob[8];
        if (att == 0) {
            float big = fmaxf(oa[8], ob[8]) * 2.7e8f;
#pragma unroll
            for (int i = 0; i < 8; ++i) big = fmaxf(fmaxf(big, __builtin_fabsf(oa[i])), __builtin_fabsf(ob[i]));
            const bool bad = !__all(fminf(oa[8], ob[8]) > 8.1e-28f && big < 1.7e38f);
            if (lane == 0) ((LAS unsigned*)((LAS char*)lds + A_VOTE))[w] = bad;
        }
        wgbar();
        const f32x4 t0 = *(LAS const f32x4*)exr, t1 = *(LAS const f32x4*)(exr + 1024);
        const float t2 = *(LAS const float*)((LAS char*)lds + A_LSUM + (w ^ 4) * 256 + lane * 4);
        unsigned redo = 0;
        if (att == 0) {
            const u32x4 v0 = *(LAS const u32x4*)((LAS char*)lds + A_VOTE), v1 = *(LAS const u32x4*)((LAS char*)lds + A_VOTE + 16);
            redo = __builtin_amdgcn_readfirstlane(v0[0] | v0[1] | v0[2] | v0[3] | v1[0] | v1[1] | v1[2] | v1[3]);
        }
        const float rinv = __builtin_amdgcn_rcpf(oa[8] + t2);
        u32x4 ov;
        ov[0] = cvtpk((oa[0] + t0[0]) * rinv, (oa[1] + t0[1]) * rinv);
        ov[1] = cvtpk((oa[2] + t0[2]) * rinv, (oa[3] + t0[3]) * rinv);
        ov[2] = cvtpk((oa[4] + t1[0]) * rinv, (oa[5] + t1[1]) * rinv);
        ov[3] = cvtpk((oa[6] + t1[2]) * rinv, (oa[7] + t1[3]) * rinv);
        if (att == 0 ? !redo : att == 1 + kh) {
            st16(ws + WS_O + (size_t)((qt + kh) * 32 + head) * 1024, lane * 16, ov);
            drain();
            st_flag(ws + WS_FO + ((qt + kh) * 32 + head) * 128, tag);
        }
        asm volatile("" : "+s"(redo));
        return redo;
    };
    ST(1);
    attn_pass(R, qsrc + kh * 1024, qsrc + (1 - kh) * 1024, ksrc, ksrc + (WS_V - WS_K), ws + WS_FQ + 32768, tag, w);
    ST(2);
    if (finish(0)) {
#pragma clang loop unroll(disable)
        for (int att = 1; att < 3; ++att) {
            drain();
            wgbar();
            const int lane = lane_id();
            const char* qa = qsrc + (att - 1) * 1024;
            bf16x8 qf;
            asm volatile("global_load_dwordx4 %0, %1, %2 sc1\n\ts_waitcnt vmcnt(0)" : "=v"(qf) : "v"(lane * 16), "s"(qa) : "memory");
            float mx = -INFINITY;
#pragma clang loop unroll(disable)
            for (int t = 0; t < 64; ++t) {
                bf16x8 kf;
                asm volatile("global_load_dwordx4 %0, %1, %2 sc1\n\ts_waitcnt vmcnt(0)" : "=v"(kf) : "v"(lane * 16), "s"(ksrc + t * 1024) : "memory");
                const f32x16 s = MFMA32(kf, qf, f32x16{});
#pragma unroll
                for (int i = 0; i < 16; ++i) mx = fmaxf(mx, s[i]);
            }
            mx = -swap32_max(mx);
            attn_prep(R, a, w);
#pragma unroll
            for (int i = 0; i < 16; ++i) R.nm[i] = mx;
            attn_pass_shift(R, qa, qa, ksrc, ksrc + (WS_V - WS_K), w);
            finish(att);
        }
    }
    ST(3); ST_OUT(0);
}

#define BPR "s_setprio 0\n\t"
#define BG1 ""
#define BG2 "s_sleep 1\n\t"
#define BG3 "s_sleep 1\n\t"
#define SISSUE                                                             \
    "s_mov_b32 m0, s44\n\t"                                                 \
    "s_add_u32 s45, %[ow], %[wst]\n\t"                                      \
    "buffer_load_dwordx4 %[g0], %[rw], %[ow] offen lds\n\t"                 \
    "buffer_load_dwordx4 %[g1], %[rw], %[ow] offen offset:1024 lds\n\t"     \
    "s_add_u32 m0, s44, 8192\n\t"                                           \
    "s_add_u32 s46, %[od], 4096\n\t"                                        \
    "buffer_load_dwordx4 %[d0], %[rd], %[od] offen sc1 lds\n\t"             \
    "buffer_load_dwordx4 %[d0], %[rd], %[od] offen offset:1024 sc1 lds\n\t" \
    "s_add_u32 s44, s44, 0x4000\n\t"                                        \
    "s_and_b32 s44, s44, 0xffff\n\t"                                        \
    "s_cmp_eq_u32 %[od], %[de]\n\t"                                         \
    "s_cselect_b32 %[ow], 0, s45\n\t"                                       \
    "s_cselect_b32 %[od], 0, s46\n\t"
#define SISW                                                               \
    "s_mov_b32 m0, s44\n\t"                                                 \
    "s_add_u32 s45, %[ow], %[wst]\n\t"                                      \
    "buffer_load_dwordx4 %[g0], %[rw], %[ow] offen lds\n\t"                 \
    "buffer_load_dwordx4 %[g1], %[rw], %[ow] offen offset:1024 lds\n\t"     \
    "s_add_u32 s44, s44, 0x4000\n\t"                                        \
    "s_mov_b32 %[ow], s45\n\t"
#define SISD                                                               \
    "s_add_u32 m0, s44, 8192\n\t"                                           \
    "s_add_u32 s44, s44, 0x4000\n\t"                                        \
    "buffer_load_dwordx4 %[d0], %[rd], %[od] offen sc1 lds\n\t"             \
    "buffer_load_dwordx4 %[d0], %[rd], %[od] offen offset:1024 sc1 lds\n\t" \
    "s_add_u32 %[od], %[od], 4096\n\t"
struct SG { u32x4 rw, rd; unsigned wst, de, ow, od, a, b, c; };
DI void sgemm_prew(SG& G, int lw) {
    asm volatile("s_mov_b32 s44, %[lo]\n\t" SISW SISW SISW
                 : [ow] "+s"(G.ow) : [wst] "s"(G.wst), [rw] "s"(G.rw), [lo] "s"(lw * 2048), [g0] "v"(G.a), [g1] "v"(G.b) : "memory", "scc", "s44", "s45");
}
#define WISSUE                                                              \
    "s_and_b32 s45, s44, 0xc000\n\t"                                        \
    "s_add_u32 m0, s45, %[lo2]\n\t"                                         \
    "s_lshl_b32 s45, s45, 3\n\t"                                            \
    "buffer_load_dwordx4 %[o0], %[ro], s45 offen lds\n\t"                   \
    "buffer_load_dwordx4 %[o1], %[ro], s45 offen offset:1024 lds\n\t"       \
    "buffer_load_dwordx4 %[o2], %[ro], s45 offen offset:2048 lds\n\t"       \
    "buffer_load_dwordx4 %[o3], %[ro], s45 offen offset:3072 lds\n\t"       \
    "s_add_u32 s44, s44, 0x4000\n\t"                                        \
    "s_and_b32 s44, s44, 0xffff\n\t"
template <int WO>
DI void sgemm_load(SG& G, int n, int lw, u32x4 ro, unsigned o0) {
    asm volatile(
        "s_mov_b32 s44, %[lo]\n\t"
        ".if %c[wo]\n\t"
        "s_sub_u32 s47, %[n], 5\n\t"
        SISD SISD SISD
        "s_waitcnt vmcnt(4)\n\ts_barrier\n\t" SISSUE
        "s_waitcnt vmcnt(6)\n\ts_barrier\n\t" SISSUE
        ".else\n\t"
        "s_sub_u32 s47, %[n], 3\n\t"
        SISSUE SISSUE SISSUE
        ".endif\n\t"
        "LL_loop%=:\n\t"
        "s_waitcnt vmcnt(8)\n\t"
        "s_barrier\n\t"
        SISSUE
        "s_sub_u32 s47, s47, 1\n\t"
        "s_cmp_lg_u32 s47, 0\n\t"
        "s_cbranch_scc1 LL_loop%=\n\t"
        ".if %c[wo]\n\t"
        "s_waitcnt vmcnt(8)\n\ts_barrier\n\t" WISSUE
        "s_waitcnt vmcnt(8)\n\ts_barrier\n\t" WISSUE
        "s_waitcnt vmcnt(8)\n\ts_barrier\n\t" WISSUE
        "s_barrier\n\t" WISSUE
        ".else\n\t"
        "s_waitcnt vmcnt(8)\n\ts_barrier\n\t"
        "s_waitcnt vmcnt(4)\n\ts_barrier\n\t"
        "s_waitcnt vmcnt(0)\n\ts_barrier\n\t"
        "s_barrier\n\t"
        ".endif\n\t"
        : [ow] "+s"(G.ow), [od] "+s"(G.od)
        : [wst] "s"(G.wst), [rw] "s"(G.rw), [rd] "s"(G.rd), [lo] "s"(lw * 2048), [de] "s"(G.de), [n] "s"(n), [g0] "v"(G.a), [g1] "v"(G.b), [d0] "v"(G.c),
          [wo] "i"(WO), [ro] "s"(ro), [lo2] "s"(lw * 4096), [o0] "v"(o0), [o1] "v"(o0 + 7168), [o2] "v"(o0 + 14336), [o3] "v"(o0 + 21504)
        : "memory", "scc", "s44", "s45", "s46", "s47");
}
#define SRD3(B, SLOT, K, TD)                                                                         \
    "ds_read_b64_tr_b16 v[" #B ":" #B "+1], %[t] offset:" #SLOT "*16384+" #K "*2048\n\t"              \
    "ds_read_b64_tr_b16 v[" #B "+2:" #B "+3], %[t] offset:" #SLOT "*16384+" #K "*2048+" #TD "\n\t"    \
    "ds_read_b128 v[" #B "+4:" #B "+7], %[dl] offset:" #SLOT "*16384+" #K "*1024\n\t"
#define SSTAGE(SLOT, MM, TD, N0, N1, N2, N3, P0, P1, P2, P3)                          \
    "s_waitcnt lgkmcnt(0)\n\t"                                                        \
    "s_barrier\n\t"                                                                   \
    MM(P0) SRD3(N0, SLOT, 0, TD) SRD3(N1, SLOT, 1, TD) BG1                            \
    MM(P1) SRD3(N2, SLOT, 2, TD) SRD3(N3, SLOT, 3, TD) BG2                            \
    MM(P2) BG3 MM(P3)
#define SST_E(SLOT, MM, TD) SSTAGE(SLOT, MM, TD, 32, 40, 48, 56, 96, 104, 112, 120)
#define SST_O(SLOT, MM, TD) SSTAGE(SLOT, MM, TD, 96, 104, 112, 120, 32, 40, 48, 56)
#define SMM_NONE(P) ""
#define SMM_WA(P) MF("v[64:79]", "v[" #P ":" #P "+3]", "v[" #P "+4:" #P "+7]", "v[64:79]")
#define SMM_WB(P) MF("v[80:95]", "v[" #P "+4:" #P "+7]", "v[" #P ":" #P "+3]", "v[80:95]")
#define SGEMM_ASM(ACC, MM, TD, NLOOP, TAIL)                                                                       \
    asm volatile(                                                                                                 \
        "s_mov_b32 s44, " #NLOOP "\n\t" BPR                                                                       \
        "s_barrier\n\t"                                                                                           \
        SRD3(32, 0, 0, TD) SRD3(40, 0, 1, TD) SRD3(48, 0, 2, TD) SRD3(56, 0, 3, TD)                               \
        "LS_loop%=:\n\t"                                                                                          \
        SST_O(1, MM, TD) SST_E(2, MM, TD) SST_O(3, MM, TD) SST_E(0, MM, TD)                                       \
        "s_sub_u32 s44, s44, 1\n\t"                                                                               \
        "s_cmp_lg_u32 s44, 0\n\t"                                                                                 \
        "s_cbranch_scc1 LS_loop%=\n\t"                                                                            \
        TAIL                                                                                                      \
        "s_waitcnt lgkmcnt(0)\n\t"                                                                                \
        MM(96) BG3 MM(104) BG3 MM(112) BG3 MM(120)                                                                \
        "s_setprio 3\n\ts_nop 15\n\t"                                                                                            \
        : ACC(acc)                                                                                                \
        : [t] "v"(G.a), [dl] "v"(G.b)                                                                             \
        : "memory", "scc", "s44",                                                                                 \
          "v32", "v33", "v34", "v35", "v36", "v37", "v38", "v39", "v40", "v41", "v42", "v43", "v44", "v45", "v46", "v47", \
          "v48", "v49", "v50", "v51", "v52", "v53", "v54", "v55", "v56", "v57", "v58", "v59", "v60", "v61", "v62", "v63", \
          "v96", "v97", "v98", "v99", "v100", "v101", "v102", "v103", "v104", "v105", "v106", "v107", "v108", "v109", "v110", "v111", \
          "v112", "v113", "v114", "v115", "v116", "v117", "v118", "v119", "v120", "v121", "v122", "v123", "v124", "v125", "v126", "v127")

template <int MODE>
DI SG sgemm_setup(const char* wbase, unsigned wbytes, int wrow, const char* dbase, int dblk, int rot, int w) {
    const int lane = lane_id(), row = lane >> 3, lw = w & 3;
    const int q = (lane & 15) >> 2, h2 = lane >> 5;
    SG G;
    G.rw = srd(wbase, wbytes); G.rd = srd(dbase, 2 * dblk);
    G.wst = 64 * wrow; G.de = (MODE == 0 ? 31 : 21) * 4096; G.ow = rot * G.wst; G.od = rot * 4096;
    if (w >= 4) {
        G.a = (16 * lw + row) * wrow + (((lane & 7) ^ (((row >> 1) & 1) << 2)) << 4);
        G.b = G.a + 8 * wrow - 1024;
        G.c = (lw >> 1) * dblk + (lw & 1) * 2048 + lane * 16;
    } else {
        G.a = ((MODE == 0 ? 8 : 4) * h2 + q) * 128 + (((lw & 1) ^ ((q >> 1) & 1)) << 6) + ((lane >> 4) & 1) * 32 + (lane & 3) * 8;
        G.b = 8192 + (lw >> 1) * 4096 + lane * 16;
        G.c = 0;
    }
    asm volatile("" : "+v"(G.a), "+v"(G.b), "+v"(G.c));
    return G;
}
#define ACC0 "+{v[64:79]}"
#define ACC1 "+{v[80:95]}"
template <int MODE>
DI void sgemm_run(f32x16& acc, SG& G) {
    if (MODE == 0) SGEMM_ASM(ACC0, SMM_WA, 512, 7, SST_O(1, SMM_WA, 512) SST_E(2, SMM_WA, 512) SST_O(3, SMM_WA, 512));
    else SGEMM_ASM(ACC1, SMM_WB, 1024, 5, SST_O(1, SMM_WB, 1024));
}
DI f32x4 sub4(const f32x16& a, int g) { return f32x4{a[4 * g], a[4 * g + 1], a[4 * g + 2], a[4 * g + 3]}; }

DI unsigned gelu2(float a, float b) {
    constexpr float c1 = -2.f * 0.7978845608028654f * 1.4426950408889634f, c2 = c1 * 0.044715f;
    const f32x2 u = {a, b};
    const f32x2 z = u * (u * u * c2 + c1);
    const f32x2 d = f32x2{__builtin_amdgcn_exp2f(z[0]), __builtin_amdgcn_exp2f(z[1])} + 1.f;
    const f32x2 o = u * f32x2{__builtin_amdgcn_rcpf(d[0]), __builtin_amdgcn_rcpf(d[1])};
    return cvtpk(o[0], o[1]);
}

#define WRD(R0, R1, R2, R3, STEP)                                                    \
    "ds_read_b64_tr_b16 " R0 ", %[t0] offset:" #STEP "*2048\n\t"                      \
    "ds_read_b64_tr_b16 " R1 ", %[t0] offset:" #STEP "*2048+1024\n\t"                 \
    "ds_read_b64_tr_b16 " R2 ", %[t1] offset:" #STEP "*2048\n\t"                      \
    "ds_read_b64_tr_b16 " R3 ", %[t1] offset:" #STEP "*2048+1024\n\t"
#define WRD0(S) WRD("v[32:33]", "v[34:35]", "v[36:37]", "v[38:39]", S)
#define WRD1(S) WRD("v[40:41]", "v[42:43]", "v[44:45]", "v[46:47]", S)
#define WRD2(S) WRD("v[48:49]", "v[50:51]", "v[52:53]", "v[54:55]", S)
#define WRD3(S) WRD("v[56:57]", "v[58:59]", "v[60:61]", "v[62:63]", S)
#define WMM(WAIT, W0, W1, O) "s_waitcnt " WAIT "\n\t" MF("v[96:111]", W0, O, "v[96:111]") MF("v[112:127]", W1, O, "v[112:127]")
#define OLD(K, R, V, OFF)                                             \
    "5:\n\t"                                                           \
    "s_bitcmp1_b32 s40, " #K "\n\t"                                    \
    "s_cbranch_scc1 6f\n\t"                                            \
    "global_load_dwordx2 v[24:25], %[fo], %[f] sc1\n\t"                       \
    "s_sub_u32 s41, s41, 1\n\t"                                        \
    "s_cselect_b32 s42, -1, 0\n\t"                                     \
    "s_waitcnt vmcnt(0)\n\t"                                           \
    "v_cmp_eq_u64 vcc, %[tag], v[24:25]\n\t"                                \
    "s_or_b32 s40, vcc_lo, s42\n\t"                                    \
    "s_bitcmp1_b32 s40, " #K "\n\t"                                    \
    "s_cbranch_scc1 6f\n\t"                                            \
    "s_sleep 1\n\t"                                                    \
    "s_branch 5b\n\t"                                                  \
    "6:\n\t"                                                           \
    "global_load_dwordx4 " R ", " V ", %[o] offset:" #OFF " sc1\n\t"
DI void wo_gemm(f32x16& a0, f32x16& a1, f32x4& k0, f32x4& k1, float& k2, const char* osrc, const char* fsrc, u64 tag, int w) {
    const int lane = lane_id();
    const int q = (lane & 15) >> 2, h2 = lane >> 5;
    const unsigned tb = (w >> 1) * 16384 + (4 * h2 + q) * 128 + ((lane >> 4) & 1) * 32 + (lane & 3) * 8;
    const unsigned t0 = tb + ((q >> 1) & 1) * 64, t1 = tb + (((q >> 1) & 1) ^ 1) * 64;
    const unsigned d0 = lane * 16, d1 = d0 + 4096, fo = (lane & 7) * 128;
    asm volatile(
        WRD0(0) WRD1(1) WRD2(2) WRD3(3)
        "s_mov_b32 s40, 0\n\t"
        "s_mov_b32 s41, 0x100000\n\t"
        "s_setprio 0\n\t"
        OLD(0, "v[64:67]", "%[d0]", 0) OLD(1, "v[68:71]", "%[d0]", 1024) OLD(2, "v[72:75]", "%[d0]", 2048) OLD(3, "v[76:79]", "%[d0]", 3072)
        OLD(4, "v[80:83]", "%[d1]", 0) OLD(5, "v[84:87]", "%[d1]", 1024) OLD(6, "v[88:91]", "%[d1]", 2048) OLD(7, "v[92:95]", "%[d1]", 3072)
        "s_setprio 3\n\t"
        "s_waitcnt vmcnt(7) lgkmcnt(0)\n\t"
        MF("v[96:111]", "v[32:35]", "v[64:67]", "0") MF("v[112:127]", "v[36:39]", "v[64:67]", "0")
        WRD0(4)
        WMM("vmcnt(6)", "v[40:43]", "v[44:47]", "v[68:71]") WRD1(5)
        WMM("vmcnt(5)", "v[48:51]", "v[52:55]", "v[72:75]") WRD2(6)
        WMM("vmcnt(4)", "v[56:59]", "v[60:63]", "v[76:79]") WRD3(7)
        WMM("vmcnt(3) lgkmcnt(12)", "v[32:35]", "v[36:39]", "v[80:83]")
        WMM("vmcnt(2) lgkmcnt(0)", "v[40:43]", "v[44:47]", "v[84:87]")
        WMM("vmcnt(1)", "v[48:51]", "v[52:55]", "v[88:91]")
        "s_barrier\n\t"
        WMM("vmcnt(0)", "v[56:59]", "v[60:63]", "v[92:95]")
        "s_nop 15\n\t"
        : "={v[96:111]}"(a0), "={v[112:127]}"(a1),
          "+{v[12:15]}"(k0), "+{v[16:19]}"(k1), "+{v20}"(k2)
        : [o] "s"(osrc), [f] "s"(fsrc), [tag] "s"(tag), [d0] "v"(d0), [d1] "v"(d1), [t0] "v"(t0), [t1] "v"(t1), [fo] "v"(fo)
        : "memory", "scc", "vcc", "s40", "s41", "s42", "v24", "v25",
          "v32", "v33", "v34", "v35", "v36", "v37", "v38", "v39", "v40", "v41", "v42", "v43", "v44", "v45", "v46", "v47",
          "v48", "v49", "v50", "v51", "v52", "v53", "v54", "v55", "v56", "v57", "v58", "v59", "v60", "v61", "v62", "v63",
          "v64", "v65", "v66", "v67", "v68", "v69", "v70", "v71", "v72", "v73", "v74", "v75", "v76", "v77", "v78", "v79",
          "v80", "v81", "v82", "v83", "v84", "v85", "v86", "v87", "v88", "v89", "v90", "v91", "v92", "v93", "v94", "v95");
}

DI void mixer_role(const char* x, const char* wqkv, const char* gw, const char* wo, const char* w1, const char* b1, const char* w2, const char* b2,
                   char* ws, char* out, char* lds, int bi, int w) {
    const int j = bi & 7, i = bi >> 3;
    __builtin_amdgcn_s_setprio(LNP);
    ST_DECL; ST(0);
    const u64 ep = ld_epoch(ws);
    u64 tag;
    {
        const int lane = lane_id(), h2 = lane >> 5, tid = 64 * w + lane;
        const u32x4 xr = *(GAS const u32x4*)(x + (size_t)(8 * bi + w) * 1024 + lane * 16);
        const u32x4 gr = *(GAS const u32x4*)(gw + lane * 16);
        tag = next_tag(ep);
        float v[8], gg[8];
#pragma unroll
        for (int k = 0; k < 4; ++k) { v[2 * k] = bflo(xr[k]); v[2 * k + 1] = bfhi(xr[k]); gg[2 * k] = bflo(gr[k]); gg[2 * k + 1] = bfhi(gr[k]); }
        float s1 = 0.f, s3 = 0.f;
#pragma unroll
        for (int k = 0; k < 8; ++k) { s1 += v[k]; s3 += v[k] * gg[k]; }
        const float mu = wave_sum(s1) * (1.f / 512.f);
        s3 = wave_sum(s3);
        float s2 = 0.f;
#pragma unroll
        for (int k = 0; k < 8; ++k) { v[k] -= mu; s2 += v[k] * v[k]; }
        const float rs = __builtin_amdgcn_rsqf(wave_sum(s2) * (1.f / 512.f) + 1e-5f);
        u32x4 hv;
#pragma unroll
        for (int k = 0; k < 4; ++k) hv[k] = cvtpk(v[2 * k] * rs, v[2 * k + 1] * rs);
        *(LAS u32x4*)((LAS char*)lds + w * 1024 + lane * 16) = hv;
        const float g = __builtin_amdgcn_rcpf(1.f + __builtin_amdgcn_exp2f(-1.4426950408889634f * s3));
        st4(ws + WS_G + (8 * bi + w) * 4, uni(g));
        wgbar();
        LAS const unsigned short* lb = (LAS const unsigned short*)lds + tid;
#pragma unroll
        for (int k = 0; k < 4; ++k) hv[k] = (unsigned)lb[(2 * k) * 512] | ((unsigned)lb[(2 * k + 1) * 512] << 16);
        st16(ws + WS_H + (size_t)(2 * w * 128 + (bi >> 1)) * 1024 + (bi & 1) * 512, h2 * 131072 + (lane & 31) * 16, hv);
        drain();
        wgbar();
        if (w == 0) st_flag(ws + WS_FL + bi * 128, tag);
    }
    __builtin_amdgcn_s_setprio(3);
    const int wb = w & 1, db = (w >> 1) & 1, hf = w >> 2;
    const int L = (j >> 1) * 22 + i, ft = L >> 2, dt1 = 4 * (j & 1) + (L & 3);
    const int st = 8 * (j >> 1) + (i & 7), dt = 4 * (j & 1) + (i >> 3);
    SG G1, G2;
    unsigned ex, l16, og, wof;
    {
        const int lane = lane_id();
        og = (64 * st + 32 * wb + (lane & 31)) * 4;
        G1 = sgemm_setup<0>(w1 + ft * 128, 2048 * 2752 - ft * 128, 2752, ws + WS_H + (size_t)2 * dt1 * 131072, 131072, ROT1, w);
        G2 = sgemm_setup<1>(w2 + st * 128, 1376 * 4096 - st * 128, 4096, ws + WS_U + (size_t)2 * dt * 86 * 1024, 86 * 1024, ROT2, w);
        l16 = lane * 16;
        ex = 65536 + (w & 3) * 2048 + l16;
        wof = (32 * (w & 3) + (lane >> 3)) * 1024 + (((lane & 7) ^ (((lane >> 4) & 1) << 2)) << 4);
        asm volatile("" : "+v"(ex), "+v"(l16), "+v"(og), "+v"(wof));
    }
    {
        const int row = 16 + 8 * (j & 1) + (i & 7), col = 8 + 8 * (i >> 4) + 2 * (j >> 1) + ((i >> 3) & 1);
        if (w == 0) {
            *(LAS unsigned*)QCNT = 0u;
            wait_flags<2>(ws + WS_FA + bi * 128, 0, tag);
        }
        wgbar();
        if (w >= 4) qkv2_loader(x, wqkv, row, col, w - 4);
        else qkv2_body(ws, row, col, bi, w, tag);
    }
    ST(6);
    if (PFA && i >= 22) {
        const int wv = ((i - 22) * 8 + j) * 8 + w;
        for (int k = 0; k < PFD; ++k) __builtin_amdgcn_s_sleep(64);
        warm(w2, 1376, wv, l16 * 4);
        warm(wo, 128, wv, l16 * 4);
    }
    if (i < 22) {
        if (w >= 4) wait_flags<8>(ws + WS_FL + (w - 4) * 8192, l16 * 8, tag);
        if (w == 4) wait_qkv(ws, bi, tag);
        wgbar();
        ST(7);
        f32x4 v0, v1;
        if (w >= 4) {
            sgemm_load<0>(G1, 32, w - 4, G1.rw, 0);
            v0 = *(LAS const f32x4*)(uintptr_t)ex; v1 = *(LAS const f32x4*)(uintptr_t)(ex + 1024);
        } else {
            f32x16 acc1;
            const char* bsrc = b1 + (64 * ft + (ft < 21 ? 32 * wb : 0)) * 2 + (l16 >> 9) * 8;
#pragma unroll
            for (int k = 0; k < 4; ++k) {
                const u32x2 bv = *(GAS const u32x2*)(bsrc + 16 * k);
                acc1[4 * k] = bflo(bv[0]); acc1[4 * k + 1] = bfhi(bv[0]); acc1[4 * k + 2] = bflo(bv[1]); acc1[4 * k + 3] = bfhi(bv[1]);
            }
            sgemm_run<0>(acc1, G1);
            *(LAS f32x4*)(uintptr_t)ex = sub4(acc1, 2); *(LAS f32x4*)(uintptr_t)(ex + 1024) = sub4(acc1, 3);
            wgbar();
            v0 = sub4(acc1, 0); v1 = sub4(acc1, 1);
        }
        ST(1);
        const u32x4 o = {gelu2(v0[0], v0[1]), gelu2(v0[2], v0[3]), gelu2(v1[0], v1[1]), gelu2(v1[2], v1[3])};
        if (ft < 21 || wb == 0) st16(ws + WS_U + (size_t)((2 * dt1 + db) * 86 + 4 * ft + 2 * wb + hf) * 1024, l16, o);
        drain();
        wgbar();
        if (w == 0) st_flag(ws + WS_FU + (dt1 * 32 + ft) * 128, tag);
    }
    ST(2);
    if (w >= 4) sgemm_prew(G2, w - 4);
    if (bi == 255 && w == 1) {
        wait_flags256(ws + WS_FQ, l16 * 8, tag);
        wait_flags256(ws + WS_FL, l16 * 8, tag);
        st_flag(ws + WS_EPOCH, tag);
    }
    if (w == 0) wait_flags<8>(ws + WS_FU + dt * 4096, (l16 < 22 * 16 ? l16 : 0) * 8, tag);
    wgbar();
    ST(3);
    f32x4 bp0, bp1;
    float g;
    {
        asm volatile("global_load_dword %0, %1, %2 sc1" : "=v"(g) : "v"(og), "s"(ws + WS_G) : "memory");
        if (w >= 4) {
            sgemm_load<1>(G2, 22, w - 4, srd(wo + dt * 128, 524288 - dt * 128), wof);
            bp0 = *(LAS const f32x4*)(uintptr_t)ex; bp1 = *(LAS const f32x4*)(uintptr_t)(ex + 1024);
        } else {
            const float bb = bflo(*(GAS const unsigned short*)(b2 + (og >> 1)));
            f32x16 acc2 = {bb, bb, bb, bb, bb, bb, bb, bb, bb, bb, bb, bb, bb, bb, bb, bb};
            sgemm_run<1>(acc2, G2);
            *(LAS f32x4*)(uintptr_t)ex = sub4(acc2, 2); *(LAS f32x4*)(uintptr_t)(ex + 1024) = sub4(acc2, 3);
            wgbar();
            bp0 = sub4(acc2, 0); bp1 = sub4(acc2, 1);
            drain();
        }
        asm volatile("" : "+v"(g));
        ST(4);
        bp0 *= 1.f - g;
        bp1 *= 1.f - g;
        asm volatile("" : "+v"(bp0), "+v"(bp1));
    }
    drain();
    wgbar();
    ST(5);
    f32x16 a0, a1;
    wo_gemm(a0, a1, bp0, bp1, g, ws + WS_O + (size_t)((2 * st + wb) * 32 + 8 * (w >> 1)) * 1024,
            ws + WS_FO + ((2 * st + wb) * 32 + 8 * (w >> 1)) * 128, tag, w);
    {
        const int lane = lane_id(), kq = 2 * db + hf;
        LAS char* ex = (LAS char*)lds + w * 8192 + lane * 16;
#pragma unroll
        for (int k = 0; k < 4; ++k) { *(LAS f32x4*)(ex + k * 1024) = sub4(a0, k); *(LAS f32x4*)(ex + 4096 + k * 1024) = sub4(a1, k); }
        wgbar();
        LAS const char* exr = (LAS const char*)lds + wb * 8192 + kq * 2048 + lane * 16;
        f32x4 s0 = *(LAS const f32x4*)exr, s1 = *(LAS const f32x4*)(exr + 1024);
#pragma unroll
        for (int k = 1; k < 4; ++k) { s0 += *(LAS const f32x4*)(exr + k * 16384); s1 += *(LAS const f32x4*)(exr + k * 16384 + 1024); }
        s0 = s0 * g + bp0;
        s1 = s1 * g + bp1;
        const auto r0 = __builtin_amdgcn_permlane32_swap(cvtpk(s0[0], s0[1]), cvtpk(s1[0], s1[1]), false, false);
        const auto r1 = __builtin_amdgcn_permlane32_swap(cvtpk(s0[2], s0[3]), cvtpk(s1[2], s1[3]), false, false);
        *(GAS u32x4*)(out + (size_t)(64 * st + 32 * wb) * 1024 + (64 * dt + 16 * kq) * 2 + (lane & 31) * 1024 + (lane >> 5) * 16) = u32x4{r0[0], r1[0], r0[1], r1[1]};
    }
    ST_OUT(0);
}

constexpr int MK_LDS = 81920;
__global__ __launch_bounds__(512, 4) void mk(const char* x, const char* wqkv, const char* wo, const char* w1, const char* b1,
                                             const char* w2, const char* b2, const char* gw, char* ws, char* out) {
    extern __shared__ __attribute__((aligned(16))) char lds[];
    const int w = __builtin_amdgcn_readfirstlane(threadIdx.x >> 6);
    if (blockIdx.x >= 256 && w == 7) {
        asm volatile("s_getpc_b64 s[40:41]\n\t"
                     "Lmk_hot_begin:\n\t"
                     "s_cmp_lt_u32 %[o], Lmk_hot_end-Lmk_hot_begin-4096\n\t"
                     "s_cbranch_scc0 1f\n\t"
                     "s_add_u32 s40, s40, %[o]\n\t"
                     "s_addc_u32 s41, s41, 0\n\t"
                     "s_and_b32 s40, s40, -4\n\t"
                     "s_mov_b32 m0, 0x13e00\n\t"
                     "s_nop 0\n\t"
                     "global_load_lds_dword %[v], s[40:41]\n\t"
                     "1:"
                     :: [o] "s"(((blockIdx.x - 256) >> 3) * 4096), [v] "v"(lane_id() * 64) : "memory", "scc", "s40", "s41");
    }
    if (blockIdx.x < 256) attn_role(x, wqkv, ws, lds, blockIdx.x, w);
    else mixer_role(x, wqkv, gw, wo, w1, b1, w2, b2, ws, out, lds, blockIdx.x - 256, w);
}

extern "C" void kernel_launch(void* const* d_in, const int* in_sizes, int n_in, void* d_out, int out_size, void* d_ws, size_t ws_size,
                              hipStream_t stream) {
    (void)hipFuncSetAttribute((const void*)mk, hipFuncAttributeMaxDynamicSharedMemorySize, MK_LDS);
    mk<<<512, 512, MK_LDS, stream>>>((const char*)d_in[0], (const char*)d_in[1], (const char*)d_in[2], (const char*)d_in[4],
                                     (const char*)d_in[5], (const char*)d_in[6], (const char*)d_in[7], (const char*)d_in[8],
                                     (char*)d_ws, (char*)d_out);
}
```

```cpp
#include <hip/hip_runtime.h>
#include <hip/hip_bf16.h>
#include <stdint.h>

typedef __attribute__((ext_vector_type(8))) short bf16x8;
typedef __attribute__((ext_vector_type(4))) short s16x4;
typedef __attribute__((ext_vector_type(16))) float f32x16;
typedef __attribute__((ext_vector_type(4))) float f32x4;
typedef __attribute__((ext_vector_type(2))) float f32x2;
typedef __attribute__((ext_vector_type(4))) unsigned u32x4;
typedef __attribute__((ext_vector_type(2))) unsigned u32x2;
typedef unsigned long long u64;
#define LAS __attribute__((address_space(3)))
#define GAS __attribute__((address_space(1)))
#define DI __device__ __forceinline__
#define MFMA32(a, b, c) __builtin_amdgcn_mfma_f32_32x32x16_bf16((a), (b), (c), 0, 0, 0)

#define ROT1 ((4 * dt1 + 10 * ft) & 31)
#define ROT2 (((st + dt) & 1) * 11)
#define LNP 0
#define PFA 3
#define PFD 2
#define PSL 2
#define STAG 1
#define CODEPF 1
#define QROT 6
#define QROTM 2
constexpr int S_ = 2048, D_ = 512, F_ = 1376;
constexpr size_t WS_EPOCH = 0;
constexpr size_t WS_G = 4096;
constexpr size_t WS_FL = 98304;
constexpr size_t WS_FQ = 458752;
constexpr size_t WS_FU = 65536;
constexpr size_t WS_FO = 131072;
constexpr size_t WS_Q = 1u << 20;
constexpr size_t WS_K = WS_Q + (2u << 20);
constexpr size_t WS_V = WS_K + (2u << 20);
constexpr size_t WS_H = WS_V + (2u << 20);
constexpr size_t WS_U = WS_H + (2u << 20);
constexpr size_t WS_O = WS_U + (2u << 20);

#define ST_DECL
#define ST_ARG
#define ST_PASS
#define ST(i)
#define ST_OUT(k)
DI unsigned cvtpk(float lo, float hi) {
    typedef float f32x2_t __attribute__((ext_vector_type(2)));
    typedef __bf16 bf16x2_t __attribute__((ext_vector_type(2)));
    return __builtin_bit_cast(unsigned, __builtin_convertvector(f32x2_t{lo, hi}, bf16x2_t));
}
DI float bflo(unsigned u) { return __uint_as_float(u << 16); }
DI float bfhi(unsigned u) { return __uint_as_float(u & 0xffff0000u); }
DI s16x4 trread(LAS const char* p) {
    typedef short v4i16_t __attribute__((ext_vector_type(4)));
    return __builtin_bit_cast(s16x4, __builtin_amdgcn_ds_read_tr16_b64_v4i16((LAS v4i16_t*)p));
}
template <int CTRL> DI float dpp(float v) {
    return __int_as_float(__builtin_amdgcn_update_dpp(0, __float_as_int(v), CTRL, 0xf, 0xf, true));
}
DI float rl(float v, int l) { return __int_as_float(__builtin_amdgcn_readlane(__float_as_int(v), l)); }
DI float wave_sum(float v) {
    v += dpp<0xB1>(v); v += dpp<0x4E>(v); v += dpp<0x141>(v); v += dpp<0x140>(v);
    return (rl(v, 0) + rl(v, 16)) + (rl(v, 32) + rl(v, 48));
}
DI float swap32_max(float v) {
    auto rr = __builtin_amdgcn_permlane32_swap(__float_as_uint(v), __float_as_uint(v), false, false);
    return fmaxf(__uint_as_float(rr[0]), __uint_as_float(rr[1]));
}

DI int lane_id() {
    int l;
    asm volatile("v_mbcnt_lo_u32_b32 %0, -1, 0\n\tv_mbcnt_hi_u32_b32 %0, -1, %0" : "=v"(l));
    __builtin_assume(l >= 0 && l < 64);
    return l;
}
DI void st16(const char* sbase, unsigned voff, u32x4 v) {
    asm volatile("global_store_dwordx4 %0, %1, %2 sc1\n\ts_nop 1" ::"v"(voff), "v"(v), "s"(sbase) : "memory");
}
DI void st4(const char* p, unsigned v) {
    unsigned t0, t1;
    asm volatile("s_nop 1\n\tv_mov_b32 %0, %2\n\tv_mov_b32 %1, 0\n\ts_mov_b64 exec, 1\n\tglobal_store_dword %1, %0, %3 sc1\n\ts_mov_b64 exec, -1"
                 : "=&v"(t0), "=&v"(t1) : "s"(v), "s"(p) : "memory");
}
DI void st_flag(const char* p, u64 v) {
    u64 t0;
    unsigned t1;
    asm volatile("s_nop 1\n\tv_mov_b64 %0, %2\n\tv_mov_b32 %1, 0\n\ts_mov_b64 exec, 1\n\tglobal_store_dwordx2 %1, %0, %3 sc1\n\ts_mov_b64 exec, -1"
                 : "=&v"(t0), "=&v"(t1) : "s"(v), "s"(p) : "memory");
}
DI u64 uni64(u64 v) {
    return (u64)(unsigned)__builtin_amdgcn_readfirstlane((unsigned)v) | ((u64)(unsigned)__builtin_amdgcn_readfirstlane((unsigned)(v >> 32)) << 32);
}
DI u64 ld_epoch(const char* ws) { return __hip_atomic_load((GAS const u64*)(ws + WS_EPOCH), __ATOMIC_RELAXED, __HIP_MEMORY_SCOPE_AGENT); }
DI u64 next_tag(u64 e) { return uni64(e) * 6364136223846793005ull + 1442695040888963407ull; }
DI unsigned uni(float v) { return __builtin_amdgcn_readfirstlane(__float_as_uint(v)); }
DI void warm(const char* base, int nchunks, int first, unsigned l64) {
#pragma clang loop unroll(disable)
    for (int n = first; n < nchunks; n += 640) {
        const char* p = base + (size_t)((n & 1) * (nchunks >> 1) + (n >> 1)) * 4096;
        asm volatile("s_mov_b32 m0, 0x13e00\n\ts_nop 0\n\tglobal_load_lds_dword %0, %1" ::"v"(l64), "s"(p) : "memory");
    }
}
DI void drain() { asm volatile("s_waitcnt vmcnt(0)" ::: "memory"); }
DI void wgbar() { asm volatile("s_waitcnt lgkmcnt(0)\n\ts_barrier" ::: "memory"); }
template <int SLEEP>
DI void wait_flags(const char* sbase, unsigned voff, u64 tag) {
#pragma clang loop unroll(disable)
    for (unsigned spin = 0; spin < (1u << 20); ++spin) {
        u64 f;
        asm volatile("global_load_dwordx2 %0, %1, %2 sc1\n\ts_waitcnt vmcnt(0)" : "=v"(f) : "v"(voff), "s"(sbase) : "memory");
        if (__all(f == tag)) break;
        __builtin_amdgcn_s_sleep(SLEEP);
    }
}

DI void wait_flags256(const char* sbase, unsigned voff, u64 tag) {
#pragma clang loop unroll(disable)
    for (unsigned spin = 0; spin < (1u << 20); ++spin) {
        u64 f0, f1, f2, f3;
        asm volatile("global_load_dwordx2 %0, %4, %5 sc1\n\tglobal_load_dwordx2 %1, %4, %6 sc1\n\tglobal_load_dwordx2 %2, %4, %7 sc1\n\t"
                     "global_load_dwordx2 %3, %4, %8 sc1\n\ts_waitcnt vmcnt(0)"
                     : "=&v"(f0), "=&v"(f1), "=&v"(f2), "=&v"(f3)
                     : "v"(voff), "s"(sbase), "s"(sbase + 8192), "s"(sbase + 16384), "s"(sbase + 24576) : "memory");
        if (__all(f0 == tag && f1 == tag && f2 == tag && f3 == tag)) break;
        __builtin_amdgcn_s_sleep(8);
    }
}

constexpr int QCNT = 73728;
#define MF(D, A, B, C) "v_mfma_f32_32x32x16_bf16 " D ", " A ", " B ", " C "\n\t"
DI u32x4 srd(const char* base, unsigned bytes) {
    return u32x4{(unsigned)(uintptr_t)base, (unsigned)((uintptr_t)base >> 32) & 0xffffu, bytes, 0x00020000u};
}
DI void qkv_store(char* ws, const f32x16& acc, int type, int head, int tile, int lane) {
    const float sc = (type == 0) ? 0.25f * 1.4426950408889634f : 1.f;
    const char* dst = ws + WS_Q + (size_t)type * (2u << 20) + (size_t)(head * 64 + tile) * 1024;
    const unsigned vo = type < 2 ? lane * 16 : ((lane >> 4) & 1) * 65536 + (lane >> 5) * 256 + (lane & 15) * 16;
#pragma unroll
    for (int hf = 0; hf < 2; ++hf) {
        u32x4 o;
#pragma unroll
        for (int i = 0; i < 4; ++i) o[i] = cvtpk(acc[8 * hf + 2 * i] * sc, acc[8 * hf + 2 * i + 1] * sc);
        st16(dst + hf * (type < 2 ? 65536 : 512), vo, o);
    }
}
DI void publish4(const char* flag, u64 tag, int lane) {
    drain();
    unsigned arr = 0;
    if (lane == 0) arr = __hip_atomic_fetch_add((LAS unsigned*)QCNT, 1u, __ATOMIC_RELAXED, __HIP_MEMORY_SCOPE_WORKGROUP);
    if (__builtin_amdgcn_readfirstlane(arr) == 3) st_flag(flag, tag);
}
DI void qkv_loader(const char* x, const char* wqkv, int row, int col, int lw) {
    const int lane = lane_id(), xr = 16 * lw + (lane >> 3), wr = 16 * lw + (lane >> 4);
    const unsigned x0 = xr * 1024 + (((lane & 7) ^ ((xr >> 1) & 7)) << 4), x1 = (xr + 8) * 1024 + (((lane & 7) ^ (((xr + 8) >> 1) & 7)) << 4) - 1024;
    const unsigned w0 = wr * 3072 + (((lane & 15) ^ ((wr & 3) << 2)) << 4);
    const u32x4 rx = srd(x + (size_t)row * 65536, 65536), rw = srd(wqkv + col * 256, 512 * 3072 - col * 256);
    const int rot = (row + col) * QROTM & QROT;
    auto issue = [&](int s, int slot) {
        const int kc = (s + rot) & 7;
        asm volatile("s_mov_b32 m0, %[m]\n\ts_nop 0\n\t"
                     "buffer_load_dwordx4 %[x0], %[rx], %[ox] offen lds\n\t"
                     "buffer_load_dwordx4 %[x1], %[rx], %[ox] offen offset:1024 lds\n\t"
                     "s_mov_b32 m0, %[m2]\n\ts_nop 0\n\t"
                     "buffer_load_dwordx4 %[w0], %[rw], %[ow] offen lds\n\t"
                     "buffer_load_dwordx4 %[w1], %[rw], %[ow] offen offset:1024 lds\n\t"
                     "buffer_load_dwordx4 %[w2], %[rw], %[ow] offen offset:2048 lds\n\t"
                     "buffer_load_dwordx4 %[w3], %[rw], %[ow] offen offset:3072 lds"
                     :: [m] "s"(slot * 24576 + lw * 2048), [m2] "s"(slot * 24576 + 8192 + lw * 4096), [ow] "s"(kc * 196608), [ox] "s"(kc * 128),
                        [rw] "s"(rw), [rx] "s"(rx), [x0] "v"(x0), [x1] "v"(x1),
                        [w0] "v"(w0), [w1] "v"(w0 + 11264), [w2] "v"(w0 + 22528), [w3] "v"(w0 + 33792)
                     : "memory");
    };
    issue(0, 0);
    __builtin_amdgcn_s_setprio(2);
    issue(1, 1);
    __builtin_amdgcn_s_setprio(1);
    issue(2, 2);
    __builtin_amdgcn_s_setprio(3);
    asm volatile("s_waitcnt vmcnt(12)\n\ts_barrier" ::: "memory");
    int slot = 0;
#pragma clang loop unroll(disable)
    for (int s = 3; s < 8; ++s) {
        asm volatile("s_waitcnt vmcnt(6)\n\ts_barrier" ::: "memory");
        issue(s, slot);
        slot = slot == 2 ? 0 : slot + 1;
    }
    asm volatile("s_waitcnt vmcnt(6)\n\ts_barrier\n\ts_waitcnt vmcnt(0)\n\ts_barrier" ::: "memory");
}
template <bool T>
DI void qkv_body(char* ws, char* lds, int row, int col, int a, int w, u64 tag ST_ARG) {
    const int lane = lane_id(), r = lane & 31, h2 = lane >> 5, q = (lane & 15) >> 2;
    f32x16 acc[2] = {};
    const int xa0 = (32 * (w & 1) + r) * 128 + ((h2 ^ ((r >> 1) & 7)) << 4);
    const int wa0 = 8192 + (8 * h2 + q) * 256 + (((2 * (w >> 1)) ^ q) << 6) + ((lane >> 4) & 1) * 32 + (lane & 3) * 8;
    bf16x8 xf[4], wf[2][2];
    auto rdw = [&](LAS const char* sl, int st, int i) {
        LAS const char* pw = sl + (wa0 ^ (i << 6)) + st * 4096;
        const s16x4 lo = trread(pw), hi = trread(pw + 1024);
        wf[st & 1][i] = __builtin_shufflevector(lo, hi, 0, 1, 2, 3, 4, 5, 6, 7);
    };
    auto mm1 = [&](int st, int i) {
        acc[i] = T ? MFMA32(wf[st & 1][i], xf[st], acc[i]) : MFMA32(xf[st], wf[st & 1][i], acc[i]);
        __builtin_amdgcn_sched_barrier(0);
    };
    auto step = [&](int st, LAS const char* sl, bool bar) {
        const int sn = (st + 2) & 3;
        mm1(st, 0);
        if (bar) wgbar();
        xf[sn] = *(LAS const bf16x8*)(sl + (xa0 ^ (sn << 5))); rdw(sl, sn, 0); __builtin_amdgcn_sched_barrier(0);
        mm1(st, 1);
        rdw(sl, sn, 1); __builtin_amdgcn_sched_barrier(0);
    };
    if (w == 0) *(LAS u32x4*)QCNT = u32x4{0u, 0u, (unsigned)tag, (unsigned)(tag >> 32)};
    wgbar();
    LAS const char* sl = (LAS const char*)lds;
#pragma unroll
    for (int st = 0; st < 2; ++st) { xf[st] = *(LAS const bf16x8*)(sl + (xa0 ^ (st << 5))); rdw(sl, st, 0); rdw(sl, st, 1); }
    __builtin_amdgcn_sched_barrier(0);
#pragma clang loop unroll(disable)
    for (int s = 0; s < 7; ++s) {
        step(0, sl, false); step(1, sl, false);
        sl = (sl == (LAS const char*)lds + 49152) ? (LAS const char*)lds : sl + 24576;
        step(2, sl, true); step(3, sl, false);
    }
    step(0, sl, false); step(1, sl, false);
    mm1(2, 0); mm1(2, 1); mm1(3, 0); mm1(3, 1);
    ST(4);
#pragma unroll
    for (int i = 0; i < 2; ++i) qkv_store(ws, acc[i], col >> 2, 8 * (col & 3) + 4 * (w >> 1) + 2 * i, 2 * row + (w & 1), lane);
    publish4(ws + WS_FQ + a * 128, tag, lane);
    ST(7);
}
DI void qkv2_loader(const char* x, const char* wqkv, int rowt, int col, int lw) {
    const int lane = lane_id(), row = 16 * lw + (lane >> 3);
    const unsigned w0 = row * 3072 + (((lane & 7) ^ (((row >> 1) & 1) << 2)) << 4), w1 = w0 + 8 * 3072 - 1024;
    const unsigned x0 = row * 1024 + (((lane & 7) ^ ((row >> 1) & 7)) << 4), x1 = (row + 8) * 1024 + (((lane & 7) ^ (((row + 8) >> 1) & 7)) << 4) - 1024;
    const u32x4 rx = srd(x + (size_t)rowt * 65536, 65536), rw = srd(wqkv + col * 128, 512 * 3072 - col * 128);
    auto issue = [&](int s) {
        asm volatile("s_mov_b32 m0, %[m]\n\ts_nop 0\n\t"
                     "buffer_load_dwordx4 %[w0], %[rw], %[ow] offen lds\n\t"
                     "buffer_load_dwordx4 %[w1], %[rw], %[ow] offen offset:1024 lds\n\t"
                     "s_add_u32 m0, %[m], 8192\n\ts_nop 0\n\t"
                     "buffer_load_dwordx4 %[x0], %[rx], %[ox] offen lds\n\t"
                     "buffer_load_dwordx4 %[x1], %[rx], %[ox] offen offset:1024 lds"
                     :: [m] "s"((s & 3) * 16384 + lw * 2048), [ow] "s"(s * 196608), [ox] "s"(s * 128), [rw] "s"(rw), [rx] "s"(rx),
                        [w0] "v"(w0), [w1] "v"(w1), [x0] "v"(x0), [x1] "v"(x1)
                     : "memory", "scc");
    };
    issue(0); issue(1); issue(2);
#pragma clang loop unroll(disable)
    for (int s = 3; s < 8; ++s) {
        asm volatile("s_waitcnt vmcnt(8)\n\ts_barrier" ::: "memory");
        issue(s);
    }
    asm volatile("s_waitcnt vmcnt(8)\n\ts_barrier\n\ts_waitcnt vmcnt(4)\n\ts_barrier\n\ts_waitcnt vmcnt(0)\n\ts_barrier" ::: "memory");
}
#define QRD3(B, SLOT, K)                                                                             \
    "ds_read_b64_tr_b16 v[" #B ":" #B "+1], %[t] offset:" #SLOT "*16384+" #K "*2048\n\t"              \
    "ds_read_b64_tr_b16 v[" #B "+2:" #B "+3], %[t] offset:" #SLOT "*16384+" #K "*2048+512\n\t"        \
    "ds_read_b128 v[" #B "+4:" #B "+7], %[x" #K "] offset:" #SLOT "*16384\n\t"
#define QSTAGE(SLOT, MM, N0, N1, N2, N3, P0, P1, P2, P3)                              \
    "s_waitcnt lgkmcnt(0)\n\t"                                                        \
    "s_barrier\n\t"                                                                   \
    MM(P0) QRD3(N0, SLOT, 0) MM(P1) QRD3(N1, SLOT, 1) MM(P2) QRD3(N2, SLOT, 2) MM(P3) QRD3(N3, SLOT, 3)
#define QST_E(SLOT, MM) QSTAGE(SLOT, MM, 32, 40, 48, 56, 96, 104, 112, 120)
#define QST_O(SLOT, MM) QSTAGE(SLOT, MM, 96, 104, 112, 120, 32, 40, 48, 56)
#define QMT(P) MF("v[16:31]", "v[" #P ":" #P "+3]", "v[" #P "+4:" #P "+7]", "v[16:31]")
#define QMV(P) MF("v[16:31]", "v[" #P "+4:" #P "+7]", "v[" #P ":" #P "+3]", "v[16:31]")
#define QTILE_ASM(MM)                                                                                             \
    asm volatile(                                                                                                 \
        "s_barrier\n\t"                                                                                           \
        QRD3(32, 0, 0) QRD3(40, 0, 1) QRD3(48, 0, 2) QRD3(56, 0, 3)                                               \
        QST_O(1, MM) QST_E(2, MM)                                                                                 \
        "s_mov_b32 s44, 0\n\t"                                                                                    \
        "LQ_loop%=:\n\t"                                                                                          \
        QST_O(3, MM)                                                                                              \
        "s_cmp_lg_u32 s44, 0\n\t"                                                                                 \
        "s_cbranch_scc1 LQ_end%=\n\t"                                                                             \
        QST_E(0, MM) QST_O(1, MM) QST_E(2, MM)                                                                    \
        "s_mov_b32 s44, 1\n\t"                                                                                    \
        "s_branch LQ_loop%=\n\t"                                                                                  \
        "LQ_end%=:\n\t"                                                                                           \
        "s_waitcnt lgkmcnt(0)\n\t"                                                                                \
        MM(96) MM(104) MM(112) MM(120)                                                                            \
        "s_nop 15\n\t"                                                                                            \
        : "+{v[16:31]}"(acc)                                                                                      \
        : [t] "v"(t), [x0] "v"(xa), [x1] "v"(xa ^ 32), [x2] "v"(xa ^ 64), [x3] "v"(xa ^ 96)                       \
        : "memory", "scc", "s44",                                                                                 \
          "v32", "v33", "v34", "v35", "v36", "v37", "v38", "v39", "v40", "v41", "v42", "v43", "v44", "v45", "v46", "v47", \
          "v48", "v49", "v50", "v51", "v52", "v53", "v54", "v55", "v56", "v57", "v58", "v59", "v60", "v61", "v62", "v63", \
          "v96", "v97", "v98", "v99", "v100", "v101", "v102", "v103", "v104", "v105", "v106", "v107", "v108", "v109", "v110", "v111", \
          "v112", "v113", "v114", "v115", "v116", "v117", "v118", "v119", "v120", "v121", "v122", "v123", "v124", "v125", "v126", "v127")
DI void qkv2_body(char* ws, int row, int col, int bi, int w, u64 tag) {
    const int lane = lane_id(), r = lane & 31, h2 = lane >> 5, q = (lane & 15) >> 2;
    const unsigned t = (8 * h2 + q) * 128 + (((w & 1) ^ ((q >> 1) & 1)) << 6) + ((lane >> 4) & 1) * 32 + (lane & 3) * 8;
    const unsigned xa = 8192 + (32 * (w >> 1) + r) * 128 + ((h2 ^ ((r >> 1) & 7)) << 4);
    f32x16 acc = {};
    if (col < 16) QTILE_ASM(QMT);
    else QTILE_ASM(QMV);
    qkv_store(ws, acc, col >> 3, 4 * (col & 7) + 2 * (w & 1), 2 * row + (w >> 1), lane);
    publish4(ws + WS_FQ + 32768 + bi * 128, tag, lane);
}

#define EXP(a) "v_exp_f32 v" #a ", v" #a "\n\t"
#define CVT(d, a, b) "v_cvt_pk_bf16_f32 v" #d ", v" #a ", v" #b "\n\t"
#define ASTEP(c0, c1, c2, c3, c4, c5, c6, c7, c8, c9, c10, c11, c12, c13, c14, c15, M1, M2, CL, M3)                  \
    EXP(c0) EXP(c1) M1 EXP(c2) EXP(c3) EXP(c4) EXP(c5) M2 CL                                                          \
    EXP(c6) EXP(c7) CVT(96, c0, c1) CVT(97, c2, c3) CVT(98, c4, c5) CVT(99, c6, c7) EXP(c8) EXP(c9) M3                \
    EXP(c10) EXP(c11) EXP(c12) EXP(c13) EXP(c14) EXP(c15)                                                             \
    CVT(100, c8, c9) CVT(101, c10, c11) CVT(102, c12, c13) CVT(103, c14, c15)
#define R_OA "v[64:79]"
#define R_OB "v[80:95]"
#define R_SA "v[32:47]"
#define R_SB "v[48:63]"
#define ATILE(R_NM, KC, KN, V0C, V1C, V0N, V1N, KOFF, VA, VOFF0, VOFF1, PRE)                                                    \
    ASTEP(32, 33, 34, 35, 36, 37, 38, 39, 40, 41, 42, 43, 44, 45, 46, 47,                                             \
          MF(R_OB, V1N, "v[100:103]", R_OB), MF(R_SB, KC, "v[8:11]", R_NM),                                           \
          PRE                                                                                                         \
          "ds_read_b128 " KC ", v12 offset:" KOFF "\n\t"                                                              \
          "ds_read_b128 " V0N ", " VA " offset:" #VOFF0 "\n\t"                                                        \
          "ds_read_b128 " V1N ", " VA " offset:" #VOFF1 "\n\t",                                                          \
          MF(R_OA, V0C, "v[96:99]", R_OA))                                                                            \
    ASTEP(48, 49, 50, 51, 52, 53, 54, 55, 56, 57, 58, 59, 60, 61, 62, 63,                                             \
          MF(R_OA, V1C, "v[100:103]", R_OA), MF(R_SA, KN, "v[4:7]", R_NM), "", MF(R_OB, V0C, "v[96:99]", R_OB))
#define ATILE_E(NM, KOFF, VA, VOFF0, VOFF1, PRE) ATILE(NM, "v[104:107]", "v[108:111]", "v[112:115]", "v[116:119]", "v[120:123]", "v[124:127]", KOFF, VA, VOFF0, VOFF1, PRE)
#define ATILE_O(NM, KOFF, VA, VOFF0, VOFF1, PRE) ATILE(NM, "v[108:111]", "v[104:107]", "v[120:123]", "v[124:127]", "v[112:115]", "v[116:119]", KOFF, VA, VOFF0, VOFF1, PRE)
#define MOV4(a, b, c, d, s) "v_mov_b32 v" #a ", " s "\n\tv_mov_b32 v" #b ", " s "\n\tv_mov_b32 v" #c ", " s "\n\tv_mov_b32 v" #d ", " s "\n\t"
#define WL "s_waitcnt lgkmcnt(0)\n\t"
#define AISSUE                                                   \
    "s_add_u32 m0, s44, %[lw]\n\t"                               \
    "s_add_u32 s44, s44, 0x4000\n\t"                             \
    "buffer_load_dwordx4 v14, %[rk], s43 offen sc1 lds\n\t"      \
    "s_add_u32 m0, m0, 0x2000\n\t"                               \
    "s_and_b32 s44, s44, 0xffff\n\t"                             \
    "buffer_load_dwordx4 v14, %[rv], s43 offen sc1 lds\n\t"      \
    "s_add_u32 s43, s43, 0x2000\n\t"
#define PR2 "s_setprio 2\n\t"
#define PR3 ""
#define PR0 "s_setprio 1\n\t"
#define PR1 "s_setprio 0\n\t"
#define PRI "s_setprio 3\n\t"
constexpr int A_ONES = 65536, A_VOTE = 69632 + 512, A_LSUM = 71680;
struct AttnRegs { f32x16 oa, ob, nm; u32x4 p1, v1; unsigned ka, va, va1, va2, va3, go, fo; };
DI unsigned qkv_dep(int a, bool early) {
    const int lane = lane_id(), r = lane & 15, v = (lane >> 4) & 1, R = 4 * (a >> 5) + (lane & 3);
    const int kv = (r >> 3) + 8 * ((early ? 8 + 8 * v : 16 * v + 8 * (a & 1)) + (r & 7));
    const int qq = ((R >> 3) & 1) + 8 * ((R >> 4) * 24 + (R & 7));
    return ((a & 6) + (lane < 32 || !early ? kv : qq)) * 128;
}
DI void attn_prep(AttnRegs& R, int a, int w) {
    const int lane = lane_id(), r = lane & 31, kh = w >> 2;
    R.ka = 4096 * kh + lane * 16;
    R.va = r < 16 ? 8192 + 4096 * kh + (lane >> 5) * 256 + r * 16 : A_ONES + (r == 16 ? 0 : r == 20 ? 64 : (r > 20 && r < 28) ? 80 : 16);
    R.go = w * 1024 + lane * 16;
#pragma unroll
    for (int i = 0; i < 16; ++i) { R.oa[i] = 0.f; R.ob[i] = 0.f; }
    R.p1 = u32x4{0u, 0u, 0u, 0u};
    R.v1 = u32x4{0u, 0u, 0u, 0u};
    asm volatile("" : "+{v[64:79]}"(R.oa), "+{v[80:95]}"(R.ob), "+{v[100:103]}"(R.p1), "+{v[124:127]}"(R.v1),
                      "+{v12}"(R.ka), "+{v13}"(R.va), "+{v14}"(R.go));
}
#define ATTN_ASM(NM, NMIN) \
    asm volatile( \
        "v_and_b32 v15, 0x3ff, v14\n\t" \
        "s_mov_b32 s43, 0\n\t" \
        "s_mov_b32 s44, 0\n\t" \
        AISSUE \
        "global_load_dwordx4 v[4:7], v15, %[qa] sc1\n\t" \
        "global_load_dwordx4 v[8:11], v15, %[qb] sc1\n\t" \
        AISSUE AISSUE \
        "s_mov_b32 s40, 0xffff\n\t" \
        "s_mov_b32 s41, 0xffff\n\t" \
        "s_mov_b32 s42, 8\n\t" \
        "s_waitcnt vmcnt(4)\n\t" \
        "s_barrier\n\t" \
        "ds_read_b128 v[104:107], v12\n\t" \
        "ds_read_b128 v[108:111], v12 offset:1024\n\t" \
        "ds_read_b128 v[112:115], v13\n\t" \
        "ds_read_b128 v[116:119], v13 offset:512\n\t" \
        PR2 \
        "s_waitcnt lgkmcnt(3)\n\t" \
        MF(R_SA, "v[104:107]", "v[4:7]", NM) \
        "s_nop 15\n\t" \
        "LA_loop%=:\n\t" \
        ATILE_E(NM, "2048", "v13", 1024, 1536, WL PR0) \
        ATILE_O(NM, "3072", "v13", 2048, 2560, WL PR1) \
        ATILE_E(NM, "0", "v13", 3072, 3584, \
                "s_waitcnt vmcnt(2) lgkmcnt(0)\n\t" \
                "s_barrier\n\t" \
                "s_setprio 3\n\t" \
                "v_add_u32 v12, 0x4000, v12\n\t" \
                AISSUE \
                "v_and_b32 v12, 0xffff, v12\n\t" \
                PR2) \
        ATILE_O(NM, "1024", "v13", 0, 512, \
                WL PR3 \
                "s_mov_b64 exec, s[40:41]\n\t" \
                "v_add_u32 v13, 0x4000, v13\n\t" \
                "s_sub_u32 s42, s42, 1\n\t" \
                "v_and_b32 v13, 0xffff, v13\n\t" \
                "s_mov_b64 exec, -1\n\t" \
                "s_cmp_lg_u32 s42, 0\n\t") \
        "s_cbranch_scc1 LA_loop%=\n\t" \
        "s_waitcnt lgkmcnt(0)\n\t" \
        "s_setprio 3\n\t" \
        MF(R_OB, "v[124:127]", "v[100:103]", R_OB) \
        "s_nop 15\n\t" \
        : "+{v[64:79]}"(R.oa), "+{v[80:95]}"(R.ob), "+{v[100:103]}"(R.p1), "+{v[124:127]}"(R.v1), "+{v12}"(R.ka), "+{v13}"(R.va) \
        : NMIN "{v14}"(R.go), [qa] "s"(qa), [qb] "s"(qb), [rk] "s"(rk), [rv] "s"(rv), [lw] "s"(w * 1024) \
        : "memory", "scc", "vcc", "s40", "s41", "s42", "s43", "s44", "s45", "v0", "v1", "v4", "v5", "v6", "v7", "v8", "v9", "v10", "v11", "v15", \
          "v32", "v33", "v34", "v35", "v36", "v37", "v38", "v39", "v40", "v41", "v42", "v43", "v44", "v45", "v46", "v47", \
          "v48", "v49", "v50", "v51", "v52", "v53", "v54", "v55", "v56", "v57", "v58", "v59", "v60", "v61", "v62", "v63", \
          "v96", "v97", "v98", "v99", "v104", "v105", "v106", "v107", "v108", "v109", "v110", "v111", \
          "v112", "v113", "v114", "v115", "v116", "v117", "v118", "v119", "v120", "v121", "v122", "v123")
DI void attn_pass_shift(AttnRegs& R, const char* qa, const char* qb, const char* ksrc, const char* vsrc, int w) {
    const u32x4 rk = srd(ksrc, 65536), rv = srd(vsrc, 65536);
#define NM_IN "{v[16:31]}"(R.nm),
    ATTN_ASM("v[16:31]", NM_IN);
}
#define FISS(S)                                                  \
    "s_add_u32 m0, %[lw], " #S "*16384\n\t"                      \
    "s_add_u32 s43, s43, 0x2000\n\t"                             \
    "buffer_load_dwordx4 v14, %[rk], s43 offen sc1 lds\n\t"      \
    "s_add_u32 m0, %[lw], " #S "*16384+8192\n\t"                 \
    "s_nop 0\n\t"                                                \
    "buffer_load_dwordx4 v14, %[rv], s43 offen sc1 lds\n\t"
#define FI2(I) "s_cbranch_vccz 7f\n\t" PRI I "7:\n\t"
#define FI3(I) "s_cbranch_vccnz 7f\n\t" PRI I PR2 "7:\n\t"
#define LA2 ""
#define LB2 PR2
#define LB3 ""
#define FCHUNK(S, N, VC, VN, BND, I2, I3) FCHUNKX(S, N, VC, VN, BND, I2, I3, "")
#define FCHUNKX(S, N, VC, VN, BND, I2, I3, I0)                        \
    ATILE_E("0", #S "*16384+2048", VC, 1024, 1536, WL I0 PR0)       \
    ATILE_O("0", #S "*16384+3072", VC, 2048, 2560, WL PR1)       \
    ATILE_E("0", #N "*16384", VC, 3072, 3584, BND LA2 I2 LB2)    \
    ATILE_O("0", #N "*16384+1024", VN, 0, 512, WL I3 LB3)
#define FLD "global_load_dwordx2 v[0:1], v2, %[ff] sc1\n\t"
#define VCCSET "s_mov_b32 vcc_lo, %[lo4]\n\ts_mov_b32 vcc_hi, 0\n\t"
#define FBAR(VM) "s_waitcnt vmcnt(" #VM ") lgkmcnt(0)\n\ts_barrier\n\t"
DI void attn_pass(AttnRegs& R, const char* qa, const char* qb, const char* ksrc, const char* vsrc, const char* late_flags, u64 tag, int w) {
    const u32x4 rk = srd(ksrc, 65536), rv = srd(vsrc, 65536);
    asm volatile(
        "v_and_b32 v15, 0x3ff, v14\n\t"
        "s_mov_b32 s43, -0x2000\n\t"
        FISS(0)
        "global_load_dwordx4 v[4:7], v15, %[qa] sc1\n\t"
        "global_load_dwordx4 v[8:11], v15, %[qb] sc1\n\t"
        FISS(1) FISS(2) VCCSET
        "s_waitcnt vmcnt(4)\n\t"
        "s_barrier\n\t"
        "ds_read_b128 v[104:107], v12\n\t"
        "ds_read_b128 v[108:111], v12 offset:1024\n\t"
        "ds_read_b128 v[112:115], v13\n\t"
        "ds_read_b128 v[116:119], v13 offset:512\n\t"
        PR2
        "s_waitcnt lgkmcnt(3)\n\t"
        MF(R_SA, "v[104:107]", "v[4:7]", "0")
        "s_nop 15\n\t"
        FCHUNK(0, 1, "v13", "v16", FBAR(2), FI2(FISS(3)), FI3(FISS(3)))
        FCHUNK(1, 2, "v16", "v17", FBAR(2), "", "")
        FCHUNKX(2, 3, "v17", "v18",
               "s_waitcnt vmcnt(0) lgkmcnt(0)\n\t"
               "s_mov_b32 s45, 0x100000\n\t"
               "LA_p%=:\n\t"
               "v_cmp_ne_u64 vcc, %[tag], v[0:1]\n\t"
               "s_sub_u32 s45, s45, 1\n\t"
               "s_cbranch_scc1 LA_f%=\n\t"
               "s_cbranch_vccz LA_f%=\n\t"
               "global_load_dwordx2 v[0:1], v2, %[ff] sc1\n\t"
               "s_waitcnt vmcnt(0)\n\t"
               "s_branch LA_p%=\n\t"
               "LA_f%=:\n\t"
               "s_barrier\n\t" VCCSET, FI2(FISS(0) FISS(1)), FI3(FISS(0) FISS(1)), PRI FLD)
        FCHUNK(3, 0, "v18", "v13", FBAR(2), FI2(FISS(2)), FI3(FISS(2)))
        FCHUNK(0, 1, "v13", "v16", FBAR(2), FI2(FISS(3)), FI3(FISS(3)))
        FCHUNK(1, 2, "v16", "v17", FBAR(2), "", "")
        FCHUNK(2, 3, "v17", "v18", FBAR(0), "", "")
        FCHUNK(3, 0, "v18", "v13", WL, "", "")
        "s_waitcnt lgkmcnt(0)\n\t"
        "s_setprio 3\n\t"
        MF(R_OB, "v[124:127]", "v[100:103]", R_OB)
        "s_nop 15\n\t"
        "Lmk_hot_end:\n\t"
        : "+{v[64:79]}"(R.oa), "+{v[80:95]}"(R.ob), "+{v[100:103]}"(R.p1), "+{v[124:127]}"(R.v1)
        : "{v12}"(R.ka), "{v13}"(R.va), "{v16}"(R.va1), "{v17}"(R.va2), "{v18}"(R.va3), "{v14}"(R.go), "{v2}"(R.fo),
          [qa] "s"(qa), [qb] "s"(qb), [rk] "s"(rk), [rv] "s"(rv), [lw] "s"(w * 1024), [tag] "s"(tag), [ff] "s"(late_flags), [lo4] "s"(1 - (w >> 2))
        : "memory", "scc", "vcc", "s43", "s45", "v0", "v1", "v4", "v5", "v6", "v7", "v8", "v9", "v10", "v11", "v15",
          "v32", "v33", "v34", "v35", "v36", "v37", "v38", "v39", "v40", "v41", "v42", "v43", "v44", "v45", "v46", "v47",
          "v48", "v49", "v50", "v51", "v52", "v53", "v54", "v55", "v56", "v57", "v58", "v59", "v60", "v61", "v62", "v63",
          "v96", "v97", "v98", "v99", "v104", "v105", "v106", "v107", "v108", "v109", "v110", "v111",
          "v112", "v113", "v114", "v115", "v116", "v117", "v118", "v119", "v120", "v121", "v122", "v123");
}

DI void wait_qkv(const char* ws, int a, u64 tag) { wait_flags<PSL>(ws + WS_FQ, qkv_dep(a, true), tag); }
DI void attn_role(const char* x, const char* wqkv, char* ws, char* lds, int a, int w) {
    const int kh = w >> 2;
    const int head = 4 * (a & 7) + ((a >> 3) & 3), qt = 8 * (a >> 5) + 2 * (w & 3);
    ST_DECL; ST(0);
    __builtin_amdgcn_s_setprio(3);
    u64 tag;
    const int ti = a >> 3, trow = 8 * (a & 1) + (ti & 7) + (ti >= 24 ? 16 : 0), tcol = (ti >= 24 ? 0 : 4 * (ti >> 3)) + ((a & 7) >> 1);
    if (w >= 4) {
        qkv_loader(x, wqkv, trow, tcol, w - 4);
        tag = uni64(*(LAS const u64*)(QCNT + 8));
        if (w == 4) wait_qkv(ws, a, tag);
    } else {
        tag = next_tag(ld_epoch(ws));
        if (tcol < 8) qkv_body<true>(ws, lds, trow, tcol, a, w, tag ST_PASS);
        else qkv_body<false>(ws, lds, trow, tcol, a, w, tag ST_PASS);
    }
    AttnRegs R;
    attn_prep(R, a, w);
    {
        const unsigned st = (lane_id() & 16) ? 0u : 16384u;
        R.va1 = R.va + st; R.va2 = R.va1 + st; R.va3 = R.va2 + st;
        R.fo = qkv_dep(a, false);
        asm volatile("" : "+{v16}"(R.va1), "+{v17}"(R.va2), "+{v18}"(R.va3), "+{v2}"(R.fo));
    }
    ST(5);
    wgbar();
    ST(6);
    {
        const int o = w * 512 + lane_id() * 8;
        const unsigned one = ((o & 0x1b0) == 0) ? 0x3f803f80u : 0u;
        *(LAS u32x2*)((LAS char*)lds + A_ONES + o) = u32x2{one, one};
        asm volatile("s_waitcnt lgkmcnt(0)" ::: "memory");
    }
    const char* ksrc = ws + WS_K + (size_t)head * 65536;
    const char* qsrc = ws + WS_Q + (size_t)(head * 64 + qt) * 1024;
    auto finish = [&](int att) {
        const int lane = lane_id();
        const f32x16 oa = R.oa, ob = R.ob;
        LAS char* ex = (LAS char*)lds + w * 2048 + lane * 16;
        LAS const char* exr = (LAS const char*)lds + (w ^ 4) * 2048 + lane * 16;
        *(LAS f32x4*)ex = f32x4{ob[0], ob[1], ob[2], ob[3]};
        *(LAS f32x4*)(ex + 1024) = f32x4{ob[4], ob[5], ob[6], ob[7]};
        *(LAS float*)((LAS char*)lds + A_LSUM + w * 256 + lane * 4) = ob[8];
        if (att == 0) {
            float big = fmaxf(oa[8], ob[8]) * 2.7e8f;
#pragma unroll
            for (int i = 0; i < 8; ++i) big = fmaxf(fmaxf(big, __builtin_fabsf(oa[i])), __builtin_fabsf(ob[i]));
            const bool bad = !__all(fminf(oa[8], ob[8]) > 8.1e-28f && big < 1.7e38f);
            if (lane == 0) ((LAS unsigned*)((LAS char*)lds + A_VOTE))[w] = bad;
        }
        wgbar();
        const f32x4 t0 = *(LAS const f32x4*)exr, t1 = *(LAS const f32x4*)(exr + 1024);
        const float t2 = *(LAS const float*)((LAS char*)lds + A_LSUM + (w ^ 4) * 256 + lane * 4);
        unsigned redo = 0;
        if (att == 0) {
            const u32x4 v0 = *(LAS const u32x4*)((LAS char*)lds + A_VOTE), v1 = *(LAS const u32x4*)((LAS char*)lds + A_VOTE + 16);
            redo = __builtin_amdgcn_readfirstlane(v0[0] | v0[1] | v0[2] | v0[3] | v1[0] | v1[1] | v1[2] | v1[3]);
        }
        const float rinv = __builtin_amdgcn_rcpf(oa[8] + t2);
        u32x4 ov;
        ov[0] = cvtpk((oa[0] + t0[0]) * rinv, (oa[1] + t0[1]) * rinv);
        ov[1] = cvtpk((oa[2] + t0[2]) * rinv, (oa[3] + t0[3]) * rinv);
        ov[2] = cvtpk((oa[4] + t1[0]) * rinv, (oa[5] + t1[1]) * rinv);
        ov[3] = cvtpk((oa[6] + t1[2]) * rinv, (oa[7] + t1[3]) * rinv);
        if (att == 0 ? !redo : att == 1 + kh) {
            st16(ws + WS_O + (size_t)((qt + kh) * 32 + head) * 1024, lane * 16, ov);
            drain();
            st_flag(ws + WS_FO + ((qt + kh) * 32 + head) * 128, tag);
        }
        asm volatile("" : "+s"(redo));
        return redo;
    };
    ST(1);
    attn_pass(R, qsrc + kh * 1024, qsrc + (1 - kh) * 1024, ksrc, ksrc + (WS_V - WS_K), ws + WS_FQ + 32768, tag, w);
    ST(2);
    if (finish(0)) {
#pragma clang loop unroll(disable)
        for (int att = 1; att < 3; ++att) {
            drain();
            wgbar();
            const int lane = lane_id();
            const char* qa = qsrc + (att - 1) * 1024;
            bf16x8 qf;
            asm volatile("global_load_dwordx4 %0, %1, %2 sc1\n\ts_waitcnt vmcnt(0)" : "=v"(qf) : "v"(lane * 16), "s"(qa) : "memory");
            float mx = -INFINITY;
#pragma clang loop unroll(disable)
            for (int t = 0; t < 64; ++t) {
                bf16x8 kf;
                asm volatile("global_load_dwordx4 %0, %1, %2 sc1\n\ts_waitcnt vmcnt(0)" : "=v"(kf) : "v"(lane * 16), "s"(ksrc + t * 1024) : "memory");
                const f32x16 s = MFMA32(kf, qf, f32x16{});
#pragma unroll
                for (int i = 0; i < 16; ++i) mx = fmaxf(mx, s[i]);
            }
            mx = -swap32_max(mx);
            attn_prep(R, a, w);
#pragma unroll
            for (int i = 0; i < 16; ++i) R.nm[i] = mx;
            attn_pass_shift(R, qa, qa, ksrc, ksrc + (WS_V - WS_K), w);
            finish(att);
        }
    }
    ST(3); ST_OUT(0);
}

#define BPR "s_setprio 0\n\t"
#define BG1 ""
#define BG2 "s_sleep 1\n\t"
#define BG3 "s_sleep 1\n\t"
#define SISSUE                                                             \
    "s_mov_b32 m0, s44\n\t"                                                 \
    "s_add_u32 s45, %[ow], %[wst]\n\t"                                      \
    "buffer_load_dwordx4 %[g0], %[rw], %[ow] offen lds\n\t"                 \
    "buffer_load_dwordx4 %[g1], %[rw], %[ow] offen offset:1024 lds\n\t"     \
    "s_add_u32 m0, s44, 8192\n\t"                                           \
    "s_add_u32 s46, %[od], 4096\n\t"                                        \
    "buffer_load_dwordx4 %[d0], %[rd], %[od] offen sc1 lds\n\t"             \
    "buffer_load_dwordx4 %[d0], %[rd], %[od] offen offset:1024 sc1 lds\n\t" \
    "s_add_u32 s44, s44, 0x4000\n\t"                                        \
    "s_and_b32 s44, s44, 0xffff\n\t"                                        \
    "s_cmp_eq_u32 %[od], %[de]\n\t"                                         \
    "s_cselect_b32 %[ow], 0, s45\n\t"                                       \
    "s_cselect_b32 %[od], 0, s46\n\t"
#define SISW                                                               \
    "s_mov_b32 m0, s44\n\t"                                                 \
    "s_add_u32 s45, %[ow], %[wst]\n\t"                                      \
    "buffer_load_dwordx4 %[g0], %[rw], %[ow] offen lds\n\t"                 \
    "buffer_load_dwordx4 %[g1], %[rw], %[ow] offen offset:1024 lds\n\t"     \
    "s_add_u32 s44, s44, 0x4000\n\t"                                        \
    "s_mov_b32 %[ow], s45\n\t"
#define SISD                                                               \
    "s_add_u32 m0, s44, 8192\n\t"                                           \
    "s_add_u32 s44, s44, 0x4000\n\t"                                        \
    "buffer_load_dwordx4 %[d0], %[rd], %[od] offen sc1 lds\n\t"             \
    "buffer_load_dwordx4 %[d0], %[rd], %[od] offen offset:1024 sc1 lds\n\t" \
    "s_add_u32 %[od], %[od], 4096\n\t"
struct SG { u32x4 rw, rd; unsigned wst, de, ow, od, a, b, c; };
DI void sgemm_prew(SG& G, int lw) {
    asm volatile("s_mov_b32 s44, %[lo]\n\t" SISW SISW SISW
                 : [ow] "+s"(G.ow) : [wst] "s"(G.wst), [rw] "s"(G.rw), [lo] "s"(lw * 2048), [g0] "v"(G.a), [g1] "v"(G.b) : "memory", "scc", "s44", "s45");
}
#define WISSUE                                                              \
    "s_and_b32 s45, s44, 0xc000\n\t"                                        \
    "s_add_u32 m0, s45, %[lo2]\n\t"                                         \
    "s_lshl_b32 s45, s45, 3\n\t"                                            \
    "buffer_load_dwordx4 %[o0], %[ro], s45 offen lds\n\t"                   \
    "buffer_load_dwordx4 %[o1], %[ro], s45 offen offset:1024 lds\n\t"       \
    "buffer_load_dwordx4 %[o2], %[ro], s45 offen offset:2048 lds\n\t"       \
    "buffer_load_dwordx4 %[o3], %[ro], s45 offen offset:3072 lds\n\t"       \
    "s_add_u32 s44, s44, 0x4000\n\t"                                        \
    "s_and_b32 s44, s44, 0xffff\n\t"
template <int WO>
DI void sgemm_load(SG& G, int n, int lw, u32x4 ro, unsigned o0) {
    asm volatile(
        "s_mov_b32 s44, %[lo]\n\t"
        ".if %c[wo]\n\t"
        "s_sub_u32 s47, %[n], 5\n\t"
        SISD SISD SISD
        "s_waitcnt vmcnt(4)\n\ts_barrier\n\t" SISSUE
        "s_waitcnt vmcnt(6)\n\ts_barrier\n\t" SISSUE
        ".else\n\t"
        "s_sub_u32 s47, %[n], 3\n\t"
        SISSUE SISSUE SISSUE
        ".endif\n\t"
        "LL_loop%=:\n\t"
        "s_waitcnt vmcnt(8)\n\t"
        "s_barrier\n\t"
        SISSUE
        "s_sub_u32 s47, s47, 1\n\t"
        "s_cmp_lg_u32 s47, 0\n\t"
        "s_cbranch_scc1 LL_loop%=\n\t"
        ".if %c[wo]\n\t"
        "s_waitcnt vmcnt(8)\n\ts_barrier\n\t" WISSUE
        "s_waitcnt vmcnt(8)\n\ts_barrier\n\t" WISSUE
        "s_waitcnt vmcnt(8)\n\ts_barrier\n\t" WISSUE
        "s_barrier\n\t" WISSUE
        ".else\n\t"
        "s_waitcnt vmcnt(8)\n\ts_barrier\n\t"
        "s_waitcnt vmcnt(4)\n\ts_barrier\n\t"
        "s_waitcnt vmcnt(0)\n\ts_barrier\n\t"
        "s_barrier\n\t"
        ".endif\n\t"
        : [ow] "+s"(G.ow), [od] "+s"(G.od)
        : [wst] "s"(G.wst), [rw] "s"(G.rw), [rd] "s"(G.rd), [lo] "s"(lw * 2048), [de] "s"(G.de), [n] "s"(n), [g0] "v"(G.a), [g1] "v"(G.b), [d0] "v"(G.c),
          [wo] "i"(WO), [ro] "s"(ro), [lo2] "s"(lw * 4096), [o0] "v"(o0), [o1] "v"(o0 + 7168), [o2] "v"(o0 + 14336), [o3] "v"(o0 + 21504)
        : "memory", "scc", "s44", "s45", "s46", "s47");
}
#define SRD3(B, SLOT, K, TD)                                                                         \
    "ds_read_b64_tr_b16 v[" #B ":" #B "+1], %[t] offset:" #SLOT "*16384+" #K "*2048\n\t"              \
    "ds_read_b64_tr_b16 v[" #B "+2:" #B "+3], %[t] offset:" #SLOT "*16384+" #K "*2048+" #TD "\n\t"    \
    "ds_read_b128 v[" #B "+4:" #B "+7], %[dl] offset:" #SLOT "*16384+" #K "*1024\n\t"
#define SSTAGE(SLOT, MM, TD, N0, N1, N2, N3, P0, P1, P2, P3)                          \
    "s_waitcnt lgkmcnt(0)\n\t"                                                        \
    "s_barrier\n\t"                                                                   \
    MM(P0) SRD3(N0, SLOT, 0, TD) SRD3(N1, SLOT, 1, TD) BG1                            \
    MM(P1) SRD3(N2, SLOT, 2, TD) SRD3(N3, SLOT, 3, TD) BG2                            \
    MM(P2) BG3 MM(P3)
#define SST_E(SLOT, MM, TD) SSTAGE(SLOT, MM, TD, 32, 40, 48, 56, 96, 104, 112, 120)
#define SST_O(SLOT, MM, TD) SSTAGE(SLOT, MM, TD, 96, 104, 112, 120, 32, 40, 48, 56)
#define SMM_NONE(P) ""
#define SMM_WA(P) MF("v[64:79]", "v[" #P ":" #P "+3]", "v[" #P "+4:" #P "+7]", "v[64:79]")
#define SMM_WB(P) MF("v[80:95]", "v[" #P "+4:" #P "+7]", "v[" #P ":" #P "+3]", "v[80:95]")
#define SGEMM_ASM(ACC, MM, TD, NLOOP, TAIL)                                                                       \
    asm volatile(                                                                                                 \
        "s_mov_b32 s44, " #NLOOP "\n\t" BPR                                                                       \
        "s_barrier\n\t"                                                                                           \
        SRD3(32, 0, 0, TD) SRD3(40, 0, 1, TD) SRD3(48, 0, 2, TD) SRD3(56, 0, 3, TD)                               \
        "LS_loop%=:\n\t"                                                                                          \
        SST_O(1, MM, TD) SST_E(2, MM, TD) SST_O(3, MM, TD) SST_E(0, MM, TD)                                       \
        "s_sub_u32 s44, s44, 1\n\t"                                                                               \
        "s_cmp_lg_u32 s44, 0\n\t"                                                                                 \
        "s_cbranch_scc1 LS_loop%=\n\t"                                                                            \
        TAIL                                                                                                      \
        "s_waitcnt lgkmcnt(0)\n\t"                                                                                \
        MM(96) BG3 MM(104) BG3 MM(112) BG3 MM(120)                                                                \
        "s_setprio 3\n\ts_nop 15\n\t"                                                                                            \
        : ACC(acc)                                                                                                \
        : [t] "v"(G.a), [dl] "v"(G.b)                                                                             \
        : "memory", "scc", "s44",                                                                                 \
          "v32", "v33", "v34", "v35", "v36", "v37", "v38", "v39", "v40", "v41", "v42", "v43", "v44", "v45", "v46", "v47", \
          "v48", "v49", "v50", "v51", "v52", "v53", "v54", "v55", "v56", "v57", "v58", "v59", "v60", "v61", "v62", "v63", \
          "v96", "v97", "v98", "v99", "v100", "v101", "v102", "v103", "v104", "v105", "v106", "v107", "v108", "v109", "v110", "v111", \
          "v112", "v113", "v114", "v115", "v116", "v117", "v118", "v119", "v120", "v121", "v122", "v123", "v124", "v125", "v126", "v127")

template <int MODE>
DI SG sgemm_setup(const char* wbase, unsigned wbytes, int wrow, const char* dbase, int dblk, int rot, int w) {
    const int lane = lane_id(), row = lane >> 3, lw = w & 3;
    const int q = (lane & 15) >> 2, h2 = lane >> 5;
    SG G;
    G.rw = srd(wbase, wbytes); G.rd = srd(dbase, 2 * dblk);
    G.wst = 64 * wrow; G.de = (MODE == 0 ? 31 : 21) * 4096; G.ow = rot * G.wst; G.od = rot * 4096;
    if (w >= 4) {
        G.a = (16 * lw + row) * wrow + (((lane & 7) ^ (((row >> 1) & 1) << 2)) << 4);
        G.b = G.a + 8 * wrow - 1024;
        G.c = (lw >> 1) * dblk + (lw & 1) * 2048 + lane * 16;
    } else {
        G.a = ((MODE == 0 ? 8 : 4) * h2 + q) * 128 + (((lw & 1) ^ ((q >> 1) & 1)) << 6) + ((lane >> 4) & 1) * 32 + (lane & 3) * 8;
        G.b = 8192 + (lw >> 1) * 4096 + lane * 16;
        G.c = 0;
    }
    asm volatile("" : "+v"(G.a), "+v"(G.b), "+v"(G.c));
    return G;
}
#define ACC0 "+{v[64:79]}"
#define ACC1 "+{v[80:95]}"
template <int MODE>
DI void sgemm_run(f32x16& acc, SG& G) {
    if (MODE == 0) SGEMM_ASM(ACC0, SMM_WA, 512, 7, SST_O(1, SMM_WA, 512) SST_E(2, SMM_WA, 512) SST_O(3, SMM_WA, 512));
    else SGEMM_ASM(ACC1, SMM_WB, 1024, 5, SST_O(1, SMM_WB, 1024));
}
DI f32x4 sub4(const f32x16& a, int g) { return f32x4{a[4 * g], a[4 * g + 1], a[4 * g + 2], a[4 * g + 3]}; }

DI unsigned gelu2(float a, float b) {
    constexpr float c1 = -2.f * 0.7978845608028654f * 1.4426950408889634f, c2 = c1 * 0.044715f;
    const f32x2 u = {a, b};
    const f32x2 z = u * (u * u * c2 + c1);
    const f32x2 d = f32x2{__builtin_amdgcn_exp2f(z[0]), __builtin_amdgcn_exp2f(z[1])} + 1.f;
    const f32x2 o = u * f32x2{__builtin_amdgcn_rcpf(d[0]), __builtin_amdgcn_rcpf(d[1])};
    return cvtpk(o[0], o[1]);
}

#define WRD(R0, R1, R2, R3, STEP)                                                    \
    "ds_read_b64_tr_b16 " R0 ", %[t0] offset:" #STEP "*2048\n\t"                      \
    "ds_read_b64_tr_b16 " R1 ", %[t0] offset:" #STEP "*2048+1024\n\t"                 \
    "ds_read_b64_tr_b16 " R2 ", %[t1] offset:" #STEP "*2048\n\t"                      \
    "ds_read_b64_tr_b16 " R3 ", %[t1] offset:" #STEP "*2048+1024\n\t"
#define WRD0(S) WRD("v[32:33]", "v[34:35]", "v[36:37]", "v[38:39]", S)
#define WRD1(S) WRD("v[40:41]", "v[42:43]", "v[44:45]", "v[46:47]", S)
#define WRD2(S) WRD("v[48:49]", "v[50:51]", "v[52:53]", "v[54:55]", S)
#define WRD3(S) WRD("v[56:57]", "v[58:59]", "v[60:61]", "v[62:63]", S)
#define WMM(WAIT, W0, W1, O) "s_waitcnt " WAIT "\n\t" MF("v[96:111]", W0, O, "v[96:111]") MF("v[112:127]", W1, O, "v[112:127]")
#define OLD(K, R, V, OFF)                                             \
    "5:\n\t"                                                           \
    "s_bitcmp1_b32 s40, " #K "\n\t"                                    \
    "s_cbranch_scc1 6f\n\t"                                            \
    "global_load_dwordx2 v[24:25], %[fo], %[f] sc1\n\t"                       \
    "s_sub_u32 s41, s41, 1\n\t"                                        \
    "s_cselect_b32 s42, -1, 0\n\t"                                     \
    "s_waitcnt vmcnt(0)\n\t"                                           \
    "v_cmp_eq_u64 vcc, %[tag], v[24:25]\n\t"                                \
    "s_or_b32 s40, vcc_lo, s42\n\t"                                    \
    "s_bitcmp1_b32 s40, " #K "\n\t"                                    \
    "s_cbranch_scc1 6f\n\t"                                            \
    "s_sleep 1\n\t"                                                    \
    "s_branch 5b\n\t"                                                  \
    "6:\n\t"                                                           \
    "global_load_dwordx4 " R ", " V ", %[o] offset:" #OFF " sc1\n\t"
DI void wo_gemm(f32x16& a0, f32x16& a1, f32x4& k0, f32x4& k1, float& k2, const char* osrc, const char* fsrc, u64 tag, int w) {
    const int lane = lane_id();
    const int q = (lane & 15) >> 2, h2 = lane >> 5;
    const unsigned tb = (w >> 1) * 16384 + (4 * h2 + q) * 128 + ((lane >> 4) & 1) * 32 + (lane & 3) * 8;
    const unsigned t0 = tb + ((q >> 1) & 1) * 64, t1 = tb + (((q >> 1) & 1) ^ 1) * 64;
    const unsigned d0 = lane * 16, d1 = d0 + 4096, fo = (lane & 7) * 128;
    asm volatile(
        WRD0(0) WRD1(1) WRD2(2) WRD3(3)
        "s_mov_b32 s40, 0\n\t"
        "s_mov_b32 s41, 0x100000\n\t"
        "s_setprio 0\n\t"
        OLD(0, "v[64:67]", "%[d0]", 0) OLD(1, "v[68:71]", "%[d0]", 1024) OLD(2, "v[72:75]", "%[d0]", 2048) OLD(3, "v[76:79]", "%[d0]", 3072)
        OLD(4, "v[80:83]", "%[d1]", 0) OLD(5, "v[84:87]", "%[d1]", 1024) OLD(6, "v[88:91]", "%[d1]", 2048) OLD(7, "v[92:95]", "%[d1]", 3072)
        "s_setprio 3\n\t"
        "s_waitcnt vmcnt(7) lgkmcnt(0)\n\t"
        MF("v[96:111]", "v[32:35]", "v[64:67]", "0") MF("v[112:127]", "v[36:39]", "v[64:67]", "0")
        WRD0(4)
        WMM("vmcnt(6)", "v[40:43]", "v[44:47]", "v[68:71]") WRD1(5)
        WMM("vmcnt(5)", "v[48:51]", "v[52:55]", "v[72:75]") WRD2(6)
        WMM("vmcnt(4)", "v[56:59]", "v[60:63]", "v[76:79]") WRD3(7)
        WMM("vmcnt(3) lgkmcnt(12)", "v[32:35]", "v[36:39]", "v[80:83]")
        WMM("vmcnt(2) lgkmcnt(0)", "v[40:43]", "v[44:47]", "v[84:87]")
        WMM("vmcnt(1)", "v[48:51]", "v[52:55]", "v[88:91]")
        "s_barrier\n\t"
        WMM("vmcnt(0)", "v[56:59]", "v[60:63]", "v[92:95]")
        "s_nop 15\n\t"
        : "={v[96:111]}"(a0), "={v[112:127]}"(a1),
          "+{v[12:15]}"(k0), "+{v[16:19]}"(k1), "+{v20}"(k2)
        : [o] "s"(osrc), [f] "s"(fsrc), [tag] "s"(tag), [d0] "v"(d0), [d1] "v"(d1), [t0] "v"(t0), [t1] "v"(t1), [fo] "v"(fo)
        : "memory", "scc", "vcc", "s40", "s41", "s42", "v24", "v25",
          "v32", "v33", "v34", "v35", "v36", "v37", "v38", "v39", "v40", "v41", "v42", "v43", "v44", "v45", "v46", "v47",
          "v48", "v49", "v50", "v51", "v52", "v53", "v54", "v55", "v56", "v57", "v58", "v59", "v60", "v61", "v62", "v63",
          "v64", "v65", "v66", "v67", "v68", "v69", "v70", "v71", "v72", "v73", "v74", "v75", "v76", "v77", "v78", "v79",
          "v80", "v81", "v82", "v83", "v84", "v85", "v86", "v87", "v88", "v89", "v90", "v91", "v92", "v93", "v94", "v95");
}

DI void mixer_role(const char* x, const char* wqkv, const char* gw, const char* wo, const char* w1, const char* b1, const char* w2, const char* b2,
                   char* ws, char* out, char* lds, int bi, int w) {
    const int j = bi & 7, i = bi >> 3;
    __builtin_amdgcn_s_setprio(LNP);
    ST_DECL; ST(0);
    const u64 ep = ld_epoch(ws);
    u64 tag;
    {
        const int lane = lane_id(), h2 = lane >> 5, tid = 64 * w + lane;
        const u32x4 xr = *(GAS const u32x4*)(x + (size_t)(8 * bi + w) * 1024 + lane * 16);
        const u32x4 gr = *(GAS const u32x4*)(gw + lane * 16);
        tag = next_tag(ep);
        float v[8], gg[8];
#pragma unroll
        for (int k = 0; k < 4; ++k) { v[2 * k] = bflo(xr[k]); v[2 * k + 1] = bfhi(xr[k]); gg[2 * k] = bflo(gr[k]); gg[2 * k + 1] = bfhi(gr[k]); }
        float s1 = 0.f, s3 = 0.f;
#pragma unroll
        for (int k = 0; k < 8; ++k) { s1 += v[k]; s3 += v[k] * gg[k]; }
        const float mu = wave_sum(s1) * (1.f / 512.f);
        s3 = wave_sum(s3);
        float s2 = 0.f;
#pragma unroll
        for (int k = 0; k < 8; ++k) { v[k] -= mu; s2 += v[k] * v[k]; }
        const float rs = __builtin_amdgcn_rsqf(wave_sum(s2) * (1.f / 512.f) + 1e-5f);
        u32x4 hv;
#pragma unroll
        for (int k = 0; k < 4; ++k) hv[k] = cvtpk(v[2 * k] * rs, v[2 * k + 1] * rs);
        *(LAS u32x4*)((LAS char*)lds + w * 1024 + lane * 16) = hv;
        const float g = __builtin_amdgcn_rcpf(1.f + __builtin_amdgcn_exp2f(-1.4426950408889634f * s3));
        st4(ws + WS_G + (8 * bi + w) * 4, uni(g));
        wgbar();
        LAS const unsigned short* lb = (LAS const unsigned short*)lds + tid;
#pragma unroll
        for (int k = 0; k < 4; ++k) hv[k] = (unsigned)lb[(2 * k) * 512] | ((unsigned)lb[(2 * k + 1) * 512] << 16);
        st16(ws + WS_H + (size_t)(2 * w * 128 + (bi >> 1)) * 1024 + (bi & 1) * 512, h2 * 131072 + (lane & 31) * 16, hv);
        drain();
        wgbar();
        if (w == 0) st_flag(ws + WS_FL + bi * 128, tag);
    }
    __builtin_amdgcn_s_setprio(3);
    const int wb = w & 1, db = (w >> 1) & 1, hf = w >> 2;
    const int L = (j >> 1) * 22 + i, ft = L >> 2, dt1 = 4 * (j & 1) + (L & 3);
    const int st = 8 * (j >> 1) + (i & 7), dt = 4 * (j & 1) + (i >> 3);
    SG G1, G2;
    unsigned ex, l16, og, wof;
    {
        const int lane = lane_id();
        og = (64 * st + 32 * wb + (lane & 31)) * 4;
        G1 = sgemm_setup<0>(w1 + ft * 128, 2048 * 2752 - ft * 128, 2752, ws + WS_H + (size_t)2 * dt1 * 131072, 131072, ROT1, w);
        G2 = sgemm_setup<1>(w2 + st * 128, 1376 * 4096 - st * 128, 4096, ws + WS_U + (size_t)2 * dt * 86 * 1024, 86 * 1024, ROT2, w);
        l16 = lane * 16;
        ex = 65536 + (w & 3) * 2048 + l16;
        wof = (32 * (w & 3) + (lane >> 3)) * 1024 + (((lane & 7) ^ (((lane >> 4) & 1) << 2)) << 4);
        asm volatile("" : "+v"(ex), "+v"(l16), "+v"(og), "+v"(wof));
    }
    {
        const int row = 16 + 8 * (j & 1) + (i & 7), col = 8 + 8 * (i >> 4) + 2 * (j >> 1) + ((i >> 3) & 1);
        if (w == 0) {
            *(LAS unsigned*)QCNT = 0u;
            wait_flags<2>(ws + WS_FQ + bi * 128, 0, tag);
        }
        wgbar();
        if (w >= 4) qkv2_loader(x, wqkv, row, col, w - 4);
        else qkv2_body(ws, row, col, bi, w, tag);
    }
    ST(6);
    if (PFA && i >= 22) {
        const int wv = ((i - 22) * 8 + j) * 8 + w;
        for (int k = 0; k < PFD; ++k) __builtin_amdgcn_s_sleep(64);
        warm(w2, 1376, wv, l16 * 4);
        warm(wo, 128, wv, l16 * 4);
    }
    if (i < 22) {
        if (w >= 4) wait_flags<8>(ws + WS_FL + (w - 4) * 8192, l16 * 8, tag);
        wgbar();
        ST(7);
        f32x4 v0, v1;
        if (w >= 4) {
            sgemm_load<0>(G1, 32, w - 4, G1.rw, 0);
            v0 = *(LAS const f32x4*)(uintptr_t)ex; v1 = *(LAS const f32x4*)(uintptr_t)(ex + 1024);
        } else {
            f32x16 acc1;
            const char* bsrc = b1 + (64 * ft + (ft < 21 ? 32 * wb : 0)) * 2 + (l16 >> 9) * 8;
#pragma unroll
            for (int k = 0; k < 4; ++k) {
                const u32x2 bv = *(GAS const u32x2*)(bsrc + 16 * k);
                acc1[4 * k] = bflo(bv[0]); acc1[4 * k + 1] = bfhi(bv[0]); acc1[4 * k + 2] = bflo(bv[1]); acc1[4 * k + 3] = bfhi(bv[1]);
            }
            sgemm_run<0>(acc1, G1);
            *(LAS f32x4*)(uintptr_t)ex = sub4(acc1, 2); *(LAS f32x4*)(uintptr_t)(ex + 1024) = sub4(acc1, 3);
            wgbar();
            v0 = sub4(acc1, 0); v1 = sub4(acc1, 1);
        }
        ST(1);
        const u32x4 o = {gelu2(v0[0], v0[1]), gelu2(v0[2], v0[3]), gelu2(v1[0], v1[1]), gelu2(v1[2], v1[3])};
        if (ft < 21 || wb == 0) st16(ws + WS_U + (size_t)((2 * dt1 + db) * 86 + 4 * ft + 2 * wb + hf) * 1024, l16, o);
        drain();
        wgbar();
        if (w == 0) st_flag(ws + WS_FU + (dt1 * 32 + ft) * 128, tag);
    }
    ST(2);
    if (w >= 4) sgemm_prew(G2, w - 4);
    if (bi == 255 && w == 1) {
        wait_flags256(ws + WS_FQ, l16 * 8, tag);
        wait_flags256(ws + WS_FL, l16 * 8, tag);
        st_flag(ws + WS_EPOCH, tag);
    }
    if (w == 0) wait_flags<8>(ws + WS_FU + dt * 4096, (l16 < 22 * 16 ? l16 : 0) * 8, tag);
    wgbar();
    ST(3);
    f32x4 bp0, bp1;
    float g;
    {
        asm volatile("global_load_dword %0, %1, %2 sc1" : "=v"(g) : "v"(og), "s"(ws + WS_G) : "memory");
        if (w >= 4) {
            sgemm_load<1>(G2, 22, w - 4, srd(wo + dt * 128, 524288 - dt * 128), wof);
            bp0 = *(LAS const f32x4*)(uintptr_t)ex; bp1 = *(LAS const f32x4*)(uintptr_t)(ex + 1024);
        } else {
            const float bb = bflo(*(GAS const unsigned short*)(b2 + (og >> 1)));
            f32x16 acc2 = {bb, bb, bb, bb, bb, bb, bb, bb, bb, bb, bb, bb, bb, bb, bb, bb};
            sgemm_run<1>(acc2, G2);
            *(LAS f32x4*)(uintptr_t)ex = sub4(acc2, 2); *(LAS f32x4*)(uintptr_t)(ex + 1024) = sub4(acc2, 3);
            wgbar();
            bp0 = sub4(acc2, 0); bp1 = sub4(acc2, 1);
            drain();
        }
        asm volatile("" : "+v"(g));
        ST(4);
        bp0 *= 1.f - g;
        bp1 *= 1.f - g;
        asm volatile("" : "+v"(bp0), "+v"(bp1));
    }
    drain();
    wgbar();
    ST(5);
    f32x16 a0, a1;
    wo_gemm(a0, a1, bp0, bp1, g, ws + WS_O + (size_t)((2 * st + wb) * 32 + 8 * (w >> 1)) * 1024,
            ws + WS_FO + ((2 * st + wb) * 32 + 8 * (w >> 1)) * 128, tag, w);
    {
        const int lane = lane_id(), kq = 2 * db + hf;
        LAS char* ex = (LAS char*)lds + w * 8192 + lane * 16;
#pragma unroll
        for (int k = 0; k < 4; ++k) { *(LAS f32x4*)(ex + k * 1024) = sub4(a0, k); *(LAS f32x4*)(ex + 4096 + k * 1024) = sub4(a1, k); }
        wgbar();
        LAS const char* exr = (LAS const char*)lds + wb * 8192 + kq * 2048 + lane * 16;
        f32x4 s0 = *(LAS const f32x4*)exr, s1 = *(LAS const f32x4*)(exr + 1024);
#pragma unroll
        for (int k = 1; k < 4; ++k) { s0 += *(LAS const f32x4*)(exr + k * 16384); s1 += *(LAS const f32x4*)(exr + k * 16384 + 1024); }
        s0 = s0 * g + bp0;
        s1 = s1 * g + bp1;
        const auto r0 = __builtin_amdgcn_permlane32_swap(cvtpk(s0[0], s0[1]), cvtpk(s1[0], s1[1]), false, false);
        const auto r1 = __builtin_amdgcn_permlane32_swap(cvtpk(s0[2], s0[3]), cvtpk(s1[2], s1[3]), false, false);
        *(GAS u32x4*)(out + (size_t)(64 * st + 32 * wb) * 1024 + (64 * dt + 16 * kq) * 2 + (lane & 31) * 1024 + (lane >> 5) * 16) = u32x4{r0[0], r1[0], r0[1], r1[1]};
    }
    ST_OUT(0);
}

constexpr int MK_LDS = 81920;
__global__ __launch_bounds__(512, 4) void mk(const char* x, const char* wqkv, const char* wo, const char* w1, const char* b1,
                                             const char* w2, const char* b2, const char* gw, char* ws, char* out) {
    extern __shared__ __attribute__((aligned(16))) char lds[];
    const int w = __builtin_amdgcn_readfirstlane(threadIdx.x >> 6);
    if (blockIdx.x >= 256 && w == 7) {
        asm volatile("s_getpc_b64 s[40:41]\n\t"
                     "Lmk_hot_begin:\n\t"
                     "s_cmp_lt_u32 %[o], Lmk_hot_end-Lmk_hot_begin-4096\n\t"
                     "s_cbranch_scc0 1f\n\t"
                     "s_add_u32 s40, s40, %[o]\n\t"
                     "s_addc_u32 s41, s41, 0\n\t"
                     "s_and_b32 s40, s40, -4\n\t"
                     "s_mov_b32 m0, 0x13e00\n\t"
                     "s_nop 0\n\t"
                     "global_load_lds_dword %[v], s[40:41]\n\t"
                     "1:"
                     :: [o] "s"(((blockIdx.x - 256) >> 3) * 4096), [v] "v"(lane_id() * 64) : "memory", "scc", "s40", "s41");
    }
    if (blockIdx.x < 256) attn_role(x, wqkv, ws, lds, blockIdx.x, w);
    else mixer_role(x, wqkv, gw, wo, w1, b1, w2, b2, ws, out, lds, blockIdx.x - 256, w);
}

extern "C" void kernel_launch(void* const* d_in, const int* in_sizes, int n_in, void* d_out, int out_size, void* d_ws, size_t ws_size,
                              hipStream_t stream) {
    (void)hipFuncSetAttribute((const void*)mk, hipFuncAttributeMaxDynamicSharedMemorySize, MK_LDS);
    mk<<<512, 512, MK_LDS, stream>>>((const char*)d_in[0], (const char*)d_in[1], (const char*)d_in[2], (const char*)d_in[4],
                                     (const char*)d_in[5], (const char*)d_in[6], (const char*)d_in[7], (const char*)d_in[8],
                                     (char*)d_ws, (char*)d_out);
}
```

```cpp
#include <hip/hip_runtime.h>
#include <hip/hip_bf16.h>
#include <stdint.h>

typedef __attribute__((ext_vector_type(8))) short bf16x8;
typedef __attribute__((ext_vector_type(4))) short s16x4;
typedef __attribute__((ext_vector_type(16))) float f32x16;
typedef __attribute__((ext_vector_type(4))) float f32x4;
typedef __attribute__((ext_vector_type(2))) float f32x2;
typedef __attribute__((ext_vector_type(4))) unsigned u32x4;
typedef __attribute__((ext_vector_type(2))) unsigned u32x2;
typedef unsigned long long u64;
#define LAS __attribute__((address_space(3)))
#define GAS __attribute__((address_space(1)))
#define DI __device__ __forceinline__
#define MFMA32(a, b, c) __builtin_amdgcn_mfma_f32_32x32x16_bf16((a), (b), (c), 0, 0, 0)

#define ROT1 ((4 * dt1 + 10 * ft) & 31)
#define ROT2 (((st + dt) & 1) * 11)
#define LNP 0
#define PFA 3
#define PFD 2
#define PSL 2
#define STAG 1
#define CODEPF 1
#define PF1 0
#define QROT 6
#define QROTM 2
constexpr int S_ = 2048, D_ = 512, F_ = 1376;
constexpr size_t WS_EPOCH = 0;
constexpr size_t WS_G = 4096;
constexpr size_t WS_FL = 98304;
constexpr size_t WS_FQ = 458752;
constexpr size_t WS_FU = 65536;
constexpr size_t WS_FO = 131072;
constexpr size_t WS_Q = 1u << 20;
constexpr size_t WS_K = WS_Q + (2u << 20);
constexpr size_t WS_V = WS_K + (2u << 20);
constexpr size_t WS_H = WS_V + (2u << 20);
constexpr size_t WS_U = WS_H + (2u << 20);
constexpr size_t WS_O = WS_U + (2u << 20);

#define ST_DECL
#define ST_ARG
#define ST_PASS
#define ST(i)
#define ST_OUT(k)
DI unsigned cvtpk(float lo, float hi) {
    typedef float f32x2_t __attribute__((ext_vector_type(2)));
    typedef __bf16 bf16x2_t __attribute__((ext_vector_type(2)));
    return __builtin_bit_cast(unsigned, __builtin_convertvector(f32x2_t{lo, hi}, bf16x2_t));
}
DI float bflo(unsigned u) { return __uint_as_float(u << 16); }
DI float bfhi(unsigned u) { return __uint_as_float(u & 0xffff0000u); }
DI s16x4 trread(LAS const char* p) {
    typedef short v4i16_t __attribute__((ext_vector_type(4)));
    return __builtin_bit_cast(s16x4, __builtin_amdgcn_ds_read_tr16_b64_v4i16((LAS v4i16_t*)p));
}
template <int CTRL> DI float dpp(float v) {
    return __int_as_float(__builtin_amdgcn_update_dpp(0, __float_as_int(v), CTRL, 0xf, 0xf, true));
}
DI float rl(float v, int l) { return __int_as_float(__builtin_amdgcn_readlane(__float_as_int(v), l)); }
DI float wave_sum(float v) {
    v += dpp<0xB1>(v); v += dpp<0x4E>(v); v += dpp<0x141>(v); v += dpp<0x140>(v);
    return (rl(v, 0) + rl(v, 16)) + (rl(v, 32) + rl(v, 48));
}
DI float swap32_max(float v) {
    auto rr = __builtin_amdgcn_permlane32_swap(__float_as_uint(v), __float_as_uint(v), false, false);
    return fmaxf(__uint_as_float(rr[0]), __uint_as_float(rr[1]));
}

DI int lane_id() {
    int l;
    asm volatile("v_mbcnt_lo_u32_b32 %0, -1, 0\n\tv_mbcnt_hi_u32_b32 %0, -1, %0" : "=v"(l));
    __builtin_assume(l >= 0 && l < 64);
    return l;
}
DI void st16(const char* sbase, unsigned voff, u32x4 v) {
    asm volatile("global_store_dwordx4 %0, %1, %2 sc1\n\ts_nop 1" ::"v"(voff), "v"(v), "s"(sbase) : "memory");
}
DI void st4(const char* p, unsigned v) {
    unsigned t0, t1;
    asm volatile("s_nop 1\n\tv_mov_b32 %0, %2\n\tv_mov_b32 %1, 0\n\ts_mov_b64 exec, 1\n\tglobal_store_dword %1, %0, %3 sc1\n\ts_mov_b64 exec, -1"
                 : "=&v"(t0), "=&v"(t1) : "s"(v), "s"(p) : "memory");
}
DI void st_flag(const char* p, u64 v) {
    u64 t0;
    unsigned t1;
    asm volatile("s_nop 1\n\tv_mov_b64 %0, %2\n\tv_mov_b32 %1, 0\n\ts_mov_b64 exec, 1\n\tglobal_store_dwordx2 %1, %0, %3 sc1\n\ts_mov_b64 exec, -1"
                 : "=&v"(t0), "=&v"(t1) : "s"(v), "s"(p) : "memory");
}
DI u64 uni64(u64 v) {
    return (u64)(unsigned)__builtin_amdgcn_readfirstlane((unsigned)v) | ((u64)(unsigned)__builtin_amdgcn_readfirstlane((unsigned)(v >> 32)) << 32);
}
DI u64 ld_epoch(const char* ws) { return __hip_atomic_load((GAS const u64*)(ws + WS_EPOCH), __ATOMIC_RELAXED, __HIP_MEMORY_SCOPE_AGENT); }
DI u64 next_tag(u64 e) { return uni64(e) * 6364136223846793005ull + 1442695040888963407ull; }
DI unsigned uni(float v) { return __builtin_amdgcn_readfirstlane(__float_as_uint(v)); }
DI void warm(const char* base, int nchunks, int first, unsigned l64) {
#pragma clang loop unroll(disable)
    for (int n = first; n < nchunks; n += 640) {
        const char* p = base + (size_t)((n & 1) * (nchunks >> 1) + (n >> 1)) * 4096;
        asm volatile("s_mov_b32 m0, 0x13e00\n\ts_nop 0\n\tglobal_load_lds_dword %0, %1" ::"v"(l64), "s"(p) : "memory");
    }
}
DI void drain() { asm volatile("s_waitcnt vmcnt(0)" ::: "memory"); }
DI void wgbar() { asm volatile("s_waitcnt lgkmcnt(0)\n\ts_barrier" ::: "memory"); }
template <int SLEEP>
DI void wait_flags(const char* sbase, unsigned voff, u64 tag) {
#pragma clang loop unroll(disable)
    for (unsigned spin = 0; spin < (1u << 20); ++spin) {
        u64 f;
        asm volatile("global_load_dwordx2 %0, %1, %2 sc1\n\ts_waitcnt vmcnt(0)" : "=v"(f) : "v"(voff), "s"(sbase) : "memory");
        if (__all(f == tag)) break;
        __builtin_amdgcn_s_sleep(SLEEP);
    }
}

DI void wait_flags256(const char* sbase, unsigned voff, u64 tag) {
#pragma clang loop unroll(disable)
    for (unsigned spin = 0; spin < (1u << 20); ++spin) {
        u64 f0, f1, f2, f3;
        asm volatile("global_load_dwordx2 %0, %4, %5 sc1\n\tglobal_load_dwordx2 %1, %4, %6 sc1\n\tglobal_load_dwordx2 %2, %4, %7 sc1\n\t"
                     "global_load_dwordx2 %3, %4, %8 sc1\n\ts_waitcnt vmcnt(0)"
                     : "=&v"(f0), "=&v"(f1), "=&v"(f2), "=&v"(f3)
                     : "v"(voff), "s"(sbase), "s"(sbase + 8192), "s"(sbase + 16384), "s"(sbase + 24576) : "memory");
        if (__all(f0 == tag && f1 == tag && f2 == tag && f3 == tag)) break;
        __builtin_amdgcn_s_sleep(8);
    }
}

constexpr int QCNT = 73728;
#define MF(D, A, B, C) "v_mfma_f32_32x32x16_bf16 " D ", " A ", " B ", " C "\n\t"
DI u32x4 srd(const char* base, unsigned bytes) {
    return u32x4{(unsigned)(uintptr_t)base, (unsigned)((uintptr_t)base >> 32) & 0xffffu, bytes, 0x00020000u};
}
DI void qkv_store(char* ws, const f32x16& acc, int type, int head, int tile, int lane) {
    const float sc = (type == 0) ? 0.25f * 1.4426950408889634f : 1.f;
    const char* dst = ws + WS_Q + (size_t)type * (2u << 20) + (size_t)(head * 64 + tile) * 1024;
    const unsigned vo = type < 2 ? lane * 16 : ((lane >> 4) & 1) * 65536 + (lane >> 5) * 256 + (lane & 15) * 16;
#pragma unroll
    for (int hf = 0; hf < 2; ++hf) {
        u32x4 o;
#pragma unroll
        for (int i = 0; i < 4; ++i) o[i] = cvtpk(acc[8 * hf + 2 * i] * sc, acc[8 * hf + 2 * i + 1] * sc);
        st16(dst + hf * (type < 2 ? 65536 : 512), vo, o);
    }
}
DI void publish4(const char* flag, u64 tag, int lane) {
    drain();
    unsigned arr = 0;
    if (lane == 0) arr = __hip_atomic_fetch_add((LAS unsigned*)QCNT, 1u, __ATOMIC_RELAXED, __HIP_MEMORY_SCOPE_WORKGROUP);
    if (__builtin_amdgcn_readfirstlane(arr) == 3) st_flag(flag, tag);
}
DI void qkv_loader(const char* x, const char* wqkv, int row, int col, int lw) {
    const int lane = lane_id(), xr = 16 * lw + (lane >> 3), wr = 16 * lw + (lane >> 4);
    const unsigned x0 = xr * 1024 + (((lane & 7) ^ ((xr >> 1) & 7)) << 4), x1 = (xr + 8) * 1024 + (((lane & 7) ^ (((xr + 8) >> 1) & 7)) << 4) - 1024;
    const unsigned w0 = wr * 3072 + (((lane & 15) ^ ((wr & 3) << 2)) << 4);
    const u32x4 rx = srd(x + (size_t)row * 65536, 65536), rw = srd(wqkv + col * 256, 512 * 3072 - col * 256);
    const int rot = (row + col) * QROTM & QROT;
    auto issue = [&](int s, int slot) {
        const int kc = (s + rot) & 7;
        asm volatile("s_mov_b32 m0, %[m]\n\ts_nop 0\n\t"
                     "buffer_load_dwordx4 %[x0], %[rx], %[ox] offen lds\n\t"
                     "buffer_load_dwordx4 %[x1], %[rx], %[ox] offen offset:1024 lds\n\t"
                     "s_mov_b32 m0, %[m2]\n\ts_nop 0\n\t"
                     "buffer_load_dwordx4 %[w0], %[rw], %[ow] offen lds\n\t"
                     "buffer_load_dwordx4 %[w1], %[rw], %[ow] offen offset:1024 lds\n\t"
                     "buffer_load_dwordx4 %[w2], %[rw], %[ow] offen offset:2048 lds\n\t"
                     "buffer_load_dwordx4 %[w3], %[rw], %[ow] offen offset:3072 lds"
                     :: [m] "s"(slot * 24576 + lw * 2048), [m2] "s"(slot * 24576 + 8192 + lw * 4096), [ow] "s"(kc * 196608), [ox] "s"(kc * 128),
                        [rw] "s"(rw), [rx] "s"(rx), [x0] "v"(x0), [x1] "v"(x1),
                        [w0] "v"(w0), [w1] "v"(w0 + 11264), [w2] "v"(w0 + 22528), [w3] "v"(w0 + 33792)
                     : "memory");
    };
    issue(0, 0);
    __builtin_amdgcn_s_setprio(2);
    issue(1, 1);
    __builtin_amdgcn_s_setprio(1);
    issue(2, 2);
    __builtin_amdgcn_s_setprio(3);
    asm volatile("s_waitcnt vmcnt(12)\n\ts_barrier" ::: "memory");
    int slot = 0;
#pragma clang loop unroll(disable)
    for (int s = 3; s < 8; ++s) {
        asm volatile("s_waitcnt vmcnt(6)\n\ts_barrier" ::: "memory");
        issue(s, slot);
        slot = slot == 2 ? 0 : slot + 1;
    }
    asm volatile("s_waitcnt vmcnt(6)\n\ts_barrier\n\ts_waitcnt vmcnt(0)\n\ts_barrier" ::: "memory");
}
template <bool T>
DI void qkv_body(char* ws, char* lds, int row, int col, int a, int w, u64 tag ST_ARG) {
    const int lane = lane_id(), r = lane & 31, h2 = lane >> 5, q = (lane & 15) >> 2;
    f32x16 acc[2] = {};
    const int xa0 = (32 * (w & 1) + r) * 128 + ((h2 ^ ((r >> 1) & 7)) << 4);
    const int wa0 = 8192 + (8 * h2 + q) * 256 + (((2 * (w >> 1)) ^ q) << 6) + ((lane >> 4) & 1) * 32 + (lane & 3) * 8;
    bf16x8 xf[4], wf[2][2];
    auto rdw = [&](LAS const char* sl, int st, int i) {
        LAS const char* pw = sl + (wa0 ^ (i << 6)) + st * 4096;
        const s16x4 lo = trread(pw), hi = trread(pw + 1024);
        wf[st & 1][i] = __builtin_shufflevector(lo, hi, 0, 1, 2, 3, 4, 5, 6, 7);
    };
    auto mm1 = [&](int st, int i) {
        acc[i] = T ? MFMA32(wf[st & 1][i], xf[st], acc[i]) : MFMA32(xf[st], wf[st & 1][i], acc[i]);
        __builtin_amdgcn_sched_barrier(0);
    };
    auto step = [&](int st, LAS const char* sl, bool bar) {
        const int sn = (st + 2) & 3;
        mm1(st, 0);
        if (bar) wgbar();
        xf[sn] = *(LAS const bf16x8*)(sl + (xa0 ^ (sn << 5))); rdw(sl, sn, 0); __builtin_amdgcn_sched_barrier(0);
        mm1(st, 1);
        rdw(sl, sn, 1); __builtin_amdgcn_sched_barrier(0);
    };
    if (w == 0) *(LAS u32x4*)QCNT = u32x4{0u, 0u, (unsigned)tag, (unsigned)(tag >> 32)};
    wgbar();
    LAS const char* sl = (LAS const char*)lds;
#pragma unroll
    for (int st = 0; st < 2; ++st) { xf[st] = *(LAS const bf16x8*)(sl + (xa0 ^ (st << 5))); rdw(sl, st, 0); rdw(sl, st, 1); }
    __builtin_amdgcn_sched_barrier(0);
#pragma clang loop unroll(disable)
    for (int s = 0; s < 7; ++s) {
        step(0, sl, false); step(1, sl, false);
        sl = (sl == (LAS const char*)lds + 49152) ? (LAS const char*)lds : sl + 24576;
        step(2, sl, true); step(3, sl, false);
    }
    step(0, sl, false); step(1, sl, false);
    mm1(2, 0); mm1(2, 1); mm1(3, 0); mm1(3, 1);
    ST(4);
#pragma unroll
    for (int i = 0; i < 2; ++i) qkv_store(ws, acc[i], col >> 2, 8 * (col & 3) + 4 * (w >> 1) + 2 * i, 2 * row + (w & 1), lane);
    publish4(ws + WS_FQ + a * 128, tag, lane);
    ST(7);
}
DI void qkv2_loader(const char* x, const char* wqkv, int rowt, int col, int lw) {
    const int lane = lane_id(), row = 16 * lw + (lane >> 3);
    const unsigned w0 = row * 3072 + (((lane & 7) ^ (((row >> 1) & 1) << 2)) << 4), w1 = w0 + 8 * 3072 - 1024;
    const unsigned x0 = row * 1024 + (((lane & 7) ^ ((row >> 1) & 7)) << 4), x1 = (row + 8) * 1024 + (((lane & 7) ^ (((row + 8) >> 1) & 7)) << 4) - 1024;
    const u32x4 rx = srd(x + (size_t)rowt * 65536, 65536), rw = srd(wqkv + col * 128, 512 * 3072 - col * 128);
    auto issue = [&](int s) {
        asm volatile("s_mov_b32 m0, %[m]\n\ts_nop 0\n\t"
                     "buffer_load_dwordx4 %[w0], %[rw], %[ow] offen lds\n\t"
                     "buffer_load_dwordx4 %[w1], %[rw], %[ow] offen offset:1024 lds\n\t"
                     "s_add_u32 m0, %[m], 8192\n\ts_nop 0\n\t"
                     "buffer_load_dwordx4 %[x0], %[rx], %[ox] offen lds\n\t"
                     "buffer_load_dwordx4 %[x1], %[rx], %[ox] offen offset:1024 lds"
                     :: [m] "s"((s & 3) * 16384 + lw * 2048), [ow] "s"(s * 196608), [ox] "s"(s * 128), [rw] "s"(rw), [rx] "s"(rx),
                        [w0] "v"(w0), [w1] "v"(w1), [x0] "v"(x0), [x1] "v"(x1)
                     : "memory", "scc");
    };
    issue(0); issue(1); issue(2);
#pragma clang loop unroll(disable)
    for (int s = 3; s < 8; ++s) {
        asm volatile("s_waitcnt vmcnt(8)\n\ts_barrier" ::: "memory");
        issue(s);
    }
    asm volatile("s_waitcnt vmcnt(8)\n\ts_barrier\n\ts_waitcnt vmcnt(4)\n\ts_barrier\n\ts_waitcnt vmcnt(0)\n\ts_barrier" ::: "memory");
}
#define QRD3(B, SLOT, K)                                                                             \
    "ds_read_b64_tr_b16 v[" #B ":" #B "+1], %[t] offset:" #SLOT "*16384+" #K "*2048\n\t"              \
    "ds_read_b64_tr_b16 v[" #B "+2:" #B "+3], %[t] offset:" #SLOT "*16384+" #K "*2048+512\n\t"        \
    "ds_read_b128 v[" #B "+4:" #B "+7], %[x" #K "] offset:" #SLOT "*16384\n\t"
#define QSTAGE(SLOT, MM, N0, N1, N2, N3, P0, P1, P2, P3)                              \
    "s_waitcnt lgkmcnt(0)\n\t"                                                        \
    "s_barrier\n\t"                                                                   \
    MM(P0) QRD3(N0, SLOT, 0) MM(P1) QRD3(N1, SLOT, 1) MM(P2) QRD3(N2, SLOT, 2) MM(P3) QRD3(N3, SLOT, 3)
#define QST_E(SLOT, MM) QSTAGE(SLOT, MM, 32, 40, 48, 56, 96, 104, 112, 120)
#define QST_O(SLOT, MM) QSTAGE(SLOT, MM, 96, 104, 112, 120, 32, 40, 48, 56)
#define QMT(P) MF("v[16:31]", "v[" #P ":" #P "+3]", "v[" #P "+4:" #P "+7]", "v[16:31]")
#define QMV(P) MF("v[16:31]", "v[" #P "+4:" #P "+7]", "v[" #P ":" #P "+3]", "v[16:31]")
#define QTILE_ASM(MM)                                                                                             \
    asm volatile(                                                                                                 \
        "s_barrier\n\t"                                                                                           \
        QRD3(32, 0, 0) QRD3(40, 0, 1) QRD3(48, 0, 2) QRD3(56, 0, 3)                                               \
        QST_O(1, MM) QST_E(2, MM)                                                                                 \
        "s_mov_b32 s44, 0\n\t"                                                                                    \
        "LQ_loop%=:\n\t"                                                                                          \
        QST_O(3, MM)                                                                                              \
        "s_cmp_lg_u32 s44, 0\n\t"                                                                                 \
        "s_cbranch_scc1 LQ_end%=\n\t"                                                                             \
        QST_E(0, MM) QST_O(1, MM) QST_E(2, MM)                                                                    \
        "s_mov_b32 s44, 1\n\t"                                                                                    \
        "s_branch LQ_loop%=\n\t"                                                                                  \
        "LQ_end%=:\n\t"                                                                                           \
        "s_waitcnt lgkmcnt(0)\n\t"                                                                                \
        MM(96) MM(104) MM(112) MM(120)                                                                            \
        "s_nop 15\n\t"                                                                                            \
        : "+{v[16:31]}"(acc)                                                                                      \
        : [t] "v"(t), [x0] "v"(xa), [x1] "v"(xa ^ 32), [x2] "v"(xa ^ 64), [x3] "v"(xa ^ 96)                       \
        : "memory", "scc", "s44",                                                                                 \
          "v32", "v33", "v34", "v35", "v36", "v37", "v38", "v39", "v40", "v41", "v42", "v43", "v44", "v45", "v46", "v47", \
          "v48", "v49", "v50", "v51", "v52", "v53", "v54", "v55", "v56", "v57", "v58", "v59", "v60", "v61", "v62", "v63", \
          "v96", "v97", "v98", "v99", "v100", "v101", "v102", "v103", "v104", "v105", "v106", "v107", "v108", "v109", "v110", "v111", \
          "v112", "v113", "v114", "v115", "v116", "v117", "v118", "v119", "v120", "v121", "v122", "v123", "v124", "v125", "v126", "v127")
DI void qkv2_body(char* ws, int row, int col, int bi, int w, u64 tag) {
    const int lane = lane_id(), r = lane & 31, h2 = lane >> 5, q = (lane & 15) >> 2;
    const unsigned t = (8 * h2 + q) * 128 + (((w & 1) ^ ((q >> 1) & 1)) << 6) + ((lane >> 4) & 1) * 32 + (lane & 3) * 8;
    const unsigned xa = 8192 + (32 * (w >> 1) + r) * 128 + ((h2 ^ ((r >> 1) & 7)) << 4);
    f32x16 acc = {};
    if (col < 16) QTILE_ASM(QMT);
    else QTILE_ASM(QMV);
    qkv_store(ws, acc, col >> 3, 4 * (col & 7) + 2 * (w & 1), 2 * row + (w >> 1), lane);
    publish4(ws + WS_FQ + 32768 + bi * 128, tag, lane);
}

#define EXP(a) "v_exp_f32 v" #a ", v" #a "\n\t"
#define CVT(d, a, b) "v_cvt_pk_bf16_f32 v" #d ", v" #a ", v" #b "\n\t"
#define ASTEP(c0, c1, c2, c3, c4, c5, c6, c7, c8, c9, c10, c11, c12, c13, c14, c15, M1, M2, CL, M3)                  \
    EXP(c0) EXP(c1) M1 EXP(c2) EXP(c3) EXP(c4) EXP(c5) M2 CL                                                          \
    EXP(c6) EXP(c7) CVT(96, c0, c1) CVT(97, c2, c3) CVT(98, c4, c5) CVT(99, c6, c7) EXP(c8) EXP(c9) M3                \
    EXP(c10) EXP(c11) EXP(c12) EXP(c13) EXP(c14) EXP(c15)                                                             \
    CVT(100, c8, c9) CVT(101, c10, c11) CVT(102, c12, c13) CVT(103, c14, c15)
#define R_OA "v[64:79]"
#define R_OB "v[80:95]"
#define R_SA "v[32:47]"
#define R_SB "v[48:63]"
#define ATILE(R_NM, KC, KN, V0C, V1C, V0N, V1N, KOFF, VA, VOFF0, VOFF1, PRE)                                                    \
    ASTEP(32, 33, 34, 35, 36, 37, 38, 39, 40, 41, 42, 43, 44, 45, 46, 47,                                             \
          MF(R_OB, V1N, "v[100:103]", R_OB), MF(R_SB, KC, "v[8:11]", R_NM),                                           \
          PRE                                                                                                         \
          "ds_read_b128 " KC ", v12 offset:" KOFF "\n\t"                                                              \
          "ds_read_b128 " V0N ", " VA " offset:" #VOFF0 "\n\t"                                                        \
          "ds_read_b128 " V1N ", " VA " offset:" #VOFF1 "\n\t",                                                          \
          MF(R_OA, V0C, "v[96:99]", R_OA))                                                                            \
    ASTEP(48, 49, 50, 51, 52, 53, 54, 55, 56, 57, 58, 59, 60, 61, 62, 63,                                             \
          MF(R_OA, V1C, "v[100:103]", R_OA), MF(R_SA, KN, "v[4:7]", R_NM), "", MF(R_OB, V0C, "v[96:99]", R_OB))
#define ATILE_E(NM, KOFF, VA, VOFF0, VOFF1, PRE) ATILE(NM, "v[104:107]", "v[108:111]", "v[112:115]", "v[116:119]", "v[120:123]", "v[124:127]", KOFF, VA, VOFF0, VOFF1, PRE)
#define ATILE_O(NM, KOFF, VA, VOFF0, VOFF1, PRE) ATILE(NM, "v[108:111]", "v[104:107]", "v[120:123]", "v[124:127]", "v[112:115]", "v[116:119]", KOFF, VA, VOFF0, VOFF1, PRE)
#define MOV4(a, b, c, d, s) "v_mov_b32 v" #a ", " s "\n\tv_mov_b32 v" #b ", " s "\n\tv_mov_b32 v" #c ", " s "\n\tv_mov_b32 v" #d ", " s "\n\t"
#define WL "s_waitcnt lgkmcnt(0)\n\t"
#define AISSUE                                                   \
    "s_add_u32 m0, s44, %[lw]\n\t"                               \
    "s_add_u32 s44, s44, 0x4000\n\t"                             \
    "buffer_load_dwordx4 v14, %[rk], s43 offen sc1 lds\n\t"      \
    "s_add_u32 m0, m0, 0x2000\n\t"                               \
    "s_and_b32 s44, s44, 0xffff\n\t"                             \
    "buffer_load_dwordx4 v14, %[rv], s43 offen sc1 lds\n\t"      \
    "s_add_u32 s43, s43, 0x2000\n\t"
#define PR2 "s_setprio 2\n\t"
#define PR3 ""
#define PR0 "s_setprio 1\n\t"
#define PR1 "s_setprio 0\n\t"
#define PRI "s_setprio 3\n\t"
constexpr int A_ONES = 65536, A_VOTE = 69632 + 512, A_LSUM = 71680;
struct AttnRegs { f32x16 oa, ob, nm; u32x4 p1, v1; unsigned ka, va, va1, va2, va3, go, fo; };
DI unsigned qkv_dep(int a, bool early) {
    const int lane = lane_id(), r = lane & 15, v = (lane >> 4) & 1, R = 4 * (a >> 5) + (lane & 3);
    const int kv = (r >> 3) + 8 * ((early ? 8 + 8 * v : 16 * v + 8 * (a & 1)) + (r & 7));
    const int qq = ((R >> 3) & 1) + 8 * ((R >> 4) * 24 + (R & 7));
    return ((a & 6) + (lane < 32 || !early ? kv : qq)) * 128;
}
DI void attn_prep(AttnRegs& R, int a, int w) {
    const int lane = lane_id(), r = lane & 31, kh = w >> 2;
    R.ka = 4096 * kh + lane * 16;
    R.va = r < 16 ? 8192 + 4096 * kh + (lane >> 5) * 256 + r * 16 : A_ONES + (r == 16 ? 0 : r == 20 ? 64 : (r > 20 && r < 28) ? 80 : 16);
    R.go = w * 1024 + lane * 16;
#pragma unroll
    for (int i = 0; i < 16; ++i) { R.oa[i] = 0.f; R.ob[i] = 0.f; }
    R.p1 = u32x4{0u, 0u, 0u, 0u};
    R.v1 = u32x4{0u, 0u, 0u, 0u};
    asm volatile("" : "+{v[64:79]}"(R.oa), "+{v[80:95]}"(R.ob), "+{v[100:103]}"(R.p1), "+{v[124:127]}"(R.v1),
                      "+{v12}"(R.ka), "+{v13}"(R.va), "+{v14}"(R.go));
}
#define ATTN_ASM(NM, NMIN) \
    asm volatile( \
        "v_and_b32 v15, 0x3ff, v14\n\t" \
        "s_mov_b32 s43, 0\n\t" \
        "s_mov_b32 s44, 0\n\t" \
        AISSUE \
        "global_load_dwordx4 v[4:7], v15, %[qa] sc1\n\t" \
        "global_load_dwordx4 v[8:11], v15, %[qb] sc1\n\t" \
        AISSUE AISSUE \
        "s_mov_b32 s40, 0xffff\n\t" \
        "s_mov_b32 s41, 0xffff\n\t" \
        "s_mov_b32 s42, 8\n\t" \
        "s_waitcnt vmcnt(4)\n\t" \
        "s_barrier\n\t" \
        "ds_read_b128 v[104:107], v12\n\t" \
        "ds_read_b128 v[108:111], v12 offset:1024\n\t" \
        "ds_read_b128 v[112:115], v13\n\t" \
        "ds_read_b128 v[116:119], v13 offset:512\n\t" \
        PR2 \
        "s_waitcnt lgkmcnt(3)\n\t" \
        MF(R_SA, "v[104:107]", "v[4:7]", NM) \
        "s_nop 15\n\t" \
        "LA_loop%=:\n\t" \
        ATILE_E(NM, "2048", "v13", 1024, 1536, WL PR0) \
        ATILE_O(NM, "3072", "v13", 2048, 2560, WL PR1) \
        ATILE_E(NM, "0", "v13", 3072, 3584, \
                "s_waitcnt vmcnt(2) lgkmcnt(0)\n\t" \
                "s_barrier\n\t" \
                "s_setprio 3\n\t" \
                "v_add_u32 v12, 0x4000, v12\n\t" \
                AISSUE \
                "v_and_b32 v12, 0xffff, v12\n\t" \
                PR2) \
        ATILE_O(NM, "1024", "v13", 0, 512, \
                WL PR3 \
                "s_mov_b64 exec, s[40:41]\n\t" \
                "v_add_u32 v13, 0x4000, v13\n\t" \
                "s_sub_u32 s42, s42, 1\n\t" \
                "v_and_b32 v13, 0xffff, v13\n\t" \
                "s_mov_b64 exec, -1\n\t" \
                "s_cmp_lg_u32 s42, 0\n\t") \
        "s_cbranch_scc1 LA_loop%=\n\t" \
        "s_waitcnt lgkmcnt(0)\n\t" \
        "s_setprio 3\n\t" \
        MF(R_OB, "v[124:127]", "v[100:103]", R_OB) \
        "s_nop 15\n\t" \
        : "+{v[64:79]}"(R.oa), "+{v[80:95]}"(R.ob), "+{v[100:103]}"(R.p1), "+{v[124:127]}"(R.v1), "+{v12}"(R.ka), "+{v13}"(R.va) \
        : NMIN "{v14}"(R.go), [qa] "s"(qa), [qb] "s"(qb), [rk] "s"(rk), [rv] "s"(rv), [lw] "s"(w * 1024) \
        : "memory", "scc", "vcc", "s40", "s41", "s42", "s43", "s44", "s45", "v0", "v1", "v4", "v5", "v6", "v7", "v8", "v9", "v10", "v11", "v15", \
          "v32", "v33", "v34", "v35", "v36", "v37", "v38", "v39", "v40", "v41", "v42", "v43", "v44", "v45", "v46", "v47", \
          "v48", "v49", "v50", "v51", "v52", "v53", "v54", "v55", "v56", "v57", "v58", "v59", "v60", "v61", "v62", "v63", \
          "v96", "v97", "v98", "v99", "v104", "v105", "v106", "v107", "v108", "v109", "v110", "v111", \
          "v112", "v113", "v114", "v115", "v116", "v117", "v118", "v119", "v120", "v121", "v122", "v123")
DI void attn_pass_shift(AttnRegs& R, const char* qa, const char* qb, const char* ksrc, const char* vsrc, int w) {
    const u32x4 rk = srd(ksrc, 65536), rv = srd(vsrc, 65536);
#define NM_IN "{v[16:31]}"(R.nm),
    ATTN_ASM("v[16:31]", NM_IN);
}
#define FISS(S)                                                  \
    "s_add_u32 m0, %[lw], " #S "*16384\n\t"                      \
    "s_add_u32 s43, s43, 0x2000\n\t"                             \
    "buffer_load_dwordx4 v14, %[rk], s43 offen sc1 lds\n\t"      \
    "s_add_u32 m0, %[lw], " #S "*16384+8192\n\t"                 \
    "s_nop 0\n\t"                                                \
    "buffer_load_dwordx4 v14, %[rv], s43 offen sc1 lds\n\t"
#define FI2(I) "s_cbranch_vccz 7f\n\t" PRI I "7:\n\t"
#define FI3(I) "s_cbranch_vccnz 7f\n\t" PRI I PR2 "7:\n\t"
#define LA2 ""
#define LB2 PR2
#define LB3 ""
#define FCHUNK(S, N, VC, VN, BND, I2, I3) FCHUNKX(S, N, VC, VN, BND, I2, I3, "")
#define FCHUNKX(S, N, VC, VN, BND, I2, I3, I0)                        \
    ATILE_E("0", #S "*16384+2048", VC, 1024, 1536, WL I0 PR0)       \
    ATILE_O("0", #S "*16384+3072", VC, 2048, 2560, WL PR1)       \
    ATILE_E("0", #N "*16384", VC, 3072, 3584, BND LA2 I2 LB2)    \
    ATILE_O("0", #N "*16384+1024", VN, 0, 512, WL I3 LB3)
#define FLD "global_load_dwordx2 v[0:1], v2, %[ff] sc1\n\t"
#define VCCSET "s_mov_b32 vcc_lo, %[lo4]\n\ts_mov_b32 vcc_hi, 0\n\t"
#define FBAR(VM) "s_waitcnt vmcnt(" #VM ") lgkmcnt(0)\n\ts_barrier\n\t"
DI void attn_pass(AttnRegs& R, const char* qa, const char* qb, const char* ksrc, const char* vsrc, const char* late_flags, u64 tag, int w) {
    const u32x4 rk = srd(ksrc, 65536), rv = srd(vsrc, 65536);
    asm volatile(
        "v_and_b32 v15, 0x3ff, v14\n\t"
        "s_mov_b32 s43, -0x2000\n\t"
        FISS(0)
        "global_load_dwordx4 v[4:7], v15, %[qa] sc1\n\t"
        "global_load_dwordx4 v[8:11], v15, %[qb] sc1\n\t"
        FISS(1) FISS(2) VCCSET
        "s_waitcnt vmcnt(4)\n\t"
        "s_barrier\n\t"
        "ds_read_b128 v[104:107], v12\n\t"
        "ds_read_b128 v[108:111], v12 offset:1024\n\t"
        "ds_read_b128 v[112:115], v13\n\t"
        "ds_read_b128 v[116:119], v13 offset:512\n\t"
        PR2
        "s_waitcnt lgkmcnt(3)\n\t"
        MF(R_SA, "v[104:107]", "v[4:7]", "0")
        "s_nop 15\n\t"
        FCHUNK(0, 1, "v13", "v16", FBAR(2), FI2(FISS(3)), FI3(FISS(3)))
        FCHUNK(1, 2, "v16", "v17", FBAR(2), "", "")
        FCHUNKX(2, 3, "v17", "v18",
               "s_waitcnt vmcnt(0) lgkmcnt(0)\n\t"
               "s_mov_b32 s45, 0x100000\n\t"
               "LA_p%=:\n\t"
               "v_cmp_ne_u64 vcc, %[tag], v[0:1]\n\t"
               "s_sub_u32 s45, s45, 1\n\t"
               "s_cbranch_scc1 LA_f%=\n\t"
               "s_cbranch_vccz LA_f%=\n\t"
               "global_load_dwordx2 v[0:1], v2, %[ff] sc1\n\t"
               "s_waitcnt vmcnt(0)\n\t"
               "s_branch LA_p%=\n\t"
               "LA_f%=:\n\t"
               "s_barrier\n\t" VCCSET, FI2(FISS(0) FISS(1)), FI3(FISS(0) FISS(1)), PRI FLD)
        FCHUNK(3, 0, "v18", "v13", FBAR(2), FI2(FISS(2)), FI3(FISS(2)))
        FCHUNK(0, 1, "v13", "v16", FBAR(2), FI2(FISS(3)), FI3(FISS(3)))
        FCHUNK(1, 2, "v16", "v17", FBAR(2), "", "")
        FCHUNK(2, 3, "v17", "v18", FBAR(0), "", "")
        FCHUNK(3, 0, "v18", "v13", WL, "", "")
        "s_waitcnt lgkmcnt(0)\n\t"
        "s_setprio 3\n\t"
        MF(R_OB, "v[124:127]", "v[100:103]", R_OB)
        "s_nop 15\n\t"
        "Lmk_hot_end:\n\t"
        : "+{v[64:79]}"(R.oa), "+{v[80:95]}"(R.ob), "+{v[100:103]}"(R.p1), "+{v[124:127]}"(R.v1)
        : "{v12}"(R.ka), "{v13}"(R.va), "{v16}"(R.va1), "{v17}"(R.va2), "{v18}"(R.va3), "{v14}"(R.go), "{v2}"(R.fo),
          [qa] "s"(qa), [qb] "s"(qb), [rk] "s"(rk), [rv] "s"(rv), [lw] "s"(w * 1024), [tag] "s"(tag), [ff] "s"(late_flags), [lo4] "s"(1 - (w >> 2))
        : "memory", "scc", "vcc", "s43", "s45", "v0", "v1", "v4", "v5", "v6", "v7", "v8", "v9", "v10", "v11", "v15",
          "v32", "v33", "v34", "v35", "v36", "v37", "v38", "v39", "v40", "v41", "v42", "v43", "v44", "v45", "v46", "v47",
          "v48", "v49", "v50", "v51", "v52", "v53", "v54", "v55", "v56", "v57", "v58", "v59", "v60", "v61", "v62", "v63",
          "v96", "v97", "v98", "v99", "v104", "v105", "v106", "v107", "v108", "v109", "v110", "v111",
          "v112", "v113", "v114", "v115", "v116", "v117", "v118", "v119", "v120", "v121", "v122", "v123");
}

DI void wait_qkv(const char* ws, int a, u64 tag) { wait_flags<PSL>(ws + WS_FQ, qkv_dep(a, true), tag); }
DI void attn_role(const char* x, const char* wqkv, char* ws, char* lds, int a, int w) {
    const int kh = w >> 2;
    const int head = 4 * (a & 7) + ((a >> 3) & 3), qt = 8 * (a >> 5) + 2 * (w & 3);
    ST_DECL; ST(0);
    __builtin_amdgcn_s_setprio(3);
    u64 tag;
    const int ti = a >> 3, trow = 8 * (a & 1) + (ti & 7) + (ti >= 24 ? 16 : 0), tcol = (ti >= 24 ? 0 : 4 * (ti >> 3)) + ((a & 7) >> 1);
    if (w >= 4) {
        qkv_loader(x, wqkv, trow, tcol, w - 4);
        tag = uni64(*(LAS const u64*)(QCNT + 8));
        if (w == 4) wait_qkv(ws, a, tag);
    } else {
        tag = next_tag(ld_epoch(ws));
        if (tcol < 8) qkv_body<true>(ws, lds, trow, tcol, a, w, tag ST_PASS);
        else qkv_body<false>(ws, lds, trow, tcol, a, w, tag ST_PASS);
    }
    AttnRegs R;
    attn_prep(R, a, w);
    {
        const unsigned st = (lane_id() & 16) ? 0u : 16384u;
        R.va1 = R.va + st; R.va2 = R.va1 + st; R.va3 = R.va2 + st;
        R.fo = qkv_dep(a, false);
        asm volatile("" : "+{v16}"(R.va1), "+{v17}"(R.va2), "+{v18}"(R.va3), "+{v2}"(R.fo));
    }
    ST(5);
    wgbar();
    ST(6);
    {
        const int o = w * 512 + lane_id() * 8;
        const unsigned one = ((o & 0x1b0) == 0) ? 0x3f803f80u : 0u;
        *(LAS u32x2*)((LAS char*)lds + A_ONES + o) = u32x2{one, one};
        asm volatile("s_waitcnt lgkmcnt(0)" ::: "memory");
    }
    const char* ksrc = ws + WS_K + (size_t)head * 65536;
    const char* qsrc = ws + WS_Q + (size_t)(head * 64 + qt) * 1024;
    auto finish = [&](int att) {
        const int lane = lane_id();
        const f32x16 oa = R.oa, ob = R.ob;
        LAS char* ex = (LAS char*)lds + w * 2048 + lane * 16;
        LAS const char* exr = (LAS const char*)lds + (w ^ 4) * 2048 + lane * 16;
        *(LAS f32x4*)ex = f32x4{ob[0], ob[1], ob[2], ob[3]};
        *(LAS f32x4*)(ex + 1024) = f32x4{ob[4], ob[5], ob[6], ob[7]};
        *(LAS float*)((LAS char*)lds + A_LSUM + w * 256 + lane * 4) = ob[8];
        if (att == 0) {
            float big = fmaxf(oa[8], ob[8]) * 2.7e8f;
#pragma unroll
            for (int i = 0; i < 8; ++i) big = fmaxf(fmaxf(big, __builtin_fabsf(oa[i])), __builtin_fabsf(ob[i]));
            const bool bad = !__all(fminf(oa[8], ob[8]) > 8.1e-28f && big < 1.7e38f);
            if (lane == 0) ((LAS unsigned*)((LAS char*)lds + A_VOTE))[w] = bad;
        }
        wgbar();
        const f32x4 t0 = *(LAS const f32x4*)exr, t1 = *(LAS const f32x4*)(exr + 1024);
        const float t2 = *(LAS const float*)((LAS char*)lds + A_LSUM + (w ^ 4) * 256 + lane * 4);
        unsigned redo = 0;
        if (att == 0) {
            const u32x4 v0 = *(LAS const u32x4*)((LAS char*)lds + A_VOTE), v1 = *(LAS const u32x4*)((LAS char*)lds + A_VOTE + 16);
            redo = __builtin_amdgcn_readfirstlane(v0[0] | v0[1] | v0[2] | v0[3] | v1[0] | v1[1] | v1[2] | v1[3]);
        }
        const float rinv = __builtin_amdgcn_rcpf(oa[8] + t2);
        u32x4 ov;
        ov[0] = cvtpk((oa[0] + t0[0]) * rinv, (oa[1] + t0[1]) * rinv);
        ov[1] = cvtpk((oa[2] + t0[2]) * rinv, (oa[3] + t0[3]) * rinv);
        ov[2] = cvtpk((oa[4] + t1[0]) * rinv, (oa[5] + t1[1]) * rinv);
        ov[3] = cvtpk((oa[6] + t1[2]) * rinv, (oa[7] + t1[3]) * rinv);
        if (att == 0 ? !redo : att == 1 + kh) {
            st16(ws + WS_O + (size_t)((qt + kh) * 32 + head) * 1024, lane * 16, ov);
            drain();
            st_flag(ws + WS_FO + ((qt + kh) * 32 + head) * 128, tag);
        }
        asm volatile("" : "+s"(redo));
        return redo;
    };
    ST(1);
    attn_pass(R, qsrc + kh * 1024, qsrc + (1 - kh) * 1024, ksrc, ksrc + (WS_V - WS_K), ws + WS_FQ + 32768, tag, w);
    ST(2);
    if (finish(0)) {
#pragma clang loop unroll(disable)
        for (int att = 1; att < 3; ++att) {
            drain();
            wgbar();
            const int lane = lane_id();
            const char* qa = qsrc + (att - 1) * 1024;
            bf16x8 qf;
            asm volatile("global_load_dwordx4 %0, %1, %2 sc1\n\ts_waitcnt vmcnt(0)" : "=v"(qf) : "v"(lane * 16), "s"(qa) : "memory");
            float mx = -INFINITY;
#pragma clang loop unroll(disable)
            for (int t = 0; t < 64; ++t) {
                bf16x8 kf;
                asm volatile("global_load_dwordx4 %0, %1, %2 sc1\n\ts_waitcnt vmcnt(0)" : "=v"(kf) : "v"(lane * 16), "s"(ksrc + t * 1024) : "memory");
                const f32x16 s = MFMA32(kf, qf, f32x16{});
#pragma unroll
                for (int i = 0; i < 16; ++i) mx = fmaxf(mx, s[i]);
            }
            mx = -swap32_max(mx);
            attn_prep(R, a, w);
#pragma unroll
            for (int i = 0; i < 16; ++i) R.nm[i] = mx;
            attn_pass_shift(R, qa, qa, ksrc, ksrc + (WS_V - WS_K), w);
            finish(att);
        }
    }
    ST(3); ST_OUT(0);
}

#define BPR "s_setprio 0\n\t"
#define BG1 ""
#define BG2 "s_sleep 1\n\t"
#define BG3 "s_sleep 1\n\t"
#define SISSUE                                                             \
    "s_mov_b32 m0, s44\n\t"                                                 \
    "s_add_u32 s45, %[ow], %[wst]\n\t"                                      \
    "buffer_load_dwordx4 %[g0], %[rw], %[ow] offen lds\n\t"                 \
    "buffer_load_dwordx4 %[g1], %[rw], %[ow] offen offset:1024 lds\n\t"     \
    "s_add_u32 m0, s44, 8192\n\t"                                           \
    "s_add_u32 s46, %[od], 4096\n\t"                                        \
    "buffer_load_dwordx4 %[d0], %[rd], %[od] offen sc1 lds\n\t"             \
    "buffer_load_dwordx4 %[d0], %[rd], %[od] offen offset:1024 sc1 lds\n\t" \
    "s_add_u32 s44, s44, 0x4000\n\t"                                        \
    "s_and_b32 s44, s44, 0xffff\n\t"                                        \
    "s_cmp_eq_u32 %[od], %[de]\n\t"                                         \
    "s_cselect_b32 %[ow], 0, s45\n\t"                                       \
    "s_cselect_b32 %[od], 0, s46\n\t"
#define SISW                                                               \
    "s_mov_b32 m0, s44\n\t"                                                 \
    "s_add_u32 s45, %[ow], %[wst]\n\t"                                      \
    "buffer_load_dwordx4 %[g0], %[rw], %[ow] offen lds\n\t"                 \
    "buffer_load_dwordx4 %[g1], %[rw], %[ow] offen offset:1024 lds\n\t"     \
    "s_add_u32 s44, s44, 0x4000\n\t"                                        \
    "s_mov_b32 %[ow], s45\n\t"
#define SISD                                                               \
    "s_add_u32 m0, s44, 8192\n\t"                                           \
    "s_add_u32 s44, s44, 0x4000\n\t"                                        \
    "buffer_load_dwordx4 %[d0], %[rd], %[od] offen sc1 lds\n\t"             \
    "buffer_load_dwordx4 %[d0], %[rd], %[od] offen offset:1024 sc1 lds\n\t" \
    "s_add_u32 %[od], %[od], 4096\n\t"
struct SG { u32x4 rw, rd; unsigned wst, de, ow, od, a, b, c; };
DI void sgemm_prew(SG& G, int lw) {
    asm volatile("s_mov_b32 s44, %[lo]\n\t" SISW SISW SISW
                 : [ow] "+s"(G.ow) : [wst] "s"(G.wst), [rw] "s"(G.rw), [lo] "s"(lw * 2048), [g0] "v"(G.a), [g1] "v"(G.b) : "memory", "scc", "s44", "s45");
}
#define WISSUE                                                              \
    "s_and_b32 s45, s44, 0xc000\n\t"                                        \
    "s_add_u32 m0, s45, %[lo2]\n\t"                                         \
    "s_lshl_b32 s45, s45, 3\n\t"                                            \
    "buffer_load_dwordx4 %[o0], %[ro], s45 offen lds\n\t"                   \
    "buffer_load_dwordx4 %[o1], %[ro], s45 offen offset:1024 lds\n\t"       \
    "buffer_load_dwordx4 %[o2], %[ro], s45 offen offset:2048 lds\n\t"       \
    "buffer_load_dwordx4 %[o3], %[ro], s45 offen offset:3072 lds\n\t"       \
    "s_add_u32 s44, s44, 0x4000\n\t"                                        \
    "s_and_b32 s44, s44, 0xffff\n\t"
template <int WO>
DI void sgemm_load(SG& G, int n, int lw, u32x4 ro, unsigned o0) {
    asm volatile(
        "s_mov_b32 s44, %[lo]\n\t"
        ".if %c[wo]\n\t"
        "s_sub_u32 s47, %[n], 5\n\t"
        SISD SISD SISD
        "s_waitcnt vmcnt(4)\n\ts_barrier\n\t" SISSUE
        "s_waitcnt vmcnt(6)\n\ts_barrier\n\t" SISSUE
        ".else\n\t"
        "s_sub_u32 s47, %[n], 3\n\t"
        SISSUE SISSUE SISSUE
        ".endif\n\t"
        "LL_loop%=:\n\t"
        "s_waitcnt vmcnt(8)\n\t"
        "s_barrier\n\t"
        SISSUE
        "s_sub_u32 s47, s47, 1\n\t"
        "s_cmp_lg_u32 s47, 0\n\t"
        "s_cbranch_scc1 LL_loop%=\n\t"
        ".if %c[wo]\n\t"
        "s_waitcnt vmcnt(8)\n\ts_barrier\n\t" WISSUE
        "s_waitcnt vmcnt(8)\n\ts_barrier\n\t" WISSUE
        "s_waitcnt vmcnt(8)\n\ts_barrier\n\t" WISSUE
        "s_barrier\n\t" WISSUE
        "s_barrier\n\t"
        ".else\n\t"
        "s_waitcnt vmcnt(8)\n\ts_barrier\n\t"
        "s_waitcnt vmcnt(4)\n\ts_barrier\n\t"
        "s_waitcnt vmcnt(0)\n\ts_barrier\n\t"
        "s_barrier\n\t"
        ".endif\n\t"
        : [ow] "+s"(G.ow), [od] "+s"(G.od)
        : [wst] "s"(G.wst), [rw] "s"(G.rw), [rd] "s"(G.rd), [lo] "s"(lw * 2048), [de] "s"(G.de), [n] "s"(n), [g0] "v"(G.a), [g1] "v"(G.b), [d0] "v"(G.c),
          [wo] "i"(WO), [ro] "s"(ro), [lo2] "s"(lw * 4096), [o0] "v"(o0), [o1] "v"(o0 + 7168), [o2] "v"(o0 + 14336), [o3] "v"(o0 + 21504)
        : "memory", "scc", "s44", "s45", "s46", "s47");
}
#define SRD3(B, SLOT, K, TD)                                                                         \
    "ds_read_b64_tr_b16 v[" #B ":" #B "+1], %[t] offset:" #SLOT "*16384+" #K "*2048\n\t"              \
    "ds_read_b64_tr_b16 v[" #B "+2:" #B "+3], %[t] offset:" #SLOT "*16384+" #K "*2048+" #TD "\n\t"    \
    "ds_read_b128 v[" #B "+4:" #B "+7], %[dl] offset:" #SLOT "*16384+" #K "*1024\n\t"
#define SSTAGE(SLOT, MM, TD, N0, N1, N2, N3, P0, P1, P2, P3)                          \
    "s_waitcnt lgkmcnt(0)\n\t"                                                        \
    "s_barrier\n\t"                                                                   \
    MM(P0) SRD3(N0, SLOT, 0, TD) SRD3(N1, SLOT, 1, TD) BG1                            \
    MM(P1) SRD3(N2, SLOT, 2, TD) SRD3(N3, SLOT, 3, TD) BG2                            \
    MM(P2) BG3 MM(P3)
#define SST_E(SLOT, MM, TD) SSTAGE(SLOT, MM, TD, 32, 40, 48, 56, 96, 104, 112, 120)
#define SST_O(SLOT, MM, TD) SSTAGE(SLOT, MM, TD, 96, 104, 112, 120, 32, 40, 48, 56)
#define SMM_NONE(P) ""
#define SMM_WA(P) MF("v[64:79]", "v[" #P ":" #P "+3]", "v[" #P "+4:" #P "+7]", "v[64:79]")
#define SMM_WB(P) MF("v[80:95]", "v[" #P "+4:" #P "+7]", "v[" #P ":" #P "+3]", "v[80:95]")
#define SGEMM_ASM(ACC, MM, TD, NLOOP, TAIL, FIN)                                                                       \
    asm volatile(                                                                                                 \
        "s_mov_b32 s44, " #NLOOP "\n\t" BPR                                                                       \
        "s_barrier\n\t"                                                                                           \
        SRD3(32, 0, 0, TD) SRD3(40, 0, 1, TD) SRD3(48, 0, 2, TD) SRD3(56, 0, 3, TD)                               \
        "LS_loop%=:\n\t"                                                                                          \
        SST_O(1, MM, TD) SST_E(2, MM, TD) SST_O(3, MM, TD) SST_E(0, MM, TD)                                       \
        "s_sub_u32 s44, s44, 1\n\t"                                                                               \
        "s_cmp_lg_u32 s44, 0\n\t"                                                                                 \
        "s_cbranch_scc1 LS_loop%=\n\t"                                                                            \
        TAIL                                                                                                      \
        "s_waitcnt lgkmcnt(0)\n\t" FIN                                                                            \
        MM(96) MM(104) MM(112) MM(120)                                                                \
        "s_setprio 3\n\ts_nop 15\n\t"                                                                                            \
        : ACC(acc)                                                                                                \
        : [t] "v"(G.a), [dl] "v"(G.b)                                                                             \
        : "memory", "scc", "s44",                                                                                 \
          "v32", "v33", "v34", "v35", "v36", "v37", "v38", "v39", "v40", "v41", "v42", "v43", "v44", "v45", "v46", "v47", \
          "v48", "v49", "v50", "v51", "v52", "v53", "v54", "v55", "v56", "v57", "v58", "v59", "v60", "v61", "v62", "v63", \
          "v96", "v97", "v98", "v99", "v100", "v101", "v102", "v103", "v104", "v105", "v106", "v107", "v108", "v109", "v110", "v111", \
          "v112", "v113", "v114", "v115", "v116", "v117", "v118", "v119", "v120", "v121", "v122", "v123", "v124", "v125", "v126", "v127")

template <int MODE>
DI SG sgemm_setup(const char* wbase, unsigned wbytes, int wrow, const char* dbase, int dblk, int rot, int w) {
    const int lane = lane_id(), row = lane >> 3, lw = w & 3;
    const int q = (lane & 15) >> 2, h2 = lane >> 5;
    SG G;
    G.rw = srd(wbase, wbytes); G.rd = srd(dbase, 2 * dblk);
    G.wst = 64 * wrow; G.de = (MODE == 0 ? 31 : 21) * 4096; G.ow = rot * G.wst; G.od = rot * 4096;
    if (w >= 4) {
        G.a = (16 * lw + row) * wrow + (((lane & 7) ^ (((row >> 1) & 1) << 2)) << 4);
        G.b = G.a + 8 * wrow - 1024;
        G.c = (lw >> 1) * dblk + (lw & 1) * 2048 + lane * 16;
    } else {
        G.a = ((MODE == 0 ? 8 : 4) * h2 + q) * 128 + (((lw & 1) ^ ((q >> 1) & 1)) << 6) + ((lane >> 4) & 1) * 32 + (lane & 3) * 8;
        G.b = 8192 + (lw >> 1) * 4096 + lane * 16;
        G.c = 0;
    }
    asm volatile("" : "+v"(G.a), "+v"(G.b), "+v"(G.c));
    return G;
}
#define ACC0 "+{v[64:79]}"
#define ACC1 "+{v[80:95]}"
template <int MODE>
DI void sgemm_run(f32x16& acc, SG& G) {
    if (MODE == 0) SGEMM_ASM(ACC0, SMM_WA, 512, 7, SST_O(1, SMM_WA, 512) SST_E(2, SMM_WA, 512) SST_O(3, SMM_WA, 512), "s_setprio 3\n\t");
    else SGEMM_ASM(ACC1, SMM_WB, 1024, 5, SST_O(1, SMM_WB, 1024), "s_barrier\n\ts_setprio 3\n\t");
}
DI f32x4 sub4(const f32x16& a, int g) { return f32x4{a[4 * g], a[4 * g + 1], a[4 * g + 2], a[4 * g + 3]}; }

DI unsigned gelu2(float a, float b) {
    constexpr float c1 = -2.f * 0.7978845608028654f * 1.4426950408889634f, c2 = c1 * 0.044715f;
    const f32x2 u = {a, b};
    const f32x2 z = u * (u * u * c2 + c1);
    const f32x2 d = f32x2{__builtin_amdgcn_exp2f(z[0]), __builtin_amdgcn_exp2f(z[1])} + 1.f;
    const f32x2 o = u * f32x2{__builtin_amdgcn_rcpf(d[0]), __builtin_amdgcn_rcpf(d[1])};
    return cvtpk(o[0], o[1]);
}

#define WRD(R0, R1, R2, R3, STEP)                                                    \
    "ds_read_b64_tr_b16 " R0 ", %[t0] offset:" #STEP "*2048\n\t"                      \
    "ds_read_b64_tr_b16 " R1 ", %[t0] offset:" #STEP "*2048+1024\n\t"                 \
    "ds_read_b64_tr_b16 " R2 ", %[t1] offset:" #STEP "*2048\n\t"                      \
    "ds_read_b64_tr_b16 " R3 ", %[t1] offset:" #STEP "*2048+1024\n\t"
#define WRD0(S) WRD("v[32:33]", "v[34:35]", "v[36:37]", "v[38:39]", S)
#define WRD1(S) WRD("v[40:41]", "v[42:43]", "v[44:45]", "v[46:47]", S)
#define WRD2(S) WRD("v[48:49]", "v[50:51]", "v[52:53]", "v[54:55]", S)
#define WRD3(S) WRD("v[56:57]", "v[58:59]", "v[60:61]", "v[62:63]", S)
#define WMM(WAIT, W0, W1, O) "s_waitcnt " WAIT "\n\t" MF("v[96:111]", W0, O, "v[96:111]") MF("v[112:127]", W1, O, "v[112:127]")
#define OLD(K, R, V, OFF)                                             \
    "5:\n\t"                                                           \
    "s_bitcmp1_b32 s40, " #K "\n\t"                                    \
    "s_cbranch_scc1 6f\n\t"                                            \
    "global_load_dwordx2 v[24:25], %[fo], %[f] sc1\n\t"                       \
    "s_sub_u32 s41, s41, 1\n\t"                                        \
    "s_cselect_b32 s42, -1, 0\n\t"                                     \
    "s_waitcnt vmcnt(0)\n\t"                                           \
    "v_cmp_eq_u64 vcc, %[tag], v[24:25]\n\t"                                \
    "s_or_b32 s40, vcc_lo, s42\n\t"                                    \
    "s_bitcmp1_b32 s40, " #K "\n\t"                                    \
    "s_cbranch_scc1 6f\n\t"                                            \
    "s_sleep 1\n\t"                                                    \
    "s_branch 5b\n\t"                                                  \
    "6:\n\t"                                                           \
    "global_load_dwordx4 " R ", " V ", %[o] offset:" #OFF " sc1\n\t"
DI void wo_gemm(f32x16& a0, f32x16& a1, f32x4& k0, f32x4& k1, float& k2, const char* osrc, const char* fsrc, unsigned seen, u64 tag, int w) {
    const int lane = lane_id();
    const int q = (lane & 15) >> 2, h2 = lane >> 5;
    const unsigned tb = (w >> 1) * 16384 + (4 * h2 + q) * 128 + ((lane >> 4) & 1) * 32 + (lane & 3) * 8;
    const unsigned t0 = tb + ((q >> 1) & 1) * 64, t1 = tb + (((q >> 1) & 1) ^ 1) * 64;
    const unsigned d0 = lane * 16, d1 = d0 + 4096, fo = (lane & 7) * 128;
    asm volatile(
        WRD0(0) WRD1(1) WRD2(2) WRD3(3)
        "s_mov_b32 s40, %[seen]\n\t"
        "s_mov_b32 s41, 0x100000\n\t"
        "s_setprio 0\n\t"
        OLD(0, "v[64:67]", "%[d0]", 0) OLD(1, "v[68:71]", "%[d0]", 1024) OLD(2, "v[72:75]", "%[d0]", 2048) OLD(3, "v[76:79]", "%[d0]", 3072)
        OLD(4, "v[80:83]", "%[d1]", 0) OLD(5, "v[84:87]", "%[d1]", 1024) OLD(6, "v[88:91]", "%[d1]", 2048) OLD(7, "v[92:95]", "%[d1]", 3072)
        "s_setprio 3\n\t"
        "s_waitcnt vmcnt(7) lgkmcnt(0)\n\t"
        MF("v[96:111]", "v[32:35]", "v[64:67]", "0") MF("v[112:127]", "v[36:39]", "v[64:67]", "0")
        WRD0(4)
        WMM("vmcnt(6)", "v[40:43]", "v[44:47]", "v[68:71]") WRD1(5)
        WMM("vmcnt(5)", "v[48:51]", "v[52:55]", "v[72:75]") WRD2(6)
        WMM("vmcnt(4)", "v[56:59]", "v[60:63]", "v[76:79]") WRD3(7)
        WMM("vmcnt(3) lgkmcnt(12)", "v[32:35]", "v[36:39]", "v[80:83]")
        WMM("vmcnt(2) lgkmcnt(0)", "v[40:43]", "v[44:47]", "v[84:87]")
        WMM("vmcnt(1)", "v[48:51]", "v[52:55]", "v[88:91]")
        "s_barrier\n\t"
        WMM("vmcnt(0)", "v[56:59]", "v[60:63]", "v[92:95]")
        "s_nop 15\n\t"
        : "={v[96:111]}"(a0), "={v[112:127]}"(a1),
          "+{v[12:15]}"(k0), "+{v[16:19]}"(k1), "+{v20}"(k2)
        : [o] "s"(osrc), [f] "s"(fsrc), [tag] "s"(tag), [seen] "s"(seen), [d0] "v"(d0), [d1] "v"(d1), [t0] "v"(t0), [t1] "v"(t1), [fo] "v"(fo)
        : "memory", "scc", "vcc", "s40", "s41", "s42", "v24", "v25",
          "v32", "v33", "v34", "v35", "v36", "v37", "v38", "v39", "v40", "v41", "v42", "v43", "v44", "v45", "v46", "v47",
          "v48", "v49", "v50", "v51", "v52", "v53", "v54", "v55", "v56", "v57", "v58", "v59", "v60", "v61", "v62", "v63",
          "v64", "v65", "v66", "v67", "v68", "v69", "v70", "v71", "v72", "v73", "v74", "v75", "v76", "v77", "v78", "v79",
          "v80", "v81", "v82", "v83", "v84", "v85", "v86", "v87", "v88", "v89", "v90", "v91", "v92", "v93", "v94", "v95");
}

DI void mixer_role(const char* x, const char* wqkv, const char* gw, const char* wo, const char* w1, const char* b1, const char* w2, const char* b2,
                   char* ws, char* out, char* lds, int bi, int w) {
    const int j = bi & 7, i = bi >> 3;
    __builtin_amdgcn_s_setprio(LNP);
    ST_DECL; ST(0);
    const u64 ep = ld_epoch(ws);
    u64 tag;
    {
        const int lane = lane_id(), h2 = lane >> 5, tid = 64 * w + lane;
        const u32x4 xr = *(GAS const u32x4*)(x + (size_t)(8 * bi + w) * 1024 + lane * 16);
        const u32x4 gr = *(GAS const u32x4*)(gw + lane * 16);
        tag = next_tag(ep);
        float v[8], gg[8];
#pragma unroll
        for (int k = 0; k < 4; ++k) { v[2 * k] = bflo(xr[k]); v[2 * k + 1] = bfhi(xr[k]); gg[2 * k] = bflo(gr[k]); gg[2 * k + 1] = bfhi(gr[k]); }
        float s1 = 0.f, s3 = 0.f;
#pragma unroll
        for (int k = 0; k < 8; ++k) { s1 += v[k]; s3 += v[k] * gg[k]; }
        const float mu = wave_sum(s1) * (1.f / 512.f);
        s3 = wave_sum(s3);
        float s2 = 0.f;
#pragma unroll
        for (int k = 0; k < 8; ++k) { v[k] -= mu; s2 += v[k] * v[k]; }
        const float rs = __builtin_amdgcn_rsqf(wave_sum(s2) * (1.f / 512.f) + 1e-5f);
        u32x4 hv;
#pragma unroll
        for (int k = 0; k < 4; ++k) hv[k] = cvtpk(v[2 * k] * rs, v[2 * k + 1] * rs);
        *(LAS u32x4*)((LAS char*)lds + w * 1024 + lane * 16) = hv;
        const float g = __builtin_amdgcn_rcpf(1.f + __builtin_amdgcn_exp2f(-1.4426950408889634f * s3));
        st4(ws + WS_G + (8 * bi + w) * 4, uni(g));
        wgbar();
        LAS const unsigned short* lb = (LAS const unsigned short*)lds + tid;
#pragma unroll
        for (int k = 0; k < 4; ++k) hv[k] = (unsigned)lb[(2 * k) * 512] | ((unsigned)lb[(2 * k + 1) * 512] << 16);
        st16(ws + WS_H + (size_t)(2 * w * 128 + (bi >> 1)) * 1024 + (bi & 1) * 512, h2 * 131072 + (lane & 31) * 16, hv);
        drain();
        wgbar();
        if (w == 0) st_flag(ws + WS_FL + bi * 128, tag);
    }
    __builtin_amdgcn_s_setprio(3);
    const int wb = w & 1, db = (w >> 1) & 1, hf = w >> 2;
    const int L = (j >> 1) * 22 + i, ft = L >> 2, dt1 = 4 * (j & 1) + (L & 3);
    const int st = 8 * (j >> 1) + (i & 7), dt = 4 * (j & 1) + (i >> 3);
    SG G1, G2;
    unsigned ex, l16, og, wof;
    {
        const int lane = lane_id();
        og = (64 * st + 32 * wb + (lane & 31)) * 4;
        G1 = sgemm_setup<0>(w1 + ft * 128, 2048 * 2752 - ft * 128, 2752, ws + WS_H + (size_t)2 * dt1 * 131072, 131072, ROT1, w);
        G2 = sgemm_setup<1>(w2 + st * 128, 1376 * 4096 - st * 128, 4096, ws + WS_U + (size_t)2 * dt * 86 * 1024, 86 * 1024, ROT2, w);
        l16 = lane * 16;
        ex = 65536 + (w & 3) * 2048 + l16;
        wof = (32 * (w & 3) + (lane >> 3)) * 1024 + (((lane & 7) ^ (((lane >> 4) & 1) << 2)) << 4);
        asm volatile("" : "+v"(ex), "+v"(l16), "+v"(og), "+v"(wof));
    }
    {
        const int row = 16 + 8 * (j & 1) + (i & 7), col = 8 + 8 * (i >> 4) + 2 * (j >> 1) + ((i >> 3) & 1);
        if (w == 0) {
            *(LAS unsigned*)QCNT = 0u;
            wait_flags<2>(ws + WS_FQ + bi * 128, 0, tag);
        }
        wgbar();
        if (w >= 4) qkv2_loader(x, wqkv, row, col, w - 4);
        else qkv2_body(ws, row, col, bi, w, tag);
    }
    ST(6);
    if (PFA && i >= 22) {
        const int wv = ((i - 22) * 8 + j) * 8 + w;
        if (PF1) warm(w1, 1376, wv, l16 * 4);
        for (int k = 0; k < PFD; ++k) __builtin_amdgcn_s_sleep(64);
        warm(w2, 1376, wv, l16 * 4);
        warm(wo, 128, wv, l16 * 4);
    }
    if (i < 22) {
        if (w >= 4) wait_flags<8>(ws + WS_FL + (w - 4) * 8192, l16 * 8, tag);
        wgbar();
        ST(7);
        f32x4 v0, v1;
        if (w >= 4) {
            sgemm_load<0>(G1, 32, w - 4, G1.rw, 0);
            v0 = *(LAS const f32x4*)(uintptr_t)ex; v1 = *(LAS const f32x4*)(uintptr_t)(ex + 1024);
        } else {
            f32x16 acc1;
            const char* bsrc = b1 + (64 * ft + (ft < 21 ? 32 * wb : 0)) * 2 + (l16 >> 9) * 8;
#pragma unroll
            for (int k = 0; k < 4; ++k) {
                const u32x2 bv = *(GAS const u32x2*)(bsrc + 16 * k);
                acc1[4 * k] = bflo(bv[0]); acc1[4 * k + 1] = bfhi(bv[0]); acc1[4 * k + 2] = bflo(bv[1]); acc1[4 * k + 3] = bfhi(bv[1]);
            }
            sgemm_run<0>(acc1, G1);
            *(LAS f32x4*)(uintptr_t)ex = sub4(acc1, 2); *(LAS f32x4*)(uintptr_t)(ex + 1024) = sub4(acc1, 3);
            wgbar();
            v0 = sub4(acc1, 0); v1 = sub4(acc1, 1);
        }
        ST(1);
        const u32x4 o = {gelu2(v0[0], v0[1]), gelu2(v0[2], v0[3]), gelu2(v1[0], v1[1]), gelu2(v1[2], v1[3])};
        if (ft < 21 || wb == 0) st16(ws + WS_U + (size_t)((2 * dt1 + db) * 86 + 4 * ft + 2 * wb + hf) * 1024, l16, o);
        drain();
        wgbar();
        if (w == 0) st_flag(ws + WS_FU + (dt1 * 32 + ft) * 128, tag);
    }
    ST(2);
    if (w >= 4) sgemm_prew(G2, w - 4);
    if (bi == 255 && w == 1) {
        wait_flags256(ws + WS_FQ, l16 * 8, tag);
        wait_flags256(ws + WS_FL, l16 * 8, tag);
        st_flag(ws + WS_EPOCH, tag);
    }
    if (w == 0) wait_flags<8>(ws + WS_FU + dt * 4096, (l16 < 22 * 16 ? l16 : 0) * 8, tag);
    wgbar();
    ST(3);
    const char* fsrc = ws + WS_FO + ((2 * st + wb) * 32 + 8 * (w >> 1)) * 128;
    u64 f0;
    f32x4 bp0, bp1;
    float g;
    {
        asm volatile("global_load_dword %0, %1, %2 sc1" : "=v"(g) : "v"(og), "s"(ws + WS_G) : "memory");
        if (w >= 4) {
            sgemm_load<1>(G2, 22, w - 4, srd(wo + dt * 128, 524288 - dt * 128), wof);
            asm volatile("global_load_dwordx2 %0, %1, %2 sc1" : "=v"(f0) : "v"((l16 & 0x70) * 8), "s"(fsrc) : "memory");
            bp0 = *(LAS const f32x4*)(uintptr_t)ex; bp1 = *(LAS const f32x4*)(uintptr_t)(ex + 1024);
        } else {
            const float bb = bflo(*(GAS const unsigned short*)(b2 + (og >> 1)));
            f32x16 acc2 = {bb, bb, bb, bb, bb, bb, bb, bb, bb, bb, bb, bb, bb, bb, bb, bb};
            sgemm_run<1>(acc2, G2);
            asm volatile("global_load_dwordx2 %0, %1, %2 sc1" : "=v"(f0) : "v"((l16 & 0x70) * 8), "s"(fsrc) : "memory");
            *(LAS f32x4*)(uintptr_t)ex = sub4(acc2, 2); *(LAS f32x4*)(uintptr_t)(ex + 1024) = sub4(acc2, 3);
            wgbar();
            bp0 = sub4(acc2, 0); bp1 = sub4(acc2, 1);
            drain();
        }
        asm volatile("" : "+v"(g));
        ST(4);
        bp0 *= 1.f - g;
        bp1 *= 1.f - g;
        asm volatile("" : "+v"(bp0), "+v"(bp1));
    }
    asm volatile("s_waitcnt vmcnt(0)\n\ts_barrier" : "+v"(f0)::"memory");
    ST(5);
    f32x16 a0, a1;
    wo_gemm(a0, a1, bp0, bp1, g, ws + WS_O + (size_t)((2 * st + wb) * 32 + 8 * (w >> 1)) * 1024,
            fsrc, (unsigned)__ballot(f0 == tag), tag, w);
    {
        const int lane = lane_id(), kq = 2 * db + hf;
        LAS char* ex = (LAS char*)lds + w * 8192 + lane * 16;
#pragma unroll
        for (int k = 0; k < 4; ++k) { *(LAS f32x4*)(ex + k * 1024) = sub4(a0, k); *(LAS f32x4*)(ex + 4096 + k * 1024) = sub4(a1, k); }
        wgbar();
        LAS const char* exr = (LAS const char*)lds + wb * 8192 + kq * 2048 + lane * 16;
        f32x4 s0 = *(LAS const f32x4*)exr, s1 = *(LAS const f32x4*)(exr + 1024);
#pragma unroll
        for (int k = 1; k < 4; ++k) { s0 += *(LAS const f32x4*)(exr + k * 16384); s1 += *(LAS const f32x4*)(exr + k * 16384 + 1024); }
        s0 = s0 * g + bp0;
        s1 = s1 * g + bp1;
        const auto r0 = __builtin_amdgcn_permlane32_swap(cvtpk(s0[0], s0[1]), cvtpk(s1[0], s1[1]), false, false);
        const auto r1 = __builtin_amdgcn_permlane32_swap(cvtpk(s0[2], s0[3]), cvtpk(s1[2], s1[3]), false, false);
        *(GAS u32x4*)(out + (size_t)(64 * st + 32 * wb) * 1024 + (64 * dt + 16 * kq) * 2 + (lane & 31) * 1024 + (lane >> 5) * 16) = u32x4{r0[0], r1[0], r0[1], r1[1]};
    }
    ST_OUT(0);
}

constexpr int MK_LDS = 81920;
__global__ __launch_bounds__(512, 4) void mk(const char* x, const char* wqkv, const char* wo, const char* w1, const char* b1,
                                             const char* w2, const char* b2, const char* gw, char* ws, char* out) {
    extern __shared__ __attribute__((aligned(16))) char lds[];
    const int w = __builtin_amdgcn_readfirstlane(threadIdx.x >> 6);
    if (blockIdx.x >= 256 && w == 7) {
        asm volatile("s_getpc_b64 s[40:41]\n\t"
                     "Lmk_hot_begin:\n\t"
                     "s_cmp_lt_u32 %[o], Lmk_hot_end-Lmk_hot_begin-4096\n\t"
                     "s_cbranch_scc0 1f\n\t"
                     "s_add_u32 s40, s40, %[o]\n\t"
                     "s_addc_u32 s41, s41, 0\n\t"
                     "s_and_b32 s40, s40, -4\n\t"
                     "s_mov_b32 m0, 0x13e00\n\t"
                     "s_nop 0\n\t"
                     "global_load_lds_dword %[v], s[40:41]\n\t"
                     "1:"
                     :: [o] "s"(((blockIdx.x - 256) >> 3) * 4096), [v] "v"(lane_id() * 64) : "memory", "scc", "s40", "s41");
    }
    if (blockIdx.x < 256) attn_role(x, wqkv, ws, lds, blockIdx.x, w);
    else mixer_role(x, wqkv, gw, wo, w1, b1, w2, b2, ws, out, lds, blockIdx.x - 256, w);
}

extern "C" void kernel_launch(void* const* d_in, const int* in_sizes, int n_in, void* d_out, int out_size, void* d_ws, size_t ws_size,
                              hipStream_t stream) {
    (void)hipFuncSetAttribute((const void*)mk, hipFuncAttributeMaxDynamicSharedMemorySize, MK_LDS);
    mk<<<512, 512, MK_LDS, stream>>>((const char*)d_in[0], (const char*)d_in[1], (const char*)d_in[2], (const char*)d_in[4],
                                     (const char*)d_in[5], (const char*)d_in[6], (const char*)d_in[7], (const char*)d_in[8],
                                     (char*)d_ws, (char*)d_out);
}
```

```cpp
#include <hip/hip_runtime.h>
#include <hip/hip_bf16.h>
#include <stdint.h>

typedef __attribute__((ext_vector_type(8))) short bf16x8;
typedef __attribute__((ext_vector_type(4))) short s16x4;
typedef __attribute__((ext_vector_type(16))) float f32x16;
typedef __attribute__((ext_vector_type(4))) float f32x4;
typedef __attribute__((ext_vector_type(2))) float f32x2;
typedef __attribute__((ext_vector_type(4))) unsigned u32x4;
typedef __attribute__((ext_vector_type(2))) unsigned u32x2;
typedef unsigned long long u64;
#define LAS __attribute__((address_space(3)))
#define GAS __attribute__((address_space(1)))
#define DI __device__ __forceinline__
#define MFMA32(a, b, c) __builtin_amdgcn_mfma_f32_32x32x16_bf16((a), (b), (c), 0, 0, 0)

constexpr size_t WS_EPOCH = 0;
constexpr size_t WS_G = 4096;
constexpr size_t WS_FL = 98304;
constexpr size_t WS_FQ = 458752;
constexpr size_t WS_FU = 65536;
constexpr size_t WS_FO = 131072;
constexpr size_t WS_Q = 1u << 20;
constexpr size_t WS_K = WS_Q + (2u << 20);
constexpr size_t WS_V = WS_K + (2u << 20);
constexpr size_t WS_H = WS_V + (2u << 20);
constexpr size_t WS_U = WS_H + (2u << 20);
constexpr size_t WS_O = WS_U + (2u << 20);

DI unsigned cvtpk(float lo, float hi) {
    typedef float f32x2_t __attribute__((ext_vector_type(2)));
    typedef __bf16 bf16x2_t __attribute__((ext_vector_type(2)));
    return __builtin_bit_cast(unsigned, __builtin_convertvector(f32x2_t{lo, hi}, bf16x2_t));
}
DI float bflo(unsigned u) { return __uint_as_float(u << 16); }
DI float bfhi(unsigned u) { return __uint_as_float(u & 0xffff0000u); }
DI s16x4 trread(LAS const char* p) {
    typedef short v4i16_t __attribute__((ext_vector_type(4)));
    return __builtin_bit_cast(s16x4, __builtin_amdgcn_ds_read_tr16_b64_v4i16((LAS v4i16_t*)p));
}
template <int CTRL> DI float dpp(float v) {
    return __int_as_float(__builtin_amdgcn_update_dpp(0, __float_as_int(v), CTRL, 0xf, 0xf, true));
}
DI float rl(float v, int l) { return __int_as_float(__builtin_amdgcn_readlane(__float_as_int(v), l)); }
DI float wave_sum(float v) {
    v += dpp<0xB1>(v); v += dpp<0x4E>(v); v += dpp<0x141>(v); v += dpp<0x140>(v);
    return (rl(v, 0) + rl(v, 16)) + (rl(v, 32) + rl(v, 48));
}
DI float swap32_max(float v) {
    auto rr = __builtin_amdgcn_permlane32_swap(__float_as_uint(v), __float_as_uint(v), false, false);
    return fmaxf(__uint_as_float(rr[0]), __uint_as_float(rr[1]));
}

DI int lane_id() {
    int l;
    asm volatile("v_mbcnt_lo_u32_b32 %0, -1, 0\n\tv_mbcnt_hi_u32_b32 %0, -1, %0" : "=v"(l));
    __builtin_assume(l >= 0 && l < 64);
    return l;
}
DI void st16(const char* sbase, unsigned voff, u32x4 v) {
    asm volatile("global_store_dwordx4 %0, %1, %2 sc1\n\ts_nop 1" ::"v"(voff), "v"(v), "s"(sbase) : "memory");
}
DI void st4(const char* p, unsigned v) {
    unsigned t0, t1;
    asm volatile("s_nop 1\n\tv_mov_b32 %0, %2\n\tv_mov_b32 %1, 0\n\ts_mov_b64 exec, 1\n\tglobal_store_dword %1, %0, %3 sc1\n\ts_mov_b64 exec, -1"
                 : "=&v"(t0), "=&v"(t1) : "s"(v), "s"(p) : "memory");
}
DI void st_flag(const char* p, u64 v) {
    u64 t0;
    unsigned t1;
    asm volatile("s_nop 1\n\tv_mov_b64 %0, %2\n\tv_mov_b32 %1, 0\n\ts_mov_b64 exec, 1\n\tglobal_store_dwordx2 %1, %0, %3 sc1\n\ts_mov_b64 exec, -1"
                 : "=&v"(t0), "=&v"(t1) : "s"(v), "s"(p) : "memory");
}
DI u64 uni64(u64 v) {
    return (u64)(unsigned)__builtin_amdgcn_readfirstlane((unsigned)v) | ((u64)(unsigned)__builtin_amdgcn_readfirstlane((unsigned)(v >> 32)) << 32);
}
DI u64 ld_epoch(const char* ws) { return __hip_atomic_load((GAS const u64*)(ws + WS_EPOCH), __ATOMIC_RELAXED, __HIP_MEMORY_SCOPE_AGENT); }
DI u64 next_tag(u64 e) { return uni64(e) * 6364136223846793005ull + 1442695040888963407ull; }
DI unsigned uni(float v) { return __builtin_amdgcn_readfirstlane(__float_as_uint(v)); }
DI void warm(const char* base, int nchunks, int first, unsigned l64) {
#pragma clang loop unroll(disable)
    for (int n = first; n < nchunks; n += 640) {
        const char* p = base + (size_t)((n & 1) * (nchunks >> 1) + (n >> 1)) * 4096;
        asm volatile("s_mov_b32 m0, 0x13e00\n\ts_nop 0\n\tglobal_load_lds_dword %0, %1" ::"v"(l64), "s"(p) : "memory");
    }
}
DI void drain() { asm volatile("s_waitcnt vmcnt(0)" ::: "memory"); }
DI void wgbar() { asm volatile("s_waitcnt lgkmcnt(0)\n\ts_barrier" ::: "memory"); }
template <int SLEEP>
DI void wait_flags(const char* sbase, unsigned voff, u64 tag) {
#pragma clang loop unroll(disable)
    for (unsigned spin = 0; spin < (1u << 20); ++spin) {
        u64 f;
        asm volatile("global_load_dwordx2 %0, %1, %2 sc1\n\ts_waitcnt vmcnt(0)" : "=v"(f) : "v"(voff), "s"(sbase) : "memory");
        if (__all(f == tag)) break;
        __builtin_amdgcn_s_sleep(SLEEP);
    }
}

DI void wait_flags256(const char* sbase, unsigned voff, u64 tag) {
#pragma clang loop unroll(disable)
    for (unsigned spin = 0; spin < (1u << 20); ++spin) {
        u64 f0, f1, f2, f3;
        asm volatile("global_load_dwordx2 %0, %4, %5 sc1\n\tglobal_load_dwordx2 %1, %4, %6 sc1\n\tglobal_load_dwordx2 %2, %4, %7 sc1\n\t"
                     "global_load_dwordx2 %3, %4, %8 sc1\n\ts_waitcnt vmcnt(0)"
                     : "=&v"(f0), "=&v"(f1), "=&v"(f2), "=&v"(f3)
                     : "v"(voff), "s"(sbase), "s"(sbase + 8192), "s"(sbase + 16384), "s"(sbase + 24576) : "memory");
        if (__all(f0 == tag && f1 == tag && f2 == tag && f3 == tag)) break;
        __builtin_amdgcn_s_sleep(8);
    }
}

constexpr int QCNT = 73728;
#define MF(D, A, B, C) "v_mfma_f32_32x32x16_bf16 " D ", " A ", " B ", " C "\n\t"
DI u32x4 srd(const char* base, unsigned bytes) {
    return u32x4{(unsigned)(uintptr_t)base, (unsigned)((uintptr_t)base >> 32) & 0xffffu, bytes, 0x00020000u};
}
DI void qkv_store(char* ws, const f32x16& acc, int type, int head, int tile, int lane) {
    const float sc = (type == 0) ? 0.25f * 1.4426950408889634f : 1.f;
    const char* dst = ws + WS_Q + (size_t)type * (2u << 20) + (size_t)(head * 64 + tile) * 1024;
    const unsigned vo = type < 2 ? lane * 16 : ((lane >> 4) & 1) * 65536 + (lane >> 5) * 256 + (lane & 15) * 16;
#pragma unroll
    for (int hf = 0; hf < 2; ++hf) {
        u32x4 o;
#pragma unroll
        for (int i = 0; i < 4; ++i) o[i] = cvtpk(acc[8 * hf + 2 * i] * sc, acc[8 * hf + 2 * i + 1] * sc);
        st16(dst + hf * (type < 2 ? 65536 : 512), vo, o);
    }
}
DI void publish4(const char* flag, u64 tag, int lane) {
    drain();
    unsigned arr = 0;
    if (lane == 0) arr = __hip_atomic_fetch_add((LAS unsigned*)QCNT, 1u, __ATOMIC_RELAXED, __HIP_MEMORY_SCOPE_WORKGROUP);
    if (__builtin_amdgcn_readfirstlane(arr) == 3) st_flag(flag, tag);
}
DI void qkv_loader(const char* x, const char* wqkv, int row, int col, int lw) {
    const int lane = lane_id(), xr = 16 * lw + (lane >> 3), wr = 16 * lw + (lane >> 4);
    const unsigned x0 = xr * 1024 + (((lane & 7) ^ ((xr >> 1) & 7)) << 4), x1 = (xr + 8) * 1024 + (((lane & 7) ^ (((xr + 8) >> 1) & 7)) << 4) - 1024;
    const unsigned w0 = wr * 3072 + (((lane & 15) ^ ((wr & 3) << 2)) << 4);
    const u32x4 rx = srd(x + (size_t)row * 65536, 65536), rw = srd(wqkv + col * 256, 512 * 3072 - col * 256);
    const int rot = (2 * (row + col)) & 6;
    auto issue = [&](int s, int slot) {
        const int kc = (s + rot) & 7;
        asm volatile("s_mov_b32 m0, %[m]\n\ts_nop 0\n\t"
                     "buffer_load_dwordx4 %[x0], %[rx], %[ox] offen lds\n\t"
                     "buffer_load_dwordx4 %[x1], %[rx], %[ox] offen offset:1024 lds\n\t"
                     "s_mov_b32 m0, %[m2]\n\ts_nop 0\n\t"
                     "buffer_load_dwordx4 %[w0], %[rw], %[ow] offen lds\n\t"
                     "buffer_load_dwordx4 %[w1], %[rw], %[ow] offen offset:1024 lds\n\t"
                     "buffer_load_dwordx4 %[w2], %[rw], %[ow] offen offset:2048 lds\n\t"
                     "buffer_load_dwordx4 %[w3], %[rw], %[ow] offen offset:3072 lds"
                     :: [m] "s"(slot * 24576 + lw * 2048), [m2] "s"(slot * 24576 + 8192 + lw * 4096), [ow] "s"(kc * 196608), [ox] "s"(kc * 128),
                        [rw] "s"(rw), [rx] "s"(rx), [x0] "v"(x0), [x1] "v"(x1),
                        [w0] "v"(w0), [w1] "v"(w0 + 11264), [w2] "v"(w0 + 22528), [w3] "v"(w0 + 33792)
                     : "memory");
    };
    issue(0, 0);
    __builtin_amdgcn_s_setprio(2);
    issue(1, 1);
    __builtin_amdgcn_s_setprio(1);
    issue(2, 2);
    __builtin_amdgcn_s_setprio(3);
    asm volatile("s_waitcnt vmcnt(12)\n\ts_barrier" ::: "memory");
    int slot = 0;
#pragma clang loop unroll(disable)
    for (int s = 3; s < 8; ++s) {
        asm volatile("s_waitcnt vmcnt(6)\n\ts_barrier" ::: "memory");
        issue(s, slot);
        slot = slot == 2 ? 0 : slot + 1;
    }
    asm volatile("s_waitcnt vmcnt(6)\n\ts_barrier\n\ts_waitcnt vmcnt(0)\n\ts_barrier" ::: "memory");
}
template <bool T>
DI void qkv_body(char* ws, char* lds, int row, int col, int a, int w, u64 tag) {
    const int lane = lane_id(), r = lane & 31, h2 = lane >> 5, q = (lane & 15) >> 2;
    f32x16 acc[2] = {};
    const int xa0 = (32 * (w & 1) + r) * 128 + ((h2 ^ ((r >> 1) & 7)) << 4);
    const int wa0 = 8192 + (8 * h2 + q) * 256 + (((2 * (w >> 1)) ^ q) << 6) + ((lane >> 4) & 1) * 32 + (lane & 3) * 8;
    bf16x8 xf[4], wf[2][2];
    auto rdw = [&](LAS const char* sl, int st, int i) {
        LAS const char* pw = sl + (wa0 ^ (i << 6)) + st * 4096;
        const s16x4 lo = trread(pw), hi = trread(pw + 1024);
        wf[st & 1][i] = __builtin_shufflevector(lo, hi, 0, 1, 2, 3, 4, 5, 6, 7);
    };
    auto mm1 = [&](int st, int i) {
        acc[i] = T ? MFMA32(wf[st & 1][i], xf[st], acc[i]) : MFMA32(xf[st], wf[st & 1][i], acc[i]);
        __builtin_amdgcn_sched_barrier(0);
    };
    auto step = [&](int st, LAS const char* sl, bool bar) {
        const int sn = (st + 2) & 3;
        mm1(st, 0);
        if (bar) wgbar();
        xf[sn] = *(LAS const bf16x8*)(sl + (xa0 ^ (sn << 5))); rdw(sl, sn, 0); __builtin_amdgcn_sched_barrier(0);
        mm1(st, 1);
        rdw(sl, sn, 1); __builtin_amdgcn_sched_barrier(0);
    };
    if (w == 0) *(LAS u32x4*)QCNT = u32x4{0u, 0u, (unsigned)tag, (unsigned)(tag >> 32)};
    wgbar();
    LAS const char* sl = (LAS const char*)lds;
#pragma unroll
    for (int st = 0; st < 2; ++st) { xf[st] = *(LAS const bf16x8*)(sl + (xa0 ^ (st << 5))); rdw(sl, st, 0); rdw(sl, st, 1); }
    __builtin_amdgcn_sched_barrier(0);
#pragma clang loop unroll(disable)
    for (int s = 0; s < 7; ++s) {
        step(0, sl, false); step(1, sl, false);
        sl = (sl == (LAS const char*)lds + 49152) ? (LAS const char*)lds : sl + 24576;
        step(2, sl, true); step(3, sl, false);
    }
    step(0, sl, false); step(1, sl, false);
    mm1(2, 0); mm1(2, 1); mm1(3, 0); mm1(3, 1);
#pragma unroll
    for (int i = 0; i < 2; ++i) qkv_store(ws, acc[i], col >> 2, 8 * (col & 3) + 4 * (w >> 1) + 2 * i, 2 * row + (w & 1), lane);
    publish4(ws + WS_FQ + a * 128, tag, lane);
}
DI void qkv2_loader(const char* x, const char* wqkv, int rowt, int col, int lw) {
    const int lane = lane_id(), row = 16 * lw + (lane >> 3);
    const unsigned w0 = row * 3072 + (((lane & 7) ^ (((row >> 1) & 1) << 2)) << 4), w1 = w0 + 8 * 3072 - 1024;
    const unsigned x0 = row * 1024 + (((lane & 7) ^ ((row >> 1) & 7)) << 4), x1 = (row + 8) * 1024 + (((lane & 7) ^ (((row + 8) >> 1) & 7)) << 4) - 1024;
    const u32x4 rx = srd(x + (size_t)rowt * 65536, 65536), rw = srd(wqkv + col * 128, 512 * 3072 - col * 128);
    auto issue = [&](int s) {
        asm volatile("s_mov_b32 m0, %[m]\n\ts_nop 0\n\t"
                     "buffer_load_dwordx4 %[w0], %[rw], %[ow] offen lds\n\t"
                     "buffer_load_dwordx4 %[w1], %[rw], %[ow] offen offset:1024 lds\n\t"
                     "s_add_u32 m0, %[m], 8192\n\ts_nop 0\n\t"
                     "buffer_load_dwordx4 %[x0], %[rx], %[ox] offen lds\n\t"
                     "buffer_load_dwordx4 %[x1], %[rx], %[ox] offen offset:1024 lds"
                     :: [m] "s"((s & 3) * 16384 + lw * 2048), [ow] "s"(s * 196608), [ox] "s"(s * 128), [rw] "s"(rw), [rx] "s"(rx),
                        [w0] "v"(w0), [w1] "v"(w1), [x0] "v"(x0), [x1] "v"(x1)
                     : "memory", "scc");
    };
    issue(0); issue(1); issue(2);
#pragma clang loop unroll(disable)
    for (int s = 3; s < 8; ++s) {
        asm volatile("s_waitcnt vmcnt(8)\n\ts_barrier" ::: "memory");
        issue(s);
    }
    asm volatile("s_waitcnt vmcnt(8)\n\ts_barrier\n\ts_waitcnt vmcnt(4)\n\ts_barrier\n\ts_waitcnt vmcnt(0)\n\ts_barrier" ::: "memory");
}
#define QRD3(B, SLOT, K)                                                                             \
    "ds_read_b64_tr_b16 v[" #B ":" #B "+1], %[t] offset:" #SLOT "*16384+" #K "*2048\n\t"              \
    "ds_read_b64_tr_b16 v[" #B "+2:" #B "+3], %[t] offset:" #SLOT "*16384+" #K "*2048+512\n\t"        \
    "ds_read_b128 v[" #B "+4:" #B "+7], %[x" #K "] offset:" #SLOT "*16384\n\t"
#define QSTAGE(SLOT, MM, N0, N1, N2, N3, P0, P1, P2, P3)                              \
    "s_waitcnt lgkmcnt(0)\n\t"                                                        \
    "s_barrier\n\t"                                                                   \
    MM(P0) QRD3(N0, SLOT, 0) MM(P1) QRD3(N1, SLOT, 1) MM(P2) QRD3(N2, SLOT, 2) MM(P3) QRD3(N3, SLOT, 3)
#define QST_E(SLOT, MM) QSTAGE(SLOT, MM, 32, 40, 48, 56, 96, 104, 112, 120)
#define QST_O(SLOT, MM) QSTAGE(SLOT, MM, 96, 104, 112, 120, 32, 40, 48, 56)
#define QMT(P) MF("v[16:31]", "v[" #P ":" #P "+3]", "v[" #P "+4:" #P "+7]", "v[16:31]")
#define QMV(P) MF("v[16:31]", "v[" #P "+4:" #P "+7]", "v[" #P ":" #P "+3]", "v[16:31]")
#define QTILE_ASM(MM)                                                                                             \
    asm volatile(                                                                                                 \
        "s_barrier\n\t"                                                                                           \
        QRD3(32, 0, 0) QRD3(40, 0, 1) QRD3(48, 0, 2) QRD3(56, 0, 3)                                               \
        QST_O(1, MM) QST_E(2, MM)                                                                                 \
        "s_mov_b32 s44, 0\n\t"                                                                                    \
        "LQ_loop%=:\n\t"                                                                                          \
        QST_O(3, MM)                                                                                              \
        "s_cmp_lg_u32 s44, 0\n\t"                                                                                 \
        "s_cbranch_scc1 LQ_end%=\n\t"                                                                             \
        QST_E(0, MM) QST_O(1, MM) QST_E(2, MM)                                                                    \
        "s_mov_b32 s44, 1\n\t"                                                                                    \
        "s_branch LQ_loop%=\n\t"                                                                                  \
        "LQ_end%=:\n\t"                                                                                           \
        "s_waitcnt lgkmcnt(0)\n\t"                                                                                \
        MM(96) MM(104) MM(112) MM(120)                                                                            \
        "s_nop 15\n\t"                                                                                            \
        : "+{v[16:31]}"(acc)                                                                                      \
        : [t] "v"(t), [x0] "v"(xa), [x1] "v"(xa ^ 32), [x2] "v"(xa ^ 64), [x3] "v"(xa ^ 96)                       \
        : "memory", "scc", "s44",                                                                                 \
          "v32", "v33", "v34", "v35", "v36", "v37", "v38", "v39", "v40", "v41", "v42", "v43", "v44", "v45", "v46", "v47", \
          "v48", "v49", "v50", "v51", "v52", "v53", "v54", "v55", "v56", "v57", "v58", "v59", "v60", "v61", "v62", "v63", \
          "v96", "v97", "v98", "v99", "v100", "v101", "v102", "v103", "v104", "v105", "v106", "v107", "v108", "v109", "v110", "v111", \
          "v112", "v113", "v114", "v115", "v116", "v117", "v118", "v119", "v120", "v121", "v122", "v123", "v124", "v125", "v126", "v127")
DI void qkv2_body(char* ws, int row, int col, int bi, int w, u64 tag) {
    const int lane = lane_id(), r = lane & 31, h2 = lane >> 5, q = (lane & 15) >> 2;
    const unsigned t = (8 * h2 + q) * 128 + (((w & 1) ^ ((q >> 1) & 1)) << 6) + ((lane >> 4) & 1) * 32 + (lane & 3) * 8;
    const unsigned xa = 8192 + (32 * (w >> 1) + r) * 128 + ((h2 ^ ((r >> 1) & 7)) << 4);
    f32x16 acc = {};
    if (col < 16) QTILE_ASM(QMT);
    else QTILE_ASM(QMV);
    qkv_store(ws, acc, col >> 3, 4 * (col & 7) + 2 * (w & 1), 2 * row + (w >> 1), lane);
    publish4(ws + WS_FQ + 32768 + bi * 128, tag, lane);
}

#define EXP(a) "v_exp_f32 v" #a ", v" #a "\n\t"
#define CVT(d, a, b) "v_cvt_pk_bf16_f32 v" #d ", v" #a ", v" #b "\n\t"
#define ASTEP(c0, c1, c2, c3, c4, c5, c6, c7, c8, c9, c10, c11, c12, c13, c14, c15, M1, M2, CL, M3)                  \
    EXP(c0) EXP(c1) M1 EXP(c2) EXP(c3) EXP(c4) EXP(c5) M2 CL                                                          \
    EXP(c6) EXP(c7) CVT(96, c0, c1) CVT(97, c2, c3) CVT(98, c4, c5) CVT(99, c6, c7) EXP(c8) EXP(c9) M3                \
    EXP(c10) EXP(c11) EXP(c12) EXP(c13) EXP(c14) EXP(c15)                                                             \
    CVT(100, c8, c9) CVT(101, c10, c11) CVT(102, c12, c13) CVT(103, c14, c15)
#define R_OA "v[64:79]"
#define R_OB "v[80:95]"
#define R_SA "v[32:47]"
#define R_SB "v[48:63]"
#define ATILE(R_NM, KC, KN, V0C, V1C, V0N, V1N, KOFF, VA, VOFF0, VOFF1, PRE)                                                    \
    ASTEP(32, 33, 34, 35, 36, 37, 38, 39, 40, 41, 42, 43, 44, 45, 46, 47,                                             \
          MF(R_OB, V1N, "v[100:103]", R_OB), MF(R_SB, KC, "v[8:11]", R_NM),                                           \
          PRE                                                                                                         \
          "ds_read_b128 " KC ", v12 offset:" KOFF "\n\t"                                                              \
          "ds_read_b128 " V0N ", " VA " offset:" #VOFF0 "\n\t"                                                        \
          "ds_read_b128 " V1N ", " VA " offset:" #VOFF1 "\n\t",                                                          \
          MF(R_OA, V0C, "v[96:99]", R_OA))                                                                            \
    ASTEP(48, 49, 50, 51, 52, 53, 54, 55, 56, 57, 58, 59, 60, 61, 62, 63,                                             \
          MF(R_OA, V1C, "v[100:103]", R_OA), MF(R_SA, KN, "v[4:7]", R_NM), "", MF(R_OB, V0C, "v[96:99]", R_OB))
#define ATILE_E(NM, KOFF, VA, VOFF0, VOFF1, PRE) ATILE(NM, "v[104:107]", "v[108:111]", "v[112:115]", "v[116:119]", "v[120:123]", "v[124:127]", KOFF, VA, VOFF0, VOFF1, PRE)
#define ATILE_O(NM, KOFF, VA, VOFF0, VOFF1, PRE) ATILE(NM, "v[108:111]", "v[104:107]", "v[120:123]", "v[124:127]", "v[112:115]", "v[116:119]", KOFF, VA, VOFF0, VOFF1, PRE)
#define WL "s_waitcnt lgkmcnt(0)\n\t"
#define AISSUE                                                   \
    "s_add_u32 m0, s44, %[lw]\n\t"                               \
    "s_add_u32 s44, s44, 0x4000\n\t"                             \
    "buffer_load_dwordx4 v14, %[rk], s43 offen sc1 lds\n\t"      \
    "s_add_u32 m0, m0, 0x2000\n\t"                               \
    "s_and_b32 s44, s44, 0xffff\n\t"                             \
    "buffer_load_dwordx4 v14, %[rv], s43 offen sc1 lds\n\t"      \
    "s_add_u32 s43, s43, 0x2000\n\t"
#define PRI "s_setprio 3\n\t"
#define PR2 "s_setprio 2\n\t"
#define PR0 "s_setprio 1\n\t"
#define PR1 "s_setprio 0\n\t"
constexpr int A_ONES = 65536, A_VOTE = 69632 + 512, A_LSUM = 71680;
struct AttnRegs { f32x16 oa, ob, nm; u32x4 p1, v1, fin; unsigned ka, va, va1, va2, va3, go, fo; };
DI unsigned qkv_dep(int a, bool early) {
    const int lane = lane_id(), r = lane & 15, v = (lane >> 4) & 1, R = 4 * (a >> 5) + (lane & 3);
    const int kv = (r >> 3) + 8 * ((early ? 8 + 8 * v : 16 * v + 8 * (a & 1)) + (r & 7));
    const int qq = ((R >> 3) & 1) + 8 * ((R >> 4) * 24 + (R & 7));
    return ((a & 6) + (lane < 32 || !early ? kv : qq)) * 128;
}
DI void attn_prep(AttnRegs& R, int w) {
    const int lane = lane_id(), r = lane & 31, kh = w >> 2;
    R.ka = 4096 * kh + lane * 16;
    R.va = r < 16 ? 8192 + 4096 * kh + (lane >> 5) * 256 + r * 16 : A_ONES + (r == 16 ? 0 : r == 20 ? 64 : (r > 20 && r < 28) ? 80 : 16);
    R.go = w * 1024 + lane * 16;
#pragma unroll
    for (int i = 0; i < 16; ++i) { R.oa[i] = 0.f; R.ob[i] = 0.f; }
    R.p1 = u32x4{0u, 0u, 0u, 0u};
    R.v1 = u32x4{0u, 0u, 0u, 0u};
    asm volatile("" : "+{v[64:79]}"(R.oa), "+{v[80:95]}"(R.ob), "+{v[100:103]}"(R.p1), "+{v[124:127]}"(R.v1),
                      "+{v12}"(R.ka), "+{v13}"(R.va), "+{v14}"(R.go));
}
#define ATTN_ASM(NM, NMIN) \
    asm volatile( \
        "v_and_b32 v15, 0x3ff, v14\n\t" \
        "s_mov_b32 s43, 0\n\t" \
        "s_mov_b32 s44, 0\n\t" \
        AISSUE \
        "global_load_dwordx4 v[4:7], v15, %[qa] sc1\n\t" \
        "global_load_dwordx4 v[8:11], v15, %[qb] sc1\n\t" \
        AISSUE AISSUE \
        "s_mov_b32 s40, 0xffff\n\t" \
        "s_mov_b32 s41, 0xffff\n\t" \
        "s_mov_b32 s42, 8\n\t" \
        "s_waitcnt vmcnt(4)\n\t" \
        "s_barrier\n\t" \
        "ds_read_b128 v[104:107], v12\n\t" \
        "ds_read_b128 v[108:111], v12 offset:1024\n\t" \
        "ds_read_b128 v[112:115], v13\n\t" \
        "ds_read_b128 v[116:119], v13 offset:512\n\t" \
        PR2 \
        "s_waitcnt lgkmcnt(3)\n\t" \
        MF(R_SA, "v[104:107]", "v[4:7]", NM) \
        "s_nop 15\n\t" \
        "LA_loop%=:\n\t" \
        ATILE_E(NM, "2048", "v13", 1024, 1536, WL PR0) \
        ATILE_O(NM, "3072", "v13", 2048, 2560, WL PR1) \
        ATILE_E(NM, "0", "v13", 3072, 3584, \
                "s_waitcnt vmcnt(2) lgkmcnt(0)\n\t" \
                "s_barrier\n\t" \
                "s_setprio 3\n\t" \
                "v_add_u32 v12, 0x4000, v12\n\t" \
                AISSUE \
                "v_and_b32 v12, 0xffff, v12\n\t" \
                PR2) \
        ATILE_O(NM, "1024", "v13", 0, 512, \
                WL \
                "s_mov_b64 exec, s[40:41]\n\t" \
                "v_add_u32 v13, 0x4000, v13\n\t" \
                "s_sub_u32 s42, s42, 1\n\t" \
                "v_and_b32 v13, 0xffff, v13\n\t" \
                "s_mov_b64 exec, -1\n\t" \
                "s_cmp_lg_u32 s42, 0\n\t") \
        "s_cbranch_scc1 LA_loop%=\n\t" \
        "s_waitcnt lgkmcnt(0)\n\t" \
        "s_setprio 3\n\t" \
        MF(R_OB, "v[124:127]", "v[100:103]", R_OB) \
        "s_nop 15\n\t" \
        : "+{v[64:79]}"(R.oa), "+{v[80:95]}"(R.ob), "+{v[100:103]}"(R.p1), "+{v[124:127]}"(R.v1), "+{v12}"(R.ka), "+{v13}"(R.va) \
        : NMIN "{v14}"(R.go), [qa] "s"(qa), [qb] "s"(qb), [rk] "s"(rk), [rv] "s"(rv), [lw] "s"(w * 1024) \
        : "memory", "scc", "vcc", "s40", "s41", "s42", "s43", "s44", "s45", "v0", "v1", "v4", "v5", "v6", "v7", "v8", "v9", "v10", "v11", "v15", \
          "v32", "v33", "v34", "v35", "v36", "v37", "v38", "v39", "v40", "v41", "v42", "v43", "v44", "v45", "v46", "v47", \
          "v48", "v49", "v50", "v51", "v52", "v53", "v54", "v55", "v56", "v57", "v58", "v59", "v60", "v61", "v62", "v63", \
          "v96", "v97", "v98", "v99", "v104", "v105", "v106", "v107", "v108", "v109", "v110", "v111", \
          "v112", "v113", "v114", "v115", "v116", "v117", "v118", "v119", "v120", "v121", "v122", "v123")
DI void attn_pass_shift(AttnRegs& R, const char* qa, const char* qb, const char* ksrc, const char* vsrc, int w) {
    const u32x4 rk = srd(ksrc, 65536), rv = srd(vsrc, 65536);
#define NM_IN "{v[16:31]}"(R.nm),
    ATTN_ASM("v[16:31]", NM_IN);
}
#define FISS(S)                                                  \
    "s_add_u32 m0, %[lw], " #S "*16384\n\t"                      \
    "s_add_u32 s43, s43, 0x2000\n\t"                             \
    "buffer_load_dwordx4 v14, %[rk], s43 offen sc1 lds\n\t"      \
    "s_add_u32 m0, %[lw], " #S "*16384+8192\n\t"                 \
    "s_nop 0\n\t"                                                \
    "buffer_load_dwordx4 v14, %[rv], s43 offen sc1 lds\n\t"
#define FI2(I) "s_cbranch_vccz 7f\n\t" PRI I "7:\n\t"
#define FI3(I) "s_cbranch_vccnz 7f\n\t" PRI I PR2 "7:\n\t"
#define FCHUNK(S, N, VC, VN, BND, I2, I3) FCHUNKX(S, N, VC, VN, BND, I2, I3, "")
#define FCHUNKX(S, N, VC, VN, BND, I2, I3, I0)                        \
    ATILE_E("0", #S "*16384+2048", VC, 1024, 1536, WL I0 PR0)       \
    ATILE_O("0", #S "*16384+3072", VC, 2048, 2560, WL PR1)       \
    ATILE_E("0", #N "*16384", VC, 3072, 3584, BND I2 PR2)    \
    ATILE_O("0", #N "*16384+1024", VN, 0, 512, WL I3)
#define FLD "global_load_dwordx2 v[0:1], v2, %[ff] sc1\n\t"
#define VCCSET "s_mov_b32 vcc_lo, %[lo4]\n\ts_mov_b32 vcc_hi, 0\n\t"
#define FBAR(VM) "s_waitcnt vmcnt(" #VM ") lgkmcnt(0)\n\ts_barrier\n\t"
DI void attn_pass(AttnRegs& R, const char* qa, const char* qb, const char* ksrc, const char* vsrc, const char* late_flags, u64 tag, int w) {
    const u32x4 rk = srd(ksrc, 65536), rv = srd(vsrc, 65536);
    asm volatile(
        "v_and_b32 v15, 0x3ff, v14\n\t"
        "s_mov_b32 s43, -0x2000\n\t"
        FISS(0)
        "global_load_dwordx4 v[4:7], v15, %[qa] sc1\n\t"
        "global_load_dwordx4 v[8:11], v15, %[qb] sc1\n\t"
        FISS(1) FISS(2) VCCSET
        "s_waitcnt vmcnt(4)\n\t"
        "s_barrier\n\t"
        "ds_read_b128 v[104:107], v12\n\t"
        "ds_read_b128 v[108:111], v12 offset:1024\n\t"
        "ds_read_b128 v[112:115], v13\n\t"
        "ds_read_b128 v[116:119], v13 offset:512\n\t"
        PR2
        "s_waitcnt lgkmcnt(3)\n\t"
        MF(R_SA, "v[104:107]", "v[4:7]", "0")
        "s_nop 15\n\t"
        FCHUNK(0, 1, "v13", "v16", FBAR(2), FI2(FISS(3)), FI3(FISS(3)))
        FCHUNK(1, 2, "v16", "v17", FBAR(2), "", "")
        FCHUNKX(2, 3, "v17", "v18",
               "s_waitcnt vmcnt(0) lgkmcnt(0)\n\t"
               "s_mov_b32 s45, 0x100000\n\t"
               "LA_p%=:\n\t"
               "v_cmp_ne_u64 vcc, %[tag], v[0:1]\n\t"
               "s_sub_u32 s45, s45, 1\n\t"
               "s_cbranch_scc1 LA_f%=\n\t"
               "s_cbranch_vccz LA_f%=\n\t"
               "global_load_dwordx2 v[0:1], v2, %[ff] sc1\n\t"
               "s_waitcnt vmcnt(0)\n\t"
               "s_branch LA_p%=\n\t"
               "LA_f%=:\n\t"
               "s_barrier\n\t" VCCSET, FI2(FISS(0) FISS(1)), FI3(FISS(0) FISS(1)), PRI FLD)
        FCHUNK(3, 0, "v18", "v13", FBAR(2), FI2(FISS(2)), FI3(FISS(2)))
        FCHUNK(0, 1, "v13", "v16", FBAR(2), FI2(FISS(3)), FI3(FISS(3)))
        FCHUNK(1, 2, "v16", "v17", FBAR(2), "", "")
        FCHUNK(2, 3, "v17", "v18", FBAR(0), "", "")
        FCHUNK(3, 0, "v18", "v13", WL, "", "")
        "s_waitcnt lgkmcnt(0)\n\t"
        "s_setprio 3\n\t"
        MF(R_OB, "v[124:127]", "v[100:103]", R_OB)
        "s_nop 15\n\t"
        "Lmk_hot_end:\n\t"
        : "+{v[64:79]}"(R.oa), "+{v[80:95]}"(R.ob), "+{v[100:103]}"(R.p1), "+{v[124:127]}"(R.v1), "+{v[20:23]}"(R.fin)
        : "{v12}"(R.ka), "{v13}"(R.va), "{v16}"(R.va1), "{v17}"(R.va2), "{v18}"(R.va3), "{v14}"(R.go), "{v2}"(R.fo),
          [qa] "s"(qa), [qb] "s"(qb), [rk] "s"(rk), [rv] "s"(rv), [lw] "s"(w * 1024), [tag] "s"(tag), [ff] "s"(late_flags), [lo4] "s"(1 - (w >> 2))
        : "memory", "scc", "vcc", "s43", "s45", "v0", "v1", "v4", "v5", "v6", "v7", "v8", "v9", "v10", "v11", "v15",
          "v32", "v33", "v34", "v35", "v36", "v37", "v38", "v39", "v40", "v41", "v42", "v43", "v44", "v45", "v46", "v47",
          "v48", "v49", "v50", "v51", "v52", "v53", "v54", "v55", "v56", "v57", "v58", "v59", "v60", "v61", "v62", "v63",
          "v96", "v97", "v98", "v99", "v104", "v105", "v106", "v107", "v108", "v109", "v110", "v111",
          "v112", "v113", "v114", "v115", "v116", "v117", "v118", "v119", "v120", "v121", "v122", "v123");
}

DI void wait_qkv(const char* ws, int a, u64 tag) { wait_flags<2>(ws + WS_FQ, qkv_dep(a, true), tag); }
DI void attn_role(const char* x, const char* wqkv, char* ws, char* lds, int a, int w) {
    const int kh = w >> 2;
    const int head = 4 * (a & 7) + ((a >> 3) & 3), qt = 8 * (a >> 5) + 2 * (w & 3);
    __builtin_amdgcn_s_setprio(3);
    u64 tag;
    const int ti = a >> 3, trow = 8 * (a & 1) + (ti & 7) + (ti >= 24 ? 16 : 0), tcol = (ti >= 24 ? 0 : 4 * (ti >> 3)) + ((a & 7) >> 1);
    if (w >= 4) {
        qkv_loader(x, wqkv, trow, tcol, w - 4);
        tag = uni64(*(LAS const u64*)(QCNT + 8));
        if (w == 4) wait_qkv(ws, a, tag);
    } else {
        tag = next_tag(ld_epoch(ws));
        if (tcol < 8) qkv_body<true>(ws, lds, trow, tcol, a, w, tag);
        else qkv_body<false>(ws, lds, trow, tcol, a, w, tag);
    }
    AttnRegs R;
    attn_prep(R, w);
    {
        const int lane = lane_id();
        const unsigned st = (lane & 16) ? 0u : 16384u;
        R.va1 = R.va + st; R.va2 = R.va1 + st; R.va3 = R.va2 + st;
        R.fo = qkv_dep(a, false);
        const int o = w * 512 + lane * 8;
        const unsigned one = ((o & 0x1b0) == 0) ? 0x3f803f80u : 0u;
        *(LAS u32x2*)((LAS char*)lds + A_ONES + o) = u32x2{one, one};
        R.fin = u32x4{(unsigned)(w * 2048 + lane * 16), (unsigned)((w ^ 4) * 2048 + lane * 16), (unsigned)(A_LSUM + w * 256 + lane * 4), (unsigned)(A_LSUM + (w ^ 4) * 256 + lane * 4)};
        asm volatile("" : "+{v16}"(R.va1), "+{v17}"(R.va2), "+{v18}"(R.va3), "+{v2}"(R.fo), "+{v[20:23]}"(R.fin));
    }
    wgbar();
    const char* ksrc = ws + WS_K + (size_t)head * 65536;
    const char* qsrc = ws + WS_Q + (size_t)(head * 64 + qt) * 1024;
    auto finish = [&](int att) {
        const f32x16 oa = R.oa, ob = R.ob;
        u32x4 fin = R.fin;
        if (att) {
            const int lane = lane_id();
            fin = u32x4{(unsigned)(w * 2048 + lane * 16), (unsigned)((w ^ 4) * 2048 + lane * 16), (unsigned)(A_LSUM + w * 256 + lane * 4), (unsigned)(A_LSUM + (w ^ 4) * 256 + lane * 4)};
        }
        LAS char* ex = (LAS char*)(uintptr_t)fin[0];
        LAS const char* exr = (LAS const char*)(uintptr_t)fin[1];
        *(LAS f32x4*)ex = f32x4{ob[0], ob[1], ob[2], ob[3]};
        *(LAS f32x4*)(ex + 1024) = f32x4{ob[4], ob[5], ob[6], ob[7]};
        *(LAS float*)(uintptr_t)fin[2] = ob[8];
        if (att == 0) {
            float big = fmaxf(oa[8], ob[8]) * 2.7e8f;
#pragma unroll
            for (int i = 0; i < 8; ++i) big = fmaxf(fmaxf(big, __builtin_fabsf(oa[i])), __builtin_fabsf(ob[i]));
            const bool bad = !__all(fminf(oa[8], ob[8]) > 8.1e-28f && big < 1.7e38f);
            ((LAS unsigned*)((LAS char*)lds + A_VOTE))[w] = bad;
        }
        wgbar();
        const f32x4 t0 = *(LAS const f32x4*)exr, t1 = *(LAS const f32x4*)(exr + 1024);
        const float t2 = *(LAS const float*)(uintptr_t)fin[3];
        unsigned redo = 0;
        if (att == 0) {
            const u32x4 v0 = *(LAS const u32x4*)((LAS char*)lds + A_VOTE), v1 = *(LAS const u32x4*)((LAS char*)lds + A_VOTE + 16);
            redo = __builtin_amdgcn_readfirstlane(v0[0] | v0[1] | v0[2] | v0[3] | v1[0] | v1[1] | v1[2] | v1[3]);
        }
        const float rinv = __builtin_amdgcn_rcpf(oa[8] + t2);
        u32x4 ov;
        ov[0] = cvtpk((oa[0] + t0[0]) * rinv, (oa[1] + t0[1]) * rinv);
        ov[1] = cvtpk((oa[2] + t0[2]) * rinv, (oa[3] + t0[3]) * rinv);
        ov[2] = cvtpk((oa[4] + t1[0]) * rinv, (oa[5] + t1[1]) * rinv);
        ov[3] = cvtpk((oa[6] + t1[2]) * rinv, (oa[7] + t1[3]) * rinv);
        if (att == 0 ? !redo : att == 1 + kh) {
            st16(ws + WS_O + (size_t)((qt + kh) * 32 + head) * 1024, fin[0] & 1023, ov);
            drain();
            st_flag(ws + WS_FO + ((qt + kh) * 32 + head) * 128, tag);
        }
        asm volatile("" : "+s"(redo));
        return redo;
    };
    attn_pass(R, qsrc + kh * 1024, qsrc + (1 - kh) * 1024, ksrc, ksrc + (WS_V - WS_K), ws + WS_FQ + 32768, tag, w);
    if (finish(0)) {
#pragma clang loop unroll(disable)
        for (int att = 1; att < 3; ++att) {
            drain();
            wgbar();
            const int lane = lane_id();
            const char* qa = qsrc + (att - 1) * 1024;
            bf16x8 qf;
            asm volatile("global_load_dwordx4 %0, %1, %2 sc1\n\ts_waitcnt vmcnt(0)" : "=v"(qf) : "v"(lane * 16), "s"(qa) : "memory");
            float mx = -INFINITY;
#pragma clang loop unroll(disable)
            for (int t = 0; t < 64; ++t) {
                bf16x8 kf;
                asm volatile("global_load_dwordx4 %0, %1, %2 sc1\n\ts_waitcnt vmcnt(0)" : "=v"(kf) : "v"(lane * 16), "s"(ksrc + t * 1024) : "memory");
                const f32x16 s = MFMA32(kf, qf, f32x16{});
#pragma unroll
                for (int i = 0; i < 16; ++i) mx = fmaxf(mx, s[i]);
            }
            mx = -swap32_max(mx);
            attn_prep(R, w);
#pragma unroll
            for (int i = 0; i < 16; ++i) R.nm[i] = mx;
            attn_pass_shift(R, qa, qa, ksrc, ksrc + (WS_V - WS_K), w);
            finish(att);
        }
    }
}

#define BGAP "s_sleep 1\n\t"
#define SISSUE                                                             \
    "s_mov_b32 m0, s44\n\t"                                                 \
    "s_add_u32 s45, %[ow], %[wst]\n\t"                                      \
    "buffer_load_dwordx4 %[g0], %[rw], %[ow] offen lds\n\t"                 \
    "buffer_load_dwordx4 %[g1], %[rw], %[ow] offen offset:1024 lds\n\t"     \
    "s_add_u32 m0, s44, 8192\n\t"                                           \
    "s_add_u32 s46, %[od], 4096\n\t"                                        \
    "buffer_load_dwordx4 %[d0], %[rd], %[od] offen sc1 lds\n\t"             \
    "buffer_load_dwordx4 %[d0], %[rd], %[od] offen offset:1024 sc1 lds\n\t" \
    "s_add_u32 s44, s44, 0x4000\n\t"                                        \
    "s_and_b32 s44, s44, 0xffff\n\t"                                        \
    "s_cmp_eq_u32 %[od], %[de]\n\t"                                         \
    "s_cselect_b32 %[ow], 0, s45\n\t"                                       \
    "s_cselect_b32 %[od], 0, s46\n\t"
#define SISW                                                               \
    "s_mov_b32 m0, s44\n\t"                                                 \
    "s_add_u32 s45, %[ow], %[wst]\n\t"                                      \
    "buffer_load_dwordx4 %[g0], %[rw], %[ow] offen lds\n\t"                 \
    "buffer_load_dwordx4 %[g1], %[rw], %[ow] offen offset:1024 lds\n\t"     \
    "s_add_u32 s44, s44, 0x4000\n\t"                                        \
    "s_mov_b32 %[ow], s45\n\t"
#define SISD                                                               \
    "s_add_u32 m0, s44, 8192\n\t"                                           \
    "s_add_u32 s44, s44, 0x4000\n\t"                                        \
    "buffer_load_dwordx4 %[d0], %[rd], %[od] offen sc1 lds\n\t"             \
    "buffer_load_dwordx4 %[d0], %[rd], %[od] offen offset:1024 sc1 lds\n\t" \
    "s_add_u32 %[od], %[od], 4096\n\t"
struct SG { u32x4 rw, rd; unsigned wst, de, ow, od, a, b, c; };
DI void sgemm_prew(SG& G, int lw) {
    asm volatile("s_mov_b32 s44, %[lo]\n\t" SISW SISW SISW
                 : [ow] "+s"(G.ow) : [wst] "s"(G.wst), [rw] "s"(G.rw), [lo] "s"(lw * 2048), [g0] "v"(G.a), [g1] "v"(G.b) : "memory", "scc", "s44", "s45");
}
#define WISSUE                                                              \
    "s_and_b32 s45, s44, 0xc000\n\t"                                        \
    "s_add_u32 m0, s45, %[lo2]\n\t"                                         \
    "s_lshl_b32 s45, s45, 3\n\t"                                            \
    "buffer_load_dwordx4 %[o0], %[ro], s45 offen lds\n\t"                   \
    "buffer_load_dwordx4 %[o1], %[ro], s45 offen offset:1024 lds\n\t"       \
    "buffer_load_dwordx4 %[o2], %[ro], s45 offen offset:2048 lds\n\t"       \
    "buffer_load_dwordx4 %[o3], %[ro], s45 offen offset:3072 lds\n\t"       \
    "s_add_u32 s44, s44, 0x4000\n\t"                                        \
    "s_and_b32 s44, s44, 0xffff\n\t"
template <int WO>
DI void sgemm_load(SG& G, int n, int lw, u32x4 ro, unsigned o0) {
    asm volatile(
        "s_mov_b32 s44, %[lo]\n\t"
        ".if %c[wo]\n\t"
        "s_sub_u32 s47, %[n], 5\n\t"
        SISD SISD SISD
        "s_waitcnt vmcnt(4)\n\ts_barrier\n\t" SISSUE
        "s_waitcnt vmcnt(6)\n\ts_barrier\n\t" SISSUE
        ".else\n\t"
        "s_sub_u32 s47, %[n], 3\n\t"
        SISSUE SISSUE SISSUE
        ".endif\n\t"
        "LL_loop%=:\n\t"
        "s_waitcnt vmcnt(8)\n\t"
        "s_barrier\n\t"
        SISSUE
        "s_sub_u32 s47, s47, 1\n\t"
        "s_cmp_lg_u32 s47, 0\n\t"
        "s_cbranch_scc1 LL_loop%=\n\t"
        ".if %c[wo]\n\t"
        "s_waitcnt vmcnt(8)\n\ts_barrier\n\t" WISSUE
        "s_waitcnt vmcnt(8)\n\ts_barrier\n\t" WISSUE
        "s_waitcnt vmcnt(8)\n\ts_barrier\n\t" WISSUE
        "s_barrier\n\t" WISSUE
        "s_barrier\n\t"
        ".else\n\t"
        "s_waitcnt vmcnt(8)\n\ts_barrier\n\t"
        "s_waitcnt vmcnt(4)\n\ts_barrier\n\t"
        "s_waitcnt vmcnt(0)\n\ts_barrier\n\t"
        "s_barrier\n\t"
        ".endif\n\t"
        : [ow] "+s"(G.ow), [od] "+s"(G.od)
        : [wst] "s"(G.wst), [rw] "s"(G.rw), [rd] "s"(G.rd), [lo] "s"(lw * 2048), [de] "s"(G.de), [n] "s"(n), [g0] "v"(G.a), [g1] "v"(G.b), [d0] "v"(G.c),
          [wo] "i"(WO), [ro] "s"(ro), [lo2] "s"(lw * 4096), [o0] "v"(o0), [o1] "v"(o0 + 7168), [o2] "v"(o0 + 14336), [o3] "v"(o0 + 21504)
        : "memory", "scc", "s44", "s45", "s46", "s47");
}
#define SRD3(B, SLOT, K, TD)                                                                         \
    "ds_read_b64_tr_b16 v[" #B ":" #B "+1], %[t] offset:" #SLOT "*16384+" #K "*2048\n\t"              \
    "ds_read_b64_tr_b16 v[" #B "+2:" #B "+3], %[t] offset:" #SLOT "*16384+" #K "*2048+" #TD "\n\t"    \
    "ds_read_b128 v[" #B "+4:" #B "+7], %[dl] offset:" #SLOT "*16384+" #K "*1024\n\t"
#define SSTAGE(SLOT, MM, TD, N0, N1, N2, N3, P0, P1, P2, P3)                          \
    "s_waitcnt lgkmcnt(0)\n\t"                                                        \
    "s_barrier\n\t"                                                                   \
    MM(P0) SRD3(N0, SLOT, 0, TD) SRD3(N1, SLOT, 1, TD)                             \
    MM(P1) SRD3(N2, SLOT, 2, TD) SRD3(N3, SLOT, 3, TD) BGAP                            \
    MM(P2) BGAP MM(P3)
#define SST_E(SLOT, MM, TD) SSTAGE(SLOT, MM, TD, 32, 40, 48, 56, 96, 104, 112, 120)
#define SST_O(SLOT, MM, TD) SSTAGE(SLOT, MM, TD, 96, 104, 112, 120, 32, 40, 48, 56)
#define SMM_WA(P) MF("v[64:79]", "v[" #P ":" #P "+3]", "v[" #P "+4:" #P "+7]", "v[64:79]")
#define SMM_WB(P) MF("v[80:95]", "v[" #P "+4:" #P "+7]", "v[" #P ":" #P "+3]", "v[80:95]")
#define SGEMM_ASM(ACC, MM, TD, NLOOP, TAIL, FIN)                                                                       \
    asm volatile(                                                                                                 \
        "s_mov_b32 s44, " #NLOOP "\n\ts_setprio 0\n\t"                                                                       \
        "s_barrier\n\t"                                                                                           \
        SRD3(32, 0, 0, TD) SRD3(40, 0, 1, TD) SRD3(48, 0, 2, TD) SRD3(56, 0, 3, TD)                               \
        "LS_loop%=:\n\t"                                                                                          \
        SST_O(1, MM, TD) SST_E(2, MM, TD) SST_O(3, MM, TD) SST_E(0, MM, TD)                                       \
        "s_sub_u32 s44, s44, 1\n\t"                                                                               \
        "s_cmp_lg_u32 s44, 0\n\t"                                                                                 \
        "s_cbranch_scc1 LS_loop%=\n\t"                                                                            \
        TAIL                                                                                                      \
        "s_waitcnt lgkmcnt(0)\n\t" FIN                                                                            \
        MM(96) MM(104) MM(112) MM(120)                                                                \
        "s_setprio 3\n\ts_nop 15\n\t"                                                                                            \
        : ACC(acc)                                                                                                \
        : [t] "v"(G.a), [dl] "v"(G.b)                                                                             \
        : "memory", "scc", "s44",                                                                                 \
          "v32", "v33", "v34", "v35", "v36", "v37", "v38", "v39", "v40", "v41", "v42", "v43", "v44", "v45", "v46", "v47", \
          "v48", "v49", "v50", "v51", "v52", "v53", "v54", "v55", "v56", "v57", "v58", "v59", "v60", "v61", "v62", "v63", \
          "v96", "v97", "v98", "v99", "v100", "v101", "v102", "v103", "v104", "v105", "v106", "v107", "v108", "v109", "v110", "v111", \
          "v112", "v113", "v114", "v115", "v116", "v117", "v118", "v119", "v120", "v121", "v122", "v123", "v124", "v125", "v126", "v127")

template <int MODE>
DI SG sgemm_setup(const char* wbase, unsigned wbytes, int wrow, const char* dbase, int dblk, int rot, int w) {
    const int lane = lane_id(), row = lane >> 3, lw = w & 3;
    const int q = (lane & 15) >> 2, h2 = lane >> 5;
    SG G;
    G.rw = srd(wbase, wbytes); G.rd = srd(dbase, 2 * dblk);
    G.wst = 64 * wrow; G.de = (MODE == 0 ? 31 : 21) * 4096; G.ow = rot * G.wst; G.od = rot * 4096;
    if (w >= 4) {
        G.a = (16 * lw + row) * wrow + (((lane & 7) ^ (((row >> 1) & 1) << 2)) << 4);
        G.b = G.a + 8 * wrow - 1024;
        G.c = (lw >> 1) * dblk + (lw & 1) * 2048 + lane * 16;
    } else {
        G.a = ((MODE == 0 ? 8 : 4) * h2 + q) * 128 + (((lw & 1) ^ ((q >> 1) & 1)) << 6) + ((lane >> 4) & 1) * 32 + (lane & 3) * 8;
        G.b = 8192 + (lw >> 1) * 4096 + lane * 16;
        G.c = 0;
    }
    asm volatile("" : "+v"(G.a), "+v"(G.b), "+v"(G.c));
    return G;
}
#define ACC0 "+{v[64:79]}"
#define ACC1 "+{v[80:95]}"
template <int MODE>
DI void sgemm_run(f32x16& acc, SG& G) {
    if (MODE == 0) SGEMM_ASM(ACC0, SMM_WA, 512, 7, SST_O(1, SMM_WA, 512) SST_E(2, SMM_WA, 512) SST_O(3, SMM_WA, 512), "s_setprio 3\n\t");
    else SGEMM_ASM(ACC1, SMM_WB, 1024, 5, SST_O(1, SMM_WB, 1024), "s_barrier\n\ts_setprio 3\n\t");
}
DI f32x4 sub4(const f32x16& a, int g) { return f32x4{a[4 * g], a[4 * g + 1], a[4 * g + 2], a[4 * g + 3]}; }

DI unsigned gelu2(float a, float b) {
    constexpr float c1 = -2.f * 0.7978845608028654f * 1.4426950408889634f, c2 = c1 * 0.044715f;
    const f32x2 u = {a, b};
    const f32x2 z = u * (u * u * c2 + c1);
    const f32x2 d = f32x2{__builtin_amdgcn_exp2f(z[0]), __builtin_amdgcn_exp2f(z[1])} + 1.f;
    const f32x2 o = u * f32x2{__builtin_amdgcn_rcpf(d[0]), __builtin_amdgcn_rcpf(d[1])};
    return cvtpk(o[0], o[1]);
}

#define WRD(R0, R1, R2, R3, STEP)                                                    \
    "ds_read_b64_tr_b16 " R0 ", %[t0] offset:" #STEP "*2048\n\t"                      \
    "ds_read_b64_tr_b16 " R1 ", %[t0] offset:" #STEP "*2048+1024\n\t"                 \
    "ds_read_b64_tr_b16 " R2 ", %[t1] offset:" #STEP "*2048\n\t"                      \
    "ds_read_b64_tr_b16 " R3 ", %[t1] offset:" #STEP "*2048+1024\n\t"
#define WRD0(S) WRD("v[32:33]", "v[34:35]", "v[36:37]", "v[38:39]", S)
#define WRD1(S) WRD("v[40:41]", "v[42:43]", "v[44:45]", "v[46:47]", S)
#define WRD2(S) WRD("v[48:49]", "v[50:51]", "v[52:53]", "v[54:55]", S)
#define WRD3(S) WRD("v[56:57]", "v[58:59]", "v[60:61]", "v[62:63]", S)
#define WMM(WAIT, W0, W1, O) "s_waitcnt " WAIT "\n\t" MF("v[96:111]", W0, O, "v[96:111]") MF("v[112:127]", W1, O, "v[112:127]")
#define OLD(K, R, V, OFF)                                             \
    "5:\n\t"                                                           \
    "s_bitcmp1_b32 s40, " #K "\n\t"                                    \
    "s_cbranch_scc1 6f\n\t"                                            \
    "global_load_dwordx2 v[24:25], %[fo], %[f] sc1\n\t"                       \
    "s_sub_u32 s41, s41, 1\n\t"                                        \
    "s_cselect_b32 s42, -1, 0\n\t"                                     \
    "s_waitcnt vmcnt(0)\n\t"                                           \
    "v_cmp_eq_u64 vcc, %[tag], v[24:25]\n\t"                                \
    "s_or_b32 s40, vcc_lo, s42\n\t"                                    \
    "s_bitcmp1_b32 s40, " #K "\n\t"                                    \
    "s_cbranch_scc1 6f\n\t"                                            \
    "s_sleep 1\n\t"                                                    \
    "s_branch 5b\n\t"                                                  \
    "6:\n\t"                                                           \
    "global_load_dwordx4 " R ", " V ", %[o] offset:" #OFF " sc1\n\t"
DI void wo_gemm(f32x16& a0, f32x16& a1, f32x4& k0, f32x4& k1, float& k2, u32x4& k3, const char* osrc, const char* fsrc, unsigned seen, u64 tag,
                unsigned t0, unsigned t1, unsigned d0, unsigned d1, unsigned fo) {
    asm volatile(
        WRD0(0) WRD1(1) WRD2(2) WRD3(3)
        "s_mov_b32 s40, %[seen]\n\t"
        "s_mov_b32 s41, 0x100000\n\t"
        "s_setprio 0\n\t"
        OLD(0, "v[64:67]", "%[d0]", 0) OLD(1, "v[68:71]", "%[d0]", 1024) OLD(2, "v[72:75]", "%[d0]", 2048) OLD(3, "v[76:79]", "%[d0]", 3072)
        OLD(4, "v[80:83]", "%[d1]", 0) OLD(5, "v[84:87]", "%[d1]", 1024) OLD(6, "v[88:91]", "%[d1]", 2048) OLD(7, "v[92:95]", "%[d1]", 3072)
        "s_setprio 3\n\t"
        "s_waitcnt vmcnt(7) lgkmcnt(0)\n\t"
        MF("v[96:111]", "v[32:35]", "v[64:67]", "0") MF("v[112:127]", "v[36:39]", "v[64:67]", "0")
        WRD0(4)
        WMM("vmcnt(6)", "v[40:43]", "v[44:47]", "v[68:71]") WRD1(5)
        WMM("vmcnt(5)", "v[48:51]", "v[52:55]", "v[72:75]") WRD2(6)
        WMM("vmcnt(4)", "v[56:59]", "v[60:63]", "v[76:79]") WRD3(7)
        WMM("vmcnt(3) lgkmcnt(12)", "v[32:35]", "v[36:39]", "v[80:83]")
        WMM("vmcnt(2) lgkmcnt(0)", "v[40:43]", "v[44:47]", "v[84:87]")
        WMM("vmcnt(1)", "v[48:51]", "v[52:55]", "v[88:91]")
        "s_barrier\n\t"
        WMM("vmcnt(0)", "v[56:59]", "v[60:63]", "v[92:95]")
        "s_nop 15\n\t"
        : "={v[96:111]}"(a0), "={v[112:127]}"(a1),
          "+{v[12:15]}"(k0), "+{v[16:19]}"(k1), "+{v20}"(k2), "+{v[28:31]}"(k3)
        : [o] "s"(osrc), [f] "s"(fsrc), [tag] "s"(tag), [seen] "s"(seen), [d0] "v"(d0), [d1] "v"(d1), [t0] "v"(t0), [t1] "v"(t1), [fo] "v"(fo)
        : "memory", "scc", "vcc", "s40", "s41", "s42", "v24", "v25",
          "v32", "v33", "v34", "v35", "v36", "v37", "v38", "v39", "v40", "v41", "v42", "v43", "v44", "v45", "v46", "v47",
          "v48", "v49", "v50", "v51", "v52", "v53", "v54", "v55", "v56", "v57", "v58", "v59", "v60", "v61", "v62", "v63",
          "v64", "v65", "v66", "v67", "v68", "v69", "v70", "v71", "v72", "v73", "v74", "v75", "v76", "v77", "v78", "v79",
          "v80", "v81", "v82", "v83", "v84", "v85", "v86", "v87", "v88", "v89", "v90", "v91", "v92", "v93", "v94", "v95");
}

DI void mixer_role(const char* x, const char* wqkv, const char* gw, const char* wo, const char* w1, const char* b1, const char* w2, const char* b2,
                   char* ws, char* out, char* lds, int bi, int w) {
    const int j = bi & 7, i = bi >> 3;
    __builtin_amdgcn_s_setprio(0);
    const u64 ep = ld_epoch(ws);
    u64 tag;
    {
        const int lane = lane_id(), h2 = lane >> 5, tid = 64 * w + lane;
        const u32x4 xr = *(GAS const u32x4*)(x + (size_t)(8 * bi + w) * 1024 + lane * 16);
        const u32x4 gr = *(GAS const u32x4*)(gw + lane * 16);
        tag = next_tag(ep);
        float v[8], gg[8];
#pragma unroll
        for (int k = 0; k < 4; ++k) { v[2 * k] = bflo(xr[k]); v[2 * k + 1] = bfhi(xr[k]); gg[2 * k] = bflo(gr[k]); gg[2 * k + 1] = bfhi(gr[k]); }
        float s1 = 0.f, s3 = 0.f;
#pragma unroll
        for (int k = 0; k < 8; ++k) { s1 += v[k]; s3 += v[k] * gg[k]; }
        const float mu = wave_sum(s1) * (1.f / 512.f);
        s3 = wave_sum(s3);
        float s2 = 0.f;
#pragma unroll
        for (int k = 0; k < 8; ++k) { v[k] -= mu; s2 += v[k] * v[k]; }
        const float rs = __builtin_amdgcn_rsqf(wave_sum(s2) * (1.f / 512.f) + 1e-5f);
        u32x4 hv;
#pragma unroll
        for (int k = 0; k < 4; ++k) hv[k] = cvtpk(v[2 * k] * rs, v[2 * k + 1] * rs);
        *(LAS u32x4*)((LAS char*)lds + w * 1024 + lane * 16) = hv;
        const float g = __builtin_amdgcn_rcpf(1.f + __builtin_amdgcn_exp2f(-1.4426950408889634f * s3));
        st4(ws + WS_G + (8 * bi + w) * 4, uni(g));
        wgbar();
        LAS const unsigned short* lb = (LAS const unsigned short*)lds + tid;
#pragma unroll
        for (int k = 0; k < 4; ++k) hv[k] = (unsigned)lb[(2 * k) * 512] | ((unsigned)lb[(2 * k + 1) * 512] << 16);
        st16(ws + WS_H + (size_t)(2 * w * 128 + (bi >> 1)) * 1024 + (bi & 1) * 512, h2 * 131072 + (lane & 31) * 16, hv);
        drain();
        wgbar();
        if (w == 0) st_flag(ws + WS_FL + bi * 128, tag);
    }
    __builtin_amdgcn_s_setprio(3);
    const int wb = w & 1, db = (w >> 1) & 1, hf = w >> 2;
    const int L = (j >> 1) * 22 + i, ft = L >> 2, dt1 = 4 * (j & 1) + (L & 3);
    const int st = 8 * (j >> 1) + (i & 7), dt = 4 * (j & 1) + (i >> 3);
    SG G1, G2;
    unsigned ex, l16, og, wof;
    {
        const int lane = lane_id();
        og = (64 * st + 32 * wb + (lane & 31)) * 4;
        G1 = sgemm_setup<0>(w1 + ft * 128, 2048 * 2752 - ft * 128, 2752, ws + WS_H + (size_t)2 * dt1 * 131072, 131072, (4 * dt1 + 10 * ft) & 31, w);
        G2 = sgemm_setup<1>(w2 + st * 128, 1376 * 4096 - st * 128, 4096, ws + WS_U + (size_t)2 * dt * 86 * 1024, 86 * 1024, ((st + dt) & 1) * 11, w);
        l16 = lane * 16;
        ex = 65536 + (w & 3) * 2048 + l16;
        wof = (32 * (w & 3) + (lane >> 3)) * 1024 + (((lane & 7) ^ (((lane >> 4) & 1) << 2)) << 4);
        asm volatile("" : "+v"(ex), "+v"(l16), "+v"(og), "+v"(wof));
    }
    {
        const int row = 16 + 8 * (j & 1) + (i & 7), col = 8 + 8 * (i >> 4) + 2 * (j >> 1) + ((i >> 3) & 1);
        if (w == 0) {
            *(LAS unsigned*)QCNT = 0u;
            wait_flags<2>(ws + WS_FQ + bi * 128, 0, tag);
        }
        wgbar();
        if (w >= 4) qkv2_loader(x, wqkv, row, col, w - 4);
        else qkv2_body(ws, row, col, bi, w, tag);
    }
    if (i >= 22) {
        const int wv = ((i - 22) * 8 + j) * 8 + w;
        __builtin_amdgcn_s_sleep(64);
        __builtin_amdgcn_s_sleep(64);
        warm(w2, 1376, wv, l16 * 4);
        warm(wo, 128, wv, l16 * 4);
    }
    if (i < 22) {
        if (w >= 4) wait_flags<8>(ws + WS_FL + (w - 4) * 8192, l16 * 8, tag);
        wgbar();
        f32x4 v0, v1;
        if (w >= 4) {
            sgemm_load<0>(G1, 32, w - 4, G1.rw, 0);
            v0 = *(LAS const f32x4*)(uintptr_t)ex; v1 = *(LAS const f32x4*)(uintptr_t)(ex + 1024);
        } else {
            f32x16 acc1;
            const char* bsrc = b1 + (64 * ft + (ft < 21 ? 32 * wb : 0)) * 2 + (l16 >> 9) * 8;
#pragma unroll
            for (int k = 0; k < 4; ++k) {
                const u32x2 bv = *(GAS const u32x2*)(bsrc + 16 * k);
                acc1[4 * k] = bflo(bv[0]); acc1[4 * k + 1] = bfhi(bv[0]); acc1[4 * k + 2] = bflo(bv[1]); acc1[4 * k + 3] = bfhi(bv[1]);
            }
            sgemm_run<0>(acc1, G1);
            *(LAS f32x4*)(uintptr_t)ex = sub4(acc1, 2); *(LAS f32x4*)(uintptr_t)(ex + 1024) = sub4(acc1, 3);
            wgbar();
            v0 = sub4(acc1, 0); v1 = sub4(acc1, 1);
        }
        const u32x4 o = {gelu2(v0[0], v0[1]), gelu2(v0[2], v0[3]), gelu2(v1[0], v1[1]), gelu2(v1[2], v1[3])};
        if (ft < 21 || wb == 0) st16(ws + WS_U + (size_t)((2 * dt1 + db) * 86 + 4 * ft + 2 * wb + hf) * 1024, l16, o);
        drain();
        wgbar();
        if (w == 0) st_flag(ws + WS_FU + (dt1 * 32 + ft) * 128, tag);
    }
    if (w >= 4) sgemm_prew(G2, w - 4);
    unsigned t0, t1, d1, fo;
    u32x4 fin;
    {
        const int lane = l16 >> 4, q = (lane & 15) >> 2, kq = 2 * db + hf;
        const unsigned tb = (w >> 1) * 16384 + (4 * (lane >> 5) + q) * 128 + ((lane >> 4) & 1) * 32 + (lane & 3) * 8;
        t0 = tb + ((q >> 1) & 1) * 64; t1 = tb + (((q >> 1) & 1) ^ 1) * 64; d1 = l16 + 4096; fo = (lane & 7) * 128;
        fin = u32x4{w * 8192 + l16, wb * 8192 + kq * 2048 + l16, (unsigned)((64 * st + 32 * wb + (lane & 31)) * 1024 + 128 * dt + kq * 32 + (lane >> 5) * 16), 0u};
        asm volatile("" : "+v"(t0), "+v"(t1), "+v"(d1), "+v"(fo), "+{v[28:31]}"(fin));
    }
    if (bi == 255 && w == 1) {
        wait_flags256(ws + WS_FQ, l16 * 8, tag);
        wait_flags256(ws + WS_FL, l16 * 8, tag);
        st_flag(ws + WS_EPOCH, tag);
    }
    if (w == 0) wait_flags<8>(ws + WS_FU + dt * 4096, (l16 < 22 * 16 ? l16 : 0) * 8, tag);
    wgbar();
    const char* fsrc = ws + WS_FO + ((2 * st + wb) * 32 + 8 * (w >> 1)) * 128;
    u64 f0;
    f32x4 bp0, bp1;
    float g;
    {
        asm volatile("global_load_dword %0, %1, %2 sc1" : "={v20}"(g) : "v"(og), "s"(ws + WS_G) : "memory");
        if (w >= 4) {
            sgemm_load<1>(G2, 22, w - 4, srd(wo + dt * 128, 524288 - dt * 128), wof);
            asm volatile("global_load_dwordx2 %0, %1, %2 sc1" : "={v[22:23]}"(f0) : "v"(fo), "s"(fsrc) : "memory");
            bp0 = *(LAS const f32x4*)(uintptr_t)ex; bp1 = *(LAS const f32x4*)(uintptr_t)(ex + 1024);
        } else {
            const float bb = bflo(*(GAS const unsigned short*)(b2 + (og >> 1)));
            f32x16 acc2 = {bb, bb, bb, bb, bb, bb, bb, bb, bb, bb, bb, bb, bb, bb, bb, bb};
            sgemm_run<1>(acc2, G2);
            asm volatile("global_load_dwordx2 %0, %1, %2 sc1" : "={v[22:23]}"(f0) : "v"(fo), "s"(fsrc) : "memory");
            *(LAS f32x4*)(uintptr_t)ex = sub4(acc2, 2); *(LAS f32x4*)(uintptr_t)(ex + 1024) = sub4(acc2, 3);
            wgbar();
            bp0 = sub4(acc2, 0); bp1 = sub4(acc2, 1);
            drain();
        }
        asm volatile("" : "+{v20}"(g));
        bp0 *= 1.f - g;
        bp1 *= 1.f - g;
        asm volatile("" : "+v"(bp0), "+v"(bp1));
    }
    asm volatile("s_waitcnt vmcnt(0)\n\ts_barrier" : "+{v[22:23]}"(f0)::"memory");
    f32x16 a0, a1;
    wo_gemm(a0, a1, bp0, bp1, g, fin, ws + WS_O + (size_t)((2 * st + wb) * 32 + 8 * (w >> 1)) * 1024,
            fsrc, (unsigned)__ballot(f0 == tag), tag, t0, t1, l16, d1, fo);
    {
        LAS char* ex = (LAS char*)(uintptr_t)fin[0];
#pragma unroll
        for (int k = 0; k < 4; ++k) { *(LAS f32x4*)(ex + k * 1024) = sub4(a0, k); *(LAS f32x4*)(ex + 4096 + k * 1024) = sub4(a1, k); }
        wgbar();
        LAS const char* exr = (LAS const char*)(uintptr_t)fin[1];
        f32x4 s0 = *(LAS const f32x4*)exr, s1 = *(LAS const f32x4*)(exr + 1024);
#pragma unroll
        for (int k = 1; k < 4; ++k) { s0 += *(LAS const f32x4*)(exr + k * 16384); s1 += *(LAS const f32x4*)(exr + k * 16384 + 1024); }
        s0 = s0 * g + bp0;
        s1 = s1 * g + bp1;
        const auto r0 = __builtin_amdgcn_permlane32_swap(cvtpk(s0[0], s0[1]), cvtpk(s1[0], s1[1]), false, false);
        const auto r1 = __builtin_amdgcn_permlane32_swap(cvtpk(s0[2], s0[3]), cvtpk(s1[2], s1[3]), false, false);
        asm volatile("global_store_dwordx4 %0, %1, %2" ::"v"(fin[2]), "v"(u32x4{r0[0], r1[0], r0[1], r1[1]}), "s"(out) : "memory");
    }
}

constexpr int MK_LDS = 81920;
__global__ __launch_bounds__(512, 4) void mk(const char* x, const char* wqkv, const char* wo, const char* w1, const char* b1,
                                             const char* w2, const char* b2, const char* gw, char* ws, char* out) {
    extern __shared__ __attribute__((aligned(16))) char lds[];
    const int w = __builtin_amdgcn_readfirstlane(threadIdx.x >> 6);
    if (blockIdx.x >= 256 && w == 7) {
        asm volatile("s_getpc_b64 s[40:41]\n\t"
                     "Lmk_hot_begin:\n\t"
                     "s_cmp_lt_u32 %[o], Lmk_hot_end-Lmk_hot_begin-4096\n\t"
                     "s_cbranch_scc0 1f\n\t"
                     "s_add_u32 s40, s40, %[o]\n\t"
                     "s_addc_u32 s41, s41, 0\n\t"
                     "s_and_b32 s40, s40, -4\n\t"
                     "s_mov_b32 m0, 0x13e00\n\t"
                     "s_nop 0\n\t"
                     "global_load_lds_dword %[v], s[40:41]\n\t"
                     "1:"
                     :: [o] "s"(((blockIdx.x - 256) >> 3) * 4096), [v] "v"(lane_id() * 64) : "memory", "scc", "s40", "s41");
    }
    if (blockIdx.x < 256) attn_role(x, wqkv, ws, lds, blockIdx.x, w);
    else mixer_role(x, wqkv, gw, wo, w1, b1, w2, b2, ws, out, lds, blockIdx.x - 256, w);
}

extern "C" void kernel_launch(void* const* d_in, const int* in_sizes, int n_in, void* d_out, int out_size, void* d_ws, size_t ws_size,
                              hipStream_t stream) {
    (void)hipFuncSetAttribute((const void*)mk, hipFuncAttributeMaxDynamicSharedMemorySize, MK_LDS);
    mk<<<512, 512, MK_LDS, stream>>>((const char*)d_in[0], (const char*)d_in[1], (const char*)d_in[2], (const char*)d_in[4],
                                     (const char*)d_in[5], (const char*)d_in[6], (const char*)d_in[7], (const char*)d_in[8],
                                     (char*)d_ws, (char*)d_out);
}
```

```cpp
#include <hip/hip_runtime.h>
#include <hip/hip_bf16.h>
#include <stdint.h>

typedef __attribute__((ext_vector_type(8))) short bf16x8;
typedef __attribute__((ext_vector_type(4))) short s16x4;
typedef __attribute__((ext_vector_type(16))) float f32x16;
typedef __attribute__((ext_vector_type(4))) float f32x4;
typedef __attribute__((ext_vector_type(2))) float f32x2;
typedef __attribute__((ext_vector_type(4))) unsigned u32x4;
typedef __attribute__((ext_vector_type(2))) unsigned u32x2;
typedef unsigned long long u64;
#define LAS __attribute__((address_space(3)))
#define GAS __attribute__((address_space(1)))
#define DI __device__ __forceinline__
#define MFMA32(a, b, c) __builtin_amdgcn_mfma_f32_32x32x16_bf16((a), (b), (c), 0, 0, 0)

constexpr size_t WS_EPOCH = 0;
constexpr size_t WS_G = 4096;
constexpr size_t WS_FL = 98304;
constexpr size_t WS_FQ = 458752;
constexpr size_t WS_FU = 65536;
constexpr size_t WS_FO = 131072;
constexpr size_t WS_Q = 1u << 20;
constexpr size_t WS_K = WS_Q + (2u << 20);
constexpr size_t WS_V = WS_K + (2u << 20);
constexpr size_t WS_H = WS_V + (2u << 20);
constexpr size_t WS_U = WS_H + (2u << 20);
constexpr size_t WS_O = WS_U + (2u << 20);

DI unsigned cvtpk(float lo, float hi) {
    typedef float f32x2_t __attribute__((ext_vector_type(2)));
    typedef __bf16 bf16x2_t __attribute__((ext_vector_type(2)));
    return __builtin_bit_cast(unsigned, __builtin_convertvector(f32x2_t{lo, hi}, bf16x2_t));
}
DI float bflo(unsigned u) { return __uint_as_float(u << 16); }
DI float bfhi(unsigned u) { return __uint_as_float(u & 0xffff0000u); }
DI s16x4 trread(LAS const char* p) {
    typedef short v4i16_t __attribute__((ext_vector_type(4)));
    return __builtin_bit_cast(s16x4, __builtin_amdgcn_ds_read_tr16_b64_v4i16((LAS v4i16_t*)p));
}
template <int CTRL> DI float dpp(float v) {
    return __int_as_float(__builtin_amdgcn_update_dpp(0, __float_as_int(v), CTRL, 0xf, 0xf, true));
}
DI float rl(float v, int l) { return __int_as_float(__builtin_amdgcn_readlane(__float_as_int(v), l)); }
DI float wave_sum(float v) {
    v += dpp<0xB1>(v); v += dpp<0x4E>(v); v += dpp<0x141>(v); v += dpp<0x140>(v);
    return (rl(v, 0) + rl(v, 16)) + (rl(v, 32) + rl(v, 48));
}
DI float swap32_max(float v) {
    auto rr = __builtin_amdgcn_permlane32_swap(__float_as_uint(v), __float_as_uint(v), false, false);
    return fmaxf(__uint_as_float(rr[0]), __uint_as_float(rr[1]));
}

DI int lane_id() {
    int l;
    asm volatile("v_mbcnt_lo_u32_b32 %0, -1, 0\n\tv_mbcnt_hi_u32_b32 %0, -1, %0" : "=v"(l));
    __builtin_assume(l >= 0 && l < 64);
    return l;
}
DI void st16(const char* sbase, unsigned voff, u32x4 v) {
    asm volatile("global_store_dwordx4 %0, %1, %2 sc1\n\ts_nop 1" ::"v"(voff), "v"(v), "s"(sbase) : "memory");
}
DI void st4(const char* p, unsigned v) {
    unsigned t0, t1;
    asm volatile("s_nop 1\n\tv_mov_b32 %0, %2\n\tv_mov_b32 %1, 0\n\ts_mov_b64 exec, 1\n\tglobal_store_dword %1, %0, %3 sc1\n\ts_mov_b64 exec, -1"
                 : "=&v"(t0), "=&v"(t1) : "s"(v), "s"(p) : "memory");
}
DI void st_flag(const char* p, u64 v) {
    u64 t0;
    unsigned t1;
    asm volatile("s_nop 1\n\tv_mov_b64 %0, %2\n\tv_mov_b32 %1, 0\n\ts_mov_b64 exec, 1\n\tglobal_store_dwordx2 %1, %0, %3 sc1\n\ts_mov_b64 exec, -1"
                 : "=&v"(t0), "=&v"(t1) : "s"(v), "s"(p) : "memory");
}
DI u64 uni64(u64 v) {
    return (u64)(unsigned)__builtin_amdgcn_readfirstlane((unsigned)v) | ((u64)(unsigned)__builtin_amdgcn_readfirstlane((unsigned)(v >> 32)) << 32);
}
DI u64 ld_epoch(const char* ws) { return __hip_atomic_load((GAS const u64*)(ws + WS_EPOCH), __ATOMIC_RELAXED, __HIP_MEMORY_SCOPE_AGENT); }
DI u64 next_tag(u64 e) { return uni64(e) * 6364136223846793005ull + 1442695040888963407ull; }
DI unsigned uni(float v) { return __builtin_amdgcn_readfirstlane(__float_as_uint(v)); }
DI void warm(const char* base, int nchunks, int first, unsigned l64) {
#pragma clang loop unroll(disable)
    for (int n = first; n < nchunks; n += 640) {
        const char* p = base + (size_t)((n & 1) * (nchunks >> 1) + (n >> 1)) * 4096;
        asm volatile("s_mov_b32 m0, 0x13e00\n\ts_nop 0\n\tglobal_load_lds_dword %0, %1" ::"v"(l64), "s"(p) : "memory");
    }
}
DI void drain() { asm volatile("s_waitcnt vmcnt(0)" ::: "memory"); }
DI void wgbar() { asm volatile("s_waitcnt lgkmcnt(0)\n\ts_barrier" ::: "memory"); }
template <int SLEEP>
DI void wait_flags(const char* sbase, unsigned voff, u64 tag) {
#pragma clang loop unroll(disable)
    for (unsigned spin = 0; spin < (1u << 20); ++spin) {
        u64 f;
        asm volatile("global_load_dwordx2 %0, %1, %2 sc1\n\ts_waitcnt vmcnt(0)" : "=v"(f) : "v"(voff), "s"(sbase) : "memory");
        if (__all(f == tag)) break;
        __builtin_amdgcn_s_sleep(SLEEP);
    }
}

DI void wait_flags256(const char* sbase, unsigned voff, u64 tag) {
#pragma clang loop unroll(disable)
    for (unsigned spin = 0; spin < (1u << 20); ++spin) {
        u64 f0, f1, f2, f3;
        asm volatile("global_load_dwordx2 %0, %4, %5 sc1\n\tglobal_load_dwordx2 %1, %4, %6 sc1\n\tglobal_load_dwordx2 %2, %4, %7 sc1\n\t"
                     "global_load_dwordx2 %3, %4, %8 sc1\n\ts_waitcnt vmcnt(0)"
                     : "=&v"(f0), "=&v"(f1), "=&v"(f2), "=&v"(f3)
                     : "v"(voff), "s"(sbase), "s"(sbase + 8192), "s"(sbase + 16384), "s"(sbase + 24576) : "memory");
        if (__all(f0 == tag && f1 == tag && f2 == tag && f3 == tag)) break;
        __builtin_amdgcn_s_sleep(8);
    }
}

constexpr int QCNT = 73728;
#define MF(D, A, B, C) "v_mfma_f32_32x32x16_bf16 " D ", " A ", " B ", " C "\n\t"
DI u32x4 srd(const char* base, unsigned bytes) {
    return u32x4{(unsigned)(uintptr_t)base, (unsigned)((uintptr_t)base >> 32) & 0xffffu, bytes, 0x00020000u};
}
DI void qkv_store(char* ws, const f32x16& acc, int type, int head, int tile, int lane) {
    const float sc = (type == 0) ? 0.25f * 1.4426950408889634f : 1.f;
    const char* dst = ws + WS_Q + (size_t)type * (2u << 20) + (size_t)(head * 64 + tile) * 1024;
    const unsigned vo = type < 2 ? lane * 16 : ((lane >> 4) & 1) * 65536 + (lane >> 5) * 256 + (lane & 15) * 16;
#pragma unroll
    for (int hf = 0; hf < 2; ++hf) {
        u32x4 o;
#pragma unroll
        for (int i = 0; i < 4; ++i) o[i] = cvtpk(acc[8 * hf + 2 * i] * sc, acc[8 * hf + 2 * i + 1] * sc);
        st16(dst + hf * (type < 2 ? 65536 : 512), vo, o);
    }
}
DI void publish4(const char* flag, u64 tag, int lane) {
    drain();
    unsigned arr = 0;
    if (lane == 0) arr = __hip_atomic_fetch_add((LAS unsigned*)QCNT, 1u, __ATOMIC_RELAXED, __HIP_MEMORY_SCOPE_WORKGROUP);
    if (__builtin_amdgcn_readfirstlane(arr) == 3) st_flag(flag, tag);
}
DI void qkv_loader(const char* x, const char* wqkv, int row, int col, int lw) {
    const int lane = lane_id(), xr = 16 * lw + (lane >> 3), wr = 16 * lw + (lane >> 4);
    const unsigned x0 = xr * 1024 + (((lane & 7) ^ ((xr >> 1) & 7)) << 4), x1 = (xr + 8) * 1024 + (((lane & 7) ^ (((xr + 8) >> 1) & 7)) << 4) - 1024;
    const unsigned w0 = wr * 3072 + (((lane & 15) ^ ((wr & 3) << 2)) << 4);
    const u32x4 rx = srd(x + (size_t)row * 65536, 65536), rw = srd(wqkv + col * 256, 512 * 3072 - col * 256);
    const int rot = (2 * (row + col)) & 6;
    auto issue = [&](int s, int slot) {
        const int kc = (s + rot) & 7;
        asm volatile("s_mov_b32 m0, %[m]\n\ts_nop 0\n\t"
                     "buffer_load_dwordx4 %[x0], %[rx], %[ox] offen lds\n\t"
                     "buffer_load_dwordx4 %[x1], %[rx], %[ox] offen offset:1024 lds\n\t"
                     "s_mov_b32 m0, %[m2]\n\ts_nop 0\n\t"
                     "buffer_load_dwordx4 %[w0], %[rw], %[ow] offen lds\n\t"
                     "buffer_load_dwordx4 %[w1], %[rw], %[ow] offen offset:1024 lds\n\t"
                     "buffer_load_dwordx4 %[w2], %[rw], %[ow] offen offset:2048 lds\n\t"
                     "buffer_load_dwordx4 %[w3], %[rw], %[ow] offen offset:3072 lds"
                     :: [m] "s"(slot * 24576 + lw * 2048), [m2] "s"(slot * 24576 + 8192 + lw * 4096), [ow] "s"(kc * 196608), [ox] "s"(kc * 128),
                        [rw] "s"(rw), [rx] "s"(rx), [x0] "v"(x0), [x1] "v"(x1),
                        [w0] "v"(w0), [w1] "v"(w0 + 11264), [w2] "v"(w0 + 22528), [w3] "v"(w0 + 33792)
                     : "memory");
    };
    issue(0, 0);
    __builtin_amdgcn_s_setprio(2);
    issue(1, 1);
    __builtin_amdgcn_s_setprio(1);
    issue(2, 2);
    __builtin_amdgcn_s_setprio(3);
    asm volatile("s_waitcnt vmcnt(12)\n\ts_barrier" ::: "memory");
    int slot = 0;
#pragma clang loop unroll(disable)
    for (int s = 3; s < 8; ++s) {
        asm volatile("s_waitcnt vmcnt(6)\n\ts_barrier" ::: "memory");
        issue(s, slot);
        slot = slot == 2 ? 0 : slot + 1;
    }
    asm volatile("s_waitcnt vmcnt(6)\n\ts_barrier\n\ts_waitcnt vmcnt(0)\n\ts_barrier" ::: "memory");
}
template <bool T>
DI void qkv_body(char* ws, char* lds, int row, int col, int a, int w, u64 tag) {
    const int lane = lane_id(), r = lane & 31, h2 = lane >> 5, q = (lane & 15) >> 2;
    f32x16 acc[2] = {};
    const int xa0 = (32 * (w & 1) + r) * 128 + ((h2 ^ ((r >> 1) & 7)) << 4);
    const int wa0 = 8192 + (8 * h2 + q) * 256 + (((2 * (w >> 1)) ^ q) << 6) + ((lane >> 4) & 1) * 32 + (lane & 3) * 8;
    bf16x8 xf[4], wf[2][2];
    auto rdw = [&](LAS const char* sl, int st, int i) {
        LAS const char* pw = sl + (wa0 ^ (i << 6)) + st * 4096;
        const s16x4 lo = trread(pw), hi = trread(pw + 1024);
        wf[st & 1][i] = __builtin_shufflevector(lo, hi, 0, 1, 2, 3, 4, 5, 6, 7);
    };
    auto mm1 = [&](int st, int i) {
        acc[i] = T ? MFMA32(wf[st & 1][i], xf[st], acc[i]) : MFMA32(xf[st], wf[st & 1][i], acc[i]);
        __builtin_amdgcn_sched_barrier(0);
    };
    auto step = [&](int st, LAS const char* sl, bool bar) {
        const int sn = (st + 2) & 3;
        mm1(st, 0);
        if (bar) wgbar();
        xf[sn] = *(LAS const bf16x8*)(sl + (xa0 ^ (sn << 5))); rdw(sl, sn, 0); __builtin_amdgcn_sched_barrier(0);
        mm1(st, 1);
        rdw(sl, sn, 1); __builtin_amdgcn_sched_barrier(0);
    };
    if (w == 0) *(LAS u32x4*)QCNT = u32x4{0u, 0u, (unsigned)tag, (unsigned)(tag >> 32)};
    wgbar();
    LAS const char* sl = (LAS const char*)lds;
#pragma unroll
    for (int st = 0; st < 2; ++st) { xf[st] = *(LAS const bf16x8*)(sl + (xa0 ^ (st << 5))); rdw(sl, st, 0); rdw(sl, st, 1); }
    __builtin_amdgcn_sched_barrier(0);
#pragma clang loop unroll(disable)
    for (int s = 0; s < 7; ++s) {
        step(0, sl, false); step(1, sl, false);
        sl = (sl == (LAS const char*)lds + 49152) ? (LAS const char*)lds : sl + 24576;
        step(2, sl, true); step(3, sl, false);
    }
    step(0, sl, false); step(1, sl, false);
    mm1(2, 0); mm1(2, 1); mm1(3, 0); mm1(3, 1);
#pragma unroll
    for (int i = 0; i < 2; ++i) qkv_store(ws, acc[i], col >> 2, 8 * (col & 3) + 4 * (w >> 1) + 2 * i, 2 * row + (w & 1), lane);
    publish4(ws + WS_FQ + a * 128, tag, lane);
}
DI void qkv2_loader(const char* x, const char* wqkv, int rowt, int col, int lw) {
    const int lane = lane_id(), row = 16 * lw + (lane >> 3);
    const unsigned w0 = row * 3072 + (((lane & 7) ^ (((row >> 1) & 1) << 2)) << 4), w1 = w0 + 8 * 3072 - 1024;
    const unsigned x0 = row * 1024 + (((lane & 7) ^ ((row >> 1) & 7)) << 4), x1 = (row + 8) * 1024 + (((lane & 7) ^ (((row + 8) >> 1) & 7)) << 4) - 1024;
    const u32x4 rx = srd(x + (size_t)rowt * 65536, 65536), rw = srd(wqkv + col * 128, 512 * 3072 - col * 128);
    auto issue = [&](int s) {
        asm volatile("s_mov_b32 m0, %[m]\n\ts_nop 0\n\t"
                     "buffer_load_dwordx4 %[w0], %[rw], %[ow] offen lds\n\t"
                     "buffer_load_dwordx4 %[w1], %[rw], %[ow] offen offset:1024 lds\n\t"
                     "s_add_u32 m0, %[m], 8192\n\ts_nop 0\n\t"
                     "buffer_load_dwordx4 %[x0], %[rx], %[ox] offen lds\n\t"
                     "buffer_load_dwordx4 %[x1], %[rx], %[ox] offen offset:1024 lds"
                     :: [m] "s"((s & 3) * 16384 + lw * 2048), [ow] "s"(s * 196608), [ox] "s"(s * 128), [rw] "s"(rw), [rx] "s"(rx),
                        [w0] "v"(w0), [w1] "v"(w1), [x0] "v"(x0), [x1] "v"(x1)
                     : "memory", "scc");
    };
    issue(0); issue(1); issue(2);
#pragma clang loop unroll(disable)
    for (int s = 3; s < 8; ++s) {
        asm volatile("s_waitcnt vmcnt(8)\n\ts_barrier" ::: "memory");
        issue(s);
    }
    asm volatile("s_waitcnt vmcnt(8)\n\ts_barrier\n\ts_waitcnt vmcnt(4)\n\ts_barrier\n\ts_waitcnt vmcnt(0)\n\ts_barrier" ::: "memory");
}
#define QRD3(B, SLOT, K)                                                                             \
    "ds_read_b64_tr_b16 v[" #B ":" #B "+1], %[t] offset:" #SLOT "*16384+" #K "*2048\n\t"              \
    "ds_read_b64_tr_b16 v[" #B "+2:" #B "+3], %[t] offset:" #SLOT "*16384+" #K "*2048+512\n\t"        \
    "ds_read_b128 v[" #B "+4:" #B "+7], %[x" #K "] offset:" #SLOT "*16384\n\t"
#define QSTAGE(SLOT, MM, N0, N1, N2, N3, P0, P1, P2, P3)                              \
    "s_waitcnt lgkmcnt(0)\n\t"                                                        \
    "s_barrier\n\t"                                                                   \
    MM(P0) QRD3(N0, SLOT, 0) MM(P1) QRD3(N1, SLOT, 1) MM(P2) QRD3(N2, SLOT, 2) MM(P3) QRD3(N3, SLOT, 3)
#define QST_E(SLOT, MM) QSTAGE(SLOT, MM, 32, 40, 48, 56, 96, 104, 112, 120)
#define QST_O(SLOT, MM) QSTAGE(SLOT, MM, 96, 104, 112, 120, 32, 40, 48, 56)
#define QMT(P) MF("v[16:31]", "v[" #P ":" #P "+3]", "v[" #P "+4:" #P "+7]", "v[16:31]")
#define QMV(P) MF("v[16:31]", "v[" #P "+4:" #P "+7]", "v[" #P ":" #P "+3]", "v[16:31]")
#define QTILE_ASM(MM)                                                                                             \
    asm volatile(                                                                                                 \
        "s_barrier\n\t"                                                                                           \
        QRD3(32, 0, 0) QRD3(40, 0, 1) QRD3(48, 0, 2) QRD3(56, 0, 3)                                               \
        QST_O(1, MM) QST_E(2, MM)                                                                                 \
        "s_mov_b32 s44, 0\n\t"                                                                                    \
        "LQ_loop%=:\n\t"                                                                                          \
        QST_O(3, MM)                                                                                              \
        "s_cmp_lg_u32 s44, 0\n\t"                                                                                 \
        "s_cbranch_scc1 LQ_end%=\n\t"                                                                             \
        QST_E(0, MM) QST_O(1, MM) QST_E(2, MM)                                                                    \
        "s_mov_b32 s44, 1\n\t"                                                                                    \
        "s_branch LQ_loop%=\n\t"                                                                                  \
        "LQ_end%=:\n\t"                                                                                           \
        "s_waitcnt lgkmcnt(0)\n\t"                                                                                \
        MM(96) MM(104) MM(112) MM(120)                                                                            \
        "s_nop 15\n\t"                                                                                            \
        : "+{v[16:31]}"(acc)                                                                                      \
        : [t] "v"(t), [x0] "v"(xa), [x1] "v"(xa ^ 32), [x2] "v"(xa ^ 64), [x3] "v"(xa ^ 96)                       \
        : "memory", "scc", "s44",                                                                                 \
          "v32", "v33", "v34", "v35", "v36", "v37", "v38", "v39", "v40", "v41", "v42", "v43", "v44", "v45", "v46", "v47", \
          "v48", "v49", "v50", "v51", "v52", "v53", "v54", "v55", "v56", "v57", "v58", "v59", "v60", "v61", "v62", "v63", \
          "v96", "v97", "v98", "v99", "v100", "v101", "v102", "v103", "v104", "v105", "v106", "v107", "v108", "v109", "v110", "v111", \
          "v112", "v113", "v114", "v115", "v116", "v117", "v118", "v119", "v120", "v121", "v122", "v123", "v124", "v125", "v126", "v127")
DI void qkv2_body(char* ws, int row, int col, int bi, int w, u64 tag) {
    const int lane = lane_id(), r = lane & 31, h2 = lane >> 5, q = (lane & 15) >> 2;
    const unsigned t = (8 * h2 + q) * 128 + (((w & 1) ^ ((q >> 1) & 1)) << 6) + ((lane >> 4) & 1) * 32 + (lane & 3) * 8;
    const unsigned xa = 8192 + (32 * (w >> 1) + r) * 128 + ((h2 ^ ((r >> 1) & 7)) << 4);
    f32x16 acc = {};
    if (col < 16) QTILE_ASM(QMT);
    else QTILE_ASM(QMV);
    qkv_store(ws, acc, col >> 3, 4 * (col & 7) + 2 * (w & 1), 2 * row + (w >> 1), lane);
    publish4(ws + WS_FQ + 32768 + bi * 128, tag, lane);
}

#define EXP(a) "v_exp_f32 v" #a ", v" #a "\n\t"
#define CVT(d, a, b) "v_cvt_pk_bf16_f32 v" #d ", v" #a ", v" #b "\n\t"
#define ASTEP(c0, c1, c2, c3, c4, c5, c6, c7, c8, c9, c10, c11, c12, c13, c14, c15, M1, M2, CL, M3)                  \
    EXP(c0) EXP(c1) M1 EXP(c2) EXP(c3) EXP(c4) EXP(c5) M2 CL                                                          \
    EXP(c6) EXP(c7) CVT(96, c0, c1) CVT(97, c2, c3) CVT(98, c4, c5) CVT(99, c6, c7) EXP(c8) EXP(c9) M3                \
    EXP(c10) EXP(c11) EXP(c12) EXP(c13) EXP(c14) EXP(c15)                                                             \
    CVT(100, c8, c9) CVT(101, c10, c11) CVT(102, c12, c13) CVT(103, c14, c15)
#define R_OA "v[64:79]"
#define R_OB "v[80:95]"
#define R_SA "v[32:47]"
#define R_SB "v[48:63]"
#define ATILE(R_NM, KC, KN, V0C, V1C, V0N, V1N, KOFF, VA, VOFF0, VOFF1, PRE)                                                    \
    ASTEP(32, 33, 34, 35, 36, 37, 38, 39, 40, 41, 42, 43, 44, 45, 46, 47,                                             \
          MF(R_OB, V1N, "v[100:103]", R_OB), MF(R_SB, KC, "v[8:11]", R_NM),                                           \
          PRE                                                                                                         \
          "ds_read_b128 " KC ", v12 offset:" KOFF "\n\t"                                                              \
          "ds_read_b128 " V0N ", " VA " offset:" #VOFF0 "\n\t"                                                        \
          "ds_read_b128 " V1N ", " VA " offset:" #VOFF1 "\n\t",                                                          \
          MF(R_OA, V0C, "v[96:99]", R_OA))                                                                            \
    ASTEP(48, 49, 50, 51, 52, 53, 54, 55, 56, 57, 58, 59, 60, 61, 62, 63,                                             \
          MF(R_OA, V1C, "v[100:103]", R_OA), MF(R_SA, KN, "v[4:7]", R_NM), "", MF(R_OB, V0C, "v[96:99]", R_OB))
#define ATILE_E(NM, KOFF, VA, VOFF0, VOFF1, PRE) ATILE(NM, "v[104:107]", "v[108:111]", "v[112:115]", "v[116:119]", "v[120:123]", "v[124:127]", KOFF, VA, VOFF0, VOFF1, PRE)
#define ATILE_O(NM, KOFF, VA, VOFF0, VOFF1, PRE) ATILE(NM, "v[108:111]", "v[104:107]", "v[120:123]", "v[124:127]", "v[112:115]", "v[116:119]", KOFF, VA, VOFF0, VOFF1, PRE)
#define WL "s_waitcnt lgkmcnt(0)\n\t"
#define AISSUE                                                   \
    "s_add_u32 m0, s44, %[lw]\n\t"                               \
    "s_add_u32 s44, s44, 0x4000\n\t"                             \
    "buffer_load_dwordx4 v14, %[rk], s43 offen sc1 lds\n\t"      \
    "s_add_u32 m0, m0, 0x2000\n\t"                               \
    "s_and_b32 s44, s44, 0xffff\n\t"                             \
    "buffer_load_dwordx4 v14, %[rv], s43 offen sc1 lds\n\t"      \
    "s_add_u32 s43, s43, 0x2000\n\t"
#define PRI "s_setprio 3\n\t"
#define PR2 "s_setprio 2\n\t"
#define PR0 "s_setprio 1\n\t"
#define PR1 "s_setprio 0\n\t"
constexpr int A_ONES = 65536, A_VOTE = 69632 + 512, A_LSUM = 71680;
struct AttnRegs { f32x16 oa, ob, nm; u32x4 p1, v1, fin; unsigned ka, va, va1, va2, va3, go, fo; };
DI unsigned qkv_dep(int a, bool early) {
    const int lane = lane_id(), r = lane & 15, v = (lane >> 4) & 1, R = 4 * (a >> 5) + (lane & 3);
    const int kv = (r >> 3) + 8 * ((early ? 8 + 8 * v : 16 * v + 8 * (a & 1)) + (r & 7));
    const int qq = ((R >> 3) & 1) + 8 * ((R >> 4) * 24 + (R & 7));
    return ((a & 6) + (lane < 32 || !early ? kv : qq)) * 128;
}
DI void attn_prep(AttnRegs& R, int w) {
    const int lane = lane_id(), r = lane & 31, kh = w >> 2;
    R.ka = 4096 * kh + lane * 16;
    R.va = r < 16 ? 8192 + 4096 * kh + (lane >> 5) * 256 + r * 16 : A_ONES + (r == 16 ? 0 : r == 20 ? 64 : (r > 20 && r < 28) ? 80 : 16);
    R.go = w * 1024 + lane * 16;
#pragma unroll
    for (int i = 0; i < 16; ++i) { R.oa[i] = 0.f; R.ob[i] = 0.f; }
    R.p1 = u32x4{0u, 0u, 0u, 0u};
    R.v1 = u32x4{0u, 0u, 0u, 0u};
    asm volatile("" : "+{v[64:79]}"(R.oa), "+{v[80:95]}"(R.ob), "+{v[100:103]}"(R.p1), "+{v[124:127]}"(R.v1),
                      "+{v12}"(R.ka), "+{v13}"(R.va), "+{v14}"(R.go));
}
#define ATTN_ASM(NM, NMIN) \
    asm volatile( \
        "v_and_b32 v15, 0x3ff, v14\n\t" \
        "s_mov_b32 s43, 0\n\t" \
        "s_mov_b32 s44, 0\n\t" \
        AISSUE \
        "global_load_dwordx4 v[4:7], v15, %[qa] sc1\n\t" \
        "global_load_dwordx4 v[8:11], v15, %[qb] sc1\n\t" \
        AISSUE AISSUE \
        "s_mov_b32 s40, 0xffff\n\t" \
        "s_mov_b32 s41, 0xffff\n\t" \
        "s_mov_b32 s42, 8\n\t" \
        "s_waitcnt vmcnt(4)\n\t" \
        "s_barrier\n\t" \
        "ds_read_b128 v[104:107], v12\n\t" \
        "ds_read_b128 v[108:111], v12 offset:1024\n\t" \
        "ds_read_b128 v[112:115], v13\n\t" \
        "ds_read_b128 v[116:119], v13 offset:512\n\t" \
        PR2 \
        "s_waitcnt lgkmcnt(3)\n\t" \
        MF(R_SA, "v[104:107]", "v[4:7]", NM) \
        "s_nop 15\n\t" \
        "LA_loop%=:\n\t" \
        ATILE_E(NM, "2048", "v13", 1024, 1536, WL PR0) \
        ATILE_O(NM, "3072", "v13", 2048, 2560, WL PR1) \
        ATILE_E(NM, "0", "v13", 3072, 3584, \
                "s_waitcnt vmcnt(2) lgkmcnt(0)\n\t" \
                "s_barrier\n\t" \
                "s_setprio 3\n\t" \
                "v_add_u32 v12, 0x4000, v12\n\t" \
                AISSUE \
                "v_and_b32 v12, 0xffff, v12\n\t" \
                PR2) \
        ATILE_O(NM, "1024", "v13", 0, 512, \
                WL \
                "s_mov_b64 exec, s[40:41]\n\t" \
                "v_add_u32 v13, 0x4000, v13\n\t" \
                "s_sub_u32 s42, s42, 1\n\t" \
                "v_and_b32 v13, 0xffff, v13\n\t" \
                "s_mov_b64 exec, -1\n\t" \
                "s_cmp_lg_u32 s42, 0\n\t") \
        "s_cbranch_scc1 LA_loop%=\n\t" \
        "s_waitcnt lgkmcnt(0)\n\t" \
        "s_setprio 3\n\t" \
        MF(R_OB, "v[124:127]", "v[100:103]", R_OB) \
        "s_nop 15\n\t" \
        : "+{v[64:79]}"(R.oa), "+{v[80:95]}"(R.ob), "+{v[100:103]}"(R.p1), "+{v[124:127]}"(R.v1), "+{v12}"(R.ka), "+{v13}"(R.va) \
        : NMIN "{v14}"(R.go), [qa] "s"(qa), [qb] "s"(qb), [rk] "s"(rk), [rv] "s"(rv), [lw] "s"(w * 1024) \
        : "memory", "scc", "vcc", "s40", "s41", "s42", "s43", "s44", "s45", "v0", "v1", "v4", "v5", "v6", "v7", "v8", "v9", "v10", "v11", "v15", \
          "v32", "v33", "v34", "v35", "v36", "v37", "v38", "v39", "v40", "v41", "v42", "v43", "v44", "v45", "v46", "v47", \
          "v48", "v49", "v50", "v51", "v52", "v53", "v54", "v55", "v56", "v57", "v58", "v59", "v60", "v61", "v62", "v63", \
          "v96", "v97", "v98", "v99", "v104", "v105", "v106", "v107", "v108", "v109", "v110", "v111", \
          "v112", "v113", "v114", "v115", "v116", "v117", "v118", "v119", "v120", "v121", "v122", "v123")
DI void attn_pass_shift(AttnRegs& R, const char* qa, const char* qb, const char* ksrc, const char* vsrc, int w) {
    const u32x4 rk = srd(ksrc, 65536), rv = srd(vsrc, 65536);
#define NM_IN "{v[16:31]}"(R.nm),
    ATTN_ASM("v[16:31]", NM_IN);
}
#define FISS(S)                                                  \
    "s_add_u32 m0, %[lw], " #S "*16384\n\t"                      \
    "s_add_u32 s43, s43, 0x2000\n\t"                             \
    "buffer_load_dwordx4 v14, %[rk], s43 offen sc1 lds\n\t"      \
    "s_add_u32 m0, %[lw], " #S "*16384+8192\n\t"                 \
    "s_nop 0\n\t"                                                \
    "buffer_load_dwordx4 v14, %[rv], s43 offen sc1 lds\n\t"
#define FCHUNK(S, N, VC, VN, I0, BND, I3)                        \
    ATILE_E("0", #S "*16384+2048", VC, 1024, 1536, WL I0 PR0)    \
    ATILE_O("0", #S "*16384+3072", VC, 2048, 2560, WL PR1)       \
    ATILE_E("0", #N "*16384", VC, 3072, 3584, BND PR2)           \
    ATILE_O("0", #N "*16384+1024", VN, 0, 512, WL I3)
#define FLD "global_load_dwordx2 v[0:1], v2, %[ff] sc1\n\t"
#define FBAR(VM) "s_waitcnt vmcnt(" #VM ") lgkmcnt(0)\n\ts_barrier\n\t"
DI void attn_pass(AttnRegs& R, const char* qa, const char* qb, const char* ksrc, const char* vsrc, const char* late_flags, u64 tag, int w) {
    const u32x4 rk = srd(ksrc, 65536), rv = srd(vsrc, 65536);
    asm volatile(
        "v_and_b32 v15, 0x3ff, v14\n\t"
        "s_mov_b32 s43, -0x2000\n\t"
        FISS(0)
        "global_load_dwordx4 v[4:7], v15, %[qa] sc1\n\t"
        "global_load_dwordx4 v[8:11], v15, %[qb] sc1\n\t"
        FISS(1) FISS(2)
        "s_waitcnt vmcnt(4)\n\t"
        "s_barrier\n\t"
        "ds_read_b128 v[104:107], v12\n\t"
        "ds_read_b128 v[108:111], v12 offset:1024\n\t"
        "ds_read_b128 v[112:115], v13\n\t"
        "ds_read_b128 v[116:119], v13 offset:512\n\t"
        PR2
        "s_waitcnt lgkmcnt(3)\n\t"
        MF(R_SA, "v[104:107]", "v[4:7]", "0")
        "s_nop 15\n\t"
        FCHUNK(0, 1, "v13", "v16", "", FBAR(2), PRI FISS(3) PR2)
        FCHUNK(1, 2, "v16", "v17", "", FBAR(2), "")
        FCHUNK(2, 3, "v17", "v18", PRI FLD,
               "s_waitcnt vmcnt(0) lgkmcnt(0)\n\t"
               "s_mov_b32 s45, 0x100000\n\t"
               "LA_p%=:\n\t"
               "v_cmp_ne_u64 vcc, %[tag], v[0:1]\n\t"
               "s_sub_u32 s45, s45, 1\n\t"
               "s_cbranch_scc1 LA_f%=\n\t"
               "s_cbranch_vccz LA_f%=\n\t"
               "global_load_dwordx2 v[0:1], v2, %[ff] sc1\n\t"
               "s_waitcnt vmcnt(0)\n\t"
               "s_branch LA_p%=\n\t"
               "LA_f%=:\n\t"
               "s_barrier\n\t", PRI FISS(0) FISS(1) PR2)
        FCHUNK(3, 0, "v18", "v13", "", FBAR(2), PRI FISS(2) PR2)
        FCHUNK(0, 1, "v13", "v16", "", FBAR(2), PRI FISS(3) PR2)
        FCHUNK(1, 2, "v16", "v17", "", FBAR(2), "")
        FCHUNK(2, 3, "v17", "v18", "", FBAR(0), "")
        FCHUNK(3, 0, "v18", "v13", "", WL, "")
        "s_waitcnt lgkmcnt(0)\n\t"
        "s_setprio 3\n\t"
        MF(R_OB, "v[124:127]", "v[100:103]", R_OB)
        "s_nop 15\n\t"
        "Lmk_hot_end:\n\t"
        : "+{v[64:79]}"(R.oa), "+{v[80:95]}"(R.ob), "+{v[100:103]}"(R.p1), "+{v[124:127]}"(R.v1), "+{v[20:23]}"(R.fin)
        : "{v12}"(R.ka), "{v13}"(R.va), "{v16}"(R.va1), "{v17}"(R.va2), "{v18}"(R.va3), "{v14}"(R.go), "{v2}"(R.fo),
          [qa] "s"(qa), [qb] "s"(qb), [rk] "s"(rk), [rv] "s"(rv), [lw] "s"(w * 1024), [tag] "s"(tag), [ff] "s"(late_flags)
        : "memory", "scc", "vcc", "s43", "s45", "v0", "v1", "v4", "v5", "v6", "v7", "v8", "v9", "v10", "v11", "v15",
          "v32", "v33", "v34", "v35", "v36", "v37", "v38", "v39", "v40", "v41", "v42", "v43", "v44", "v45", "v46", "v47",
          "v48", "v49", "v50", "v51", "v52", "v53", "v54", "v55", "v56", "v57", "v58", "v59", "v60", "v61", "v62", "v63",
          "v96", "v97", "v98", "v99", "v104", "v105", "v106", "v107", "v108", "v109", "v110", "v111",
          "v112", "v113", "v114", "v115", "v116", "v117", "v118", "v119", "v120", "v121", "v122", "v123");
}

DI void wait_qkv(const char* ws, int a, u64 tag) { wait_flags<2>(ws + WS_FQ, qkv_dep(a, true), tag); }
DI void attn_role(const char* x, const char* wqkv, char* ws, char* lds, int a, int w) {
    const int kh = w >> 2;
    const int head = 4 * (a & 7) + ((a >> 3) & 3), qt = 8 * (a >> 5) + 2 * (w & 3);
    __builtin_amdgcn_s_setprio(3);
    u64 tag;
    const int ti = a >> 3, trow = 8 * (a & 1) + (ti & 7) + (ti >= 24 ? 16 : 0), tcol = (ti >= 24 ? 0 : 4 * (ti >> 3)) + ((a & 7) >> 1);
    if (w >= 4) {
        qkv_loader(x, wqkv, trow, tcol, w - 4);
        tag = uni64(*(LAS const u64*)(QCNT + 8));
        if (w == 4) wait_qkv(ws, a, tag);
    } else {
        tag = next_tag(ld_epoch(ws));
        if (tcol < 8) qkv_body<true>(ws, lds, trow, tcol, a, w, tag);
        else qkv_body<false>(ws, lds, trow, tcol, a, w, tag);
    }
    AttnRegs R;
    attn_prep(R, w);
    {
        const int lane = lane_id();
        const unsigned st = (lane & 16) ? 0u : 16384u;
        R.va1 = R.va + st; R.va2 = R.va1 + st; R.va3 = R.va2 + st;
        R.fo = qkv_dep(a, false);
        const int o = w * 512 + lane * 8;
        const unsigned one = ((o & 0x1b0) == 0) ? 0x3f803f80u : 0u;
        *(LAS u32x2*)((LAS char*)lds + A_ONES + o) = u32x2{one, one};
        R.fin = u32x4{(unsigned)(w * 2048 + lane * 16), (unsigned)((w ^ 4) * 2048 + lane * 16), (unsigned)(A_LSUM + w * 256 + lane * 4), (unsigned)(A_LSUM + (w ^ 4) * 256 + lane * 4)};
        asm volatile("" : "+{v16}"(R.va1), "+{v17}"(R.va2), "+{v18}"(R.va3), "+{v2}"(R.fo), "+{v[20:23]}"(R.fin));
    }
    wgbar();
    const char* ksrc = ws + WS_K + (size_t)head * 65536;
    const char* qsrc = ws + WS_Q + (size_t)(head * 64 + qt) * 1024;
    auto finish = [&](int att) {
        const f32x16 oa = R.oa, ob = R.ob;
        u32x4 fin = R.fin;
        if (att) {
            const int lane = lane_id();
            fin = u32x4{(unsigned)(w * 2048 + lane * 16), (unsigned)((w ^ 4) * 2048 + lane * 16), (unsigned)(A_LSUM + w * 256 + lane * 4), (unsigned)(A_LSUM + (w ^ 4) * 256 + lane * 4)};
        }
        LAS char* ex = (LAS char*)(uintptr_t)fin[0];
        LAS const char* exr = (LAS const char*)(uintptr_t)fin[1];
        *(LAS f32x4*)ex = f32x4{ob[0], ob[1], ob[2], ob[3]};
        *(LAS f32x4*)(ex + 1024) = f32x4{ob[4], ob[5], ob[6], ob[7]};
        *(LAS float*)(uintptr_t)fin[2] = ob[8];
        if (att == 0) {
            float big = fmaxf(oa[8], ob[8]) * 2.7e8f;
#pragma unroll
            for (int i = 0; i < 8; ++i) big = fmaxf(fmaxf(big, __builtin_fabsf(oa[i])), __builtin_fabsf(ob[i]));
            const bool bad = !__all(fminf(oa[8], ob[8]) > 8.1e-28f && big < 1.7e38f);
            ((LAS unsigned*)((LAS char*)lds + A_VOTE))[w] = bad;
        }
        wgbar();
        const f32x4 t0 = *(LAS const f32x4*)exr, t1 = *(LAS const f32x4*)(exr + 1024);
        const float t2 = *(LAS const float*)(uintptr_t)fin[3];
        unsigned redo = 0;
        if (att == 0) {
            const u32x4 v0 = *(LAS const u32x4*)((LAS char*)lds + A_VOTE), v1 = *(LAS const u32x4*)((LAS char*)lds + A_VOTE + 16);
            redo = __builtin_amdgcn_readfirstlane(v0[0] | v0[1] | v0[2] | v0[3] | v1[0] | v1[1] | v1[2] | v1[3]);
        }
        const float rinv = __builtin_amdgcn_rcpf(oa[8] + t2);
        u32x4 ov;
        ov[0] = cvtpk((oa[0] + t0[0]) * rinv, (oa[1] + t0[1]) * rinv);
        ov[1] = cvtpk((oa[2] + t0[2]) * rinv, (oa[3] + t0[3]) * rinv);
        ov[2] = cvtpk((oa[4] + t1[0]) * rinv, (oa[5] + t1[1]) * rinv);
        ov[3] = cvtpk((oa[6] + t1[2]) * rinv, (oa[7] + t1[3]) * rinv);
        if (att == 0 ? !redo : att == 1 + kh) {
            st16(ws + WS_O + (size_t)((qt + kh) * 32 + head) * 1024, fin[0] & 1023, ov);
            drain();
            st_flag(ws + WS_FO + ((qt + kh) * 32 + head) * 128, tag);
        }
        asm volatile("" : "+s"(redo));
        return redo;
    };
    attn_pass(R, qsrc + kh * 1024, qsrc + (1 - kh) * 1024, ksrc, ksrc + (WS_V - WS_K), ws + WS_FQ + 32768, tag, w);
    if (finish(0)) {
#pragma clang loop unroll(disable)
        for (int att = 1; att < 3; ++att) {
            drain();
            wgbar();
            const int lane = lane_id();
            const char* qa = qsrc + (att - 1) * 1024;
            bf16x8 qf;
            asm volatile("global_load_dwordx4 %0, %1, %2 sc1\n\ts_waitcnt vmcnt(0)" : "=v"(qf) : "v"(lane * 16), "s"(qa) : "memory");
            float mx = -INFINITY;
#pragma clang loop unroll(disable)
            for (int t = 0; t < 64; ++t) {
                bf16x8 kf;
                asm volatile("global_load_dwordx4 %0, %1, %2 sc1\n\ts_waitcnt vmcnt(0)" : "=v"(kf) : "v"(lane * 16), "s"(ksrc + t * 1024) : "memory");
                const f32x16 s = MFMA32(kf, qf, f32x16{});
#pragma unroll
                for (int i = 0; i < 16; ++i) mx = fmaxf(mx, s[i]);
            }
            mx = -swap32_max(mx);
            attn_prep(R, w);
#pragma unroll
            for (int i = 0; i < 16; ++i) R.nm[i] = mx;
            attn_pass_shift(R, qa, qa, ksrc, ksrc + (WS_V - WS_K), w);
            finish(att);
        }
    }
}

#define BGAP "s_sleep 1\n\t"
#define SISSUE                                                             \
    "s_mov_b32 m0, s44\n\t"                                                 \
    "s_add_u32 s45, %[ow], %[wst]\n\t"                                      \
    "buffer_load_dwordx4 %[g0], %[rw], %[ow] offen lds\n\t"                 \
    "buffer_load_dwordx4 %[g1], %[rw], %[ow] offen offset:1024 lds\n\t"     \
    "s_add_u32 m0, s44, 8192\n\t"                                           \
    "s_add_u32 s46, %[od], 4096\n\t"                                        \
    "buffer_load_dwordx4 %[d0], %[rd], %[od] offen sc1 lds\n\t"             \
    "buffer_load_dwordx4 %[d0], %[rd], %[od] offen offset:1024 sc1 lds\n\t" \
    "s_add_u32 s44, s44, 0x4000\n\t"                                        \
    "s_and_b32 s44, s44, 0xffff\n\t"                                        \
    "s_cmp_eq_u32 %[od], %[de]\n\t"                                         \
    "s_cselect_b32 %[ow], 0, s45\n\t"                                       \
    "s_cselect_b32 %[od], 0, s46\n\t"
#define SISW                                                               \
    "s_mov_b32 m0, s44\n\t"                                                 \
    "s_add_u32 s45, %[ow], %[wst]\n\t"                                      \
    "buffer_load_dwordx4 %[g0], %[rw], %[ow] offen lds\n\t"                 \
    "buffer_load_dwordx4 %[g1], %[rw], %[ow] offen offset:1024 lds\n\t"     \
    "s_add_u32 s44, s44, 0x4000\n\t"                                        \
    "s_mov_b32 %[ow], s45\n\t"
#define SISD                                                               \
    "s_add_u32 m0, s44, 8192\n\t"                                           \
    "s_add_u32 s44, s44, 0x4000\n\t"                                        \
    "buffer_load_dwordx4 %[d0], %[rd], %[od] offen sc1 lds\n\t"             \
    "buffer_load_dwordx4 %[d0], %[rd], %[od] offen offset:1024 sc1 lds\n\t" \
    "s_add_u32 %[od], %[od], 4096\n\t"
struct SG { u32x4 rw, rd; unsigned wst, de, ow, od, a, b, c; };
DI void sgemm_prew(SG& G, int lw) {
    asm volatile("s_mov_b32 s44, %[lo]\n\t" SISW SISW SISW
                 : [ow] "+s"(G.ow) : [wst] "s"(G.wst), [rw] "s"(G.rw), [lo] "s"(lw * 2048), [g0] "v"(G.a), [g1] "v"(G.b) : "memory", "scc", "s44", "s45");
}
#define WISSUE                                                              \
    "s_and_b32 s45, s44, 0xc000\n\t"                                        \
    "s_add_u32 m0, s45, %[lo2]\n\t"                                         \
    "s_lshl_b32 s45, s45, 3\n\t"                                            \
    "buffer_load_dwordx4 %[o0], %[ro], s45 offen lds\n\t"                   \
    "buffer_load_dwordx4 %[o1], %[ro], s45 offen offset:1024 lds\n\t"       \
    "buffer_load_dwordx4 %[o2], %[ro], s45 offen offset:2048 lds\n\t"       \
    "buffer_load_dwordx4 %[o3], %[ro], s45 offen offset:3072 lds\n\t"       \
    "s_add_u32 s44, s44, 0x4000\n\t"                                        \
    "s_and_b32 s44, s44, 0xffff\n\t"
template <int WO>
DI void sgemm_load(SG& G, int n, int lw, u32x4 ro, unsigned o0) {
    asm volatile(
        "s_mov_b32 s44, %[lo]\n\t"
        ".if %c[wo]\n\t"
        "s_sub_u32 s47, %[n], 5\n\t"
        SISD SISD SISD
        "s_waitcnt vmcnt(4)\n\ts_barrier\n\t" SISSUE
        "s_waitcnt vmcnt(6)\n\ts_barrier\n\t" SISSUE
        ".else\n\t"
        "s_sub_u32 s47, %[n], 3\n\t"
        SISSUE SISSUE SISSUE
        ".endif\n\t"
        "LL_loop%=:\n\t"
        "s_waitcnt vmcnt(8)\n\t"
        "s_barrier\n\t"
        SISSUE
        "s_sub_u32 s47, s47, 1\n\t"
        "s_cmp_lg_u32 s47, 0\n\t"
        "s_cbranch_scc1 LL_loop%=\n\t"
        ".if %c[wo]\n\t"
        "s_waitcnt vmcnt(8)\n\ts_barrier\n\t" WISSUE
        "s_waitcnt vmcnt(8)\n\ts_barrier\n\t" WISSUE
        "s_waitcnt vmcnt(8)\n\ts_barrier\n\t" WISSUE
        "s_barrier\n\t" WISSUE
        "s_barrier\n\t"
        ".else\n\t"
        "s_waitcnt vmcnt(8)\n\ts_barrier\n\t"
        "s_waitcnt vmcnt(4)\n\ts_barrier\n\t"
        "s_waitcnt vmcnt(0)\n\ts_barrier\n\t"
        "s_barrier\n\t"
        ".endif\n\t"
        : [ow] "+s"(G.ow), [od] "+s"(G.od)
        : [wst] "s"(G.wst), [rw] "s"(G.rw), [rd] "s"(G.rd), [lo] "s"(lw * 2048), [de] "s"(G.de), [n] "s"(n), [g0] "v"(G.a), [g1] "v"(G.b), [d0] "v"(G.c),
          [wo] "i"(WO), [ro] "s"(ro), [lo2] "s"(lw * 4096), [o0] "v"(o0), [o1] "v"(o0 + 7168), [o2] "v"(o0 + 14336), [o3] "v"(o0 + 21504)
        : "memory", "scc", "s44", "s45", "s46", "s47");
}
#define SRD3(B, SLOT, K, TD)                                                                         \
    "ds_read_b64_tr_b16 v[" #B ":" #B "+1], %[t] offset:" #SLOT "*16384+" #K "*2048\n\t"              \
    "ds_read_b64_tr_b16 v[" #B "+2:" #B "+3], %[t] offset:" #SLOT "*16384+" #K "*2048+" #TD "\n\t"    \
    "ds_read_b128 v[" #B "+4:" #B "+7], %[dl] offset:" #SLOT "*16384+" #K "*1024\n\t"
#define SSTAGE(SLOT, MM, TD, N0, N1, N2, N3, P0, P1, P2, P3)                          \
    "s_waitcnt lgkmcnt(0)\n\t"                                                        \
    "s_barrier\n\t"                                                                   \
    MM(P0) SRD3(N0, SLOT, 0, TD) SRD3(N1, SLOT, 1, TD)                             \
    MM(P1) SRD3(N2, SLOT, 2, TD) SRD3(N3, SLOT, 3, TD) BGAP                            \
    MM(P2) BGAP MM(P3)
#define SST_E(SLOT, MM, TD) SSTAGE(SLOT, MM, TD, 32, 40, 48, 56, 96, 104, 112, 120)
#define SST_O(SLOT, MM, TD) SSTAGE(SLOT, MM, TD, 96, 104, 112, 120, 32, 40, 48, 56)
#define SMM_WA(P) MF("v[64:79]", "v[" #P ":" #P "+3]", "v[" #P "+4:" #P "+7]", "v[64:79]")
#define SMM_WB(P) MF("v[80:95]", "v[" #P "+4:" #P "+7]", "v[" #P ":" #P "+3]", "v[80:95]")
#define SGEMM_ASM(ACC, MM, TD, NLOOP, TAIL, FIN)                                                                       \
    asm volatile(                                                                                                 \
        "s_mov_b32 s44, " #NLOOP "\n\ts_setprio 0\n\t"                                                                       \
        "s_barrier\n\t"                                                                                           \
        SRD3(32, 0, 0, TD) SRD3(40, 0, 1, TD) SRD3(48, 0, 2, TD) SRD3(56, 0, 3, TD)                               \
        "LS_loop%=:\n\t"                                                                                          \
        SST_O(1, MM, TD) SST_E(2, MM, TD) SST_O(3, MM, TD) SST_E(0, MM, TD)                                       \
        "s_sub_u32 s44, s44, 1\n\t"                                                                               \
        "s_cmp_lg_u32 s44, 0\n\t"                                                                                 \
        "s_cbranch_scc1 LS_loop%=\n\t"                                                                            \
        TAIL                                                                                                      \
        "s_waitcnt lgkmcnt(0)\n\t" FIN                                                                            \
        MM(96) MM(104) MM(112) MM(120)                                                                \
        "s_setprio 3\n\ts_nop 15\n\t"                                                                                            \
        : ACC(acc)                                                                                                \
        : [t] "v"(G.a), [dl] "v"(G.b)                                                                             \
        : "memory", "scc", "s44",                                                                                 \
          "v32", "v33", "v34", "v35", "v36", "v37", "v38", "v39", "v40", "v41", "v42", "v43", "v44", "v45", "v46", "v47", \
          "v48", "v49", "v50", "v51", "v52", "v53", "v54", "v55", "v56", "v57", "v58", "v59", "v60", "v61", "v62", "v63", \
          "v96", "v97", "v98", "v99", "v100", "v101", "v102", "v103", "v104", "v105", "v106", "v107", "v108", "v109", "v110", "v111", \
          "v112", "v113", "v114", "v115", "v116", "v117", "v118", "v119", "v120", "v121", "v122", "v123", "v124", "v125", "v126", "v127")

template <int MODE>
DI SG sgemm_setup(const char* wbase, unsigned wbytes, int wrow, const char* dbase, int dblk, int rot, int w) {
    const int lane = lane_id(), row = lane >> 3, lw = w & 3;
    const int q = (lane & 15) >> 2, h2 = lane >> 5;
    SG G;
    G.rw = srd(wbase, wbytes); G.rd = srd(dbase, 2 * dblk);
    G.wst = 64 * wrow; G.de = (MODE == 0 ? 31 : 21) * 4096; G.ow = rot * G.wst; G.od = rot * 4096;
    if (w >= 4) {
        G.a = (16 * lw + row) * wrow + (((lane & 7) ^ (((row >> 1) & 1) << 2)) << 4);
        G.b = G.a + 8 * wrow - 1024;
        G.c = (lw >> 1) * dblk + (lw & 1) * 2048 + lane * 16;
    } else {
        G.a = ((MODE == 0 ? 8 : 4) * h2 + q) * 128 + (((lw & 1) ^ ((q >> 1) & 1)) << 6) + ((lane >> 4) & 1) * 32 + (lane & 3) * 8;
        G.b = 8192 + (lw >> 1) * 4096 + lane * 16;
        G.c = 0;
    }
    asm volatile("" : "+v"(G.a), "+v"(G.b), "+v"(G.c));
    return G;
}
#define ACC0 "+{v[64:79]}"
#define ACC1 "+{v[80:95]}"
template <int MODE>
DI void sgemm_run(f32x16& acc, SG& G) {
    if (MODE == 0) SGEMM_ASM(ACC0, SMM_WA, 512, 7, SST_O(1, SMM_WA, 512) SST_E(2, SMM_WA, 512) SST_O(3, SMM_WA, 512), "s_setprio 3\n\t");
    else SGEMM_ASM(ACC1, SMM_WB, 1024, 5, SST_O(1, SMM_WB, 1024), "s_barrier\n\ts_setprio 3\n\t");
}
DI f32x4 sub4(const f32x16& a, int g) { return f32x4{a[4 * g], a[4 * g + 1], a[4 * g + 2], a[4 * g + 3]}; }

DI unsigned gelu2(float a, float b) {
    constexpr float c1 = -2.f * 0.7978845608028654f * 1.4426950408889634f, c2 = c1 * 0.044715f;
    const f32x2 u = {a, b};
    const f32x2 z = u * (u * u * c2 + c1);
    const f32x2 d = f32x2{__builtin_amdgcn_exp2f(z[0]), __builtin_amdgcn_exp2f(z[1])} + 1.f;
    const f32x2 o = u * f32x2{__builtin_amdgcn_rcpf(d[0]), __builtin_amdgcn_rcpf(d[1])};
    return cvtpk(o[0], o[1]);
}

#define WRD(R0, R1, R2, R3, STEP)                                                    \
    "ds_read_b64_tr_b16 " R0 ", %[t0] offset:" #STEP "*2048\n\t"                      \
    "ds_read_b64_tr_b16 " R1 ", %[t0] offset:" #STEP "*2048+1024\n\t"                 \
    "ds_read_b64_tr_b16 " R2 ", %[t1] offset:" #STEP "*2048\n\t"                      \
    "ds_read_b64_tr_b16 " R3 ", %[t1] offset:" #STEP "*2048+1024\n\t"
#define WRD0(S) WRD("v[32:33]", "v[34:35]", "v[36:37]", "v[38:39]", S)
#define WRD1(S) WRD("v[40:41]", "v[42:43]", "v[44:45]", "v[46:47]", S)
#define WRD2(S) WRD("v[48:49]", "v[50:51]", "v[52:53]", "v[54:55]", S)
#define WRD3(S) WRD("v[56:57]", "v[58:59]", "v[60:61]", "v[62:63]", S)
#define WMM(WAIT, W0, W1, O) "s_waitcnt " WAIT "\n\t" MF("v[96:111]", W0, O, "v[96:111]") MF("v[112:127]", W1, O, "v[112:127]")
#define OLD(K, R, V, OFF)                                             \
    "5:\n\t"                                                           \
    "s_bitcmp1_b32 s40, " #K "\n\t"                                    \
    "s_cbranch_scc1 6f\n\t"                                            \
    "global_load_dwordx2 v[24:25], %[fo], %[f] sc1\n\t"                       \
    "s_sub_u32 s41, s41, 1\n\t"                                        \
    "s_cselect_b32 s42, -1, 0\n\t"                                     \
    "s_waitcnt vmcnt(0)\n\t"                                           \
    "v_cmp_eq_u64 vcc, %[tag], v[24:25]\n\t"                                \
    "s_or_b32 s40, vcc_lo, s42\n\t"                                    \
    "s_bitcmp1_b32 s40, " #K "\n\t"                                    \
    "s_cbranch_scc1 6f\n\t"                                            \
    "s_sleep 1\n\t"                                                    \
    "s_branch 5b\n\t"                                                  \
    "6:\n\t"                                                           \
    "global_load_dwordx4 " R ", " V ", %[o] offset:" #OFF " sc1\n\t"
DI void wo_gemm(f32x16& a0, f32x16& a1, f32x4& k0, f32x4& k1, float& k2, u32x4& k3, const char* osrc, const char* fsrc, unsigned seen, u64 tag,
                unsigned t0, unsigned t1, unsigned d0, unsigned d1, unsigned fo) {
    asm volatile(
        WRD0(0) WRD1(1) WRD2(2) WRD3(3)
        "s_mov_b32 s40, %[seen]\n\t"
        "s_mov_b32 s41, 0x100000\n\t"
        "s_setprio 0\n\t"
        OLD(0, "v[64:67]", "%[d0]", 0) OLD(1, "v[68:71]", "%[d0]", 1024) OLD(2, "v[72:75]", "%[d0]", 2048) OLD(3, "v[76:79]", "%[d0]", 3072)
        OLD(4, "v[80:83]", "%[d1]", 0) OLD(5, "v[84:87]", "%[d1]", 1024) OLD(6, "v[88:91]", "%[d1]", 2048) OLD(7, "v[92:95]", "%[d1]", 3072)
        "s_setprio 3\n\t"
        "s_waitcnt vmcnt(7) lgkmcnt(0)\n\t"
        MF("v[96:111]", "v[32:35]", "v[64:67]", "0") MF("v[112:127]", "v[36:39]", "v[64:67]", "0")
        WRD0(4)
        WMM("vmcnt(6)", "v[40:43]", "v[44:47]", "v[68:71]") WRD1(5)
        WMM("vmcnt(5)", "v[48:51]", "v[52:55]", "v[72:75]") WRD2(6)
        WMM("vmcnt(4)", "v[56:59]", "v[60:63]", "v[76:79]") WRD3(7)
        WMM("vmcnt(3) lgkmcnt(12)", "v[32:35]", "v[36:39]", "v[80:83]")
        WMM("vmcnt(2) lgkmcnt(0)", "v[40:43]", "v[44:47]", "v[84:87]")
        WMM("vmcnt(1)", "v[48:51]", "v[52:55]", "v[88:91]")
        "s_barrier\n\t"
        WMM("vmcnt(0)", "v[56:59]", "v[60:63]", "v[92:95]")
        "s_nop 15\n\t"
        : "={v[96:111]}"(a0), "={v[112:127]}"(a1),
          "+{v[12:15]}"(k0), "+{v[16:19]}"(k1), "+{v20}"(k2), "+{v[28:31]}"(k3)
        : [o] "s"(osrc), [f] "s"(fsrc), [tag] "s"(tag), [seen] "s"(seen), [d0] "v"(d0), [d1] "v"(d1), [t0] "v"(t0), [t1] "v"(t1), [fo] "v"(fo)
        : "memory", "scc", "vcc", "s40", "s41", "s42", "v24", "v25",
          "v32", "v33", "v34", "v35", "v36", "v37", "v38", "v39", "v40", "v41", "v42", "v43", "v44", "v45", "v46", "v47",
          "v48", "v49", "v50", "v51", "v52", "v53", "v54", "v55", "v56", "v57", "v58", "v59", "v60", "v61", "v62", "v63",
          "v64", "v65", "v66", "v67", "v68", "v69", "v70", "v71", "v72", "v73", "v74", "v75", "v76", "v77", "v78", "v79",
          "v80", "v81", "v82", "v83", "v84", "v85", "v86", "v87", "v88", "v89", "v90", "v91", "v92", "v93", "v94", "v95");
}

DI void mixer_role(const char* x, const char* wqkv, const char* gw, const char* wo, const char* w1, const char* b1, const char* w2, const char* b2,
                   char* ws, char* out, char* lds, int bi, int w) {
    const int j = bi & 7, i = bi >> 3;
    __builtin_amdgcn_s_setprio(0);
    const u64 ep = ld_epoch(ws);
    u64 tag;
    {
        const int lane = lane_id(), h2 = lane >> 5, tid = 64 * w + lane;
        const u32x4 xr = *(GAS const u32x4*)(x + (size_t)(8 * bi + w) * 1024 + lane * 16);
        const u32x4 gr = *(GAS const u32x4*)(gw + lane * 16);
        tag = next_tag(ep);
        float v[8], gg[8];
#pragma unroll
        for (int k = 0; k < 4; ++k) { v[2 * k] = bflo(xr[k]); v[2 * k + 1] = bfhi(xr[k]); gg[2 * k] = bflo(gr[k]); gg[2 * k + 1] = bfhi(gr[k]); }
        float s1 = 0.f, s3 = 0.f;
#pragma unroll
        for (int k = 0; k < 8; ++k) { s1 += v[k]; s3 += v[k] * gg[k]; }
        const float mu = wave_sum(s1) * (1.f / 512.f);
        s3 = wave_sum(s3);
        float s2 = 0.f;
#pragma unroll
        for (int k = 0; k < 8; ++k) { v[k] -= mu; s2 += v[k] * v[k]; }
        const float rs = __builtin_amdgcn_rsqf(wave_sum(s2) * (1.f / 512.f) + 1e-5f);
        u32x4 hv;
#pragma unroll
        for (int k = 0; k < 4; ++k) hv[k] = cvtpk(v[2 * k] * rs, v[2 * k + 1] * rs);
        *(LAS u32x4*)((LAS char*)lds + w * 1024 + lane * 16) = hv;
        const float g = __builtin_amdgcn_rcpf(1.f + __builtin_amdgcn_exp2f(-1.4426950408889634f * s3));
        st4(ws + WS_G + (8 * bi + w) * 4, uni(g));
        wgbar();
        LAS const unsigned short* lb = (LAS const unsigned short*)lds + tid;
#pragma unroll
        for (int k = 0; k < 4; ++k) hv[k] = (unsigned)lb[(2 * k) * 512] | ((unsigned)lb[(2 * k + 1) * 512] << 16);
        st16(ws + WS_H + (size_t)(2 * w * 128 + (bi >> 1)) * 1024 + (bi & 1) * 512, h2 * 131072 + (lane & 31) * 16, hv);
        drain();
        wgbar();
        if (w == 0) st_flag(ws + WS_FL + bi * 128, tag);
    }
    __builtin_amdgcn_s_setprio(3);
    const int wb = w & 1, db = (w >> 1) & 1, hf = w >> 2;
    const int L = (j >> 1) * 22 + i, ft = L >> 2, dt1 = 4 * (j & 1) + (L & 3);
    const int st = 8 * (j >> 1) + (i & 7), dt = 4 * (j & 1) + (i >> 3);
    SG G1, G2;
    unsigned ex, l16, og, wof;
    {
        const int lane = lane_id();
        og = (64 * st + 32 * wb + (lane & 31)) * 4;
        G1 = sgemm_setup<0>(w1 + ft * 128, 2048 * 2752 - ft * 128, 2752, ws + WS_H + (size_t)2 * dt1 * 131072, 131072, (4 * dt1 + 10 * ft) & 31, w);
        G2 = sgemm_setup<1>(w2 + st * 128, 1376 * 4096 - st * 128, 4096, ws + WS_U + (size_t)2 * dt * 86 * 1024, 86 * 1024, ((st + dt) & 1) * 11, w);
        l16 = lane * 16;
        ex = 65536 + (w & 3) * 2048 + l16;
        wof = (32 * (w & 3) + (lane >> 3)) * 1024 + (((lane & 7) ^ (((lane >> 4) & 1) << 2)) << 4);
        asm volatile("" : "+v"(ex), "+v"(l16), "+v"(og), "+v"(wof));
    }
    {
        const int row = 16 + 8 * (j & 1) + (i & 7), col = 8 + 8 * (i >> 4) + 2 * (j >> 1) + ((i >> 3) & 1);
        if (w == 0) {
            *(LAS unsigned*)QCNT = 0u;
            wait_flags<2>(ws + WS_FQ + bi * 128, 0, tag);
        }
        wgbar();
        if (w >= 4) qkv2_loader(x, wqkv, row, col, w - 4);
        else qkv2_body(ws, row, col, bi, w, tag);
    }
    if (i >= 22) {
        const int wv = ((i - 22) * 8 + j) * 8 + w;
        __builtin_amdgcn_s_sleep(64);
        __builtin_amdgcn_s_sleep(64);
        warm(w2, 1376, wv, l16 * 4);
        warm(wo, 128, wv, l16 * 4);
    }
    if (i < 22) {
        if (w >= 4) wait_flags<8>(ws + WS_FL + (w - 4) * 8192, l16 * 8, tag);
        wgbar();
        f32x4 v0, v1;
        if (w >= 4) {
            sgemm_load<0>(G1, 32, w - 4, G1.rw, 0);
            v0 = *(LAS const f32x4*)(uintptr_t)ex; v1 = *(LAS const f32x4*)(uintptr_t)(ex + 1024);
        } else {
            f32x16 acc1;
            const char* bsrc = b1 + (64 * ft + (ft < 21 ? 32 * wb : 0)) * 2 + (l16 >> 9) * 8;
#pragma unroll
            for (int k = 0; k < 4; ++k) {
                const u32x2 bv = *(GAS const u32x2*)(bsrc + 16 * k);
                acc1[4 * k] = bflo(bv[0]); acc1[4 * k + 1] = bfhi(bv[0]); acc1[4 * k + 2] = bflo(bv[1]); acc1[4 * k + 3] = bfhi(bv[1]);
            }
            sgemm_run<0>(acc1, G1);
            *(LAS f32x4*)(uintptr_t)ex = sub4(acc1, 2); *(LAS f32x4*)(uintptr_t)(ex + 1024) = sub4(acc1, 3);
            wgbar();
            v0 = sub4(acc1, 0); v1 = sub4(acc1, 1);
        }
        const u32x4 o = {gelu2(v0[0], v0[1]), gelu2(v0[2], v0[3]), gelu2(v1[0], v1[1]), gelu2(v1[2], v1[3])};
        if (ft < 21 || wb == 0) st16(ws + WS_U + (size_t)((2 * dt1 + db) * 86 + 4 * ft + 2 * wb + hf) * 1024, l16, o);
        drain();
        wgbar();
        if (w == 0) st_flag(ws + WS_FU + (dt1 * 32 + ft) * 128, tag);
    }
    if (w >= 4) sgemm_prew(G2, w - 4);
    unsigned t0, t1, d1, fo;
    u32x4 fin;
    {
        const int lane = l16 >> 4, q = (lane & 15) >> 2, kq = 2 * db + hf;
        const unsigned tb = (w >> 1) * 16384 + (4 * (lane >> 5) + q) * 128 + ((lane >> 4) & 1) * 32 + (lane & 3) * 8;
        t0 = tb + ((q >> 1) & 1) * 64; t1 = tb + (((q >> 1) & 1) ^ 1) * 64; d1 = l16 + 4096; fo = (lane & 7) * 128;
        fin = u32x4{w * 8192 + l16, wb * 8192 + kq * 2048 + l16, (unsigned)((64 * st + 32 * wb + (lane & 31)) * 1024 + 128 * dt + kq * 32 + (lane >> 5) * 16), 0u};
        asm volatile("" : "+v"(t0), "+v"(t1), "+v"(d1), "+v"(fo), "+{v[28:31]}"(fin));
    }
    if (bi == 255 && w == 1) {
        wait_flags256(ws + WS_FQ, l16 * 8, tag);
        wait_flags256(ws + WS_FL, l16 * 8, tag);
        st_flag(ws + WS_EPOCH, tag);
    }
    if (w == 0) wait_flags<8>(ws + WS_FU + dt * 4096, (l16 < 22 * 16 ? l16 : 0) * 8, tag);
    wgbar();
    const char* fsrc = ws + WS_FO + ((2 * st + wb) * 32 + 8 * (w >> 1)) * 128;
    u64 f0;
    f32x4 bp0, bp1;
    float g;
    {
        asm volatile("global_load_dword %0, %1, %2 sc1" : "={v20}"(g) : "v"(og), "s"(ws + WS_G) : "memory");
        if (w >= 4) {
            sgemm_load<1>(G2, 22, w - 4, srd(wo + dt * 128, 524288 - dt * 128), wof);
            asm volatile("global_load_dwordx2 %0, %1, %2 sc1" : "={v[22:23]}"(f0) : "v"(fo), "s"(fsrc) : "memory");
            bp0 = *(LAS const f32x4*)(uintptr_t)ex; bp1 = *(LAS const f32x4*)(uintptr_t)(ex + 1024);
        } else {
            const float bb = bflo(*(GAS const unsigned short*)(b2 + (og >> 1)));
            f32x16 acc2 = {bb, bb, bb, bb, bb, bb, bb, bb, bb, bb, bb, bb, bb, bb, bb, bb};
            sgemm_run<1>(acc2, G2);
            asm volatile("global_load_dwordx2 %0, %1, %2 sc1" : "={v[22:23]}"(f0) : "v"(fo), "s"(fsrc) : "memory");
            *(LAS f32x4*)(uintptr_t)ex = sub4(acc2, 2); *(LAS f32x4*)(uintptr_t)(ex + 1024) = sub4(acc2, 3);
            wgbar();
            bp0 = sub4(acc2, 0); bp1 = sub4(acc2, 1);
            drain();
        }
        asm volatile("" : "+{v20}"(g));
        bp0 *= 1.f - g;
        bp1 *= 1.f - g;
        asm volatile("" : "+v"(bp0), "+v"(bp1));
    }
    asm volatile("s_waitcnt vmcnt(0)\n\ts_barrier" : "+{v[22:23]}"(f0)::"memory");
    f32x16 a0, a1;
    wo_gemm(a0, a1, bp0, bp1, g, fin, ws + WS_O + (size_t)((2 * st + wb) * 32 + 8 * (w >> 1)) * 1024,
            fsrc, (unsigned)__ballot(f0 == tag), tag, t0, t1, l16, d1, fo);
    {
        LAS char* ex = (LAS char*)(uintptr_t)fin[0];
#pragma unroll
        for (int k = 0; k < 4; ++k) { *(LAS f32x4*)(ex + k * 1024) = sub4(a0, k); *(LAS f32x4*)(ex + 4096 + k * 1024) = sub4(a1, k); }
        wgbar();
        LAS const char* exr = (LAS const char*)(uintptr_t)fin[1];
        f32x4 s0 = *(LAS const f32x4*)exr, s1 = *(LAS const f32x4*)(exr + 1024);
#pragma unroll
        for (int k = 1; k < 4; ++k) { s0 += *(LAS const f32x4*)(exr + k * 16384); s1 += *(LAS const f32x4*)(exr + k * 16384 + 1024); }
        s0 = s0 * g + bp0;
        s1 = s1 * g + bp1;
        const auto r0 = __builtin_amdgcn_permlane32_swap(cvtpk(s0[0], s0[1]), cvtpk(s1[0], s1[1]), false, false);
        const auto r1 = __builtin_amdgcn_permlane32_swap(cvtpk(s0[2], s0[3]), cvtpk(s1[2], s1[3]), false, false);
        asm volatile("global_store_dwordx4 %0, %1, %2" ::"v"(fin[2]), "v"(u32x4{r0[0], r1[0], r0[1], r1[1]}), "s"(out) : "memory");
    }
}

constexpr int MK_LDS = 81920;
__global__ __launch_bounds__(512, 4) void mk(const char* x, const char* wqkv, const char* wo, const char* w1, const char* b1,
                                             const char* w2, const char* b2, const char* gw, char* ws, char* out) {
    extern __shared__ __attribute__((aligned(16))) char lds[];
    const int w = __builtin_amdgcn_readfirstlane(threadIdx.x >> 6);
    if (blockIdx.x >= 256 && w == 7) {
        asm volatile("s_getpc_b64 s[40:41]\n\t"
                     "Lmk_hot_begin:\n\t"
                     "s_cmp_lt_u32 %[o], Lmk_hot_end-Lmk_hot_begin-4096\n\t"
                     "s_cbranch_scc0 1f\n\t"
                     "s_add_u32 s40, s40, %[o]\n\t"
                     "s_addc_u32 s41, s41, 0\n\t"
                     "s_and_b32 s40, s40, -4\n\t"
                     "s_mov_b32 m0, 0x13e00\n\t"
                     "s_nop 0\n\t"
                     "global_load_lds_dword %[v], s[40:41]\n\t"
                     "1:"
                     :: [o] "s"(((blockIdx.x - 256) >> 3) * 4096), [v] "v"(lane_id() * 64) : "memory", "scc", "s40", "s41");
    }
    if (blockIdx.x < 256) attn_role(x, wqkv, ws, lds, blockIdx.x, w);
    else mixer_role(x, wqkv, gw, wo, w1, b1, w2, b2, ws, out, lds, blockIdx.x - 256, w);
}

extern "C" void kernel_launch(void* const* d_in, const int* in_sizes, int n_in, void* d_out, int out_size, void* d_ws, size_t ws_size,
                              hipStream_t stream) {
    (void)hipFuncSetAttribute((const void*)mk, hipFuncAttributeMaxDynamicSharedMemorySize, MK_LDS);
    mk<<<512, 512, MK_LDS, stream>>>((const char*)d_in[0], (const char*)d_in[1], (const char*)d_in[2], (const char*)d_in[4],
                                     (const char*)d_in[5], (const char*)d_in[6], (const char*)d_in[7], (const char*)d_in[8],
                                     (char*)d_ws, (char*)d_out);
}
```

```cpp
#include <hip/hip_runtime.h>
#include <hip/hip_bf16.h>
#include <stdint.h>

typedef __attribute__((ext_vector_type(8))) short bf16x8;
typedef __attribute__((ext_vector_type(4))) short s16x4;
typedef __attribute__((ext_vector_type(16))) float f32x16;
typedef __attribute__((ext_vector_type(4))) float f32x4;
typedef __attribute__((ext_vector_type(2))) float f32x2;
typedef __attribute__((ext_vector_type(4))) unsigned u32x4;
typedef __attribute__((ext_vector_type(2))) unsigned u32x2;
typedef unsigned long long u64;
#define LAS __attribute__((address_space(3)))
#define GAS __attribute__((address_space(1)))
#define DI __device__ __forceinline__
#define MFMA32(a, b, c) __builtin_amdgcn_mfma_f32_32x32x16_bf16((a), (b), (c), 0, 0, 0)

#define ROT1 ((4 * dt1 + 10 * ft) & 31)
#define ROT2 (((st + dt) & 1) * 11)
#define LNP 0
#define PFA 3
#define PFD 2
#define PSL 2
#define STAG 1
#define CODEPF 1
#define PF1 0
#define QROT 6
#define QROTM 2
constexpr int S_ = 2048, D_ = 512, F_ = 1376;
constexpr size_t WS_EPOCH = 0;
constexpr size_t WS_G = 4096;
constexpr size_t WS_FL = 98304;
constexpr size_t WS_FQ = 458752;
constexpr size_t WS_FU = 65536;
constexpr size_t WS_FO = 131072;
constexpr size_t WS_Q = 1u << 20;
constexpr size_t WS_K = WS_Q + (2u << 20);
constexpr size_t WS_V = WS_K + (2u << 20);
constexpr size_t WS_H = WS_V + (2u << 20);
constexpr size_t WS_U = WS_H + (2u << 20);
constexpr size_t WS_O = WS_U + (2u << 20);

#define ST_DECL
#define ST_ARG
#define ST_PASS
#define ST(i)
#define ST_OUT(k)
DI unsigned cvtpk(float lo, float hi) {
    typedef float f32x2_t __attribute__((ext_vector_type(2)));
    typedef __bf16 bf16x2_t __attribute__((ext_vector_type(2)));
    return __builtin_bit_cast(unsigned, __builtin_convertvector(f32x2_t{lo, hi}, bf16x2_t));
}
DI float bflo(unsigned u) { return __uint_as_float(u << 16); }
DI float bfhi(unsigned u) { return __uint_as_float(u & 0xffff0000u); }
DI s16x4 trread(LAS const char* p) {
    typedef short v4i16_t __attribute__((ext_vector_type(4)));
    return __builtin_bit_cast(s16x4, __builtin_amdgcn_ds_read_tr16_b64_v4i16((LAS v4i16_t*)p));
}
template <int CTRL> DI float dpp(float v) {
    return __int_as_float(__builtin_amdgcn_update_dpp(0, __float_as_int(v), CTRL, 0xf, 0xf, true));
}
DI float rl(float v, int l) { return __int_as_float(__builtin_amdgcn_readlane(__float_as_int(v), l)); }
DI float wave_sum(float v) {
    v += dpp<0xB1>(v); v += dpp<0x4E>(v); v += dpp<0x141>(v); v += dpp<0x140>(v);
    return (rl(v, 0) + rl(v, 16)) + (rl(v, 32) + rl(v, 48));
}
DI float swap32_max(float v) {
    auto rr = __builtin_amdgcn_permlane32_swap(__float_as_uint(v), __float_as_uint(v), false, false);
    return fmaxf(__uint_as_float(rr[0]), __uint_as_float(rr[1]));
}

DI int lane_id() {
    int l;
    asm volatile("v_mbcnt_lo_u32_b32 %0, -1, 0\n\tv_mbcnt_hi_u32_b32 %0, -1, %0" : "=v"(l));
    __builtin_assume(l >= 0 && l < 64);
    return l;
}
DI void st16(const char* sbase, unsigned voff, u32x4 v) {
    asm volatile("global_store_dwordx4 %0, %1, %2 sc1\n\ts_nop 1" ::"v"(voff), "v"(v), "s"(sbase) : "memory");
}
DI void st4(const char* p, unsigned v) {
    unsigned t0, t1;
    asm volatile("s_nop 1\n\tv_mov_b32 %0, %2\n\tv_mov_b32 %1, 0\n\ts_mov_b64 exec, 1\n\tglobal_store_dword %1, %0, %3 sc1\n\ts_mov_b64 exec, -1"
                 : "=&v"(t0), "=&v"(t1) : "s"(v), "s"(p) : "memory");
}
DI void st_flag(const char* p, u64 v) {
    u64 t0;
    unsigned t1;
    asm volatile("s_nop 1\n\tv_mov_b64 %0, %2\n\tv_mov_b32 %1, 0\n\ts_mov_b64 exec, 1\n\tglobal_store_dwordx2 %1, %0, %3 sc1\n\ts_mov_b64 exec, -1"
                 : "=&v"(t0), "=&v"(t1) : "s"(v), "s"(p) : "memory");
}
DI u64 uni64(u64 v) {
    return (u64)(unsigned)__builtin_amdgcn_readfirstlane((unsigned)v) | ((u64)(unsigned)__builtin_amdgcn_readfirstlane((unsigned)(v >> 32)) << 32);
}
DI u64 ld_epoch(const char* ws) { return __hip_atomic_load((GAS const u64*)(ws + WS_EPOCH), __ATOMIC_RELAXED, __HIP_MEMORY_SCOPE_AGENT); }
DI u64 next_tag(u64 e) { return uni64(e) * 6364136223846793005ull + 1442695040888963407ull; }
DI unsigned uni(float v) { return __builtin_amdgcn_readfirstlane(__float_as_uint(v)); }
DI void warm(const char* base, int nchunks, int first, unsigned l64) {
#pragma clang loop unroll(disable)
    for (int n = first; n < nchunks; n += 640) {
        const char* p = base + (size_t)((n & 1) * (nchunks >> 1) + (n >> 1)) * 4096;
        asm volatile("s_mov_b32 m0, 0x13e00\n\ts_nop 0\n\tglobal_load_lds_dword %0, %1" ::"v"(l64), "s"(p) : "memory");
    }
}
DI void drain() { asm volatile("s_waitcnt vmcnt(0)" ::: "memory"); }
DI void wgbar() { asm volatile("s_waitcnt lgkmcnt(0)\n\ts_barrier" ::: "memory"); }
template <int SLEEP>
DI void wait_flags(const char* sbase, unsigned voff, u64 tag) {
#pragma clang loop unroll(disable)
    for (unsigned spin = 0; spin < (1u << 20); ++spin) {
        u64 f;
        asm volatile("global_load_dwordx2 %0, %1, %2 sc1\n\ts_waitcnt vmcnt(0)" : "=v"(f) : "v"(voff), "s"(sbase) : "memory");
        if (__all(f == tag)) break;
        __builtin_amdgcn_s_sleep(SLEEP);
    }
}

DI void wait_flags256(const char* sbase, unsigned voff, u64 tag) {
#pragma clang loop unroll(disable)
    for (unsigned spin = 0; spin < (1u << 20); ++spin) {
        u64 f0, f1, f2, f3;
        asm volatile("global_load_dwordx2 %0, %4, %5 sc1\n\tglobal_load_dwordx2 %1, %4, %6 sc1\n\tglobal_load_dwordx2 %2, %4, %7 sc1\n\t"
                     "global_load_dwordx2 %3, %4, %8 sc1\n\ts_waitcnt vmcnt(0)"
                     : "=&v"(f0), "=&v"(f1), "=&v"(f2), "=&v"(f3)
                     : "v"(voff), "s"(sbase), "s"(sbase + 8192), "s"(sbase + 16384), "s"(sbase + 24576) : "memory");
        if (__all(f0 == tag && f1 == tag && f2 == tag && f3 == tag)) break;
        __builtin_amdgcn_s_sleep(8);
    }
}

constexpr int QCNT = 73728;
#define MF(D, A, B, C) "v_mfma_f32_32x32x16_bf16 " D ", " A ", " B ", " C "\n\t"
DI u32x4 srd(const char* base, unsigned bytes) {
    return u32x4{(unsigned)(uintptr_t)base, (unsigned)((uintptr_t)base >> 32) & 0xffffu, bytes, 0x00020000u};
}
DI void qkv_store(char* ws, const f32x16& acc, int type, int head, int tile, int lane) {
    const float sc = (type == 0) ? 0.25f * 1.4426950408889634f : 1.f;
    const char* dst = ws + WS_Q + (size_t)type * (2u << 20) + (size_t)(head * 64 + tile) * 1024;
    const unsigned vo = type < 2 ? lane * 16 : ((lane >> 4) & 1) * 65536 + (lane >> 5) * 256 + (lane & 15) * 16;
#pragma unroll
    for (int hf = 0; hf < 2; ++hf) {
        u32x4 o;
#pragma unroll
        for (int i = 0; i < 4; ++i) o[i] = cvtpk(acc[8 * hf + 2 * i] * sc, acc[8 * hf + 2 * i + 1] * sc);
        st16(dst + hf * (type < 2 ? 65536 : 512), vo, o);
    }
}
DI void publish4(const char* flag, u64 tag, int lane) {
    drain();
    unsigned arr = 0;
    if (lane == 0) arr = __hip_atomic_fetch_add((LAS unsigned*)QCNT, 1u, __ATOMIC_RELAXED, __HIP_MEMORY_SCOPE_WORKGROUP);
    if (__builtin_amdgcn_readfirstlane(arr) == 3) st_flag(flag, tag);
}
DI void qkv_loader(const char* x, const char* wqkv, int row, int col, int lw) {
    const int lane = lane_id(), xr = 16 * lw + (lane >> 3), wr = 16 * lw + (lane >> 4);
    const unsigned x0 = xr * 1024 + (((lane & 7) ^ ((xr >> 1) & 7)) << 4), x1 = (xr + 8) * 1024 + (((lane & 7) ^ (((xr + 8) >> 1) & 7)) << 4) - 1024;
    const unsigned w0 = wr * 3072 + (((lane & 15) ^ ((wr & 3) << 2)) << 4);
    const u32x4 rx = srd(x + (size_t)row * 65536, 65536), rw = srd(wqkv + col * 256, 512 * 3072 - col * 256);
    const int rot = (row + col) * QROTM & QROT;
    auto issue = [&](int s, int slot) {
        const int kc = (s + rot) & 7;
        asm volatile("s_mov_b32 m0, %[m]\n\ts_nop 0\n\t"
                     "buffer_load_dwordx4 %[x0], %[rx], %[ox] offen lds\n\t"
                     "buffer_load_dwordx4 %[x1], %[rx], %[ox] offen offset:1024 lds\n\t"
                     "s_mov_b32 m0, %[m2]\n\ts_nop 0\n\t"
                     "buffer_load_dwordx4 %[w0], %[rw], %[ow] offen lds\n\t"
                     "buffer_load_dwordx4 %[w1], %[rw], %[ow] offen offset:1024 lds\n\t"
                     "buffer_load_dwordx4 %[w2], %[rw], %[ow] offen offset:2048 lds\n\t"
                     "buffer_load_dwordx4 %[w3], %[rw], %[ow] offen offset:3072 lds"
                     :: [m] "s"(slot * 24576 + lw * 2048), [m2] "s"(slot * 24576 + 8192 + lw * 4096), [ow] "s"(kc * 196608), [ox] "s"(kc * 128),
                        [rw] "s"(rw), [rx] "s"(rx), [x0] "v"(x0), [x1] "v"(x1),
                        [w0] "v"(w0), [w1] "v"(w0 + 11264), [w2] "v"(w0 + 22528), [w3] "v"(w0 + 33792)
                     : "memory");
    };
    issue(0, 0);
    __builtin_amdgcn_s_setprio(2);
    issue(1, 1);
    __builtin_amdgcn_s_setprio(1);
    issue(2, 2);
    __builtin_amdgcn_s_setprio(3);
    asm volatile("s_waitcnt vmcnt(12)\n\ts_barrier" ::: "memory");
    int slot = 0;
#pragma clang loop unroll(disable)
    for (int s = 3; s < 8; ++s) {
        asm volatile("s_waitcnt vmcnt(6)\n\ts_barrier" ::: "memory");
        issue(s, slot);
        slot = slot == 2 ? 0 : slot + 1;
    }
    asm volatile("s_waitcnt vmcnt(6)\n\ts_barrier\n\ts_waitcnt vmcnt(0)\n\ts_barrier" ::: "memory");
}
template <bool T>
DI void qkv_body(char* ws, char* lds, int row, int col, int a, int w, u64 tag ST_ARG) {
    const int lane = lane_id(), r = lane & 31, h2 = lane >> 5, q = (lane & 15) >> 2;
    f32x16 acc[2] = {};
    const int xa0 = (32 * (w & 1) + r) * 128 + ((h2 ^ ((r >> 1) & 7)) << 4);
    const int wa0 = 8192 + (8 * h2 + q) * 256 + (((2 * (w >> 1)) ^ q) << 6) + ((lane >> 4) & 1) * 32 + (lane & 3) * 8;
    bf16x8 xf[4], wf[2][2];
    auto rdw = [&](LAS const char* sl, int st, int i) {
        LAS const char* pw = sl + (wa0 ^ (i << 6)) + st * 4096;
        const s16x4 lo = trread(pw), hi = trread(pw + 1024);
        wf[st & 1][i] = __builtin_shufflevector(lo, hi, 0, 1, 2, 3, 4, 5, 6, 7);
    };
    auto mm1 = [&](int st, int i) {
        acc[i] = T ? MFMA32(wf[st & 1][i], xf[st], acc[i]) : MFMA32(xf[st], wf[st & 1][i], acc[i]);
        __builtin_amdgcn_sched_barrier(0);
    };
    auto step = [&](int st, LAS const char* sl, bool bar) {
        const int sn = (st + 2) & 3;
        mm1(st, 0);
        if (bar) wgbar();
        xf[sn] = *(LAS const bf16x8*)(sl + (xa0 ^ (sn << 5))); rdw(sl, sn, 0); __builtin_amdgcn_sched_barrier(0);
        mm1(st, 1);
        rdw(sl, sn, 1); __builtin_amdgcn_sched_barrier(0);
    };
    if (w == 0) *(LAS u32x4*)QCNT = u32x4{0u, 0u, (unsigned)tag, (unsigned)(tag >> 32)};
    wgbar();
    LAS const char* sl = (LAS const char*)lds;
#pragma unroll
    for (int st = 0; st < 2; ++st) { xf[st] = *(LAS const bf16x8*)(sl + (xa0 ^ (st << 5))); rdw(sl, st, 0); rdw(sl, st, 1); }
    __builtin_amdgcn_sched_barrier(0);
#pragma clang loop unroll(disable)
    for (int s = 0; s < 7; ++s) {
        step(0, sl, false); step(1, sl, false);
        sl = (sl == (LAS const char*)lds + 49152) ? (LAS const char*)lds : sl + 24576;
        step(2, sl, true); step(3, sl, false);
    }
    step(0, sl, false); step(1, sl, false);
    mm1(2, 0); mm1(2, 1); mm1(3, 0); mm1(3, 1);
    ST(4);
#pragma unroll
    for (int i = 0; i < 2; ++i) qkv_store(ws, acc[i], col >> 2, 8 * (col & 3) + 4 * (w >> 1) + 2 * i, 2 * row + (w & 1), lane);
    publish4(ws + WS_FQ + a * 128, tag, lane);
    ST(7);
}
DI void qkv2_loader(const char* x, const char* wqkv, int rowt, int col, int lw) {
    const int lane = lane_id(), row = 16 * lw + (lane >> 3);
    const unsigned w0 = row * 3072 + (((lane & 7) ^ (((row >> 1) & 1) << 2)) << 4), w1 = w0 + 8 * 3072 - 1024;
    const unsigned x0 = row * 1024 + (((lane & 7) ^ ((row >> 1) & 7)) << 4), x1 = (row + 8) * 1024 + (((lane & 7) ^ (((row + 8) >> 1) & 7)) << 4) - 1024;
    const u32x4 rx = srd(x + (size_t)rowt * 65536, 65536), rw = srd(wqkv + col * 128, 512 * 3072 - col * 128);
    auto issue = [&](int s) {
        asm volatile("s_mov_b32 m0, %[m]\n\ts_nop 0\n\t"
                     "buffer_load_dwordx4 %[w0], %[rw], %[ow] offen lds\n\t"
                     "buffer_load_dwordx4 %[w1], %[rw], %[ow] offen offset:1024 lds\n\t"
                     "s_add_u32 m0, %[m], 8192\n\ts_nop 0\n\t"
                     "buffer_load_dwordx4 %[x0], %[rx], %[ox] offen lds\n\t"
                     "buffer_load_dwordx4 %[x1], %[rx], %[ox] offen offset:1024 lds"
                     :: [m] "s"((s & 3) * 16384 + lw * 2048), [ow] "s"(s * 196608), [ox] "s"(s * 128), [rw] "s"(rw), [rx] "s"(rx),
                        [w0] "v"(w0), [w1] "v"(w1), [x0] "v"(x0), [x1] "v"(x1)
                     : "memory", "scc");
    };
    issue(0); issue(1); issue(2);
#pragma clang loop unroll(disable)
    for (int s = 3; s < 8; ++s) {
        asm volatile("s_waitcnt vmcnt(8)\n\ts_barrier" ::: "memory");
        issue(s);
    }
    asm volatile("s_waitcnt vmcnt(8)\n\ts_barrier\n\ts_waitcnt vmcnt(4)\n\ts_barrier\n\ts_waitcnt vmcnt(0)\n\ts_barrier" ::: "memory");
}
#define QRD3(B, SLOT, K)                                                                             \
    "ds_read_b64_tr_b16 v[" #B ":" #B "+1], %[t] offset:" #SLOT "*16384+" #K "*2048\n\t"              \
    "ds_read_b64_tr_b16 v[" #B "+2:" #B "+3], %[t] offset:" #SLOT "*16384+" #K "*2048+512\n\t"        \
    "ds_read_b128 v[" #B "+4:" #B "+7], %[x" #K "] offset:" #SLOT "*16384\n\t"
#define QSTAGE(SLOT, MM, N0, N1, N2, N3, P0, P1, P2, P3)                              \
    "s_waitcnt lgkmcnt(0)\n\t"                                                        \
    "s_barrier\n\t"                                                                   \
    MM(P0) QRD3(N0, SLOT, 0) MM(P1) QRD3(N1, SLOT, 1) MM(P2) QRD3(N2, SLOT, 2) MM(P3) QRD3(N3, SLOT, 3)
#define QST_E(SLOT, MM) QSTAGE(SLOT, MM, 32, 40, 48, 56, 96, 104, 112, 120)
#define QST_O(SLOT, MM) QSTAGE(SLOT, MM, 96, 104, 112, 120, 32, 40, 48, 56)
#define QMT(P) MF("v[16:31]", "v[" #P ":" #P "+3]", "v[" #P "+4:" #P "+7]", "v[16:31]")
#define QMV(P) MF("v[16:31]", "v[" #P "+4:" #P "+7]", "v[" #P ":" #P "+3]", "v[16:31]")
#define QTILE_ASM(MM)                                                                                             \
    asm volatile(                                                                                                 \
        "s_barrier\n\t"                                                                                           \
        QRD3(32, 0, 0) QRD3(40, 0, 1) QRD3(48, 0, 2) QRD3(56, 0, 3)                                               \
        QST_O(1, MM) QST_E(2, MM)                                                                                 \
        "s_mov_b32 s44, 0\n\t"                                                                                    \
        "LQ_loop%=:\n\t"                                                                                          \
        QST_O(3, MM)                                                                                              \
        "s_cmp_lg_u32 s44, 0\n\t"                                                                                 \
        "s_cbranch_scc1 LQ_end%=\n\t"                                                                             \
        QST_E(0, MM) QST_O(1, MM) QST_E(2, MM)                                                                    \
        "s_mov_b32 s44, 1\n\t"                                                                                    \
        "s_branch LQ_loop%=\n\t"                                                                                  \
        "LQ_end%=:\n\t"                                                                                           \
        "s_waitcnt lgkmcnt(0)\n\t"                                                                                \
        MM(96) MM(104) MM(112) MM(120)                                                                            \
        "s_nop 15\n\t"                                                                                            \
        : "+{v[16:31]}"(acc)                                                                                      \
        : [t] "v"(t), [x0] "v"(xa), [x1] "v"(xa ^ 32), [x2] "v"(xa ^ 64), [x3] "v"(xa ^ 96)                       \
        : "memory", "scc", "s44",                                                                                 \
          "v32", "v33", "v34", "v35", "v36", "v37", "v38", "v39", "v40", "v41", "v42", "v43", "v44", "v45", "v46", "v47", \
          "v48", "v49", "v50", "v51", "v52", "v53", "v54", "v55", "v56", "v57", "v58", "v59", "v60", "v61", "v62", "v63", \
          "v96", "v97", "v98", "v99", "v100", "v101", "v102", "v103", "v104", "v105", "v106", "v107", "v108", "v109", "v110", "v111", \
          "v112", "v113", "v114", "v115", "v116", "v117", "v118", "v119", "v120", "v121", "v122", "v123", "v124", "v125", "v126", "v127")
DI void qkv2_body(char* ws, int row, int col, int bi, int w, u64 tag) {
    const int lane = lane_id(), r = lane & 31, h2 = lane >> 5, q = (lane & 15) >> 2;
    const unsigned t = (8 * h2 + q) * 128 + (((w & 1) ^ ((q >> 1) & 1)) << 6) + ((lane >> 4) & 1) * 32 + (lane & 3) * 8;
    const unsigned xa = 8192 + (32 * (w >> 1) + r) * 128 + ((h2 ^ ((r >> 1) & 7)) << 4);
    f32x16 acc = {};
    if (col < 16) QTILE_ASM(QMT);
    else QTILE_ASM(QMV);
    qkv_store(ws, acc, col >> 3, 4 * (col & 7) + 2 * (w & 1), 2 * row + (w >> 1), lane);
    publish4(ws + WS_FQ + 32768 + bi * 128, tag, lane);
}

#define EXP(a) "v_exp_f32 v" #a ", v" #a "\n\t"
#define CVT(d, a, b) "v_cvt_pk_bf16_f32 v" #d ", v" #a ", v" #b "\n\t"
#define ASTEP(c0, c1, c2, c3, c4, c5, c6, c7, c8, c9, c10, c11, c12, c13, c14, c15, M1, M2, CL, M3)                  \
    EXP(c0) EXP(c1) M1 EXP(c2) EXP(c3) EXP(c4) EXP(c5) M2 CL                                                          \
    EXP(c6) EXP(c7) CVT(96, c0, c1) CVT(97, c2, c3) CVT(98, c4, c5) CVT(99, c6, c7) EXP(c8) EXP(c9) M3                \
    EXP(c10) EXP(c11) EXP(c12) EXP(c13) EXP(c14) EXP(c15)                                                             \
    CVT(100, c8, c9) CVT(101, c10, c11) CVT(102, c12, c13) CVT(103, c14, c15)
#define R_OA "v[64:79]"
#define R_OB "v[80:95]"
#define R_SA "v[32:47]"
#define R_SB "v[48:63]"
#define ATILE(R_NM, KC, KN, V0C, V1C, V0N, V1N, KOFF, VA, VOFF0, VOFF1, PRE)                                                    \
    ASTEP(32, 33, 34, 35, 36, 37, 38, 39, 40, 41, 42, 43, 44, 45, 46, 47,                                             \
          MF(R_OB, V1N, "v[100:103]", R_OB), MF(R_SB, KC, "v[8:11]", R_NM),                                           \
          PRE                                                                                                         \
          "ds_read_b128 " KC ", v12 offset:" KOFF "\n\t"                                                              \
          "ds_read_b128 " V0N ", " VA " offset:" #VOFF0 "\n\t"                                                        \
          "ds_read_b128 " V1N ", " VA " offset:" #VOFF1 "\n\t",                                                          \
          MF(R_OA, V0C, "v[96:99]", R_OA))                                                                            \
    ASTEP(48, 49, 50, 51, 52, 53, 54, 55, 56, 57, 58, 59, 60, 61, 62, 63,                                             \
          MF(R_OA, V1C, "v[100:103]", R_OA), MF(R_SA, KN, "v[4:7]", R_NM), "", MF(R_OB, V0C, "v[96:99]", R_OB))
#define ATILE_E(NM, KOFF, VA, VOFF0, VOFF1, PRE) ATILE(NM, "v[104:107]", "v[108:111]", "v[112:115]", "v[116:119]", "v[120:123]", "v[124:127]", KOFF, VA, VOFF0, VOFF1, PRE)
#define ATILE_O(NM, KOFF, VA, VOFF0, VOFF1, PRE) ATILE(NM, "v[108:111]", "v[104:107]", "v[120:123]", "v[124:127]", "v[112:115]", "v[116:119]", KOFF, VA, VOFF0, VOFF1, PRE)
#define MOV4(a, b, c, d, s) "v_mov_b32 v" #a ", " s "\n\tv_mov_b32 v" #b ", " s "\n\tv_mov_b32 v" #c ", " s "\n\tv_mov_b32 v" #d ", " s "\n\t"
#define WL "s_waitcnt lgkmcnt(0)\n\t"
#define AISSUE                                                   \
    "s_add_u32 m0, s44, %[lw]\n\t"                               \
    "s_add_u32 s44, s44, 0x4000\n\t"                             \
    "buffer_load_dwordx4 v14, %[rk], s43 offen sc1 lds\n\t"      \
    "s_add_u32 m0, m0, 0x2000\n\t"                               \
    "s_and_b32 s44, s44, 0xffff\n\t"                             \
    "buffer_load_dwordx4 v14, %[rv], s43 offen sc1 lds\n\t"      \
    "s_add_u32 s43, s43, 0x2000\n\t"
#define PR2 "s_setprio 2\n\t"
#define PR3 ""
#define PR0 "s_setprio 1\n\t"
#define PR1 "s_setprio 0\n\t"
#define PRI "s_setprio 3\n\t"
constexpr int A_ONES = 65536, A_VOTE = 69632 + 512, A_LSUM = 71680;
struct AttnRegs { f32x16 oa, ob, nm; u32x4 p1, v1, fin; unsigned ka, va, va1, va2, va3, go, fo; };
DI unsigned qkv_dep(int a, bool early) {
    const int lane = lane_id(), r = lane & 15, v = (lane >> 4) & 1, R = 4 * (a >> 5) + (lane & 3);
    const int kv = (r >> 3) + 8 * ((early ? 8 + 8 * v : 16 * v + 8 * (a & 1)) + (r & 7));
    const int qq = ((R >> 3) & 1) + 8 * ((R >> 4) * 24 + (R & 7));
    return ((a & 6) + (lane < 32 || !early ? kv : qq)) * 128;
}
DI void attn_prep(AttnRegs& R, int a, int w) {
    const int lane = lane_id(), r = lane & 31, kh = w >> 2;
    R.ka = 4096 * kh + lane * 16;
    R.va = r < 16 ? 8192 + 4096 * kh + (lane >> 5) * 256 + r * 16 : A_ONES + (r == 16 ? 0 : r == 20 ? 64 : (r > 20 && r < 28) ? 80 : 16);
    R.go = w * 1024 + lane * 16;
#pragma unroll
    for (int i = 0; i < 16; ++i) { R.oa[i] = 0.f; R.ob[i] = 0.f; }
    R.p1 = u32x4{0u, 0u, 0u, 0u};
    R.v1 = u32x4{0u, 0u, 0u, 0u};
    asm volatile("" : "+{v[64:79]}"(R.oa), "+{v[80:95]}"(R.ob), "+{v[100:103]}"(R.p1), "+{v[124:127]}"(R.v1),
                      "+{v12}"(R.ka), "+{v13}"(R.va), "+{v14}"(R.go));
}
#define ATTN_ASM(NM, NMIN) \
    asm volatile( \
        "v_and_b32 v15, 0x3ff, v14\n\t" \
        "s_mov_b32 s43, 0\n\t" \
        "s_mov_b32 s44, 0\n\t" \
        AISSUE \
        "global_load_dwordx4 v[4:7], v15, %[qa] sc1\n\t" \
        "global_load_dwordx4 v[8:11], v15, %[qb] sc1\n\t" \
        AISSUE AISSUE \
        "s_mov_b32 s40, 0xffff\n\t" \
        "s_mov_b32 s41, 0xffff\n\t" \
        "s_mov_b32 s42, 8\n\t" \
        "s_waitcnt vmcnt(4)\n\t" \
        "s_barrier\n\t" \
        "ds_read_b128 v[104:107], v12\n\t" \
        "ds_read_b128 v[108:111], v12 offset:1024\n\t" \
        "ds_read_b128 v[112:115], v13\n\t" \
        "ds_read_b128 v[116:119], v13 offset:512\n\t" \
        PR2 \
        "s_waitcnt lgkmcnt(3)\n\t" \
        MF(R_SA, "v[104:107]", "v[4:7]", NM) \
        "s_nop 15\n\t" \
        "LA_loop%=:\n\t" \
        ATILE_E(NM, "2048", "v13", 1024, 1536, WL PR0) \
        ATILE_O(NM, "3072", "v13", 2048, 2560, WL PR1) \
        ATILE_E(NM, "0", "v13", 3072, 3584, \
                "s_waitcnt vmcnt(2) lgkmcnt(0)\n\t" \
                "s_barrier\n\t" \
                "s_setprio 3\n\t" \
                "v_add_u32 v12, 0x4000, v12\n\t" \
                AISSUE \
                "v_and_b32 v12, 0xffff, v12\n\t" \
                PR2) \
        ATILE_O(NM, "1024", "v13", 0, 512, \
                WL PR3 \
                "s_mov_b64 exec, s[40:41]\n\t" \
                "v_add_u32 v13, 0x4000, v13\n\t" \
                "s_sub_u32 s42, s42, 1\n\t" \
                "v_and_b32 v13, 0xffff, v13\n\t" \
                "s_mov_b64 exec, -1\n\t" \
                "s_cmp_lg_u32 s42, 0\n\t") \
        "s_cbranch_scc1 LA_loop%=\n\t" \
        "s_waitcnt lgkmcnt(0)\n\t" \
        "s_setprio 3\n\t" \
        MF(R_OB, "v[124:127]", "v[100:103]", R_OB) \
        "s_nop 15\n\t" \
        : "+{v[64:79]}"(R.oa), "+{v[80:95]}"(R.ob), "+{v[100:103]}"(R.p1), "+{v[124:127]}"(R.v1), "+{v12}"(R.ka), "+{v13}"(R.va) \
        : NMIN "{v14}"(R.go), [qa] "s"(qa), [qb] "s"(qb), [rk] "s"(rk), [rv] "s"(rv), [lw] "s"(w * 1024) \
        : "memory", "scc", "vcc", "s40", "s41", "s42", "s43", "s44", "s45", "v0", "v1", "v4", "v5", "v6", "v7", "v8", "v9", "v10", "v11", "v15", \
          "v32", "v33", "v34", "v35", "v36", "v37", "v38", "v39", "v40", "v41", "v42", "v43", "v44", "v45", "v46", "v47", \
          "v48", "v49", "v50", "v51", "v52", "v53", "v54", "v55", "v56", "v57", "v58", "v59", "v60", "v61", "v62", "v63", \
          "v96", "v97", "v98", "v99", "v104", "v105", "v106", "v107", "v108", "v109", "v110", "v111", \
          "v112", "v113", "v114", "v115", "v116", "v117", "v118", "v119", "v120", "v121", "v122", "v123")
DI void attn_pass_shift(AttnRegs& R, const char* qa, const char* qb, const char* ksrc, const char* vsrc, int w) {
    const u32x4 rk = srd(ksrc, 65536), rv = srd(vsrc, 65536);
#define NM_IN "{v[16:31]}"(R.nm),
    ATTN_ASM("v[16:31]", NM_IN);
}
#define FISS(S)                                                  \
    "s_add_u32 m0, %[lw], " #S "*16384\n\t"                      \
    "s_add_u32 s43, s43, 0x2000\n\t"                             \
    "buffer_load_dwordx4 v14, %[rk], s43 offen sc1 lds\n\t"      \
    "s_add_u32 m0, %[lw], " #S "*16384+8192\n\t"                 \
    "s_nop 0\n\t"                                                \
    "buffer_load_dwordx4 v14, %[rv], s43 offen sc1 lds\n\t"
#define FI2(I) "s_cbranch_vccz 7f\n\t" PRI I "7:\n\t"
#define FI3(I) "s_cbranch_vccnz 7f\n\t" PRI I PR2 "7:\n\t"
#define LA2 ""
#define LB2 PR2
#define LB3 ""
#define FCHUNK(S, N, VC, VN, BND, I2, I3) FCHUNKX(S, N, VC, VN, BND, I2, I3, "")
#define FCHUNKX(S, N, VC, VN, BND, I2, I3, I0)                        \
    ATILE_E("0", #S "*16384+2048", VC, 1024, 1536, WL I0 PR0)       \
    ATILE_O("0", #S "*16384+3072", VC, 2048, 2560, WL PR1)       \
    ATILE_E("0", #N "*16384", VC, 3072, 3584, BND LA2 I2 LB2)    \
    ATILE_O("0", #N "*16384+1024", VN, 0, 512, WL I3 LB3)
#define FLD "global_load_dwordx2 v[0:1], v2, %[ff] sc1\n\t"
#define VCCSET "s_mov_b32 vcc_lo, %[lo4]\n\ts_mov_b32 vcc_hi, 0\n\t"
#define FBAR(VM) "s_waitcnt vmcnt(" #VM ") lgkmcnt(0)\n\ts_barrier\n\t"
DI void attn_pass(AttnRegs& R, const char* qa, const char* qb, const char* ksrc, const char* vsrc, const char* late_flags, u64 tag, int w) {
    const u32x4 rk = srd(ksrc, 65536), rv = srd(vsrc, 65536);
    asm volatile(
        "v_and_b32 v15, 0x3ff, v14\n\t"
        "s_mov_b32 s43, -0x2000\n\t"
        FISS(0)
        "global_load_dwordx4 v[4:7], v15, %[qa] sc1\n\t"
        "global_load_dwordx4 v[8:11], v15, %[qb] sc1\n\t"
        FISS(1) FISS(2) VCCSET
        "s_waitcnt vmcnt(4)\n\t"
        "s_barrier\n\t"
        "ds_read_b128 v[104:107], v12\n\t"
        "ds_read_b128 v[108:111], v12 offset:1024\n\t"
        "ds_read_b128 v[112:115], v13\n\t"
        "ds_read_b128 v[116:119], v13 offset:512\n\t"
        PR2
        "s_waitcnt lgkmcnt(3)\n\t"
        MF(R_SA, "v[104:107]", "v[4:7]", "0")
        "s_nop 15\n\t"
        FCHUNK(0, 1, "v13", "v16", FBAR(2), FI2(FISS(3)), FI3(FISS(3)))
        FCHUNK(1, 2, "v16", "v17", FBAR(2), "", "")
        FCHUNKX(2, 3, "v17", "v18",
               "s_waitcnt vmcnt(0) lgkmcnt(0)\n\t"
               "s_mov_b32 s45, 0x100000\n\t"
               "LA_p%=:\n\t"
               "v_cmp_ne_u64 vcc, %[tag], v[0:1]\n\t"
               "s_sub_u32 s45, s45, 1\n\t"
               "s_cbranch_scc1 LA_f%=\n\t"
               "s_cbranch_vccz LA_f%=\n\t"
               "global_load_dwordx2 v[0:1], v2, %[ff] sc1\n\t"
               "s_waitcnt vmcnt(0)\n\t"
               "s_branch LA_p%=\n\t"
               "LA_f%=:\n\t"
               "s_barrier\n\t" VCCSET, FI2(FISS(0) FISS(1)), FI3(FISS(0) FISS(1)), PRI FLD)
        FCHUNK(3, 0, "v18", "v13", FBAR(2), FI2(FISS(2)), FI3(FISS(2)))
        FCHUNK(0, 1, "v13", "v16", FBAR(2), FI2(FISS(3)), FI3(FISS(3)))
        FCHUNK(1, 2, "v16", "v17", FBAR(2), "", "")
        FCHUNK(2, 3, "v17", "v18", FBAR(0), "", "")
        FCHUNK(3, 0, "v18", "v13", WL, "", "")
        "s_waitcnt lgkmcnt(0)\n\t"
        "s_setprio 3\n\t"
        MF(R_OB, "v[124:127]", "v[100:103]", R_OB)
        "s_nop 15\n\t"
        "Lmk_hot_end:\n\t"
        : "+{v[64:79]}"(R.oa), "+{v[80:95]}"(R.ob), "+{v[100:103]}"(R.p1), "+{v[124:127]}"(R.v1), "+{v[20:23]}"(R.fin)
        : "{v12}"(R.ka), "{v13}"(R.va), "{v16}"(R.va1), "{v17}"(R.va2), "{v18}"(R.va3), "{v14}"(R.go), "{v2}"(R.fo),
          [qa] "s"(qa), [qb] "s"(qb), [rk] "s"(rk), [rv] "s"(rv), [lw] "s"(w * 1024), [tag] "s"(tag), [ff] "s"(late_flags), [lo4] "s"(1 - (w >> 2))
        : "memory", "scc", "vcc", "s43", "s45", "v0", "v1", "v4", "v5", "v6", "v7", "v8", "v9", "v10", "v11", "v15",
          "v32", "v33", "v34", "v35", "v36", "v37", "v38", "v39", "v40", "v41", "v42", "v43", "v44", "v45", "v46", "v47",
          "v48", "v49", "v50", "v51", "v52", "v53", "v54", "v55", "v56", "v57", "v58", "v59", "v60", "v61", "v62", "v63",
          "v96", "v97", "v98", "v99", "v104", "v105", "v106", "v107", "v108", "v109", "v110", "v111",
          "v112", "v113", "v114", "v115", "v116", "v117", "v118", "v119", "v120", "v121", "v122", "v123");
}

DI void wait_qkv(const char* ws, int a, u64 tag) { wait_flags<PSL>(ws + WS_FQ, qkv_dep(a, true), tag); }
DI void attn_role(const char* x, const char* wqkv, char* ws, char* lds, int a, int w) {
    const int kh = w >> 2;
    const int head = 4 * (a & 7) + ((a >> 3) & 3), qt = 8 * (a >> 5) + 2 * (w & 3);
    ST_DECL; ST(0);
    __builtin_amdgcn_s_setprio(3);
    u64 tag;
    const int ti = a >> 3, trow = 8 * (a & 1) + (ti & 7) + (ti >= 24 ? 16 : 0), tcol = (ti >= 24 ? 0 : 4 * (ti >> 3)) + ((a & 7) >> 1);
    if (w >= 4) {
        qkv_loader(x, wqkv, trow, tcol, w - 4);
        tag = uni64(*(LAS const u64*)(QCNT + 8));
        if (w == 4) wait_qkv(ws, a, tag);
    } else {
        tag = next_tag(ld_epoch(ws));
        if (tcol < 8) qkv_body<true>(ws, lds, trow, tcol, a, w, tag ST_PASS);
        else qkv_body<false>(ws, lds, trow, tcol, a, w, tag ST_PASS);
    }
    AttnRegs R;
    attn_prep(R, a, w);
    {
        const int lane = lane_id();
        const unsigned st = (lane & 16) ? 0u : 16384u;
        R.va1 = R.va + st; R.va2 = R.va1 + st; R.va3 = R.va2 + st;
        R.fo = qkv_dep(a, false);
        const int o = w * 512 + lane * 8;
        const unsigned one = ((o & 0x1b0) == 0) ? 0x3f803f80u : 0u;
        *(LAS u32x2*)((LAS char*)lds + A_ONES + o) = u32x2{one, one};
        R.fin = u32x4{(unsigned)(w * 2048 + lane * 16), (unsigned)((w ^ 4) * 2048 + lane * 16), (unsigned)(A_LSUM + w * 256 + lane * 4), (unsigned)(A_LSUM + (w ^ 4) * 256 + lane * 4)};
        asm volatile("" : "+{v16}"(R.va1), "+{v17}"(R.va2), "+{v18}"(R.va3), "+{v2}"(R.fo), "+{v[20:23]}"(R.fin));
    }
    ST(5);
    wgbar();
    ST(6);
    const char* ksrc = ws + WS_K + (size_t)head * 65536;
    const char* qsrc = ws + WS_Q + (size_t)(head * 64 + qt) * 1024;
    auto finish = [&](int att) {
        const f32x16 oa = R.oa, ob = R.ob;
        u32x4 fin = R.fin;
        if (att) {
            const int lane = lane_id();
            fin = u32x4{(unsigned)(w * 2048 + lane * 16), (unsigned)((w ^ 4) * 2048 + lane * 16), (unsigned)(A_LSUM + w * 256 + lane * 4), (unsigned)(A_LSUM + (w ^ 4) * 256 + lane * 4)};
        }
        LAS char* ex = (LAS char*)(uintptr_t)fin[0];
        LAS const char* exr = (LAS const char*)(uintptr_t)fin[1];
        *(LAS f32x4*)ex = f32x4{ob[0], ob[1], ob[2], ob[3]};
        *(LAS f32x4*)(ex + 1024) = f32x4{ob[4], ob[5], ob[6], ob[7]};
        *(LAS float*)(uintptr_t)fin[2] = ob[8];
        if (att == 0) {
            float big = fmaxf(oa[8], ob[8]) * 2.7e8f;
#pragma unroll
            for (int i = 0; i < 8; ++i) big = fmaxf(fmaxf(big, __builtin_fabsf(oa[i])), __builtin_fabsf(ob[i]));
            const bool bad = !__all(fminf(oa[8], ob[8]) > 8.1e-28f && big < 1.7e38f);
            ((LAS unsigned*)((LAS char*)lds + A_VOTE))[w] = bad;
        }
        wgbar();
        const f32x4 t0 = *(LAS const f32x4*)exr, t1 = *(LAS const f32x4*)(exr + 1024);
        const float t2 = *(LAS const float*)(uintptr_t)fin[3];
        unsigned redo = 0;
        if (att == 0) {
            const u32x4 v0 = *(LAS const u32x4*)((LAS char*)lds + A_VOTE), v1 = *(LAS const u32x4*)((LAS char*)lds + A_VOTE + 16);
            redo = __builtin_amdgcn_readfirstlane(v0[0] | v0[1] | v0[2] | v0[3] | v1[0] | v1[1] | v1[2] | v1[3]);
        }
        const float rinv = __builtin_amdgcn_rcpf(oa[8] + t2);
        u32x4 ov;
        ov[0] = cvtpk((oa[0] + t0[0]) * rinv, (oa[1] + t0[1]) * rinv);
        ov[1] = cvtpk((oa[2] + t0[2]) * rinv, (oa[3] + t0[3]) * rinv);
        ov[2] = cvtpk((oa[4] + t1[0]) * rinv, (oa[5] + t1[1]) * rinv);
        ov[3] = cvtpk((oa[6] + t1[2]) * rinv, (oa[7] + t1[3]) * rinv);
        if (att == 0 ? !redo : att == 1 + kh) {
            st16(ws + WS_O + (size_t)((qt + kh) * 32 + head) * 1024, fin[0] & 1023, ov);
            drain();
            st_flag(ws + WS_FO + ((qt + kh) * 32 + head) * 128, tag);
        }
        asm volatile("" : "+s"(redo));
        return redo;
    };
    ST(1);
    attn_pass(R, qsrc + kh * 1024, qsrc + (1 - kh) * 1024, ksrc, ksrc + (WS_V - WS_K), ws + WS_FQ + 32768, tag, w);
    ST(2);
    if (finish(0)) {
#pragma clang loop unroll(disable)
        for (int att = 1; att < 3; ++att) {
            drain();
            wgbar();
            const int lane = lane_id();
            const char* qa = qsrc + (att - 1) * 1024;
            bf16x8 qf;
            asm volatile("global_load_dwordx4 %0, %1, %2 sc1\n\ts_waitcnt vmcnt(0)" : "=v"(qf) : "v"(lane * 16), "s"(qa) : "memory");
            float mx = -INFINITY;
#pragma clang loop unroll(disable)
            for (int t = 0; t < 64; ++t) {
                bf16x8 kf;
                asm volatile("global_load_dwordx4 %0, %1, %2 sc1\n\ts_waitcnt vmcnt(0)" : "=v"(kf) : "v"(lane * 16), "s"(ksrc + t * 1024) : "memory");
                const f32x16 s = MFMA32(kf, qf, f32x16{});
#pragma unroll
                for (int i = 0; i < 16; ++i) mx = fmaxf(mx, s[i]);
            }
            mx = -swap32_max(mx);
            attn_prep(R, a, w);
#pragma unroll
            for (int i = 0; i < 16; ++i) R.nm[i] = mx;
            attn_pass_shift(R, qa, qa, ksrc, ksrc + (WS_V - WS_K), w);
            finish(att);
        }
    }
    ST(3); ST_OUT(0);
}

#define BPR "s_setprio 0\n\t"
#define BG1 ""
#define BG2 "s_sleep 1\n\t"
#define BG3 "s_sleep 1\n\t"
#define SISSUE                                                             \
    "s_mov_b32 m0, s44\n\t"                                                 \
    "s_add_u32 s45, %[ow], %[wst]\n\t"                                      \
    "buffer_load_dwordx4 %[g0], %[rw], %[ow] offen lds\n\t"                 \
    "buffer_load_dwordx4 %[g1], %[rw], %[ow] offen offset:1024 lds\n\t"     \
    "s_add_u32 m0, s44, 8192\n\t"                                           \
    "s_add_u32 s46, %[od], 4096\n\t"                                        \
    "buffer_load_dwordx4 %[d0], %[rd], %[od] offen sc1 lds\n\t"             \
    "buffer_load_dwordx4 %[d0], %[rd], %[od] offen offset:1024 sc1 lds\n\t" \
    "s_add_u32 s44, s44, 0x4000\n\t"                                        \
    "s_and_b32 s44, s44, 0xffff\n\t"                                        \
    "s_cmp_eq_u32 %[od], %[de]\n\t"                                         \
    "s_cselect_b32 %[ow], 0, s45\n\t"                                       \
    "s_cselect_b32 %[od], 0, s46\n\t"
#define SISW                                                               \
    "s_mov_b32 m0, s44\n\t"                                                 \
    "s_add_u32 s45, %[ow], %[wst]\n\t"                                      \
    "buffer_load_dwordx4 %[g0], %[rw], %[ow] offen lds\n\t"                 \
    "buffer_load_dwordx4 %[g1], %[rw], %[ow] offen offset:1024 lds\n\t"     \
    "s_add_u32 s44, s44, 0x4000\n\t"                                        \
    "s_mov_b32 %[ow], s45\n\t"
#define SISD                                                               \
    "s_add_u32 m0, s44, 8192\n\t"                                           \
    "s_add_u32 s44, s44, 0x4000\n\t"                                        \
    "buffer_load_dwordx4 %[d0], %[rd], %[od] offen sc1 lds\n\t"             \
    "buffer_load_dwordx4 %[d0], %[rd], %[od] offen offset:1024 sc1 lds\n\t" \
    "s_add_u32 %[od], %[od], 4096\n\t"
struct SG { u32x4 rw, rd; unsigned wst, de, ow, od, a, b, c; };
DI void sgemm_prew(SG& G, int lw) {
    asm volatile("s_mov_b32 s44, %[lo]\n\t" SISW SISW SISW
                 : [ow] "+s"(G.ow) : [wst] "s"(G.wst), [rw] "s"(G.rw), [lo] "s"(lw * 2048), [g0] "v"(G.a), [g1] "v"(G.b) : "memory", "scc", "s44", "s45");
}
#define WISSUE                                                              \
    "s_and_b32 s45, s44, 0xc000\n\t"                                        \
    "s_add_u32 m0, s45, %[lo2]\n\t"                                         \
    "s_lshl_b32 s45, s45, 3\n\t"                                            \
    "buffer_load_dwordx4 %[o0], %[ro], s45 offen lds\n\t"                   \
    "buffer_load_dwordx4 %[o1], %[ro], s45 offen offset:1024 lds\n\t"       \
    "buffer_load_dwordx4 %[o2], %[ro], s45 offen offset:2048 lds\n\t"       \
    "buffer_load_dwordx4 %[o3], %[ro], s45 offen offset:3072 lds\n\t"       \
    "s_add_u32 s44, s44, 0x4000\n\t"                                        \
    "s_and_b32 s44, s44, 0xffff\n\t"
template <int WO>
DI void sgemm_load(SG& G, int n, int lw, u32x4 ro, unsigned o0) {
    asm volatile(
        "s_mov_b32 s44, %[lo]\n\t"
        ".if %c[wo]\n\t"
        "s_sub_u32 s47, %[n], 5\n\t"
        SISD SISD SISD
        "s_waitcnt vmcnt(4)\n\ts_barrier\n\t" SISSUE
        "s_waitcnt vmcnt(6)\n\ts_barrier\n\t" SISSUE
        ".else\n\t"
        "s_sub_u32 s47, %[n], 3\n\t"
        SISSUE SISSUE SISSUE
        ".endif\n\t"
        "LL_loop%=:\n\t"
        "s_waitcnt vmcnt(8)\n\t"
        "s_barrier\n\t"
        SISSUE
        "s_sub_u32 s47, s47, 1\n\t"
        "s_cmp_lg_u32 s47, 0\n\t"
        "s_cbranch_scc1 LL_loop%=\n\t"
        ".if %c[wo]\n\t"
        "s_waitcnt vmcnt(8)\n\ts_barrier\n\t" WISSUE
        "s_waitcnt vmcnt(8)\n\ts_barrier\n\t" WISSUE
        "s_waitcnt vmcnt(8)\n\ts_barrier\n\t" WISSUE
        "s_barrier\n\t" WISSUE
        "s_barrier\n\t"
        ".else\n\t"
        "s_waitcnt vmcnt(8)\n\ts_barrier\n\t"
        "s_waitcnt vmcnt(4)\n\ts_barrier\n\t"
        "s_waitcnt vmcnt(0)\n\ts_barrier\n\t"
        "s_barrier\n\t"
        ".endif\n\t"
        : [ow] "+s"(G.ow), [od] "+s"(G.od)
        : [wst] "s"(G.wst), [rw] "s"(G.rw), [rd] "s"(G.rd), [lo] "s"(lw * 2048), [de] "s"(G.de), [n] "s"(n), [g0] "v"(G.a), [g1] "v"(G.b), [d0] "v"(G.c),
          [wo] "i"(WO), [ro] "s"(ro), [lo2] "s"(lw * 4096), [o0] "v"(o0), [o1] "v"(o0 + 7168), [o2] "v"(o0 + 14336), [o3] "v"(o0 + 21504)
        : "memory", "scc", "s44", "s45", "s46", "s47");
}
#define SRD3(B, SLOT, K, TD)                                                                         \
    "ds_read_b64_tr_b16 v[" #B ":" #B "+1], %[t] offset:" #SLOT "*16384+" #K "*2048\n\t"              \
    "ds_read_b64_tr_b16 v[" #B "+2:" #B "+3], %[t] offset:" #SLOT "*16384+" #K "*2048+" #TD "\n\t"    \
    "ds_read_b128 v[" #B "+4:" #B "+7], %[dl] offset:" #SLOT "*16384+" #K "*1024\n\t"
#define SSTAGE(SLOT, MM, TD, N0, N1, N2, N3, P0, P1, P2, P3)                          \
    "s_waitcnt lgkmcnt(0)\n\t"                                                        \
    "s_barrier\n\t"                                                                   \
    MM(P0) SRD3(N0, SLOT, 0, TD) SRD3(N1, SLOT, 1, TD) BG1                            \
    MM(P1) SRD3(N2, SLOT, 2, TD) SRD3(N3, SLOT, 3, TD) BG2                            \
    MM(P2) BG3 MM(P3)
#define SST_E(SLOT, MM, TD) SSTAGE(SLOT, MM, TD, 32, 40, 48, 56, 96, 104, 112, 120)
#define SST_O(SLOT, MM, TD) SSTAGE(SLOT, MM, TD, 96, 104, 112, 120, 32, 40, 48, 56)
#define SMM_NONE(P) ""
#define SMM_WA(P) MF("v[64:79]", "v[" #P ":" #P "+3]", "v[" #P "+4:" #P "+7]", "v[64:79]")
#define SMM_WB(P) MF("v[80:95]", "v[" #P "+4:" #P "+7]", "v[" #P ":" #P "+3]", "v[80:95]")
#define SGEMM_ASM(ACC, MM, TD, NLOOP, TAIL, FIN)                                                                       \
    asm volatile(                                                                                                 \
        "s_mov_b32 s44, " #NLOOP "\n\t" BPR                                                                       \
        "s_barrier\n\t"                                                                                           \
        SRD3(32, 0, 0, TD) SRD3(40, 0, 1, TD) SRD3(48, 0, 2, TD) SRD3(56, 0, 3, TD)                               \
        "LS_loop%=:\n\t"                                                                                          \
        SST_O(1, MM, TD) SST_E(2, MM, TD) SST_O(3, MM, TD) SST_E(0, MM, TD)                                       \
        "s_sub_u32 s44, s44, 1\n\t"                                                                               \
        "s_cmp_lg_u32 s44, 0\n\t"                                                                                 \
        "s_cbranch_scc1 LS_loop%=\n\t"                                                                            \
        TAIL                                                                                                      \
        "s_waitcnt lgkmcnt(0)\n\t" FIN                                                                            \
        MM(96) MM(104) MM(112) MM(120)                                                                \
        "s_setprio 3\n\ts_nop 15\n\t"                                                                                            \
        : ACC(acc)                                                                                                \
        : [t] "v"(G.a), [dl] "v"(G.b)                                                                             \
        : "memory", "scc", "s44",                                                                                 \
          "v32", "v33", "v34", "v35", "v36", "v37", "v38", "v39", "v40", "v41", "v42", "v43", "v44", "v45", "v46", "v47", \
          "v48", "v49", "v50", "v51", "v52", "v53", "v54", "v55", "v56", "v57", "v58", "v59", "v60", "v61", "v62", "v63", \
          "v96", "v97", "v98", "v99", "v100", "v101", "v102", "v103", "v104", "v105", "v106", "v107", "v108", "v109", "v110", "v111", \
          "v112", "v113", "v114", "v115", "v116", "v117", "v118", "v119", "v120", "v121", "v122", "v123", "v124", "v125", "v126", "v127")

template <int MODE>
DI SG sgemm_setup(const char* wbase, unsigned wbytes, int wrow, const char* dbase, int dblk, int rot, int w) {
    const int lane = lane_id(), row = lane >> 3, lw = w & 3;
    const int q = (lane & 15) >> 2, h2 = lane >> 5;
    SG G;
    G.rw = srd(wbase, wbytes); G.rd = srd(dbase, 2 * dblk);
    G.wst = 64 * wrow; G.de = (MODE == 0 ? 31 : 21) * 4096; G.ow = rot * G.wst; G.od = rot * 4096;
    if (w >= 4) {
        G.a = (16 * lw + row) * wrow + (((lane & 7) ^ (((row >> 1) & 1) << 2)) << 4);
        G.b = G.a + 8 * wrow - 1024;
        G.c = (lw >> 1) * dblk + (lw & 1) * 2048 + lane * 16;
    } else {
        G.a = ((MODE == 0 ? 8 : 4) * h2 + q) * 128 + (((lw & 1) ^ ((q >> 1) & 1)) << 6) + ((lane >> 4) & 1) * 32 + (lane & 3) * 8;
        G.b = 8192 + (lw >> 1) * 4096 + lane * 16;
        G.c = 0;
    }
    asm volatile("" : "+v"(G.a), "+v"(G.b), "+v"(G.c));
    return G;
}
#define ACC0 "+{v[64:79]}"
#define ACC1 "+{v[80:95]}"
template <int MODE>
DI void sgemm_run(f32x16& acc, SG& G) {
    if (MODE == 0) SGEMM_ASM(ACC0, SMM_WA, 512, 7, SST_O(1, SMM_WA, 512) SST_E(2, SMM_WA, 512) SST_O(3, SMM_WA, 512), "s_setprio 3\n\t");
    else SGEMM_ASM(ACC1, SMM_WB, 1024, 5, SST_O(1, SMM_WB, 1024), "s_barrier\n\ts_setprio 3\n\t");
}
DI f32x4 sub4(const f32x16& a, int g) { return f32x4{a[4 * g], a[4 * g + 1], a[4 * g + 2], a[4 * g + 3]}; }

DI unsigned gelu2(float a, float b) {
    constexpr float c1 = -2.f * 0.7978845608028654f * 1.4426950408889634f, c2 = c1 * 0.044715f;
    const f32x2 u = {a, b};
    const f32x2 z = u * (u * u * c2 + c1);
    const f32x2 d = f32x2{__builtin_amdgcn_exp2f(z[0]), __builtin_amdgcn_exp2f(z[1])} + 1.f;
    const f32x2 o = u * f32x2{__builtin_amdgcn_rcpf(d[0]), __builtin_amdgcn_rcpf(d[1])};
    return cvtpk(o[0], o[1]);
}

#define WRD(R0, R1, R2, R3, STEP)                                                    \
    "ds_read_b64_tr_b16 " R0 ", %[t0] offset:" #STEP "*2048\n\t"                      \
    "ds_read_b64_tr_b16 " R1 ", %[t0] offset:" #STEP "*2048+1024\n\t"                 \
    "ds_read_b64_tr_b16 " R2 ", %[t1] offset:" #STEP "*2048\n\t"                      \
    "ds_read_b64_tr_b16 " R3 ", %[t1] offset:" #STEP "*2048+1024\n\t"
#define WRD0(S) WRD("v[32:33]", "v[34:35]", "v[36:37]", "v[38:39]", S)
#define WRD1(S) WRD("v[40:41]", "v[42:43]", "v[44:45]", "v[46:47]", S)
#define WRD2(S) WRD("v[48:49]", "v[50:51]", "v[52:53]", "v[54:55]", S)
#define WRD3(S) WRD("v[56:57]", "v[58:59]", "v[60:61]", "v[62:63]", S)
#define WMM(WAIT, W0, W1, O) "s_waitcnt " WAIT "\n\t" MF("v[96:111]", W0, O, "v[96:111]") MF("v[112:127]", W1, O, "v[112:127]")
#define OLD(K, R, V, OFF)                                             \
    "5:\n\t"                                                           \
    "s_bitcmp1_b32 s40, " #K "\n\t"                                    \
    "s_cbranch_scc1 6f\n\t"                                            \
    "global_load_dwordx2 v[24:25], %[fo], %[f] sc1\n\t"                       \
    "s_sub_u32 s41, s41, 1\n\t"                                        \
    "s_cselect_b32 s42, -1, 0\n\t"                                     \
    "s_waitcnt vmcnt(0)\n\t"                                           \
    "v_cmp_eq_u64 vcc, %[tag], v[24:25]\n\t"                                \
    "s_or_b32 s40, vcc_lo, s42\n\t"                                    \
    "s_bitcmp1_b32 s40, " #K "\n\t"                                    \
    "s_cbranch_scc1 6f\n\t"                                            \
    "s_sleep 1\n\t"                                                    \
    "s_branch 5b\n\t"                                                  \
    "6:\n\t"                                                           \
    "global_load_dwordx4 " R ", " V ", %[o] offset:" #OFF " sc1\n\t"
DI void wo_gemm(f32x16& a0, f32x16& a1, f32x4& k0, f32x4& k1, float& k2, u32x4& k3, const char* osrc, const char* fsrc, unsigned seen, u64 tag,
                unsigned t0, unsigned t1, unsigned d0, unsigned d1, unsigned fo) {
    asm volatile(
        WRD0(0) WRD1(1) WRD2(2) WRD3(3)
        "s_mov_b32 s40, %[seen]\n\t"
        "s_mov_b32 s41, 0x100000\n\t"
        "s_setprio 0\n\t"
        OLD(0, "v[64:67]", "%[d0]", 0) OLD(1, "v[68:71]", "%[d0]", 1024) OLD(2, "v[72:75]", "%[d0]", 2048) OLD(3, "v[76:79]", "%[d0]", 3072)
        OLD(4, "v[80:83]", "%[d1]", 0) OLD(5, "v[84:87]", "%[d1]", 1024) OLD(6, "v[88:91]", "%[d1]", 2048) OLD(7, "v[92:95]", "%[d1]", 3072)
        "s_setprio 3\n\t"
        "s_waitcnt vmcnt(7) lgkmcnt(0)\n\t"
        MF("v[96:111]", "v[32:35]", "v[64:67]", "0") MF("v[112:127]", "v[36:39]", "v[64:67]", "0")
        WRD0(4)
        WMM("vmcnt(6)", "v[40:43]", "v[44:47]", "v[68:71]") WRD1(5)
        WMM("vmcnt(5)", "v[48:51]", "v[52:55]", "v[72:75]") WRD2(6)
        WMM("vmcnt(4)", "v[56:59]", "v[60:63]", "v[76:79]") WRD3(7)
        WMM("vmcnt(3) lgkmcnt(12)", "v[32:35]", "v[36:39]", "v[80:83]")
        WMM("vmcnt(2) lgkmcnt(0)", "v[40:43]", "v[44:47]", "v[84:87]")
        WMM("vmcnt(1)", "v[48:51]", "v[52:55]", "v[88:91]")
        "s_barrier\n\t"
        WMM("vmcnt(0)", "v[56:59]", "v[60:63]", "v[92:95]")
        "s_nop 15\n\t"
        : "={v[96:111]}"(a0), "={v[112:127]}"(a1),
          "+{v[12:15]}"(k0), "+{v[16:19]}"(k1), "+{v20}"(k2), "+{v[28:31]}"(k3)
        : [o] "s"(osrc), [f] "s"(fsrc), [tag] "s"(tag), [seen] "s"(seen), [d0] "v"(d0), [d1] "v"(d1), [t0] "v"(t0), [t1] "v"(t1), [fo] "v"(fo)
        : "memory", "scc", "vcc", "s40", "s41", "s42", "v24", "v25",
          "v32", "v33", "v34", "v35", "v36", "v37", "v38", "v39", "v40", "v41", "v42", "v43", "v44", "v45", "v46", "v47",
          "v48", "v49", "v50", "v51", "v52", "v53", "v54", "v55", "v56", "v57", "v58", "v59", "v60", "v61", "v62", "v63",
          "v64", "v65", "v66", "v67", "v68", "v69", "v70", "v71", "v72", "v73", "v74", "v75", "v76", "v77", "v78", "v79",
          "v80", "v81", "v82", "v83", "v84", "v85", "v86", "v87", "v88", "v89", "v90", "v91", "v92", "v93", "v94", "v95");
}

DI void mixer_role(const char* x, const char* wqkv, const char* gw, const char* wo, const char* w1, const char* b1, const char* w2, const char* b2,
                   char* ws, char* out, char* lds, int bi, int w) {
    const int j = bi & 7, i = bi >> 3;
    __builtin_amdgcn_s_setprio(LNP);
    ST_DECL; ST(0);
    const u64 ep = ld_epoch(ws);
    u64 tag;
    {
        const int lane = lane_id(), h2 = lane >> 5, tid = 64 * w + lane;
        const u32x4 xr = *(GAS const u32x4*)(x + (size_t)(8 * bi + w) * 1024 + lane * 16);
        const u32x4 gr = *(GAS const u32x4*)(gw + lane * 16);
        tag = next_tag(ep);
        float v[8], gg[8];
#pragma unroll
        for (int k = 0; k < 4; ++k) { v[2 * k] = bflo(xr[k]); v[2 * k + 1] = bfhi(xr[k]); gg[2 * k] = bflo(gr[k]); gg[2 * k + 1] = bfhi(gr[k]); }
        float s1 = 0.f, s3 = 0.f;
#pragma unroll
        for (int k = 0; k < 8; ++k) { s1 += v[k]; s3 += v[k] * gg[k]; }
        const float mu = wave_sum(s1) * (1.f / 512.f);
        s3 = wave_sum(s3);
        float s2 = 0.f;
#pragma unroll
        for (int k = 0; k < 8; ++k) { v[k] -= mu; s2 += v[k] * v[k]; }
        const float rs = __builtin_amdgcn_rsqf(wave_sum(s2) * (1.f / 512.f) + 1e-5f);
        u32x4 hv;
#pragma unroll
        for (int k = 0; k < 4; ++k) hv[k] = cvtpk(v[2 * k] * rs, v[2 * k + 1] * rs);
        *(LAS u32x4*)((LAS char*)lds + w * 1024 + lane * 16) = hv;
        const float g = __builtin_amdgcn_rcpf(1.f + __builtin_amdgcn_exp2f(-1.4426950408889634f * s3));
        st4(ws + WS_G + (8 * bi + w) * 4, uni(g));
        wgbar();
        LAS const unsigned short* lb = (LAS const unsigned short*)lds + tid;
#pragma unroll
        for (int k = 0; k < 4; ++k) hv[k] = (unsigned)lb[(2 * k) * 512] | ((unsigned)lb[(2 * k + 1) * 512] << 16);
        st16(ws + WS_H + (size_t)(2 * w * 128 + (bi >> 1)) * 1024 + (bi & 1) * 512, h2 * 131072 + (lane & 31) * 16, hv);
        drain();
        wgbar();
        if (w == 0) st_flag(ws + WS_FL + bi * 128, tag);
    }
    __builtin_amdgcn_s_setprio(3);
    const int wb = w & 1, db = (w >> 1) & 1, hf = w >> 2;
    const int L = (j >> 1) * 22 + i, ft = L >> 2, dt1 = 4 * (j & 1) + (L & 3);
    const int st = 8 * (j >> 1) + (i & 7), dt = 4 * (j & 1) + (i >> 3);
    SG G1, G2;
    unsigned ex, l16, og, wof;
    {
        const int lane = lane_id();
        og = (64 * st + 32 * wb + (lane & 31)) * 4;
        G1 = sgemm_setup<0>(w1 + ft * 128, 2048 * 2752 - ft * 128, 2752, ws + WS_H + (size_t)2 * dt1 * 131072, 131072, ROT1, w);
        G2 = sgemm_setup<1>(w2 + st * 128, 1376 * 4096 - st * 128, 4096, ws + WS_U + (size_t)2 * dt * 86 * 1024, 86 * 1024, ROT2, w);
        l16 = lane * 16;
        ex = 65536 + (w & 3) * 2048 + l16;
        wof = (32 * (w & 3) + (lane >> 3)) * 1024 + (((lane & 7) ^ (((lane >> 4) & 1) << 2)) << 4);
        asm volatile("" : "+v"(ex), "+v"(l16), "+v"(og), "+v"(wof));
    }
    {
        const int row = 16 + 8 * (j & 1) + (i & 7), col = 8 + 8 * (i >> 4) + 2 * (j >> 1) + ((i >> 3) & 1);
        if (w == 0) {
            *(LAS unsigned*)QCNT = 0u;
            wait_flags<2>(ws + WS_FQ + bi * 128, 0, tag);
        }
        wgbar();
        if (w >= 4) qkv2_loader(x, wqkv, row, col, w - 4);
        else qkv2_body(ws, row, col, bi, w, tag);
    }
    ST(6);
    if (PFA && i >= 22) {
        const int wv = ((i - 22) * 8 + j) * 8 + w;
        if (PF1) warm(w1, 1376, wv, l16 * 4);
        for (int k = 0; k < PFD; ++k) __builtin_amdgcn_s_sleep(64);
        warm(w2, 1376, wv, l16 * 4);
        warm(wo, 128, wv, l16 * 4);
    }
    if (i < 22) {
        if (w >= 4) wait_flags<8>(ws + WS_FL + (w - 4) * 8192, l16 * 8, tag);
        wgbar();
        ST(7);
        f32x4 v0, v1;
        if (w >= 4) {
            sgemm_load<0>(G1, 32, w - 4, G1.rw, 0);
            v0 = *(LAS const f32x4*)(uintptr_t)ex; v1 = *(LAS const f32x4*)(uintptr_t)(ex + 1024);
        } else {
            f32x16 acc1;
            const char* bsrc = b1 + (64 * ft + (ft < 21 ? 32 * wb : 0)) * 2 + (l16 >> 9) * 8;
#pragma unroll
            for (int k = 0; k < 4; ++k) {
                const u32x2 bv = *(GAS const u32x2*)(bsrc + 16 * k);
                acc1[4 * k] = bflo(bv[0]); acc1[4 * k + 1] = bfhi(bv[0]); acc1[4 * k + 2] = bflo(bv[1]); acc1[4 * k + 3] = bfhi(bv[1]);
            }
            sgemm_run<0>(acc1, G1);
            *(LAS f32x4*)(uintptr_t)ex = sub4(acc1, 2); *(LAS f32x4*)(uintptr_t)(ex + 1024) = sub4(acc1, 3);
            wgbar();
            v0 = sub4(acc1, 0); v1 = sub4(acc1, 1);
        }
        ST(1);
        const u32x4 o = {gelu2(v0[0], v0[1]), gelu2(v0[2], v0[3]), gelu2(v1[0], v1[1]), gelu2(v1[2], v1[3])};
        if (ft < 21 || wb == 0) st16(ws + WS_U + (size_t)((2 * dt1 + db) * 86 + 4 * ft + 2 * wb + hf) * 1024, l16, o);
        drain();
        wgbar();
        if (w == 0) st_flag(ws + WS_FU + (dt1 * 32 + ft) * 128, tag);
    }
    ST(2);
    if (w >= 4) sgemm_prew(G2, w - 4);
    unsigned t0, t1, d1, fo;
    u32x4 fin;
    {
        const int lane = l16 >> 4, q = (lane & 15) >> 2, kq = 2 * db + hf;
        const unsigned tb = (w >> 1) * 16384 + (4 * (lane >> 5) + q) * 128 + ((lane >> 4) & 1) * 32 + (lane & 3) * 8;
        t0 = tb + ((q >> 1) & 1) * 64; t1 = tb + (((q >> 1) & 1) ^ 1) * 64; d1 = l16 + 4096; fo = (lane & 7) * 128;
        fin = u32x4{w * 8192 + l16, wb * 8192 + kq * 2048 + l16, (unsigned)((64 * st + 32 * wb + (lane & 31)) * 1024 + 128 * dt + kq * 32 + (lane >> 5) * 16), 0u};
        asm volatile("" : "+v"(t0), "+v"(t1), "+v"(d1), "+v"(fo), "+{v[28:31]}"(fin));
    }
    if (bi == 255 && w == 1) {
        wait_flags256(ws + WS_FQ, l16 * 8, tag);
        wait_flags256(ws + WS_FL, l16 * 8, tag);
        st_flag(ws + WS_EPOCH, tag);
    }
    if (w == 0) wait_flags<8>(ws + WS_FU + dt * 4096, (l16 < 22 * 16 ? l16 : 0) * 8, tag);
    wgbar();
    ST(3);
    const char* fsrc = ws + WS_FO + ((2 * st + wb) * 32 + 8 * (w >> 1)) * 128;
    u64 f0;
    f32x4 bp0, bp1;
    float g;
    {
        asm volatile("global_load_dword %0, %1, %2 sc1" : "={v20}"(g) : "v"(og), "s"(ws + WS_G) : "memory");
        if (w >= 4) {
            sgemm_load<1>(G2, 22, w - 4, srd(wo + dt * 128, 524288 - dt * 128), wof);
            asm volatile("global_load_dwordx2 %0, %1, %2 sc1" : "={v[22:23]}"(f0) : "v"(fo), "s"(fsrc) : "memory");
            bp0 = *(LAS const f32x4*)(uintptr_t)ex; bp1 = *(LAS const f32x4*)(uintptr_t)(ex + 1024);
        } else {
            const float bb = bflo(*(GAS const unsigned short*)(b2 + (og >> 1)));
            f32x16 acc2 = {bb, bb, bb, bb, bb, bb, bb, bb, bb, bb, bb, bb, bb, bb, bb, bb};
            sgemm_run<1>(acc2, G2);
            asm volatile("global_load_dwordx2 %0, %1, %2 sc1" : "={v[22:23]}"(f0) : "v"(fo), "s"(fsrc) : "memory");
            *(LAS f32x4*)(uintptr_t)ex = sub4(acc2, 2); *(LAS f32x4*)(uintptr_t)(ex + 1024) = sub4(acc2, 3);
            wgbar();
            bp0 = sub4(acc2, 0); bp1 = sub4(acc2, 1);
            drain();
        }
        asm volatile("" : "+{v20}"(g));
        ST(4);
        bp0 *= 1.f - g;
        bp1 *= 1.f - g;
        asm volatile("" : "+v"(bp0), "+v"(bp1));
    }
    asm volatile("s_waitcnt vmcnt(0)\n\ts_barrier" : "+{v[22:23]}"(f0)::"memory");
    ST(5);
    f32x16 a0, a1;
    wo_gemm(a0, a1, bp0, bp1, g, fin, ws + WS_O + (size_t)((2 * st + wb) * 32 + 8 * (w >> 1)) * 1024,
            fsrc, (unsigned)__ballot(f0 == tag), tag, t0, t1, l16, d1, fo);
    {
        LAS char* ex = (LAS char*)(uintptr_t)fin[0];
#pragma unroll
        for (int k = 0; k < 4; ++k) { *(LAS f32x4*)(ex + k * 1024) = sub4(a0, k); *(LAS f32x4*)(ex + 4096 + k * 1024) = sub4(a1, k); }
        wgbar();
        LAS const char* exr = (LAS const char*)(uintptr_t)fin[1];
        f32x4 s0 = *(LAS const f32x4*)exr, s1 = *(LAS const f32x4*)(exr + 1024);
#pragma unroll
        for (int k = 1; k < 4; ++k) { s0 += *(LAS const f32x4*)(exr + k * 16384); s1 += *(LAS const f32x4*)(exr + k * 16384 + 1024); }
        s0 = s0 * g + bp0;
        s1 = s1 * g + bp1;
        const auto r0 = __builtin_amdgcn_permlane32_swap(cvtpk(s0[0], s0[1]), cvtpk(s1[0], s1[1]), false, false);
        const auto r1 = __builtin_amdgcn_permlane32_swap(cvtpk(s0[2], s0[3]), cvtpk(s1[2], s1[3]), false, false);
        asm volatile("global_store_dwordx4 %0, %1, %2" ::"v"(fin[2]), "v"(u32x4{r0[0], r1[0], r0[1], r1[1]}), "s"(out) : "memory");
    }
    ST_OUT(0);
}

constexpr int MK_LDS = 81920;
__global__ __launch_bounds__(512, 4) void mk(const char* x, const char* wqkv, const char* wo, const char* w1, const char* b1,
                                             const char* w2, const char* b2, const char* gw, char* ws, char* out) {
    extern __shared__ __attribute__((aligned(16))) char lds[];
    const int w = __builtin_amdgcn_readfirstlane(threadIdx.x >> 6);
    if (blockIdx.x >= 256 && w == 7) {
        asm volatile("s_getpc_b64 s[40:41]\n\t"
                     "Lmk_hot_begin:\n\t"
                     "s_cmp_lt_u32 %[o], Lmk_hot_end-Lmk_hot_begin-4096\n\t"
                     "s_cbranch_scc0 1f\n\t"
                     "s_add_u32 s40, s40, %[o]\n\t"
                     "s_addc_u32 s41, s41, 0\n\t"
                     "s_and_b32 s40, s40, -4\n\t"
                     "s_mov_b32 m0, 0x13e00\n\t"
                     "s_nop 0\n\t"
                     "global_load_lds_dword %[v], s[40:41]\n\t"
                     "1:"
                     :: [o] "s"(((blockIdx.x - 256) >> 3) * 4096), [v] "v"(lane_id() * 64) : "memory", "scc", "s40", "s41");
    }
    if (blockIdx.x < 256) attn_role(x, wqkv, ws, lds, blockIdx.x, w);
    else mixer_role(x, wqkv, gw, wo, w1, b1, w2, b2, ws, out, lds, blockIdx.x - 256, w);
}

extern "C" void kernel_launch(void* const* d_in, const int* in_sizes, int n_in, void* d_out, int out_size, void* d_ws, size_t ws_size,
                              hipStream_t stream) {
    (void)hipFuncSetAttribute((const void*)mk, hipFuncAttributeMaxDynamicSharedMemorySize, MK_LDS);
    mk<<<512, 512, MK_LDS, stream>>>((const char*)d_in[0], (const char*)d_in[1], (const char*)d_in[2], (const char*)d_in[4],
                                     (const char*)d_in[5], (const char*)d_in[6], (const char*)d_in[7], (const char*)d_in[8],
                                     (char*)d_ws, (char*)d_out);
}
```

```cpp
#include <hip/hip_runtime.h>
#include <hip/hip_bf16.h>
#include <stdint.h>

typedef __attribute__((ext_vector_type(8))) short bf16x8;
typedef __attribute__((ext_vector_type(4))) short s16x4;
typedef __attribute__((ext_vector_type(16))) float f32x16;
typedef __attribute__((ext_vector_type(4))) float f32x4;
typedef __attribute__((ext_vector_type(2))) float f32x2;
typedef __attribute__((ext_vector_type(4))) unsigned u32x4;
typedef __attribute__((ext_vector_type(2))) unsigned u32x2;
typedef unsigned long long u64;
#define LAS __attribute__((address_space(3)))
#define GAS __attribute__((address_space(1)))
#define DI __device__ __forceinline__
#define MFMA32(a, b, c) __builtin_amdgcn_mfma_f32_32x32x16_bf16((a), (b), (c), 0, 0, 0)

#define ROT1 ((4 * dt1 + 10 * ft) & 31)
#define ROT2 (((st + dt) & 1) * 11)
#define LNP 0
#define PFA 3
#define PFD 2
#define PSL 2
#define STAG 3
#define CODEPF 1
#define PF1 0
#define MAP1 0
#define MAP2 0
#define QROT 6
#define QROTM 2
constexpr int S_ = 2048, D_ = 512, F_ = 1376;
constexpr size_t WS_EPOCH = 0;
constexpr size_t WS_G = 4096;
constexpr size_t WS_FL = 98304;
constexpr size_t WS_FQ = 458752;
constexpr size_t WS_FU = 65536;
constexpr size_t WS_FO = 131072;
constexpr size_t WS_Q = 1u << 20;
constexpr size_t WS_K = WS_Q + (2u << 20);
constexpr size_t WS_V = WS_K + (2u << 20);
constexpr size_t WS_H = WS_V + (2u << 20);
constexpr size_t WS_U = WS_H + (2u << 20);
constexpr size_t WS_O = WS_U + (2u << 20);

struct Stamps { unsigned long long t0, t1; };
#define ST_DECL Stamps st_; st_.t0 = __builtin_amdgcn_s_memrealtime(); st_.t1 = st_.t0
#define ST_ARG , Stamps& st_
#define ST_PASS , st_
#define ST(i) do { if ((i) == PROBE_I) { asm volatile("" ::: "memory"); st_.t1 = __builtin_amdgcn_s_memrealtime(); asm volatile("s_waitcnt lgkmcnt(0)" ::: "memory"); } } while (0)
#define ST_OUT(k) do { if ((blockIdx.x >= 256) == PROBE_B) { while (__builtin_amdgcn_s_memrealtime() - st_.t1 < 4000ull) __builtin_amdgcn_s_sleep(1); } } while (0)
#define PROBE_B 0
#define PROBE_I 9
DI unsigned cvtpk(float lo, float hi) {
    typedef float f32x2_t __attribute__((ext_vector_type(2)));
    typedef __bf16 bf16x2_t __attribute__((ext_vector_type(2)));
    return __builtin_bit_cast(unsigned, __builtin_convertvector(f32x2_t{lo, hi}, bf16x2_t));
}
DI float bflo(unsigned u) { return __uint_as_float(u << 16); }
DI float bfhi(unsigned u) { return __uint_as_float(u & 0xffff0000u); }
DI s16x4 trread(LAS const char* p) {
    typedef short v4i16_t __attribute__((ext_vector_type(4)));
    return __builtin_bit_cast(s16x4, __builtin_amdgcn_ds_read_tr16_b64_v4i16((LAS v4i16_t*)p));
}
template <int CTRL> DI float dpp(float v) {
    return __int_as_float(__builtin_amdgcn_update_dpp(0, __float_as_int(v), CTRL, 0xf, 0xf, true));
}
DI float rl(float v, int l) { return __int_as_float(__builtin_amdgcn_readlane(__float_as_int(v), l)); }
DI float wave_sum(float v) {
    v += dpp<0xB1>(v); v += dpp<0x4E>(v); v += dpp<0x141>(v); v += dpp<0x140>(v);
    return (rl(v, 0) + rl(v, 16)) + (rl(v, 32) + rl(v, 48));
}
DI float swap32_max(float v) {
    auto rr = __builtin_amdgcn_permlane32_swap(__float_as_uint(v), __float_as_uint(v), false, false);
    return fmaxf(__uint_as_float(rr[0]), __uint_as_float(rr[1]));
}

DI int lane_id() {
    int l;
    asm volatile("v_mbcnt_lo_u32_b32 %0, -1, 0\n\tv_mbcnt_hi_u32_b32 %0, -1, %0" : "=v"(l));
    __builtin_assume(l >= 0 && l < 64);
    return l;
}
DI void st16(const char* sbase, unsigned voff, u32x4 v) {
    asm volatile("global_store_dwordx4 %0, %1, %2 sc1\n\ts_nop 1" ::"v"(voff), "v"(v), "s"(sbase) : "memory");
}
DI void st4(const char* p, unsigned v) {
    unsigned t0, t1;
    asm volatile("s_nop 1\n\tv_mov_b32 %0, %2\n\tv_mov_b32 %1, 0\n\ts_mov_b64 exec, 1\n\tglobal_store_dword %1, %0, %3 sc1\n\ts_mov_b64 exec, -1"
                 : "=&v"(t0), "=&v"(t1) : "s"(v), "s"(p) : "memory");
}
DI void st_flag(const char* p, u64 v) {
    u64 t0;
    unsigned t1;
    asm volatile("s_nop 1\n\tv_mov_b64 %0, %2\n\tv_mov_b32 %1, 0\n\ts_mov_b64 exec, 1\n\tglobal_store_dwordx2 %1, %0, %3 sc1\n\ts_mov_b64 exec, -1"
                 : "=&v"(t0), "=&v"(t1) : "s"(v), "s"(p) : "memory");
}
DI u64 uni64(u64 v) {
    return (u64)(unsigned)__builtin_amdgcn_readfirstlane((unsigned)v) | ((u64)(unsigned)__builtin_amdgcn_readfirstlane((unsigned)(v >> 32)) << 32);
}
DI u64 ld_epoch(const char* ws) { return __hip_atomic_load((GAS const u64*)(ws + WS_EPOCH), __ATOMIC_RELAXED, __HIP_MEMORY_SCOPE_AGENT); }
DI u64 next_tag(u64 e) { return uni64(e) * 6364136223846793005ull + 1442695040888963407ull; }
DI unsigned uni(float v) { return __builtin_amdgcn_readfirstlane(__float_as_uint(v)); }
DI void warm(const char* base, int nchunks, int first, unsigned l64) {
#pragma clang loop unroll(disable)
    for (int n = first; n < nchunks; n += 640) {
        const char* p = base + (size_t)((n & 1) * (nchunks >> 1) + (n >> 1)) * 4096;
        asm volatile("s_mov_b32 m0, 0x13e00\n\ts_nop 0\n\tglobal_load_lds_dword %0, %1" ::"v"(l64), "s"(p) : "memory");
    }
}
DI void drain() { asm volatile("s_waitcnt vmcnt(0)" ::: "memory"); }
DI void wgbar() { asm volatile("s_waitcnt lgkmcnt(0)\n\ts_barrier" ::: "memory"); }
template <int SLEEP>
DI void wait_flags(const char* sbase, unsigned voff, u64 tag) {
#pragma clang loop unroll(disable)
    for (unsigned spin = 0; spin < (1u << 20); ++spin) {
        u64 f;
        asm volatile("global_load_dwordx2 %0, %1, %2 sc1\n\ts_waitcnt vmcnt(0)" : "=v"(f) : "v"(voff), "s"(sbase) : "memory");
        if (__all(f == tag)) break;
        __builtin_amdgcn_s_sleep(SLEEP);
    }
}

DI void wait_flags256(const char* sbase, unsigned voff, u64 tag) {
#pragma clang loop unroll(disable)
    for (unsigned spin = 0; spin < (1u << 20); ++spin) {
        u64 f0, f1, f2, f3;
        asm volatile("global_load_dwordx2 %0, %4, %5 sc1\n\tglobal_load_dwordx2 %1, %4, %6 sc1\n\tglobal_load_dwordx2 %2, %4, %7 sc1\n\t"
                     "global_load_dwordx2 %3, %4, %8 sc1\n\ts_waitcnt vmcnt(0)"
                     : "=&v"(f0), "=&v"(f1), "=&v"(f2), "=&v"(f3)
                     : "v"(voff), "s"(sbase), "s"(sbase + 8192), "s"(sbase + 16384), "s"(sbase + 24576) : "memory");
        if (__all(f0 == tag && f1 == tag && f2 == tag && f3 == tag)) break;
        __builtin_amdgcn_s_sleep(8);
    }
}

constexpr int QCNT = 73728;
#define MF(D, A, B, C) "v_mfma_f32_32x32x16_bf16 " D ", " A ", " B ", " C "\n\t"
DI u32x4 srd(const char* base, unsigned bytes) {
    return u32x4{(unsigned)(uintptr_t)base, (unsigned)((uintptr_t)base >> 32) & 0xffffu, bytes, 0x00020000u};
}
DI void qkv_store(char* ws, const f32x16& acc, int type, int head, int tile, int lane) {
    const float sc = (type == 0) ? 0.25f * 1.4426950408889634f : 1.f;
    const char* dst = ws + WS_Q + (size_t)type * (2u << 20) + (size_t)(head * 64 + tile) * 1024;
    const unsigned vo = type < 2 ? lane * 16 : ((lane >> 4) & 1) * 65536 + (lane >> 5) * 256 + (lane & 15) * 16;
#pragma unroll
    for (int hf = 0; hf < 2; ++hf) {
        u32x4 o;
#pragma unroll
        for (int i = 0; i < 4; ++i) o[i] = cvtpk(acc[8 * hf + 2 * i] * sc, acc[8 * hf + 2 * i + 1] * sc);
        st16(dst + hf * (type < 2 ? 65536 : 512), vo, o);
    }
}
DI void publish4(const char* flag, u64 tag, int lane) {
    drain();
    unsigned arr = 0;
    if (lane == 0) arr = __hip_atomic_fetch_add((LAS unsigned*)QCNT, 1u, __ATOMIC_RELAXED, __HIP_MEMORY_SCOPE_WORKGROUP);
    if (__builtin_amdgcn_readfirstlane(arr) == 3) st_flag(flag, tag);
}
DI void qkv_loader(const char* x, const char* wqkv, int row, int col, int lw) {
    const int lane = lane_id(), xr = 16 * lw + (lane >> 3), wr = 16 * lw + (lane >> 4);
    const unsigned x0 = xr * 1024 + (((lane & 7) ^ ((xr >> 1) & 7)) << 4), x1 = (xr + 8) * 1024 + (((lane & 7) ^ (((xr + 8) >> 1) & 7)) << 4) - 1024;
    const unsigned w0 = wr * 3072 + (((lane & 15) ^ ((wr & 3) << 2)) << 4);
    const u32x4 rx = srd(x + (size_t)row * 65536, 65536), rw = srd(wqkv + col * 256, 512 * 3072 - col * 256);
    const int rot = (row + col) * QROTM & QROT;
    auto issue = [&](int s, int slot) {
        const int kc = (s + rot) & 7;
        asm volatile("s_mov_b32 m0, %[m]\n\ts_nop 0\n\t"
                     "buffer_load_dwordx4 %[x0], %[rx], %[ox] offen lds\n\t"
                     "buffer_load_dwordx4 %[x1], %[rx], %[ox] offen offset:1024 lds\n\t"
                     "s_mov_b32 m0, %[m2]\n\ts_nop 0\n\t"
                     "buffer_load_dwordx4 %[w0], %[rw], %[ow] offen lds\n\t"
                     "buffer_load_dwordx4 %[w1], %[rw], %[ow] offen offset:1024 lds\n\t"
                     "buffer_load_dwordx4 %[w2], %[rw], %[ow] offen offset:2048 lds\n\t"
                     "buffer_load_dwordx4 %[w3], %[rw], %[ow] offen offset:3072 lds"
                     :: [m] "s"(slot * 24576 + lw * 2048), [m2] "s"(slot * 24576 + 8192 + lw * 4096), [ow] "s"(kc * 196608), [ox] "s"(kc * 128),
                        [rw] "s"(rw), [rx] "s"(rx), [x0] "v"(x0), [x1] "v"(x1),
                        [w0] "v"(w0), [w1] "v"(w0 + 11264), [w2] "v"(w0 + 22528), [w3] "v"(w0 + 33792)
                     : "memory");
    };
    issue(0, 0);
    __builtin_amdgcn_s_setprio(2);
    issue(1, 1);
    __builtin_amdgcn_s_setprio(1);
    issue(2, 2);
    __builtin_amdgcn_s_setprio(3);
    asm volatile("s_waitcnt vmcnt(12)\n\ts_barrier" ::: "memory");
    int slot = 0;
#pragma clang loop unroll(disable)
    for (int s = 3; s < 8; ++s) {
        asm volatile("s_waitcnt vmcnt(6)\n\ts_barrier" ::: "memory");
        issue(s, slot);
        slot = slot == 2 ? 0 : slot + 1;
    }
    asm volatile("s_waitcnt vmcnt(6)\n\ts_barrier\n\ts_waitcnt vmcnt(0)\n\ts_barrier" ::: "memory");
}
template <bool T>
DI void qkv_body(char* ws, char* lds, int row, int col, int a, int w, u64 tag ST_ARG) {
    const int lane = lane_id(), r = lane & 31, h2 = lane >> 5, q = (lane & 15) >> 2;
    f32x16 acc[2] = {};
    const int xa0 = (32 * (w & 1) + r) * 128 + ((h2 ^ ((r >> 1) & 7)) << 4);
    const int wa0 = 8192 + (8 * h2 + q) * 256 + (((2 * (w >> 1)) ^ q) << 6) + ((lane >> 4) & 1) * 32 + (lane & 3) * 8;
    bf16x8 xf[4], wf[2][2];
    auto rdw = [&](LAS const char* sl, int st, int i) {
        LAS const char* pw = sl + (wa0 ^ (i << 6)) + st * 4096;
        const s16x4 lo = trread(pw), hi = trread(pw + 1024);
        wf[st & 1][i] = __builtin_shufflevector(lo, hi, 0, 1, 2, 3, 4, 5, 6, 7);
    };
    auto mm1 = [&](int st, int i) {
        acc[i] = T ? MFMA32(wf[st & 1][i], xf[st], acc[i]) : MFMA32(xf[st], wf[st & 1][i], acc[i]);
        __builtin_amdgcn_sched_barrier(0);
    };
    auto step = [&](int st, LAS const char* sl, bool bar) {
        const int sn = (st + 2) & 3;
        mm1(st, 0);
        if (bar) wgbar();
        xf[sn] = *(LAS const bf16x8*)(sl + (xa0 ^ (sn << 5))); rdw(sl, sn, 0); __builtin_amdgcn_sched_barrier(0);
        mm1(st, 1);
        rdw(sl, sn, 1); __builtin_amdgcn_sched_barrier(0);
    };
    if (w == 0) *(LAS u32x4*)QCNT = u32x4{0u, 0u, (unsigned)tag, (unsigned)(tag >> 32)};
    wgbar();
    LAS const char* sl = (LAS const char*)lds;
#pragma unroll
    for (int st = 0; st < 2; ++st) { xf[st] = *(LAS const bf16x8*)(sl + (xa0 ^ (st << 5))); rdw(sl, st, 0); rdw(sl, st, 1); }
    __builtin_amdgcn_sched_barrier(0);
#pragma clang loop unroll(disable)
    for (int s = 0; s < 7; ++s) {
        step(0, sl, false); step(1, sl, false);
        sl = (sl == (LAS const char*)lds + 49152) ? (LAS const char*)lds : sl + 24576;
        step(2, sl, true); step(3, sl, false);
    }
    step(0, sl, false); step(1, sl, false);
    mm1(2, 0); mm1(2, 1); mm1(3, 0); mm1(3, 1);
    ST(4);
#pragma unroll
    for (int i = 0; i < 2; ++i) qkv_store(ws, acc[i], col >> 2, 8 * (col & 3) + 4 * (w >> 1) + 2 * i, 2 * row + (w & 1), lane);
    publish4(ws + WS_FQ + a * 128, tag, lane);
    ST(7);
}
DI void qkv2_loader(const char* x, const char* wqkv, int rowt, int col, int lw) {
    const int lane = lane_id(), row = 16 * lw + (lane >> 3);
    const unsigned w0 = row * 3072 + (((lane & 7) ^ (((row >> 1) & 1) << 2)) << 4), w1 = w0 + 8 * 3072 - 1024;
    const unsigned x0 = row * 1024 + (((lane & 7) ^ ((row >> 1) & 7)) << 4), x1 = (row + 8) * 1024 + (((lane & 7) ^ (((row + 8) >> 1) & 7)) << 4) - 1024;
    const u32x4 rx = srd(x + (size_t)rowt * 65536, 65536), rw = srd(wqkv + col * 128, 512 * 3072 - col * 128);
    auto issue = [&](int s) {
        asm volatile("s_mov_b32 m0, %[m]\n\ts_nop 0\n\t"
                     "buffer_load_dwordx4 %[w0], %[rw], %[ow] offen lds\n\t"
                     "buffer_load_dwordx4 %[w1], %[rw], %[ow] offen offset:1024 lds\n\t"
                     "s_add_u32 m0, %[m], 8192\n\ts_nop 0\n\t"
                     "buffer_load_dwordx4 %[x0], %[rx], %[ox] offen lds\n\t"
                     "buffer_load_dwordx4 %[x1], %[rx], %[ox] offen offset:1024 lds"
                     :: [m] "s"((s & 3) * 16384 + lw * 2048), [ow] "s"(s * 196608), [ox] "s"(s * 128), [rw] "s"(rw), [rx] "s"(rx),
                        [w0] "v"(w0), [w1] "v"(w1), [x0] "v"(x0), [x1] "v"(x1)
                     : "memory", "scc");
    };
    issue(0); issue(1); issue(2);
#pragma clang loop unroll(disable)
    for (int s = 3; s < 8; ++s) {
        asm volatile("s_waitcnt vmcnt(8)\n\ts_barrier" ::: "memory");
        issue(s);
    }
    asm volatile("s_waitcnt vmcnt(8)\n\ts_barrier\n\ts_waitcnt vmcnt(4)\n\ts_barrier\n\ts_waitcnt vmcnt(0)\n\ts_barrier" ::: "memory");
}
#define QRD3(B, SLOT, K)                                                                             \
    "ds_read_b64_tr_b16 v[" #B ":" #B "+1], %[t] offset:" #SLOT "*16384+" #K "*2048\n\t"              \
    "ds_read_b64_tr_b16 v[" #B "+2:" #B "+3], %[t] offset:" #SLOT "*16384+" #K "*2048+512\n\t"        \
    "ds_read_b128 v[" #B "+4:" #B "+7], %[x" #K "] offset:" #SLOT "*16384\n\t"
#define QSTAGE(SLOT, MM, N0, N1, N2, N3, P0, P1, P2, P3)                              \
    "s_waitcnt lgkmcnt(0)\n\t"                                                        \
    "s_barrier\n\t"                                                                   \
    MM(P0) QRD3(N0, SLOT, 0) MM(P1) QRD3(N1, SLOT, 1) MM(P2) QRD3(N2, SLOT, 2) MM(P3) QRD3(N3, SLOT, 3)
#define QST_E(SLOT, MM) QSTAGE(SLOT, MM, 32, 40, 48, 56, 96, 104, 112, 120)
#define QST_O(SLOT, MM) QSTAGE(SLOT, MM, 96, 104, 112, 120, 32, 40, 48, 56)
#define QMT(P) MF("v[16:31]", "v[" #P ":" #P "+3]", "v[" #P "+4:" #P "+7]", "v[16:31]")
#define QMV(P) MF("v[16:31]", "v[" #P "+4:" #P "+7]", "v[" #P ":" #P "+3]", "v[16:31]")
#define QTILE_ASM(MM)                                                                                             \
    asm volatile(                                                                                                 \
        "s_barrier\n\t"                                                                                           \
        QRD3(32, 0, 0) QRD3(40, 0, 1) QRD3(48, 0, 2) QRD3(56, 0, 3)                                               \
        QST_O(1, MM) QST_E(2, MM)                                                                                 \
        "s_mov_b32 s44, 0\n\t"                                                                                    \
        "LQ_loop%=:\n\t"                                                                                          \
        QST_O(3, MM)                                                                                              \
        "s_cmp_lg_u32 s44, 0\n\t"                                                                                 \
        "s_cbranch_scc1 LQ_end%=\n\t"                                                                             \
        QST_E(0, MM) QST_O(1, MM) QST_E(2, MM)                                                                    \
        "s_mov_b32 s44, 1\n\t"                                                                                    \
        "s_branch LQ_loop%=\n\t"                                                                                  \
        "LQ_end%=:\n\t"                                                                                           \
        "s_waitcnt lgkmcnt(0)\n\t"                                                                                \
        MM(96) MM(104) MM(112) MM(120)                                                                            \
        "s_nop 15\n\t"                                                                                            \
        : "+{v[16:31]}"(acc)                                                                                      \
        : [t] "v"(t), [x0] "v"(xa), [x1] "v"(xa ^ 32), [x2] "v"(xa ^ 64), [x3] "v"(xa ^ 96)                       \
        : "memory", "scc", "s44",                                                                                 \
          "v32", "v33", "v34", "v35", "v36", "v37", "v38", "v39", "v40", "v41", "v42", "v43", "v44", "v45", "v46", "v47", \
          "v48", "v49", "v50", "v51", "v52", "v53", "v54", "v55", "v56", "v57", "v58", "v59", "v60", "v61", "v62", "v63", \
          "v96", "v97", "v98", "v99", "v100", "v101", "v102", "v103", "v104", "v105", "v106", "v107", "v108", "v109", "v110", "v111", \
          "v112", "v113", "v114", "v115", "v116", "v117", "v118", "v119", "v120", "v121", "v122", "v123", "v124", "v125", "v126", "v127")
DI void qkv2_body(char* ws, int row, int col, int bi, int w, u64 tag) {
    const int lane = lane_id(), r = lane & 31, h2 = lane >> 5, q = (lane & 15) >> 2;
    const unsigned t = (8 * h2 + q) * 128 + (((w & 1) ^ ((q >> 1) & 1)) << 6) + ((lane >> 4) & 1) * 32 + (lane & 3) * 8;
    const unsigned xa = 8192 + (32 * (w >> 1) + r) * 128 + ((h2 ^ ((r >> 1) & 7)) << 4);
    f32x16 acc = {};
    if (col < 16) QTILE_ASM(QMT);
    else QTILE_ASM(QMV);
    qkv_store(ws, acc, col >> 3, 4 * (col & 7) + 2 * (w & 1), 2 * row + (w >> 1), lane);
    publish4(ws + WS_FQ + 32768 + bi * 128, tag, lane);
}

#define EXP(a) "v_exp_f32 v" #a ", v" #a "\n\t"
#define CVT(d, a, b) "v_cvt_pk_bf16_f32 v" #d ", v" #a ", v" #b "\n\t"
#define ASTEP(c0, c1, c2, c3, c4, c5, c6, c7, c8, c9, c10, c11, c12, c13, c14, c15, M1, M2, CL, M3)                  \
    EXP(c0) EXP(c1) M1 EXP(c2) EXP(c3) EXP(c4) EXP(c5) M2 CL                                                          \
    EXP(c6) EXP(c7) CVT(96, c0, c1) CVT(97, c2, c3) CVT(98, c4, c5) CVT(99, c6, c7) EXP(c8) EXP(c9) M3                \
    EXP(c10) EXP(c11) EXP(c12) EXP(c13) EXP(c14) EXP(c15)                                                             \
    CVT(100, c8, c9) CVT(101, c10, c11) CVT(102, c12, c13) CVT(103, c14, c15)
#define R_OA "v[64:79]"
#define R_OB "v[80:95]"
#define R_SA "v[32:47]"
#define R_SB "v[48:63]"
#define ATILE(R_NM, KC, KN, V0C, V1C, V0N, V1N, KOFF, VA, VOFF0, VOFF1, PRE)                                                    \
    ASTEP(32, 33, 34, 35, 36, 37, 38, 39, 40, 41, 42, 43, 44, 45, 46, 47,                                             \
          MF(R_OB, V1N, "v[100:103]", R_OB), MF(R_SB, KC, "v[8:11]", R_NM),                                           \
          PRE                                                                                                         \
          "ds_read_b128 " KC ", v12 offset:" KOFF "\n\t"                                                              \
          "ds_read_b128 " V0N ", " VA " offset:" #VOFF0 "\n\t"                                                        \
          "ds_read_b128 " V1N ", " VA " offset:" #VOFF1 "\n\t",                                                          \
          MF(R_OA, V0C, "v[96:99]", R_OA))                                                                            \
    ASTEP(48, 49, 50, 51, 52, 53, 54, 55, 56, 57, 58, 59, 60, 61, 62, 63,                                             \
          MF(R_OA, V1C, "v[100:103]", R_OA), MF(R_SA, KN, "v[4:7]", R_NM), "", MF(R_OB, V0C, "v[96:99]", R_OB))
#define ATILE_E(NM, KOFF, VA, VOFF0, VOFF1, PRE) ATILE(NM, "v[104:107]", "v[108:111]", "v[112:115]", "v[116:119]", "v[120:123]", "v[124:127]", KOFF, VA, VOFF0, VOFF1, PRE)
#define ATILE_O(NM, KOFF, VA, VOFF0, VOFF1, PRE) ATILE(NM, "v[108:111]", "v[104:107]", "v[120:123]", "v[124:127]", "v[112:115]", "v[116:119]", KOFF, VA, VOFF0, VOFF1, PRE)
#define MOV4(a, b, c, d, s) "v_mov_b32 v" #a ", " s "\n\tv_mov_b32 v" #b ", " s "\n\tv_mov_b32 v" #c ", " s "\n\tv_mov_b32 v" #d ", " s "\n\t"
#define WL "s_waitcnt lgkmcnt(0)\n\t"
#define AISSUE                                                   \
    "s_add_u32 m0, s44, %[lw]\n\t"                               \
    "s_add_u32 s44, s44, 0x4000\n\t"                             \
    "buffer_load_dwordx4 v14, %[rk], s43 offen sc1 lds\n\t"      \
    "s_add_u32 m0, m0, 0x2000\n\t"                               \
    "s_and_b32 s44, s44, 0xffff\n\t"                             \
    "buffer_load_dwordx4 v14, %[rv], s43 offen sc1 lds\n\t"      \
    "s_add_u32 s43, s43, 0x2000\n\t"
#define PR2 "s_setprio 2\n\t"
#define PR3 ""
#define PR0 "s_setprio 1\n\t"
#define PR1 "s_setprio 0\n\t"
#define PRI "s_setprio 3\n\t"
constexpr int A_ONES = 65536, A_VOTE = 69632 + 512, A_LSUM = 71680;
struct AttnRegs { f32x16 oa, ob, nm; u32x4 p1, v1, fin; unsigned ka, va, va1, va2, va3, go, fo; };
DI unsigned qkv_dep(int a, bool early) {
    const int lane = lane_id(), r = lane & 15, v = (lane >> 4) & 1, R = 4 * (a >> 5) + (lane & 3);
    const int kv = (r >> 3) + 8 * ((early ? 8 + 8 * v : 16 * v + 8 * (a & 1)) + (r & 7));
    const int qq = ((R >> 3) & 1) + 8 * ((R >> 4) * 24 + (R & 7));
    return ((a & 6) + (lane < 32 || !early ? kv : qq)) * 128;
}
DI void attn_prep(AttnRegs& R, int a, int w) {
    const int lane = lane_id(), r = lane & 31, kh = w >> 2;
    R.ka = 4096 * kh + lane * 16;
    R.va = r < 16 ? 8192 + 4096 * kh + (lane >> 5) * 256 + r * 16 : A_ONES + (r == 16 ? 0 : r == 20 ? 64 : (r > 20 && r < 28) ? 80 : 16);
    R.go = w * 1024 + lane * 16;
#pragma unroll
    for (int i = 0; i < 16; ++i) { R.oa[i] = 0.f; R.ob[i] = 0.f; }
    R.p1 = u32x4{0u, 0u, 0u, 0u};
    R.v1 = u32x4{0u, 0u, 0u, 0u};
    asm volatile("" : "+{v[64:79]}"(R.oa), "+{v[80:95]}"(R.ob), "+{v[100:103]}"(R.p1), "+{v[124:127]}"(R.v1),
                      "+{v12}"(R.ka), "+{v13}"(R.va), "+{v14}"(R.go));
}
#define ATTN_ASM(NM, NMIN) \
    asm volatile( \
        "v_and_b32 v15, 0x3ff, v14\n\t" \
        "s_mov_b32 s43, 0\n\t" \
        "s_mov_b32 s44, 0\n\t" \
        AISSUE \
        "global_load_dwordx4 v[4:7], v15, %[qa] sc1\n\t" \
        "global_load_dwordx4 v[8:11], v15, %[qb] sc1\n\t" \
        AISSUE AISSUE \
        "s_mov_b32 s40, 0xffff\n\t" \
        "s_mov_b32 s41, 0xffff\n\t" \
        "s_mov_b32 s42, 8\n\t" \
        "s_waitcnt vmcnt(4)\n\t" \
        "s_barrier\n\t" \
        "ds_read_b128 v[104:107], v12\n\t" \
        "ds_read_b128 v[108:111], v12 offset:1024\n\t" \
        "ds_read_b128 v[112:115], v13\n\t" \
        "ds_read_b128 v[116:119], v13 offset:512\n\t" \
        PR2 \
        "s_waitcnt lgkmcnt(3)\n\t" \
        MF(R_SA, "v[104:107]", "v[4:7]", NM) \
        "s_nop 15\n\t" \
        "LA_loop%=:\n\t" \
        ATILE_E(NM, "2048", "v13", 1024, 1536, WL PR0) \
        ATILE_O(NM, "3072", "v13", 2048, 2560, WL PR1) \
        ATILE_E(NM, "0", "v13", 3072, 3584, \
                "s_waitcnt vmcnt(2) lgkmcnt(0)\n\t" \
                "s_barrier\n\t" \
                "s_setprio 3\n\t" \
                "v_add_u32 v12, 0x4000, v12\n\t" \
                AISSUE \
                "v_and_b32 v12, 0xffff, v12\n\t" \
                PR2) \
        ATILE_O(NM, "1024", "v13", 0, 512, \
                WL PR3 \
                "s_mov_b64 exec, s[40:41]\n\t" \
                "v_add_u32 v13, 0x4000, v13\n\t" \
                "s_sub_u32 s42, s42, 1\n\t" \
                "v_and_b32 v13, 0xffff, v13\n\t" \
                "s_mov_b64 exec, -1\n\t" \
                "s_cmp_lg_u32 s42, 0\n\t") \
        "s_cbranch_scc1 LA_loop%=\n\t" \
        "s_waitcnt lgkmcnt(0)\n\t" \
        "s_setprio 3\n\t" \
        MF(R_OB, "v[124:127]", "v[100:103]", R_OB) \
        "s_nop 15\n\t" \
        : "+{v[64:79]}"(R.oa), "+{v[80:95]}"(R.ob), "+{v[100:103]}"(R.p1), "+{v[124:127]}"(R.v1), "+{v12}"(R.ka), "+{v13}"(R.va) \
        : NMIN "{v14}"(R.go), [qa] "s"(qa), [qb] "s"(qb), [rk] "s"(rk), [rv] "s"(rv), [lw] "s"(w * 1024) \
        : "memory", "scc", "vcc", "s40", "s41", "s42", "s43", "s44", "s45", "v0", "v1", "v4", "v5", "v6", "v7", "v8", "v9", "v10", "v11", "v15", \
          "v32", "v33", "v34", "v35", "v36", "v37", "v38", "v39", "v40", "v41", "v42", "v43", "v44", "v45", "v46", "v47", \
          "v48", "v49", "v50", "v51", "v52", "v53", "v54", "v55", "v56", "v57", "v58", "v59", "v60", "v61", "v62", "v63", \
          "v96", "v97", "v98", "v99", "v104", "v105", "v106", "v107", "v108", "v109", "v110", "v111", \
          "v112", "v113", "v114", "v115", "v116", "v117", "v118", "v119", "v120", "v121", "v122", "v123")
DI void attn_pass_shift(AttnRegs& R, const char* qa, const char* qb, const char* ksrc, const char* vsrc, int w) {
    const u32x4 rk = srd(ksrc, 65536), rv = srd(vsrc, 65536);
#define NM_IN "{v[16:31]}"(R.nm),
    ATTN_ASM("v[16:31]", NM_IN);
}
#define FISS(S)                                                  \
    "s_add_u32 m0, %[lw], " #S "*16384\n\t"                      \
    "s_add_u32 s43, s43, 0x2000\n\t"                             \
    "buffer_load_dwordx4 v14, %[rk], s43 offen sc1 lds\n\t"      \
    "s_add_u32 m0, %[lw], " #S "*16384+8192\n\t"                 \
    "s_nop 0\n\t"                                                \
    "buffer_load_dwordx4 v14, %[rv], s43 offen sc1 lds\n\t"
#define FI2(I) ""
#define FI3(I) PRI I PR2
#define LA2 ""
#define LB2 PR2
#define LB3 ""
#define FCHUNK(S, N, VC, VN, BND, I2, I3) FCHUNKX(S, N, VC, VN, BND, I2, I3, "")
#define FCHUNKX(S, N, VC, VN, BND, I2, I3, I0)                        \
    ATILE_E("0", #S "*16384+2048", VC, 1024, 1536, WL I0 PR0)       \
    ATILE_O("0", #S "*16384+3072", VC, 2048, 2560, WL PR1)       \
    ATILE_E("0", #N "*16384", VC, 3072, 3584, BND LA2 I2 LB2)    \
    ATILE_O("0", #N "*16384+1024", VN, 0, 512, WL I3 LB3)
#define FLD "global_load_dwordx2 v[0:1], v2, %[ff] sc1\n\t"
#define VCCSET "s_mov_b32 vcc_lo, %[lo4]\n\ts_mov_b32 vcc_hi, 0\n\ts_mov_b64 vcc, vcc\n\t"
#define FBAR(VM) "s_waitcnt vmcnt(" #VM ") lgkmcnt(0)\n\ts_barrier\n\t"
DI void attn_pass(AttnRegs& R, const char* qa, const char* qb, const char* ksrc, const char* vsrc, const char* late_flags, u64 tag, int w) {
    const u32x4 rk = srd(ksrc, 65536), rv = srd(vsrc, 65536);
    asm volatile(
        "v_and_b32 v15, 0x3ff, v14\n\t"
        "s_mov_b32 s43, -0x2000\n\t"
        FISS(0)
        "global_load_dwordx4 v[4:7], v15, %[qa] sc1\n\t"
        "global_load_dwordx4 v[8:11], v15, %[qb] sc1\n\t"
        FISS(1) FISS(2) VCCSET
        "s_waitcnt vmcnt(4)\n\t"
        "s_barrier\n\t"
        "ds_read_b128 v[104:107], v12\n\t"
        "ds_read_b128 v[108:111], v12 offset:1024\n\t"
        "ds_read_b128 v[112:115], v13\n\t"
        "ds_read_b128 v[116:119], v13 offset:512\n\t"
        PR2
        "s_waitcnt lgkmcnt(3)\n\t"
        MF(R_SA, "v[104:107]", "v[4:7]", "0")
        "s_nop 15\n\t"
        FCHUNK(0, 1, "v13", "v16", FBAR(2), FI2(FISS(3)), FI3(FISS(3)))
        FCHUNK(1, 2, "v16", "v17", FBAR(2), "", "")
        FCHUNKX(2, 3, "v17", "v18",
               "s_waitcnt vmcnt(0) lgkmcnt(0)\n\t"
               "s_mov_b32 s45, 0x100000\n\t"
               "LA_p%=:\n\t"
               "v_cmp_ne_u64 vcc, %[tag], v[0:1]\n\t"
               "s_sub_u32 s45, s45, 1\n\t"
               "s_cbranch_scc1 LA_f%=\n\t"
               "s_cbranch_vccz LA_f%=\n\t"
               "global_load_dwordx2 v[0:1], v2, %[ff] sc1\n\t"
               "s_waitcnt vmcnt(0)\n\t"
               "s_branch LA_p%=\n\t"
               "LA_f%=:\n\t"
               "s_barrier\n\t" VCCSET, FI2(FISS(0) FISS(1)), FI3(FISS(0) FISS(1)), PRI FLD)
        FCHUNK(3, 0, "v18", "v13", FBAR(2), FI2(FISS(2)), FI3(FISS(2)))
        FCHUNK(0, 1, "v13", "v16", FBAR(2), FI2(FISS(3)), FI3(FISS(3)))
        FCHUNK(1, 2, "v16", "v17", FBAR(2), "", "")
        FCHUNK(2, 3, "v17", "v18", FBAR(0), "", "")
        FCHUNK(3, 0, "v18", "v13", WL, "", "")
        "s_waitcnt lgkmcnt(0)\n\t"
        "s_setprio 3\n\t"
        MF(R_OB, "v[124:127]", "v[100:103]", R_OB)
        "s_nop 15\n\t"
        "Lmk_hot_end:\n\t"
        : "+{v[64:79]}"(R.oa), "+{v[80:95]}"(R.ob), "+{v[100:103]}"(R.p1), "+{v[124:127]}"(R.v1), "+{v[20:23]}"(R.fin)
        : "{v12}"(R.ka), "{v13}"(R.va), "{v16}"(R.va1), "{v17}"(R.va2), "{v18}"(R.va3), "{v14}"(R.go), "{v2}"(R.fo),
          [qa] "s"(qa), [qb] "s"(qb), [rk] "s"(rk), [rv] "s"(rv), [lw] "s"(w * 1024), [tag] "s"(tag), [ff] "s"(late_flags), [lo4] "s"(1 - (w >> 2))
        : "memory", "scc", "vcc", "s43", "s45", "v0", "v1", "v4", "v5", "v6", "v7", "v8", "v9", "v10", "v11", "v15",
          "v32", "v33", "v34", "v35", "v36", "v37", "v38", "v39", "v40", "v41", "v42", "v43", "v44", "v45", "v46", "v47",
          "v48", "v49", "v50", "v51", "v52", "v53", "v54", "v55", "v56", "v57", "v58", "v59", "v60", "v61", "v62", "v63",
          "v96", "v97", "v98", "v99", "v104", "v105", "v106", "v107", "v108", "v109", "v110", "v111",
          "v112", "v113", "v114", "v115", "v116", "v117", "v118", "v119", "v120", "v121", "v122", "v123");
}

DI void wait_qkv(const char* ws, int a, u64 tag) { wait_flags<PSL>(ws + WS_FQ, qkv_dep(a, true), tag); }
DI void attn_role(const char* x, const char* wqkv, char* ws, char* lds, int a, int w) {
    const int kh = w >> 2;
    const int head = 4 * (a & 7) + ((a >> 3) & 3), qt = 8 * (a >> 5) + 2 * (w & 3);
    ST_DECL; ST(0);
    __builtin_amdgcn_s_setprio(3);
    u64 tag;
    const int ti = a >> 3, trow = 8 * (a & 1) + (ti & 7) + (ti >= 24 ? 16 : 0), tcol = (ti >= 24 ? 0 : 4 * (ti >> 3)) + ((a & 7) >> 1);
    if (w >= 4) {
        qkv_loader(x, wqkv, trow, tcol, w - 4);
        tag = uni64(*(LAS const u64*)(QCNT + 8));
        if (w == 4) wait_qkv(ws, a, tag);
    } else {
        tag = next_tag(ld_epoch(ws));
        if (tcol < 8) qkv_body<true>(ws, lds, trow, tcol, a, w, tag ST_PASS);
        else qkv_body<false>(ws, lds, trow, tcol, a, w, tag ST_PASS);
    }
    AttnRegs R;
    attn_prep(R, a, w);
    {
        const int lane = lane_id();
        const unsigned st = (lane & 16) ? 0u : 16384u;
        R.va1 = R.va + st; R.va2 = R.va1 + st; R.va3 = R.va2 + st;
        R.fo = qkv_dep(a, false);
        const int o = w * 512 + lane * 8;
        const unsigned one = ((o & 0x1b0) == 0) ? 0x3f803f80u : 0u;
        *(LAS u32x2*)((LAS char*)lds + A_ONES + o) = u32x2{one, one};
        R.fin = u32x4{(unsigned)(w * 2048 + lane * 16), (unsigned)((w ^ 4) * 2048 + lane * 16), (unsigned)(A_LSUM + w * 256 + lane * 4), (unsigned)(A_LSUM + (w ^ 4) * 256 + lane * 4)};
        asm volatile("" : "+{v16}"(R.va1), "+{v17}"(R.va2), "+{v18}"(R.va3), "+{v2}"(R.fo), "+{v[20:23]}"(R.fin));
    }
    ST(5);
    wgbar();
    ST(6);
    const char* ksrc = ws + WS_K + (size_t)head * 65536;
    const char* qsrc = ws + WS_Q + (size_t)(head * 64 + qt) * 1024;
    auto finish = [&](int att) {
        const f32x16 oa = R.oa, ob = R.ob;
        u32x4 fin = R.fin;
        if (att) {
            const int lane = lane_id();
            fin = u32x4{(unsigned)(w * 2048 + lane * 16), (unsigned)((w ^ 4) * 2048 + lane * 16), (unsigned)(A_LSUM + w * 256 + lane * 4), (unsigned)(A_LSUM + (w ^ 4) * 256 + lane * 4)};
        }
        LAS char* ex = (LAS char*)(uintptr_t)fin[0];
        LAS const char* exr = (LAS const char*)(uintptr_t)fin[1];
        *(LAS f32x4*)ex = f32x4{ob[0], ob[1], ob[2], ob[3]};
        *(LAS f32x4*)(ex + 1024) = f32x4{ob[4], ob[5], ob[6], ob[7]};
        *(LAS float*)(uintptr_t)fin[2] = ob[8];
        if (att == 0) {
            float big = fmaxf(oa[8], ob[8]) * 2.7e8f;
#pragma unroll
            for (int i = 0; i < 8; ++i) big = fmaxf(fmaxf(big, __builtin_fabsf(oa[i])), __builtin_fabsf(ob[i]));
            const bool bad = !__all(fminf(oa[8], ob[8]) > 8.1e-28f && big < 1.7e38f);
            ((LAS unsigned*)((LAS char*)lds + A_VOTE))[w] = bad;
        }
        wgbar();
        const f32x4 t0 = *(LAS const f32x4*)exr, t1 = *(LAS const f32x4*)(exr + 1024);
        const float t2 = *(LAS const float*)(uintptr_t)fin[3];
        unsigned redo = 0;
        if (att == 0) {
            const u32x4 v0 = *(LAS const u32x4*)((LAS char*)lds + A_VOTE), v1 = *(LAS const u32x4*)((LAS char*)lds + A_VOTE + 16);
            redo = __builtin_amdgcn_readfirstlane(v0[0] | v0[1] | v0[2] | v0[3] | v1[0] | v1[1] | v1[2] | v1[3]);
        }
        const float rinv = __builtin_amdgcn_rcpf(oa[8] + t2);
        u32x4 ov;
        ov[0] = cvtpk((oa[0] + t0[0]) * rinv, (oa[1] + t0[1]) * rinv);
        ov[1] = cvtpk((oa[2] + t0[2]) * rinv, (oa[3] + t0[3]) * rinv);
        ov[2] = cvtpk((oa[4] + t1[0]) * rinv, (oa[5] + t1[1]) * rinv);
        ov[3] = cvtpk((oa[6] + t1[2]) * rinv, (oa[7] + t1[3]) * rinv);
        if (att == 0 ? !redo : att == 1 + kh) {
            st16(ws + WS_O + (size_t)((qt + kh) * 32 + head) * 1024, fin[0] & 1023, ov);
            drain();
            st_flag(ws + WS_FO + ((qt + kh) * 32 + head) * 128, tag);
        }
        asm volatile("" : "+s"(redo));
        return redo;
    };
    ST(1);
    attn_pass(R, qsrc + kh * 1024, qsrc + (1 - kh) * 1024, ksrc, ksrc + (WS_V - WS_K), ws + WS_FQ + 32768, tag, w);
    ST(2);
    if (finish(0)) {
#pragma clang loop unroll(disable)
        for (int att = 1; att < 3; ++att) {
            drain();
            wgbar();
            const int lane = lane_id();
            const char* qa = qsrc + (att - 1) * 1024;
            bf16x8 qf;
            asm volatile("global_load_dwordx4 %0, %1, %2 sc1\n\ts_waitcnt vmcnt(0)" : "=v"(qf) : "v"(lane * 16), "s"(qa) : "memory");
            float mx = -INFINITY;
#pragma clang loop unroll(disable)
            for (int t = 0; t < 64; ++t) {
                bf16x8 kf;
                asm volatile("global_load_dwordx4 %0, %1, %2 sc1\n\ts_waitcnt vmcnt(0)" : "=v"(kf) : "v"(lane * 16), "s"(ksrc + t * 1024) : "memory");
                const f32x16 s = MFMA32(kf, qf, f32x16{});
#pragma unroll
                for (int i = 0; i < 16; ++i) mx = fmaxf(mx, s[i]);
            }
            mx = -swap32_max(mx);
            attn_prep(R, a, w);
#pragma unroll
            for (int i = 0; i < 16; ++i) R.nm[i] = mx;
            attn_pass_shift(R, qa, qa, ksrc, ksrc + (WS_V - WS_K), w);
            finish(att);
        }
    }
    ST(3); ST_OUT(0);
}

#define BPR "s_setprio 0\n\t"
#define BG1 ""
#define BG2 "s_sleep 1\n\t"
#define BG3 "s_sleep 1\n\t"
#define SISSUE                                                             \
    "s_mov_b32 m0, s44\n\t"                                                 \
    "s_add_u32 s45, %[ow], %[wst]\n\t"                                      \
    "buffer_load_dwordx4 %[g0], %[rw], %[ow] offen lds\n\t"                 \
    "buffer_load_dwordx4 %[g1], %[rw], %[ow] offen offset:1024 lds\n\t"     \
    "s_add_u32 m0, s44, 8192\n\t"                                           \
    "s_add_u32 s46, %[od], 4096\n\t"                                        \
    "buffer_load_dwordx4 %[d0], %[rd], %[od] offen sc1 lds\n\t"             \
    "buffer_load_dwordx4 %[d0], %[rd], %[od] offen offset:1024 sc1 lds\n\t" \
    "s_add_u32 s44, s44, 0x4000\n\t"                                        \
    "s_and_b32 s44, s44, 0xffff\n\t"                                        \
    "s_cmp_eq_u32 %[od], %[de]\n\t"                                         \
    "s_cselect_b32 %[ow], 0, s45\n\t"                                       \
    "s_cselect_b32 %[od], 0, s46\n\t"
#define SISW                                                               \
    "s_mov_b32 m0, s44\n\t"                                                 \
    "s_add_u32 s45, %[ow], %[wst]\n\t"                                      \
    "buffer_load_dwordx4 %[g0], %[rw], %[ow] offen lds\n\t"                 \
    "buffer_load_dwordx4 %[g1], %[rw], %[ow] offen offset:1024 lds\n\t"     \
    "s_add_u32 s44, s44, 0x4000\n\t"                                        \
    "s_mov_b32 %[ow], s45\n\t"
#define SISD                                                               \
    "s_add_u32 m0, s44, 8192\n\t"                                           \
    "s_add_u32 s44, s44, 0x4000\n\t"                                        \
    "buffer_load_dwordx4 %[d0], %[rd], %[od] offen sc1 lds\n\t"             \
    "buffer_load_dwordx4 %[d0], %[rd], %[od] offen offset:1024 sc1 lds\n\t" \
    "s_add_u32 %[od], %[od], 4096\n\t"
struct SG { u32x4 rw, rd; unsigned wst, de, ow, od, a, b, c; };
DI void sgemm_prew(SG& G, int lw) {
    asm volatile("s_mov_b32 s44, %[lo]\n\t" SISW SISW SISW
                 : [ow] "+s"(G.ow) : [wst] "s"(G.wst), [rw] "s"(G.rw), [lo] "s"(lw * 2048), [g0] "v"(G.a), [g1] "v"(G.b) : "memory", "scc", "s44", "s45");
}
#define WISSUE                                                              \
    "s_and_b32 s45, s44, 0xc000\n\t"                                        \
    "s_add_u32 m0, s45, %[lo2]\n\t"                                         \
    "s_lshl_b32 s45, s45, 3\n\t"                                            \
    "buffer_load_dwordx4 %[o0], %[ro], s45 offen lds\n\t"                   \
    "buffer_load_dwordx4 %[o1], %[ro], s45 offen offset:1024 lds\n\t"       \
    "buffer_load_dwordx4 %[o2], %[ro], s45 offen offset:2048 lds\n\t"       \
    "buffer_load_dwordx4 %[o3], %[ro], s45 offen offset:3072 lds\n\t"       \
    "s_add_u32 s44, s44, 0x4000\n\t"                                        \
    "s_and_b32 s44, s44, 0xffff\n\t"
template <int WO>
DI void sgemm_load(SG& G, int n, int lw, u32x4 ro, unsigned o0) {
    asm volatile(
        "s_mov_b32 s44, %[lo]\n\t"
        ".if %c[wo]\n\t"
        "s_sub_u32 s47, %[n], 5\n\t"
        SISD SISD SISD
        "s_waitcnt vmcnt(4)\n\ts_barrier\n\t" SISSUE
        "s_waitcnt vmcnt(6)\n\ts_barrier\n\t" SISSUE
        ".else\n\t"
        "s_sub_u32 s47, %[n], 3\n\t"
        SISSUE SISSUE SISSUE
        ".endif\n\t"
        "LL_loop%=:\n\t"
        "s_waitcnt vmcnt(8)\n\t"
        "s_barrier\n\t"
        SISSUE
        "s_sub_u32 s47, s47, 1\n\t"
        "s_cmp_lg_u32 s47, 0\n\t"
        "s_cbranch_scc1 LL_loop%=\n\t"
        ".if %c[wo]\n\t"
        "s_waitcnt vmcnt(8)\n\ts_barrier\n\t" WISSUE
        "s_waitcnt vmcnt(8)\n\ts_barrier\n\t" WISSUE
        "s_waitcnt vmcnt(8)\n\ts_barrier\n\t" WISSUE
        "s_barrier\n\t" WISSUE
        "s_barrier\n\t"
        ".else\n\t"
        "s_waitcnt vmcnt(8)\n\ts_barrier\n\t"
        "s_waitcnt vmcnt(4)\n\ts_barrier\n\t"
        "s_waitcnt vmcnt(0)\n\ts_barrier\n\t"
        "s_barrier\n\t"
        ".endif\n\t"
        : [ow] "+s"(G.ow), [od] "+s"(G.od)
        : [wst] "s"(G.wst), [rw] "s"(G.rw), [rd] "s"(G.rd), [lo] "s"(lw * 2048), [de] "s"(G.de), [n] "s"(n), [g0] "v"(G.a), [g1] "v"(G.b), [d0] "v"(G.c),
          [wo] "i"(WO), [ro] "s"(ro), [lo2] "s"(lw * 4096), [o0] "v"(o0), [o1] "v"(o0 + 7168), [o2] "v"(o0 + 14336), [o3] "v"(o0 + 21504)
        : "memory", "scc", "s44", "s45", "s46", "s47");
}
#define SRD3(B, SLOT, K, TD)                                                                         \
    "ds_read_b64_tr_b16 v[" #B ":" #B "+1], %[t] offset:" #SLOT "*16384+" #K "*2048\n\t"              \
    "ds_read_b64_tr_b16 v[" #B "+2:" #B "+3], %[t] offset:" #SLOT "*16384+" #K "*2048+" #TD "\n\t"    \
    "ds_read_b128 v[" #B "+4:" #B "+7], %[dl] offset:" #SLOT "*16384+" #K "*1024\n\t"
#define SSTAGE(SLOT, MM, TD, N0, N1, N2, N3, P0, P1, P2, P3)                          \
    "s_waitcnt lgkmcnt(0)\n\t"                                                        \
    "s_barrier\n\t"                                                                   \
    MM(P0) SRD3(N0, SLOT, 0, TD) SRD3(N1, SLOT, 1, TD) BG1                            \
    MM(P1) SRD3(N2, SLOT, 2, TD) SRD3(N3, SLOT, 3, TD) BG2                            \
    MM(P2) BG3 MM(P3)
#define SST_E(SLOT, MM, TD) SSTAGE(SLOT, MM, TD, 32, 40, 48, 56, 96, 104, 112, 120)
#define SST_O(SLOT, MM, TD) SSTAGE(SLOT, MM, TD, 96, 104, 112, 120, 32, 40, 48, 56)
#define SMM_NONE(P) ""
#define SMM_WA(P) MF("v[64:79]", "v[" #P ":" #P "+3]", "v[" #P "+4:" #P "+7]", "v[64:79]")
#define SMM_WB(P) MF("v[80:95]", "v[" #P "+4:" #P "+7]", "v[" #P ":" #P "+3]", "v[80:95]")
#define SGEMM_ASM(ACC, MM, TD, NLOOP, TAIL, FIN)                                                                       \
    asm volatile(                                                                                                 \
        "s_mov_b32 s44, " #NLOOP "\n\t" BPR                                                                       \
        "s_barrier\n\t"                                                                                           \
        SRD3(32, 0, 0, TD) SRD3(40, 0, 1, TD) SRD3(48, 0, 2, TD) SRD3(56, 0, 3, TD)                               \
        "LS_loop%=:\n\t"                                                                                          \
        SST_O(1, MM, TD) SST_E(2, MM, TD) SST_O(3, MM, TD) SST_E(0, MM, TD)                                       \
        "s_sub_u32 s44, s44, 1\n\t"                                                                               \
        "s_cmp_lg_u32 s44, 0\n\t"                                                                                 \
        "s_cbranch_scc1 LS_loop%=\n\t"                                                                            \
        TAIL                                                                                                      \
        "s_waitcnt lgkmcnt(0)\n\t" FIN                                                                            \
        MM(96) MM(104) MM(112) MM(120)                                                                \
        "s_setprio 3\n\ts_nop 15\n\t"                                                                                            \
        : ACC(acc)                                                                                                \
        : [t] "v"(G.a), [dl] "v"(G.b)                                                                             \
        : "memory", "scc", "s44",                                                                                 \
          "v32", "v33", "v34", "v35", "v36", "v37", "v38", "v39", "v40", "v41", "v42", "v43", "v44", "v45", "v46", "v47", \
          "v48", "v49", "v50", "v51", "v52", "v53", "v54", "v55", "v56", "v57", "v58", "v59", "v60", "v61", "v62", "v63", \
          "v96", "v97", "v98", "v99", "v100", "v101", "v102", "v103", "v104", "v105", "v106", "v107", "v108", "v109", "v110", "v111", \
          "v112", "v113", "v114", "v115", "v116", "v117", "v118", "v119", "v120", "v121", "v122", "v123", "v124", "v125", "v126", "v127")

template <int MODE>
DI SG sgemm_setup(const char* wbase, unsigned wbytes, int wrow, const char* dbase, int dblk, int rot, int w) {
    const int lane = lane_id(), row = lane >> 3, lw = w & 3;
    const int q = (lane & 15) >> 2, h2 = lane >> 5;
    SG G;
    G.rw = srd(wbase, wbytes); G.rd = srd(dbase, 2 * dblk);
    G.wst = 64 * wrow; G.de = (MODE == 0 ? 31 : 21) * 4096; G.ow = rot * G.wst; G.od = rot * 4096;
    if (w >= 4) {
        G.a = (16 * lw + row) * wrow + (((lane & 7) ^ (((row >> 1) & 1) << 2)) << 4);
        G.b = G.a + 8 * wrow - 1024;
        G.c = (lw >> 1) * dblk + (lw & 1) * 2048 + lane * 16;
    } else {
        G.a = ((MODE == 0 ? 8 : 4) * h2 + q) * 128 + (((lw & 1) ^ ((q >> 1) & 1)) << 6) + ((lane >> 4) & 1) * 32 + (lane & 3) * 8;
        G.b = 8192 + (lw >> 1) * 4096 + lane * 16;
        G.c = 0;
    }
    asm volatile("" : "+v"(G.a), "+v"(G.b), "+v"(G.c));
    return G;
}
#define ACC0 "+{v[64:79]}"
#define ACC1 "+{v[80:95]}"
template <int MODE>
DI void sgemm_run(f32x16& acc, SG& G) {
    if (MODE == 0) SGEMM_ASM(ACC0, SMM_WA, 512, 7, SST_O(1, SMM_WA, 512) SST_E(2, SMM_WA, 512) SST_O(3, SMM_WA, 512), "s_setprio 3\n\t");
    else SGEMM_ASM(ACC1, SMM_WB, 1024, 5, SST_O(1, SMM_WB, 1024), "s_barrier\n\ts_setprio 3\n\t");
}
DI f32x4 sub4(const f32x16& a, int g) { return f32x4{a[4 * g], a[4 * g + 1], a[4 * g + 2], a[4 * g + 3]}; }

DI unsigned gelu2(float a, float b) {
    constexpr float c1 = -2.f * 0.7978845608028654f * 1.4426950408889634f, c2 = c1 * 0.044715f;
    const f32x2 u = {a, b};
    const f32x2 z = u * (u * u * c2 + c1);
    const f32x2 d = f32x2{__builtin_amdgcn_exp2f(z[0]), __builtin_amdgcn_exp2f(z[1])} + 1.f;
    const f32x2 o = u * f32x2{__builtin_amdgcn_rcpf(d[0]), __builtin_amdgcn_rcpf(d[1])};
    return cvtpk(o[0], o[1]);
}

#define WRD(R0, R1, R2, R3, STEP)                                                    \
    "ds_read_b64_tr_b16 " R0 ", %[t0] offset:" #STEP "*2048\n\t"                      \
    "ds_read_b64_tr_b16 " R1 ", %[t0] offset:" #STEP "*2048+1024\n\t"                 \
    "ds_read_b64_tr_b16 " R2 ", %[t1] offset:" #STEP "*2048\n\t"                      \
    "ds_read_b64_tr_b16 " R3 ", %[t1] offset:" #STEP "*2048+1024\n\t"
#define WRD0(S) WRD("v[32:33]", "v[34:35]", "v[36:37]", "v[38:39]", S)
#define WRD1(S) WRD("v[40:41]", "v[42:43]", "v[44:45]", "v[46:47]", S)
#define WRD2(S) WRD("v[48:49]", "v[50:51]", "v[52:53]", "v[54:55]", S)
#define WRD3(S) WRD("v[56:57]", "v[58:59]", "v[60:61]", "v[62:63]", S)
#define WMM(WAIT, W0, W1, O) "s_waitcnt " WAIT "\n\t" MF("v[96:111]", W0, O, "v[96:111]") MF("v[112:127]", W1, O, "v[112:127]")
#define OLD(K, R, V, OFF)                                             \
    "5:\n\t"                                                           \
    "s_bitcmp1_b32 s40, " #K "\n\t"                                    \
    "s_cbranch_scc1 6f\n\t"                                            \
    "global_load_dwordx2 v[24:25], %[fo], %[f] sc1\n\t"                       \
    "s_sub_u32 s41, s41, 1\n\t"                                        \
    "s_cselect_b32 s42, -1, 0\n\t"                                     \
    "s_waitcnt vmcnt(0)\n\t"                                           \
    "v_cmp_eq_u64 vcc, %[tag], v[24:25]\n\t"                                \
    "s_or_b32 s40, vcc_lo, s42\n\t"                                    \
    "s_bitcmp1_b32 s40, " #K "\n\t"                                    \
    "s_cbranch_scc1 6f\n\t"                                            \
    "s_sleep 1\n\t"                                                    \
    "s_branch 5b\n\t"                                                  \
    "6:\n\t"                                                           \
    "global_load_dwordx4 " R ", " V ", %[o] offset:" #OFF " sc1\n\t"
DI void wo_gemm(f32x16& a0, f32x16& a1, f32x4& k0, f32x4& k1, float& k2, u32x4& k3, const char* osrc, const char* fsrc, unsigned seen, u64 tag,
                unsigned t0, unsigned t1, unsigned d0, unsigned d1, unsigned fo) {
    asm volatile(
        WRD0(0) WRD1(1) WRD2(2) WRD3(3)
        "s_mov_b32 s40, %[seen]\n\t"
        "s_mov_b32 s41, 0x100000\n\t"
        "s_setprio 0\n\t"
        OLD(0, "v[64:67]", "%[d0]", 0) OLD(1, "v[68:71]", "%[d0]", 1024) OLD(2, "v[72:75]", "%[d0]", 2048) OLD(3, "v[76:79]", "%[d0]", 3072)
        OLD(4, "v[80:83]", "%[d1]", 0) OLD(5, "v[84:87]", "%[d1]", 1024) OLD(6, "v[88:91]", "%[d1]", 2048) OLD(7, "v[92:95]", "%[d1]", 3072)
        "s_setprio 3\n\t"
        "s_waitcnt vmcnt(7) lgkmcnt(0)\n\t"
        MF("v[96:111]", "v[32:35]", "v[64:67]", "0") MF("v[112:127]", "v[36:39]", "v[64:67]", "0")
        WRD0(4)
        WMM("vmcnt(6)", "v[40:43]", "v[44:47]", "v[68:71]") WRD1(5)
        WMM("vmcnt(5)", "v[48:51]", "v[52:55]", "v[72:75]") WRD2(6)
        WMM("vmcnt(4)", "v[56:59]", "v[60:63]", "v[76:79]") WRD3(7)
        WMM("vmcnt(3) lgkmcnt(12)", "v[32:35]", "v[36:39]", "v[80:83]")
        WMM("vmcnt(2) lgkmcnt(0)", "v[40:43]", "v[44:47]", "v[84:87]")
        WMM("vmcnt(1)", "v[48:51]", "v[52:55]", "v[88:91]")
        "s_barrier\n\t"
        WMM("vmcnt(0)", "v[56:59]", "v[60:63]", "v[92:95]")
        "s_nop 15\n\t"
        : "={v[96:111]}"(a0), "={v[112:127]}"(a1),
          "+{v[12:15]}"(k0), "+{v[16:19]}"(k1), "+{v20}"(k2), "+{v[28:31]}"(k3)
        : [o] "s"(osrc), [f] "s"(fsrc), [tag] "s"(tag), [seen] "s"(seen), [d0] "v"(d0), [d1] "v"(d1), [t0] "v"(t0), [t1] "v"(t1), [fo] "v"(fo)
        : "memory", "scc", "vcc", "s40", "s41", "s42", "v24", "v25",
          "v32", "v33", "v34", "v35", "v36", "v37", "v38", "v39", "v40", "v41", "v42", "v43", "v44", "v45", "v46", "v47",
          "v48", "v49", "v50", "v51", "v52", "v53", "v54", "v55", "v56", "v57", "v58", "v59", "v60", "v61", "v62", "v63",
          "v64", "v65", "v66", "v67", "v68", "v69", "v70", "v71", "v72", "v73", "v74", "v75", "v76", "v77", "v78", "v79",
          "v80", "v81", "v82", "v83", "v84", "v85", "v86", "v87", "v88", "v89", "v90", "v91", "v92", "v93", "v94", "v95");
}

DI void mixer_role(const char* x, const char* wqkv, const char* gw, const char* wo, const char* w1, const char* b1, const char* w2, const char* b2,
                   char* ws, char* out, char* lds, int bi, int w) {
    const int j = bi & 7, i = bi >> 3;
    __builtin_amdgcn_s_setprio(LNP);
    ST_DECL; ST(0);
    const u64 ep = ld_epoch(ws);
    u64 tag;
    {
        const int lane = lane_id(), h2 = lane >> 5, tid = 64 * w + lane;
        const u32x4 xr = *(GAS const u32x4*)(x + (size_t)(8 * bi + w) * 1024 + lane * 16);
        const u32x4 gr = *(GAS const u32x4*)(gw + lane * 16);
        tag = next_tag(ep);
        float v[8], gg[8];
#pragma unroll
        for (int k = 0; k < 4; ++k) { v[2 * k] = bflo(xr[k]); v[2 * k + 1] = bfhi(xr[k]); gg[2 * k] = bflo(gr[k]); gg[2 * k + 1] = bfhi(gr[k]); }
        float s1 = 0.f, s3 = 0.f;
#pragma unroll
        for (int k = 0; k < 8; ++k) { s1 += v[k]; s3 += v[k] * gg[k]; }
        const float mu = wave_sum(s1) * (1.f / 512.f);
        s3 = wave_sum(s3);
        float s2 = 0.f;
#pragma unroll
        for (int k = 0; k < 8; ++k) { v[k] -= mu; s2 += v[k] * v[k]; }
        const float rs = __builtin_amdgcn_rsqf(wave_sum(s2) * (1.f / 512.f) + 1e-5f);
        u32x4 hv;
#pragma unroll
        for (int k = 0; k < 4; ++k) hv[k] = cvtpk(v[2 * k] * rs, v[2 * k + 1] * rs);
        *(LAS u32x4*)((LAS char*)lds + w * 1024 + lane * 16) = hv;
        const float g = __builtin_amdgcn_rcpf(1.f + __builtin_amdgcn_exp2f(-1.4426950408889634f * s3));
        st4(ws + WS_G + (8 * bi + w) * 4, uni(g));
        wgbar();
        LAS const unsigned short* lb = (LAS const unsigned short*)lds + tid;
#pragma unroll
        for (int k = 0; k < 4; ++k) hv[k] = (unsigned)lb[(2 * k) * 512] | ((unsigned)lb[(2 * k + 1) * 512] << 16);
        st16(ws + WS_H + (size_t)(2 * w * 128 + (bi >> 1)) * 1024 + (bi & 1) * 512, h2 * 131072 + (lane & 31) * 16, hv);
        drain();
        wgbar();
        if (w == 0) st_flag(ws + WS_FL + bi * 128, tag);
    }
    __builtin_amdgcn_s_setprio(3);
    const int wb = w & 1, db = (w >> 1) & 1, hf = w >> 2;
    const int L = (j >> 1) * 22 + i, ft = L >> 2, dt1 = 4 * (j & 1) + (L & 3);
    const int st = 8 * (j >> 1) + (i & 7), dt = 4 * (j & 1) + (i >> 3);
    SG G1, G2;
    unsigned ex, l16, og, wof;
    {
        const int lane = lane_id();
        og = (64 * st + 32 * wb + (lane & 31)) * 4;
        G1 = sgemm_setup<0>(w1 + ft * 128, 2048 * 2752 - ft * 128, 2752, ws + WS_H + (size_t)2 * dt1 * 131072, 131072, ROT1, w);
        G2 = sgemm_setup<1>(w2 + st * 128, 1376 * 4096 - st * 128, 4096, ws + WS_U + (size_t)2 * dt * 86 * 1024, 86 * 1024, ROT2, w);
        l16 = lane * 16;
        ex = 65536 + (w & 3) * 2048 + l16;
        wof = (32 * (w & 3) + (lane >> 3)) * 1024 + (((lane & 7) ^ (((lane >> 4) & 1) << 2)) << 4);
        asm volatile("" : "+v"(ex), "+v"(l16), "+v"(og), "+v"(wof));
    }
    {
        const int row = 16 + 8 * (j & 1) + (i & 7), col = 8 + 8 * (i >> 4) + 2 * (j >> 1) + ((i >> 3) & 1);
        if (w == 0) {
            *(LAS unsigned*)QCNT = 0u;
            wait_flags<2>(ws + WS_FQ + bi * 128, 0, tag);
        }
        wgbar();
        if (w >= 4) qkv2_loader(x, wqkv, row, col, w - 4);
        else qkv2_body(ws, row, col, bi, w, tag);
    }
    ST(6);
    if (PFA && i >= 22) {
        const int wv = ((i - 22) * 8 + j) * 8 + w;
        if (PF1) warm(w1, 1376, wv, l16 * 4);
        for (int k = 0; k < PFD; ++k) __builtin_amdgcn_s_sleep(64);
        warm(w2, 1376, wv, l16 * 4);
        warm(wo, 128, wv, l16 * 4);
    }
    if (i < 22) {
        if (w >= 4) wait_flags<8>(ws + WS_FL + (w - 4) * 8192, l16 * 8, tag);
        wgbar();
        ST(7);
        f32x4 v0, v1;
        if (w >= 4) {
            sgemm_load<0>(G1, 32, w - 4, G1.rw, 0);
            v0 = *(LAS const f32x4*)(uintptr_t)ex; v1 = *(LAS const f32x4*)(uintptr_t)(ex + 1024);
        } else {
            f32x16 acc1;
            const char* bsrc = b1 + (64 * ft + (ft < 21 ? 32 * wb : 0)) * 2 + (l16 >> 9) * 8;
#pragma unroll
            for (int k = 0; k < 4; ++k) {
                const u32x2 bv = *(GAS const u32x2*)(bsrc + 16 * k);
                acc1[4 * k] = bflo(bv[0]); acc1[4 * k + 1] = bfhi(bv[0]); acc1[4 * k + 2] = bflo(bv[1]); acc1[4 * k + 3] = bfhi(bv[1]);
            }
            sgemm_run<0>(acc1, G1);
            *(LAS f32x4*)(uintptr_t)ex = sub4(acc1, 2); *(LAS f32x4*)(uintptr_t)(ex + 1024) = sub4(acc1, 3);
            wgbar();
            v0 = sub4(acc1, 0); v1 = sub4(acc1, 1);
        }
        ST(1);
        const u32x4 o = {gelu2(v0[0], v0[1]), gelu2(v0[2], v0[3]), gelu2(v1[0], v1[1]), gelu2(v1[2], v1[3])};
        if (ft < 21 || wb == 0) st16(ws + WS_U + (size_t)((2 * dt1 + db) * 86 + 4 * ft + 2 * wb + hf) * 1024, l16, o);
        drain();
        wgbar();
        if (w == 0) st_flag(ws + WS_FU + (dt1 * 32 + ft) * 128, tag);
    }
    ST(2);
    if (w >= 4) sgemm_prew(G2, w - 4);
    unsigned t0, t1, d1, fo;
    u32x4 fin;
    {
        const int lane = l16 >> 4, q = (lane & 15) >> 2, kq = 2 * db + hf;
        const unsigned tb = (w >> 1) * 16384 + (4 * (lane >> 5) + q) * 128 + ((lane >> 4) & 1) * 32 + (lane & 3) * 8;
        t0 = tb + ((q >> 1) & 1) * 64; t1 = tb + (((q >> 1) & 1) ^ 1) * 64; d1 = l16 + 4096; fo = (lane & 7) * 128;
        fin = u32x4{w * 8192 + l16, wb * 8192 + kq * 2048 + l16, (unsigned)((64 * st + 32 * wb + (lane & 31)) * 1024 + 128 * dt + kq * 32 + (lane >> 5) * 16), 0u};
        asm volatile("" : "+v"(t0), "+v"(t1), "+v"(d1), "+v"(fo), "+{v[28:31]}"(fin));
    }
    if (bi == 255 && w == 1) {
        wait_flags256(ws + WS_FQ, l16 * 8, tag);
        wait_flags256(ws + WS_FL, l16 * 8, tag);
        st_flag(ws + WS_EPOCH, tag);
    }
    if (w == 0) wait_flags<8>(ws + WS_FU + dt * 4096, (l16 < 22 * 16 ? l16 : 0) * 8, tag);
    wgbar();
    ST(3);
    const char* fsrc = ws + WS_FO + ((2 * st + wb) * 32 + 8 * (w >> 1)) * 128;
    u64 f0;
    f32x4 bp0, bp1;
    float g;
    {
        asm volatile("global_load_dword %0, %1, %2 sc1" : "={v20}"(g) : "v"(og), "s"(ws + WS_G) : "memory");
        if (w >= 4) {
            sgemm_load<1>(G2, 22, w - 4, srd(wo + dt * 128, 524288 - dt * 128), wof);
            asm volatile("global_load_dwordx2 %0, %1, %2 sc1" : "={v[22:23]}"(f0) : "v"(fo), "s"(fsrc) : "memory");
            bp0 = *(LAS const f32x4*)(uintptr_t)ex; bp1 = *(LAS const f32x4*)(uintptr_t)(ex + 1024);
        } else {
            const float bb = bflo(*(GAS const unsigned short*)(b2 + (og >> 1)));
            f32x16 acc2 = {bb, bb, bb, bb, bb, bb, bb, bb, bb, bb, bb, bb, bb, bb, bb, bb};
            sgemm_run<1>(acc2, G2);
            asm volatile("global_load_dwordx2 %0, %1, %2 sc1" : "={v[22:23]}"(f0) : "v"(fo), "s"(fsrc) : "memory");
            *(LAS f32x4*)(uintptr_t)ex = sub4(acc2, 2); *(LAS f32x4*)(uintptr_t)(ex + 1024) = sub4(acc2, 3);
            wgbar();
            bp0 = sub4(acc2, 0); bp1 = sub4(acc2, 1);
            drain();
        }
        asm volatile("" : "+{v20}"(g));
        ST(4);
        bp0 *= 1.f - g;
        bp1 *= 1.f - g;
        asm volatile("" : "+v"(bp0), "+v"(bp1));
    }
    asm volatile("s_waitcnt vmcnt(0)\n\ts_barrier" : "+{v[22:23]}"(f0)::"memory");
    ST(5);
    f32x16 a0, a1;
    wo_gemm(a0, a1, bp0, bp1, g, fin, ws + WS_O + (size_t)((2 * st + wb) * 32 + 8 * (w >> 1)) * 1024,
            fsrc, (unsigned)__ballot(f0 == tag), tag, t0, t1, l16, d1, fo);
    {
        LAS char* ex = (LAS char*)(uintptr_t)fin[0];
#pragma unroll
        for (int k = 0; k < 4; ++k) { *(LAS f32x4*)(ex + k * 1024) = sub4(a0, k); *(LAS f32x4*)(ex + 4096 + k * 1024) = sub4(a1, k); }
        wgbar();
        LAS const char* exr = (LAS const char*)(uintptr_t)fin[1];
        f32x4 s0 = *(LAS const f32x4*)exr, s1 = *(LAS const f32x4*)(exr + 1024);
#pragma unroll
        for (int k = 1; k < 4; ++k) { s0 += *(LAS const f32x4*)(exr + k * 16384); s1 += *(LAS const f32x4*)(exr + k * 16384 + 1024); }
        s0 = s0 * g + bp0;
        s1 = s1 * g + bp1;
        const auto r0 = __builtin_amdgcn_permlane32_swap(cvtpk(s0[0], s0[1]), cvtpk(s1[0], s1[1]), false, false);
        const auto r1 = __builtin_amdgcn_permlane32_swap(cvtpk(s0[2], s0[3]), cvtpk(s1[2], s1[3]), false, false);
        asm volatile("global_store_dwordx4 %0, %1, %2" ::"v"(fin[2]), "v"(u32x4{r0[0], r1[0], r0[1], r1[1]}), "s"(out) : "memory");
    }
    ST_OUT(0);
}

constexpr int MK_LDS = 81920;
__global__ __launch_bounds__(512, 4) void mk(const char* x, const char* wqkv, const char* wo, const char* w1, const char* b1,
                                             const char* w2, const char* b2, const char* gw, char* ws, char* out) {
    extern __shared__ __attribute__((aligned(16))) char lds[];
    const int w = __builtin_amdgcn_readfirstlane(threadIdx.x >> 6);
    if (blockIdx.x >= 256 && w == 7) {
        asm volatile("s_getpc_b64 s[40:41]\n\t"
                     "Lmk_hot_begin:\n\t"
                     "s_cmp_lt_u32 %[o], Lmk_hot_end-Lmk_hot_begin-4096\n\t"
                     "s_cbranch_scc0 1f\n\t"
                     "s_add_u32 s40, s40, %[o]\n\t"
                     "s_addc_u32 s41, s41, 0\n\t"
                     "s_and_b32 s40, s40, -4\n\t"
                     "s_mov_b32 m0, 0x13e00\n\t"
                     "s_nop 0\n\t"
                     "global_load_lds_dword %[v], s[40:41]\n\t"
                     "1:"
                     :: [o] "s"(((blockIdx.x - 256) >> 3) * 4096), [v] "v"(lane_id() * 64) : "memory", "scc", "s40", "s41");
    }
    if (blockIdx.x < 256) attn_role(x, wqkv, ws, lds, blockIdx.x, w);
    else mixer_role(x, wqkv, gw, wo, w1, b1, w2, b2, ws, out, lds, blockIdx.x - 256, w);
}

extern "C" void kernel_launch(void* const* d_in, const int* in_sizes, int n_in, void* d_out, int out_size, void* d_ws, size_t ws_size,
                              hipStream_t stream) {
    (void)hipFuncSetAttribute((const void*)mk, hipFuncAttributeMaxDynamicSharedMemorySize, MK_LDS);
    mk<<<512, 512, MK_LDS, stream>>>((const char*)d_in[0], (const char*)d_in[1], (const char*)d_in[2], (const char*)d_in[4],
                                     (const char*)d_in[5], (const char*)d_in[6], (const char*)d_in[7], (const char*)d_in[8],
                                     (char*)d_ws, (char*)d_out);
}
```

```cpp
#include <hip/hip_runtime.h>
#include <hip/hip_bf16.h>
#include <stdint.h>

typedef __attribute__((ext_vector_type(8))) short bf16x8;
typedef __attribute__((ext_vector_type(4))) short s16x4;
typedef __attribute__((ext_vector_type(16))) float f32x16;
typedef __attribute__((ext_vector_type(4))) float f32x4;
typedef __attribute__((ext_vector_type(2))) float f32x2;
typedef __attribute__((ext_vector_type(4))) unsigned u32x4;
typedef __attribute__((ext_vector_type(2))) unsigned u32x2;
typedef unsigned long long u64;
#define LAS __attribute__((address_space(3)))
#define GAS __attribute__((address_space(1)))
#define DI __device__ __forceinline__
#define MFMA32(a, b, c) __builtin_amdgcn_mfma_f32_32x32x16_bf16((a), (b), (c), 0, 0, 0)

constexpr size_t WS_EPOCH = 0;
constexpr size_t WS_G = 4096;
constexpr size_t WS_FL = 98304;
constexpr size_t WS_FQ = 458752;
constexpr size_t WS_FU = 65536;
constexpr size_t WS_FO = 131072;
constexpr size_t WS_Q = 1u << 20;
constexpr size_t WS_K = WS_Q + (2u << 20);
constexpr size_t WS_V = WS_K + (2u << 20);
constexpr size_t WS_H = WS_V + (2u << 20);
constexpr size_t WS_U = WS_H + (2u << 20);
constexpr size_t WS_O = WS_U + (2u << 20);

DI unsigned cvtpk(float lo, float hi) {
    typedef float f32x2_t __attribute__((ext_vector_type(2)));
    typedef __bf16 bf16x2_t __attribute__((ext_vector_type(2)));
    return __builtin_bit_cast(unsigned, __builtin_convertvector(f32x2_t{lo, hi}, bf16x2_t));
}
DI float bflo(unsigned u) { return __uint_as_float(u << 16); }
DI float bfhi(unsigned u) { return __uint_as_float(u & 0xffff0000u); }
DI s16x4 trread(LAS const char* p) {
    typedef short v4i16_t __attribute__((ext_vector_type(4)));
    return __builtin_bit_cast(s16x4, __builtin_amdgcn_ds_read_tr16_b64_v4i16((LAS v4i16_t*)p));
}
template <int CTRL> DI float dpp(float v) {
    return __int_as_float(__builtin_amdgcn_update_dpp(0, __float_as_int(v), CTRL, 0xf, 0xf, true));
}
DI float rl(float v, int l) { return __int_as_float(__builtin_amdgcn_readlane(__float_as_int(v), l)); }
DI float wave_sum(float v) {
    v += dpp<0xB1>(v); v += dpp<0x4E>(v); v += dpp<0x141>(v); v += dpp<0x140>(v);
    return (rl(v, 0) + rl(v, 16)) + (rl(v, 32) + rl(v, 48));
}
DI float swap32_max(float v) {
    auto rr = __builtin_amdgcn_permlane32_swap(__float_as_uint(v), __float_as_uint(v), false, false);
    return fmaxf(__uint_as_float(rr[0]), __uint_as_float(rr[1]));
}

DI int lane_id() {
    int l;
    asm volatile("v_mbcnt_lo_u32_b32 %0, -1, 0\n\tv_mbcnt_hi_u32_b32 %0, -1, %0" : "=v"(l));
    __builtin_assume(l >= 0 && l < 64);
    return l;
}
DI void st16(const char* sbase, unsigned voff, u32x4 v) {
    asm volatile("global_store_dwordx4 %0, %1, %2 sc1\n\ts_nop 1" ::"v"(voff), "v"(v), "s"(sbase) : "memory");
}
DI void st4(const char* p, unsigned v) {
    unsigned t0, t1;
    asm volatile("s_nop 1\n\tv_mov_b32 %0, %2\n\tv_mov_b32 %1, 0\n\ts_mov_b64 exec, 1\n\tglobal_store_dword %1, %0, %3 sc1\n\ts_mov_b64 exec, -1"
                 : "=&v"(t0), "=&v"(t1) : "s"(v), "s"(p) : "memory");
}
DI void st_flag(const char* p, u64 v) {
    u64 t0;
    unsigned t1;
    asm volatile("s_nop 1\n\tv_mov_b64 %0, %2\n\tv_mov_b32 %1, 0\n\ts_mov_b64 exec, 1\n\tglobal_store_dwordx2 %1, %0, %3 sc1\n\ts_mov_b64 exec, -1"
                 : "=&v"(t0), "=&v"(t1) : "s"(v), "s"(p) : "memory");
}
DI u64 uni64(u64 v) {
    return (u64)(unsigned)__builtin_amdgcn_readfirstlane((unsigned)v) | ((u64)(unsigned)__builtin_amdgcn_readfirstlane((unsigned)(v >> 32)) << 32);
}
DI u64 ld_epoch(const char* ws) { return __hip_atomic_load((GAS const u64*)(ws + WS_EPOCH), __ATOMIC_RELAXED, __HIP_MEMORY_SCOPE_AGENT); }
DI u64 next_tag(u64 e) { return uni64(e) * 6364136223846793005ull + 1442695040888963407ull; }
DI unsigned uni(float v) { return __builtin_amdgcn_readfirstlane(__float_as_uint(v)); }
DI void warm(const char* base, int nchunks, int first, unsigned l64) {
#pragma clang loop unroll(disable)
    for (int n = first; n < nchunks; n += 640) {
        const char* p = base + (size_t)((n & 1) * (nchunks >> 1) + (n >> 1)) * 4096;
        asm volatile("s_mov_b32 m0, 0x13e00\n\ts_nop 0\n\tglobal_load_lds_dword %0, %1" ::"v"(l64), "s"(p) : "memory");
    }
}
DI void drain() { asm volatile("s_waitcnt vmcnt(0)" ::: "memory"); }
DI void wgbar() { asm volatile("s_waitcnt lgkmcnt(0)\n\ts_barrier" ::: "memory"); }
template <int SLEEP>
DI void wait_flags(const char* sbase, unsigned voff, u64 tag) {
#pragma clang loop unroll(disable)
    for (unsigned spin = 0; spin < (1u << 20); ++spin) {
        u64 f;
        asm volatile("global_load_dwordx2 %0, %1, %2 sc1\n\ts_waitcnt vmcnt(0)" : "=v"(f) : "v"(voff), "s"(sbase) : "memory");
        if (__all(f == tag)) break;
        __builtin_amdgcn_s_sleep(SLEEP);
    }
}

DI void wait_flags256(const char* sbase, unsigned voff, u64 tag) {
#pragma clang loop unroll(disable)
    for (unsigned spin = 0; spin < (1u << 20); ++spin) {
        u64 f0, f1, f2, f3;
        asm volatile("global_load_dwordx2 %0, %4, %5 sc1\n\tglobal_load_dwordx2 %1, %4, %6 sc1\n\tglobal_load_dwordx2 %2, %4, %7 sc1\n\t"
                     "global_load_dwordx2 %3, %4, %8 sc1\n\ts_waitcnt vmcnt(0)"
                     : "=&v"(f0), "=&v"(f1), "=&v"(f2), "=&v"(f3)
                     : "v"(voff), "s"(sbase), "s"(sbase + 8192), "s"(sbase + 16384), "s"(sbase + 24576) : "memory");
        if (__all(f0 == tag && f1 == tag && f2 == tag && f3 == tag)) break;
        __builtin_amdgcn_s_sleep(8);
    }
}

constexpr int QCNT = 73728;
#define MF(D, A, B, C) "v_mfma_f32_32x32x16_bf16 " D ", " A ", " B ", " C "\n\t"
DI u32x4 srd(const char* base, unsigned bytes) {
    return u32x4{(unsigned)(uintptr_t)base, (unsigned)((uintptr_t)base >> 32) & 0xffffu, bytes, 0x00020000u};
}
DI void qkv_store(char* ws, const f32x16& acc, int type, int head, int tile, int lane) {
    const float sc = (type == 0) ? 0.25f * 1.4426950408889634f : 1.f;
    const char* dst = ws + WS_Q + (size_t)type * (2u << 20) + (size_t)(head * 64 + tile) * 1024;
    const unsigned vo = type < 2 ? lane * 16 : ((lane >> 4) & 1) * 65536 + (lane >> 5) * 256 + (lane & 15) * 16;
#pragma unroll
    for (int hf = 0; hf < 2; ++hf) {
        u32x4 o;
#pragma unroll
        for (int i = 0; i < 4; ++i) o[i] = cvtpk(acc[8 * hf + 2 * i] * sc, acc[8 * hf + 2 * i + 1] * sc);
        st16(dst + hf * (type < 2 ? 65536 : 512), vo, o);
    }
}
DI void publish4(const char* flag, u64 tag, int lane) {
    drain();
    unsigned arr = 0;
    if (lane == 0) arr = __hip_atomic_fetch_add((LAS unsigned*)QCNT, 1u, __ATOMIC_RELAXED, __HIP_MEMORY_SCOPE_WORKGROUP);
    if (__builtin_amdgcn_readfirstlane(arr) == 3) st_flag(flag, tag);
}
DI void qkv_loader(const char* x, const char* wqkv, int row, int col, int lw) {
    const int lane = lane_id(), xr = 16 * lw + (lane >> 3), wr = 16 * lw + (lane >> 4);
    const unsigned x0 = xr * 1024 + (((lane & 7) ^ ((xr >> 1) & 7)) << 4), x1 = (xr + 8) * 1024 + (((lane & 7) ^ (((xr + 8) >> 1) & 7)) << 4) - 1024;
    const unsigned w0 = wr * 3072 + (((lane & 15) ^ ((wr & 3) << 2)) << 4);
    const u32x4 rx = srd(x + (size_t)row * 65536, 65536), rw = srd(wqkv + col * 256, 512 * 3072 - col * 256);
    const int rot = (2 * (row + col)) & 6;
    auto issue = [&](int s, int slot) {
        const int kc = (s + rot) & 7;
        asm volatile("s_mov_b32 m0, %[m]\n\ts_nop 0\n\t"
                     "buffer_load_dwordx4 %[x0], %[rx], %[ox] offen lds\n\t"
                     "buffer_load_dwordx4 %[x1], %[rx], %[ox] offen offset:1024 lds\n\t"
                     "s_mov_b32 m0, %[m2]\n\ts_nop 0\n\t"
                     "buffer_load_dwordx4 %[w0], %[rw], %[ow] offen lds\n\t"
                     "buffer_load_dwordx4 %[w1], %[rw], %[ow] offen offset:1024 lds\n\t"
                     "buffer_load_dwordx4 %[w2], %[rw], %[ow] offen offset:2048 lds\n\t"
                     "buffer_load_dwordx4 %[w3], %[rw], %[ow] offen offset:3072 lds"
                     :: [m] "s"(slot * 24576 + lw * 2048), [m2] "s"(slot * 24576 + 8192 + lw * 4096), [ow] "s"(kc * 196608), [ox] "s"(kc * 128),
                        [rw] "s"(rw), [rx] "s"(rx), [x0] "v"(x0), [x1] "v"(x1),
                        [w0] "v"(w0), [w1] "v"(w0 + 11264), [w2] "v"(w0 + 22528), [w3] "v"(w0 + 33792)
                     : "memory");
    };
    issue(0, 0);
    __builtin_amdgcn_s_setprio(2);
    issue(1, 1);
    __builtin_amdgcn_s_setprio(1);
    issue(2, 2);
    __builtin_amdgcn_s_setprio(3);
    asm volatile("s_waitcnt vmcnt(12)\n\ts_barrier" ::: "memory");
    int slot = 0;
#pragma clang loop unroll(disable)
    for (int s = 3; s < 8; ++s) {
        asm volatile("s_waitcnt vmcnt(6)\n\ts_barrier" ::: "memory");
        issue(s, slot);
        slot = slot == 2 ? 0 : slot + 1;
    }
    asm volatile("s_waitcnt vmcnt(6)\n\ts_barrier\n\ts_waitcnt vmcnt(0)\n\ts_barrier" ::: "memory");
}
template <bool T>
DI void qkv_body(char* ws, char* lds, int row, int col, int a, int w, u64 tag) {
    const int lane = lane_id(), r = lane & 31, h2 = lane >> 5, q = (lane & 15) >> 2;
    f32x16 acc[2] = {};
    const int xa0 = (32 * (w & 1) + r) * 128 + ((h2 ^ ((r >> 1) & 7)) << 4);
    const int wa0 = 8192 + (8 * h2 + q) * 256 + (((2 * (w >> 1)) ^ q) << 6) + ((lane >> 4) & 1) * 32 + (lane & 3) * 8;
    bf16x8 xf[4], wf[2][2];
    auto rdw = [&](LAS const char* sl, int st, int i) {
        LAS const char* pw = sl + (wa0 ^ (i << 6)) + st * 4096;
        const s16x4 lo = trread(pw), hi = trread(pw + 1024);
        wf[st & 1][i] = __builtin_shufflevector(lo, hi, 0, 1, 2, 3, 4, 5, 6, 7);
    };
    auto mm1 = [&](int st, int i) {
        acc[i] = T ? MFMA32(wf[st & 1][i], xf[st], acc[i]) : MFMA32(xf[st], wf[st & 1][i], acc[i]);
        __builtin_amdgcn_sched_barrier(0);
    };
    auto step = [&](int st, LAS const char* sl, bool bar) {
        const int sn = (st + 2) & 3;
        mm1(st, 0);
        if (bar) wgbar();
        xf[sn] = *(LAS const bf16x8*)(sl + (xa0 ^ (sn << 5))); rdw(sl, sn, 0); __builtin_amdgcn_sched_barrier(0);
        mm1(st, 1);
        rdw(sl, sn, 1); __builtin_amdgcn_sched_barrier(0);
    };
    if (w == 0) *(LAS u32x4*)QCNT = u32x4{0u, 0u, (unsigned)tag, (unsigned)(tag >> 32)};
    wgbar();
    LAS const char* sl = (LAS const char*)lds;
#pragma unroll
    for (int st = 0; st < 2; ++st) { xf[st] = *(LAS const bf16x8*)(sl + (xa0 ^ (st << 5))); rdw(sl, st, 0); rdw(sl, st, 1); }
    __builtin_amdgcn_sched_barrier(0);
#pragma clang loop unroll(disable)
    for (int s = 0; s < 7; ++s) {
        step(0, sl, false); step(1, sl, false);
        sl = (sl == (LAS const char*)lds + 49152) ? (LAS const char*)lds : sl + 24576;
        step(2, sl, true); step(3, sl, false);
    }
    step(0, sl, false); step(1, sl, false);
    mm1(2, 0); mm1(2, 1); mm1(3, 0); mm1(3, 1);
#pragma unroll
    for (int i = 0; i < 2; ++i) qkv_store(ws, acc[i], col >> 2, 8 * (col & 3) + 4 * (w >> 1) + 2 * i, 2 * row + (w & 1), lane);
    publish4(ws + WS_FQ + a * 128, tag, lane);
}
DI void qkv2_loader(const char* x, const char* wqkv, int rowt, int col, int lw) {
    const int lane = lane_id(), row = 16 * lw + (lane >> 3);
    const unsigned w0 = row * 3072 + (((lane & 7) ^ (((row >> 1) & 1) << 2)) << 4), w1 = w0 + 8 * 3072 - 1024;
    const unsigned x0 = row * 1024 + (((lane & 7) ^ ((row >> 1) & 7)) << 4), x1 = (row + 8) * 1024 + (((lane & 7) ^ (((row + 8) >> 1) & 7)) << 4) - 1024;
    const u32x4 rx = srd(x + (size_t)rowt * 65536, 65536), rw = srd(wqkv + col * 128, 512 * 3072 - col * 128);
    auto issue = [&](int s) {
        asm volatile("s_mov_b32 m0, %[m]\n\ts_nop 0\n\t"
                     "buffer_load_dwordx4 %[w0], %[rw], %[ow] offen lds\n\t"
                     "buffer_load_dwordx4 %[w1], %[rw], %[ow] offen offset:1024 lds\n\t"
                     "s_add_u32 m0, %[m], 8192\n\ts_nop 0\n\t"
                     "buffer_load_dwordx4 %[x0], %[rx], %[ox] offen lds\n\t"
                     "buffer_load_dwordx4 %[x1], %[rx], %[ox] offen offset:1024 lds"
                     :: [m] "s"((s & 3) * 16384 + lw * 2048), [ow] "s"(s * 196608), [ox] "s"(s * 128), [rw] "s"(rw), [rx] "s"(rx),
                        [w0] "v"(w0), [w1] "v"(w1), [x0] "v"(x0), [x1] "v"(x1)
                     : "memory", "scc");
    };
    issue(0); issue(1); issue(2);
#pragma clang loop unroll(disable)
    for (int s = 3; s < 8; ++s) {
        asm volatile("s_waitcnt vmcnt(8)\n\ts_barrier" ::: "memory");
        issue(s);
    }
    asm volatile("s_waitcnt vmcnt(8)\n\ts_barrier\n\ts_waitcnt vmcnt(4)\n\ts_barrier\n\ts_waitcnt vmcnt(0)\n\ts_barrier" ::: "memory");
}
#define QRD3(B, SLOT, K)                                                                             \
    "ds_read_b64_tr_b16 v[" #B ":" #B "+1], %[t] offset:" #SLOT "*16384+" #K "*2048\n\t"              \
    "ds_read_b64_tr_b16 v[" #B "+2:" #B "+3], %[t] offset:" #SLOT "*16384+" #K "*2048+512\n\t"        \
    "ds_read_b128 v[" #B "+4:" #B "+7], %[x" #K "] offset:" #SLOT "*16384\n\t"
#define QSTAGE(SLOT, MM, N0, N1, N2, N3, P0, P1, P2, P3)                              \
    "s_waitcnt lgkmcnt(0)\n\t"                                                        \
    "s_barrier\n\t"                                                                   \
    MM(P0) QRD3(N0, SLOT, 0) MM(P1) QRD3(N1, SLOT, 1) MM(P2) QRD3(N2, SLOT, 2) MM(P3) QRD3(N3, SLOT, 3)
#define QST_E(SLOT, MM) QSTAGE(SLOT, MM, 32, 40, 48, 56, 96, 104, 112, 120)
#define QST_O(SLOT, MM) QSTAGE(SLOT, MM, 96, 104, 112, 120, 32, 40, 48, 56)
#define QMT(P) MF("v[16:31]", "v[" #P ":" #P "+3]", "v[" #P "+4:" #P "+7]", "v[16:31]")
#define QMV(P) MF("v[16:31]", "v[" #P "+4:" #P "+7]", "v[" #P ":" #P "+3]", "v[16:31]")
#define QTILE_ASM(MM)                                                                                             \
    asm volatile(                                                                                                 \
        "s_barrier\n\t"                                                                                           \
        QRD3(32, 0, 0) QRD3(40, 0, 1) QRD3(48, 0, 2) QRD3(56, 0, 3)                                               \
        QST_O(1, MM) QST_E(2, MM)                                                                                 \
        "s_mov_b32 s44, 0\n\t"                                                                                    \
        "LQ_loop%=:\n\t"                                                                                          \
        QST_O(3, MM)                                                                                              \
        "s_cmp_lg_u32 s44, 0\n\t"                                                                                 \
        "s_cbranch_scc1 LQ_end%=\n\t"                                                                             \
        QST_E(0, MM) QST_O(1, MM) QST_E(2, MM)                                                                    \
        "s_mov_b32 s44, 1\n\t"                                                                                    \
        "s_branch LQ_loop%=\n\t"                                                                                  \
        "LQ_end%=:\n\t"                                                                                           \
        "s_waitcnt lgkmcnt(0)\n\t"                                                                                \
        MM(96) MM(104) MM(112) MM(120)                                                                            \
        "s_nop 15\n\t"                                                                                            \
        : "+{v[16:31]}"(acc)                                                                                      \
        : [t] "v"(t), [x0] "v"(xa), [x1] "v"(xa ^ 32), [x2] "v"(xa ^ 64), [x3] "v"(xa ^ 96)                       \
        : "memory", "scc", "s44",                                                                                 \
          "v32", "v33", "v34", "v35", "v36", "v37", "v38", "v39", "v40", "v41", "v42", "v43", "v44", "v45", "v46", "v47", \
          "v48", "v49", "v50", "v51", "v52", "v53", "v54", "v55", "v56", "v57", "v58", "v59", "v60", "v61", "v62", "v63", \
          "v96", "v97", "v98", "v99", "v100", "v101", "v102", "v103", "v104", "v105", "v106", "v107", "v108", "v109", "v110", "v111", \
          "v112", "v113", "v114", "v115", "v116", "v117", "v118", "v119", "v120", "v121", "v122", "v123", "v124", "v125", "v126", "v127")
DI void qkv2_body(char* ws, int row, int col, int bi, int w, u64 tag) {
    const int lane = lane_id(), r = lane & 31, h2 = lane >> 5, q = (lane & 15) >> 2;
    const unsigned t = (8 * h2 + q) * 128 + (((w & 1) ^ ((q >> 1) & 1)) << 6) + ((lane >> 4) & 1) * 32 + (lane & 3) * 8;
    const unsigned xa = 8192 + (32 * (w >> 1) + r) * 128 + ((h2 ^ ((r >> 1) & 7)) << 4);
    f32x16 acc = {};
    if (col < 16) QTILE_ASM(QMT);
    else QTILE_ASM(QMV);
    qkv_store(ws, acc, col >> 3, 4 * (col & 7) + 2 * (w & 1), 2 * row + (w >> 1), lane);
    publish4(ws + WS_FQ + 32768 + bi * 128, tag, lane);
}

#define EXP(a) "v_exp_f32 v" #a ", v" #a "\n\t"
#define CVT(d, a, b) "v_cvt_pk_bf16_f32 v" #d ", v" #a ", v" #b "\n\t"
#define ASTEP(c0, c1, c2, c3, c4, c5, c6, c7, c8, c9, c10, c11, c12, c13, c14, c15, M1, M2, CL, M3)                  \
    EXP(c0) EXP(c1) M1 EXP(c2) EXP(c3) EXP(c4) EXP(c5) M2 CL                                                          \
    EXP(c6) EXP(c7) CVT(96, c0, c1) CVT(97, c2, c3) CVT(98, c4, c5) CVT(99, c6, c7) EXP(c8) EXP(c9) M3                \
    EXP(c10) EXP(c11) EXP(c12) EXP(c13) EXP(c14) EXP(c15)                                                             \
    CVT(100, c8, c9) CVT(101, c10, c11) CVT(102, c12, c13) CVT(103, c14, c15)
#define R_OA "v[64:79]"
#define R_OB "v[80:95]"
#define R_SA "v[32:47]"
#define R_SB "v[48:63]"
#define ATILE(R_NM, KC, KN, V0C, V1C, V0N, V1N, KOFF, VA, VOFF0, VOFF1, PRE)                                                    \
    ASTEP(32, 33, 34, 35, 36, 37, 38, 39, 40, 41, 42, 43, 44, 45, 46, 47,                                             \
          MF(R_OB, V1N, "v[100:103]", R_OB), MF(R_SB, KC, "v[8:11]", R_NM),                                           \
          PRE                                                                                                         \
          "ds_read_b128 " KC ", v12 offset:" KOFF "\n\t"                                                              \
          "ds_read_b128 " V0N ", " VA " offset:" #VOFF0 "\n\t"                                                        \
          "ds_read_b128 " V1N ", " VA " offset:" #VOFF1 "\n\t",                                                          \
          MF(R_OA, V0C, "v[96:99]", R_OA))                                                                            \
    ASTEP(48, 49, 50, 51, 52, 53, 54, 55, 56, 57, 58, 59, 60, 61, 62, 63,                                             \
          MF(R_OA, V1C, "v[100:103]", R_OA), MF(R_SA, KN, "v[4:7]", R_NM), "", MF(R_OB, V0C, "v[96:99]", R_OB))
#define ATILE_E(NM, KOFF, VA, VOFF0, VOFF1, PRE) ATILE(NM, "v[104:107]", "v[108:111]", "v[112:115]", "v[116:119]", "v[120:123]", "v[124:127]", KOFF, VA, VOFF0, VOFF1, PRE)
#define ATILE_O(NM, KOFF, VA, VOFF0, VOFF1, PRE) ATILE(NM, "v[108:111]", "v[104:107]", "v[120:123]", "v[124:127]", "v[112:115]", "v[116:119]", KOFF, VA, VOFF0, VOFF1, PRE)
#define WL "s_waitcnt lgkmcnt(0)\n\t"
#define AISSUE                                                   \
    "s_add_u32 m0, s44, %[lw]\n\t"                               \
    "s_add_u32 s44, s44, 0x4000\n\t"                             \
    "buffer_load_dwordx4 v14, %[rk], s43 offen sc1 lds\n\t"      \
    "s_add_u32 m0, m0, 0x2000\n\t"                               \
    "s_and_b32 s44, s44, 0xffff\n\t"                             \
    "buffer_load_dwordx4 v14, %[rv], s43 offen sc1 lds\n\t"      \
    "s_add_u32 s43, s43, 0x2000\n\t"
#define PRI "s_setprio 3\n\t"
#define PR2 "s_setprio 2\n\t"
#define PR0 "s_setprio 1\n\t"
#define PR1 "s_setprio 0\n\t"
constexpr int A_ONES = 65536, A_VOTE = 69632 + 512, A_LSUM = 71680;
struct AttnRegs { f32x16 oa, ob, nm; u32x4 p1, v1, fin; unsigned ka, va, va1, va2, va3, go, fo; };
DI unsigned qkv_dep(int a, bool early) {
    const int lane = lane_id(), r = lane & 15, v = (lane >> 4) & 1, R = 4 * (a >> 5) + (lane & 3);
    const int kv = (r >> 3) + 8 * ((early ? 8 + 8 * v : 16 * v + 8 * (a & 1)) + (r & 7));
    const int qq = ((R >> 3) & 1) + 8 * ((R >> 4) * 24 + (R & 7));
    return ((a & 6) + (lane < 32 || !early ? kv : qq)) * 128;
}
DI void attn_prep(AttnRegs& R, int w) {
    const int lane = lane_id(), r = lane & 31, kh = w >> 2;
    R.ka = 4096 * kh + lane * 16;
    R.va = r < 16 ? 8192 + 4096 * kh + (lane >> 5) * 256 + r * 16 : A_ONES + (r == 16 ? 0 : r == 20 ? 64 : (r > 20 && r < 28) ? 80 : 16);
    R.go = w * 1024 + lane * 16;
#pragma unroll
    for (int i = 0; i < 16; ++i) { R.oa[i] = 0.f; R.ob[i] = 0.f; }
    R.p1 = u32x4{0u, 0u, 0u, 0u};
    R.v1 = u32x4{0u, 0u, 0u, 0u};
    asm volatile("" : "+{v[64:79]}"(R.oa), "+{v[80:95]}"(R.ob), "+{v[100:103]}"(R.p1), "+{v[124:127]}"(R.v1),
                      "+{v12}"(R.ka), "+{v13}"(R.va), "+{v14}"(R.go));
}
#define ATTN_ASM(NM, NMIN) \
    asm volatile( \
        "v_and_b32 v15, 0x3ff, v14\n\t" \
        "s_mov_b32 s43, 0\n\t" \
        "s_mov_b32 s44, 0\n\t" \
        AISSUE \
        "global_load_dwordx4 v[4:7], v15, %[qa] sc1\n\t" \
        "global_load_dwordx4 v[8:11], v15, %[qb] sc1\n\t" \
        AISSUE AISSUE \
        "s_mov_b32 s40, 0xffff\n\t" \
        "s_mov_b32 s41, 0xffff\n\t" \
        "s_mov_b32 s42, 8\n\t" \
        "s_waitcnt vmcnt(4)\n\t" \
        "s_barrier\n\t" \
        "ds_read_b128 v[104:107], v12\n\t" \
        "ds_read_b128 v[108:111], v12 offset:1024\n\t" \
        "ds_read_b128 v[112:115], v13\n\t" \
        "ds_read_b128 v[116:119], v13 offset:512\n\t" \
        PR2 \
        "s_waitcnt lgkmcnt(3)\n\t" \
        MF(R_SA, "v[104:107]", "v[4:7]", NM) \
        "s_nop 15\n\t" \
        "LA_loop%=:\n\t" \
        ATILE_E(NM, "2048", "v13", 1024, 1536, WL PR0) \
        ATILE_O(NM, "3072", "v13", 2048, 2560, WL PR1) \
        ATILE_E(NM, "0", "v13", 3072, 3584, \
                "s_waitcnt vmcnt(2) lgkmcnt(0)\n\t" \
                "s_barrier\n\t" \
                "s_setprio 3\n\t" \
                "v_add_u32 v12, 0x4000, v12\n\t" \
                AISSUE \
                "v_and_b32 v12, 0xffff, v12\n\t" \
                PR2) \
        ATILE_O(NM, "1024", "v13", 0, 512, \
                WL \
                "s_mov_b64 exec, s[40:41]\n\t" \
                "v_add_u32 v13, 0x4000, v13\n\t" \
                "s_sub_u32 s42, s42, 1\n\t" \
                "v_and_b32 v13, 0xffff, v13\n\t" \
                "s_mov_b64 exec, -1\n\t" \
                "s_cmp_lg_u32 s42, 0\n\t") \
        "s_cbranch_scc1 LA_loop%=\n\t" \
        "s_waitcnt lgkmcnt(0)\n\t" \
        "s_setprio 3\n\t" \
        MF(R_OB, "v[124:127]", "v[100:103]", R_OB) \
        "s_nop 15\n\t" \
        : "+{v[64:79]}"(R.oa), "+{v[80:95]}"(R.ob), "+{v[100:103]}"(R.p1), "+{v[124:127]}"(R.v1), "+{v12}"(R.ka), "+{v13}"(R.va) \
        : NMIN "{v14}"(R.go), [qa] "s"(qa), [qb] "s"(qb), [rk] "s"(rk), [rv] "s"(rv), [lw] "s"(w * 1024) \
        : "memory", "scc", "vcc", "s40", "s41", "s42", "s43", "s44", "s45", "v0", "v1", "v4", "v5", "v6", "v7", "v8", "v9", "v10", "v11", "v15", \
          "v32", "v33", "v34", "v35", "v36", "v37", "v38", "v39", "v40", "v41", "v42", "v43", "v44", "v45", "v46", "v47", \
          "v48", "v49", "v50", "v51", "v52", "v53", "v54", "v55", "v56", "v57", "v58", "v59", "v60", "v61", "v62", "v63", \
          "v96", "v97", "v98", "v99", "v104", "v105", "v106", "v107", "v108", "v109", "v110", "v111", \
          "v112", "v113", "v114", "v115", "v116", "v117", "v118", "v119", "v120", "v121", "v122", "v123")
DI void attn_pass_shift(AttnRegs& R, const char* qa, const char* qb, const char* ksrc, const char* vsrc, int w) {
    const u32x4 rk = srd(ksrc, 65536), rv = srd(vsrc, 65536);
#define NM_IN "{v[16:31]}"(R.nm),
    ATTN_ASM("v[16:31]", NM_IN);
}
#define FISS(S)                                                  \
    "s_add_u32 m0, %[lw], " #S "*16384\n\t"                      \
    "s_add_u32 s43, s43, 0x2000\n\t"                             \
    "buffer_load_dwordx4 v14, %[rk], s43 offen sc1 lds\n\t"      \
    "s_add_u32 m0, %[lw], " #S "*16384+8192\n\t"                 \
    "s_nop 0\n\t"                                                \
    "buffer_load_dwordx4 v14, %[rv], s43 offen sc1 lds\n\t"
#define FCHUNK(S, N, VC, VN, I0, BND, I3)                        \
    ATILE_E("0", #S "*16384+2048", VC, 1024, 1536, WL I0 PR0)    \
    ATILE_O("0", #S "*16384+3072", VC, 2048, 2560, WL PR1)       \
    ATILE_E("0", #N "*16384", VC, 3072, 3584, BND PR2)           \
    ATILE_O("0", #N "*16384+1024", VN, 0, 512, WL I3)
#define FLD "global_load_dwordx2 v[0:1], v2, %[ff] sc1\n\t"
#define FBAR(VM) "s_waitcnt vmcnt(" #VM ") lgkmcnt(0)\n\ts_barrier\n\t"
DI void attn_pass(AttnRegs& R, const char* qa, const char* qb, const char* ksrc, const char* vsrc, const char* late_flags, u64 tag, int w) {
    const u32x4 rk = srd(ksrc, 65536), rv = srd(vsrc, 65536);
    asm volatile(
        "v_and_b32 v15, 0x3ff, v14\n\t"
        "s_mov_b32 s43, -0x2000\n\t"
        FISS(0)
        "global_load_dwordx4 v[4:7], v15, %[qa] sc1\n\t"
        "global_load_dwordx4 v[8:11], v15, %[qb] sc1\n\t"
        FISS(1) FISS(2)
        "s_waitcnt vmcnt(4)\n\t"
        "s_barrier\n\t"
        "ds_read_b128 v[104:107], v12\n\t"
        "ds_read_b128 v[108:111], v12 offset:1024\n\t"
        "ds_read_b128 v[112:115], v13\n\t"
        "ds_read_b128 v[116:119], v13 offset:512\n\t"
        PR2
        "s_waitcnt lgkmcnt(3)\n\t"
        MF(R_SA, "v[104:107]", "v[4:7]", "0")
        "s_nop 15\n\t"
        FCHUNK(0, 1, "v13", "v16", "", FBAR(2), PRI FISS(3) PR2)
        FCHUNK(1, 2, "v16", "v17", "", FBAR(2), "")
        FCHUNK(2, 3, "v17", "v18", PRI FLD,
               "s_waitcnt vmcnt(0) lgkmcnt(0)\n\t"
               "s_mov_b32 s45, 0x100000\n\t"
               "LA_p%=:\n\t"
               "v_cmp_ne_u64 vcc, %[tag], v[0:1]\n\t"
               "s_sub_u32 s45, s45, 1\n\t"
               "s_cbranch_scc1 LA_f%=\n\t"
               "s_cbranch_vccz LA_f%=\n\t"
               "global_load_dwordx2 v[0:1], v2, %[ff] sc1\n\t"
               "s_waitcnt vmcnt(0)\n\t"
               "s_branch LA_p%=\n\t"
               "LA_f%=:\n\t"
               "s_barrier\n\t", PRI FISS(0) FISS(1) PR2)
        FCHUNK(3, 0, "v18", "v13", "", FBAR(2), PRI FISS(2) PR2)
        FCHUNK(0, 1, "v13", "v16", "", FBAR(2), PRI FISS(3) PR2)
        FCHUNK(1, 2, "v16", "v17", "", FBAR(2), "")
        FCHUNK(2, 3, "v17", "v18", "", FBAR(0), "")
        FCHUNK(3, 0, "v18", "v13", "", WL, "")
        "s_waitcnt lgkmcnt(0)\n\t"
        "s_setprio 3\n\t"
        MF(R_OB, "v[124:127]", "v[100:103]", R_OB)
        "s_nop 15\n\t"
        "Lmk_hot_end:\n\t"
        : "+{v[64:79]}"(R.oa), "+{v[80:95]}"(R.ob), "+{v[100:103]}"(R.p1), "+{v[124:127]}"(R.v1), "+{v[20:23]}"(R.fin)
        : "{v12}"(R.ka), "{v13}"(R.va), "{v16}"(R.va1), "{v17}"(R.va2), "{v18}"(R.va3), "{v14}"(R.go), "{v2}"(R.fo),
          [qa] "s"(qa), [qb] "s"(qb), [rk] "s"(rk), [rv] "s"(rv), [lw] "s"(w * 1024), [tag] "s"(tag), [ff] "s"(late_flags)
        : "memory", "scc", "vcc", "s43", "s45", "v0", "v1", "v4", "v5", "v6", "v7", "v8", "v9", "v10", "v11", "v15",
          "v32", "v33", "v34", "v35", "v36", "v37", "v38", "v39", "v40", "v41", "v42", "v43", "v44", "v45", "v46", "v47",
          "v48", "v49", "v50", "v51", "v52", "v53", "v54", "v55", "v56", "v57", "v58", "v59", "v60", "v61", "v62", "v63",
          "v96", "v97", "v98", "v99", "v104", "v105", "v106", "v107", "v108", "v109", "v110", "v111",
          "v112", "v113", "v114", "v115", "v116", "v117", "v118", "v119", "v120", "v121", "v122", "v123");
}

DI void wait_qkv(const char* ws, int a, u64 tag) { wait_flags<2>(ws + WS_FQ, qkv_dep(a, true), tag); }
DI void attn_role(const char* x, const char* wqkv, char* ws, char* lds, int a, int w) {
    const int kh = w >> 2;
    const int head = 4 * (a & 7) + ((a >> 3) & 3), qt = 8 * (a >> 5) + 2 * (w & 3);
    __builtin_amdgcn_s_setprio(3);
    u64 tag;
    const int ti = a >> 3, trow = 8 * (a & 1) + (ti & 7) + (ti >= 24 ? 16 : 0), tcol = (ti >= 24 ? 0 : 4 * (ti >> 3)) + ((a & 7) >> 1);
    if (w >= 4) {
        qkv_loader(x, wqkv, trow, tcol, w - 4);
        tag = uni64(*(LAS const u64*)(QCNT + 8));
        if (w == 4) wait_qkv(ws, a, tag);
    } else {
        tag = next_tag(ld_epoch(ws));
        if (tcol < 8) qkv_body<true>(ws, lds, trow, tcol, a, w, tag);
        else qkv_body<false>(ws, lds, trow, tcol, a, w, tag);
    }
    AttnRegs R;
    attn_prep(R, w);
    {
        const int lane = lane_id();
        const unsigned st = (lane & 16) ? 0u : 16384u;
        R.va1 = R.va + st; R.va2 = R.va1 + st; R.va3 = R.va2 + st;
        R.fo = qkv_dep(a, false);
        const int o = w * 512 + lane * 8;
        const unsigned one = ((o & 0x1b0) == 0) ? 0x3f803f80u : 0u;
        *(LAS u32x2*)((LAS char*)lds + A_ONES + o) = u32x2{one, one};
        R.fin = u32x4{(unsigned)(w * 2048 + lane * 16), (unsigned)((w ^ 4) * 2048 + lane * 16), (unsigned)(A_LSUM + w * 256 + lane * 4), (unsigned)(A_LSUM + (w ^ 4) * 256 + lane * 4)};
        asm volatile("" : "+{v16}"(R.va1), "+{v17}"(R.va2), "+{v18}"(R.va3), "+{v2}"(R.fo), "+{v[20:23]}"(R.fin));
    }
    wgbar();
    const char* ksrc = ws + WS_K + (size_t)head * 65536;
    const char* qsrc = ws + WS_Q + (size_t)(head * 64 + qt) * 1024;
    auto finish = [&](int att) {
        const f32x16 oa = R.oa, ob = R.ob;
        u32x4 fin = R.fin;
        if (att) {
            const int lane = lane_id();
            fin = u32x4{(unsigned)(w * 2048 + lane * 16), (unsigned)((w ^ 4) * 2048 + lane * 16), (unsigned)(A_LSUM + w * 256 + lane * 4), (unsigned)(A_LSUM + (w ^ 4) * 256 + lane * 4)};
        }
        LAS char* ex = (LAS char*)(uintptr_t)fin[0];
        LAS const char* exr = (LAS const char*)(uintptr_t)fin[1];
        *(LAS f32x4*)ex = f32x4{ob[0], ob[1], ob[2], ob[3]};
        *(LAS f32x4*)(ex + 1024) = f32x4{ob[4], ob[5], ob[6], ob[7]};
        *(LAS float*)(uintptr_t)fin[2] = ob[8];
        if (att == 0) {
            float big = fmaxf(oa[8], ob[8]) * 2.7e8f;
#pragma unroll
            for (int i = 0; i < 8; ++i) big = fmaxf(fmaxf(big, __builtin_fabsf(oa[i])), __builtin_fabsf(ob[i]));
            const bool bad = !__all(fminf(oa[8], ob[8]) > 9.1e-13f && big < 1.7e38f);
            ((LAS unsigned*)((LAS char*)lds + A_VOTE))[w] = bad;
        }
        wgbar();
        const f32x4 t0 = *(LAS const f32x4*)exr, t1 = *(LAS const f32x4*)(exr + 1024);
        const float t2 = *(LAS const float*)(uintptr_t)fin[3];
        unsigned redo = 0;
        if (att == 0) {
            const u32x4 v0 = *(LAS const u32x4*)((LAS char*)lds + A_VOTE), v1 = *(LAS const u32x4*)((LAS char*)lds + A_VOTE + 16);
            redo = __builtin_amdgcn_readfirstlane(v0[0] | v0[1] | v0[2] | v0[3] | v1[0] | v1[1] | v1[2] | v1[3]);
        }
        const float rinv = __builtin_amdgcn_rcpf(oa[8] + t2);
        u32x4 ov;
        ov[0] = cvtpk((oa[0] + t0[0]) * rinv, (oa[1] + t0[1]) * rinv);
        ov[1] = cvtpk((oa[2] + t0[2]) * rinv, (oa[3] + t0[3]) * rinv);
        ov[2] = cvtpk((oa[4] + t1[0]) * rinv, (oa[5] + t1[1]) * rinv);
        ov[3] = cvtpk((oa[6] + t1[2]) * rinv, (oa[7] + t1[3]) * rinv);
        if (att == 0 ? !redo : att == 1 + kh) {
            st16(ws + WS_O + (size_t)((qt + kh) * 32 + head) * 1024, fin[0] & 1023, ov);
            drain();
            st_flag(ws + WS_FO + ((qt + kh) * 32 + head) * 128, tag);
        }
        asm volatile("" : "+s"(redo));
        return redo;
    };
    attn_pass(R, qsrc + kh * 1024, qsrc + (1 - kh) * 1024, ksrc, ksrc + (WS_V - WS_K), ws + WS_FQ + 32768, tag, w);
    if (finish(0)) {
#pragma clang loop unroll(disable)
        for (int att = 1; att < 3; ++att) {
            drain();
            wgbar();
            const int lane = lane_id();
            const char* qa = qsrc + (att - 1) * 1024;
            bf16x8 qf;
            asm volatile("global_load_dwordx4 %0, %1, %2 sc1\n\ts_waitcnt vmcnt(0)" : "=v"(qf) : "v"(lane * 16), "s"(qa) : "memory");
            float mx = -INFINITY;
#pragma clang loop unroll(disable)
            for (int t = 0; t < 64; ++t) {
                bf16x8 kf;
                asm volatile("global_load_dwordx4 %0, %1, %2 sc1\n\ts_waitcnt vmcnt(0)" : "=v"(kf) : "v"(lane * 16), "s"(ksrc + t * 1024) : "memory");
                const f32x16 s = MFMA32(kf, qf, f32x16{});
#pragma unroll
                for (int i = 0; i < 16; ++i) mx = fmaxf(mx, s[i]);
            }
            mx = -swap32_max(mx);
            attn_prep(R, w);
#pragma unroll
            for (int i = 0; i < 16; ++i) R.nm[i] = mx;
            attn_pass_shift(R, qa, qa, ksrc, ksrc + (WS_V - WS_K), w);
            finish(att);
        }
    }
}

#define BGAP "s_sleep 1\n\t"
#define SISSUE                                                             \
    "s_mov_b32 m0, s44\n\t"                                                 \
    "s_add_u32 s45, %[ow], %[wst]\n\t"                                      \
    "buffer_load_dwordx4 %[g0], %[rw], %[ow] offen lds\n\t"                 \
    "buffer_load_dwordx4 %[g1], %[rw], %[ow] offen offset:1024 lds\n\t"     \
    "s_add_u32 m0, s44, 8192\n\t"                                           \
    "s_add_u32 s46, %[od], 4096\n\t"                                        \
    "buffer_load_dwordx4 %[d0], %[rd], %[od] offen sc1 lds\n\t"             \
    "buffer_load_dwordx4 %[d0], %[rd], %[od] offen offset:1024 sc1 lds\n\t" \
    "s_add_u32 s44, s44, 0x4000\n\t"                                        \
    "s_and_b32 s44, s44, 0xffff\n\t"                                        \
    "s_cmp_eq_u32 %[od], %[de]\n\t"                                         \
    "s_cselect_b32 %[ow], 0, s45\n\t"                                       \
    "s_cselect_b32 %[od], 0, s46\n\t"
#define SISW                                                               \
    "s_mov_b32 m0, s44\n\t"                                                 \
    "s_add_u32 s45, %[ow], %[wst]\n\t"                                      \
    "buffer_load_dwordx4 %[g0], %[rw], %[ow] offen lds\n\t"                 \
    "buffer_load_dwordx4 %[g1], %[rw], %[ow] offen offset:1024 lds\n\t"     \
    "s_add_u32 s44, s44, 0x4000\n\t"                                        \
    "s_mov_b32 %[ow], s45\n\t"
#define SISD                                                               \
    "s_add_u32 m0, s44, 8192\n\t"                                           \
    "s_add_u32 s44, s44, 0x4000\n\t"                                        \
    "buffer_load_dwordx4 %[d0], %[rd], %[od] offen sc1 lds\n\t"             \
    "buffer_load_dwordx4 %[d0], %[rd], %[od] offen offset:1024 sc1 lds\n\t" \
    "s_add_u32 %[od], %[od], 4096\n\t"
struct SG { u32x4 rw, rd; unsigned wst, de, ow, od, a, b, c; };
DI void sgemm_prew(SG& G, int lw) {
    asm volatile("s_mov_b32 s44, %[lo]\n\t" SISW SISW SISW
                 : [ow] "+s"(G.ow) : [wst] "s"(G.wst), [rw] "s"(G.rw), [lo] "s"(lw * 2048), [g0] "v"(G.a), [g1] "v"(G.b) : "memory", "scc", "s44", "s45");
}
#define WISSUE                                                              \
    "s_and_b32 s45, s44, 0xc000\n\t"                                        \
    "s_add_u32 m0, s45, %[lo2]\n\t"                                         \
    "s_lshl_b32 s45, s45, 3\n\t"                                            \
    "buffer_load_dwordx4 %[o0], %[ro], s45 offen lds\n\t"                   \
    "buffer_load_dwordx4 %[o1], %[ro], s45 offen offset:1024 lds\n\t"       \
    "buffer_load_dwordx4 %[o2], %[ro], s45 offen offset:2048 lds\n\t"       \
    "buffer_load_dwordx4 %[o3], %[ro], s45 offen offset:3072 lds\n\t"       \
    "s_add_u32 s44, s44, 0x4000\n\t"                                        \
    "s_and_b32 s44, s44, 0xffff\n\t"
template <int WO>
DI void sgemm_load(SG& G, int n, int lw, u32x4 ro, unsigned o0) {
    asm volatile(
        "s_mov_b32 s44, %[lo]\n\t"
        ".if %c[wo]\n\t"
        "s_sub_u32 s47, %[n], 5\n\t"
        SISD SISD SISD
        "s_waitcnt vmcnt(4)\n\ts_barrier\n\t" SISSUE
        "s_waitcnt vmcnt(6)\n\ts_barrier\n\t" SISSUE
        ".else\n\t"
        "s_sub_u32 s47, %[n], 3\n\t"
        SISSUE SISSUE SISSUE
        ".endif\n\t"
        "LL_loop%=:\n\t"
        "s_waitcnt vmcnt(8)\n\t"
        "s_barrier\n\t"
        SISSUE
        "s_sub_u32 s47, s47, 1\n\t"
        "s_cmp_lg_u32 s47, 0\n\t"
        "s_cbranch_scc1 LL_loop%=\n\t"
        ".if %c[wo]\n\t"
        "s_waitcnt vmcnt(8)\n\ts_barrier\n\t" WISSUE
        "s_waitcnt vmcnt(8)\n\ts_barrier\n\t" WISSUE
        "s_waitcnt vmcnt(8)\n\ts_barrier\n\t" WISSUE
        "s_barrier\n\t" WISSUE
        "s_barrier\n\t"
        ".else\n\t"
        "s_waitcnt vmcnt(8)\n\ts_barrier\n\t"
        "s_waitcnt vmcnt(4)\n\ts_barrier\n\t"
        "s_waitcnt vmcnt(0)\n\ts_barrier\n\t"
        "s_barrier\n\t"
        ".endif\n\t"
        : [ow] "+s"(G.ow), [od] "+s"(G.od)
        : [wst] "s"(G.wst), [rw] "s"(G.rw), [rd] "s"(G.rd), [lo] "s"(lw * 2048), [de] "s"(G.de), [n] "s"(n), [g0] "v"(G.a), [g1] "v"(G.b), [d0] "v"(G.c),
          [wo] "i"(WO), [ro] "s"(ro), [lo2] "s"(lw * 4096), [o0] "v"(o0), [o1] "v"(o0 + 7168), [o2] "v"(o0 + 14336), [o3] "v"(o0 + 21504)
        : "memory", "scc", "s44", "s45", "s46", "s47");
}
#define SRD3(B, SLOT, K, TD)                                                                         \
    "ds_read_b64_tr_b16 v[" #B ":" #B "+1], %[t] offset:" #SLOT "*16384+" #K "*2048\n\t"              \
    "ds_read_b64_tr_b16 v[" #B "+2:" #B "+3], %[t] offset:" #SLOT "*16384+" #K "*2048+" #TD "\n\t"    \
    "ds_read_b128 v[" #B "+4:" #B "+7], %[dl] offset:" #SLOT "*16384+" #K "*1024\n\t"
#define SSTAGE(SLOT, MM, TD, N0, N1, N2, N3, P0, P1, P2, P3)                          \
    "s_waitcnt lgkmcnt(0)\n\t"                                                        \
    "s_barrier\n\t"                                                                   \
    MM(P0) SRD3(N0, SLOT, 0, TD) SRD3(N1, SLOT, 1, TD)                             \
    MM(P1) SRD3(N2, SLOT, 2, TD) SRD3(N3, SLOT, 3, TD) BGAP                            \
    MM(P2) BGAP MM(P3)
#define SST_E(SLOT, MM, TD) SSTAGE(SLOT, MM, TD, 32, 40, 48, 56, 96, 104, 112, 120)
#define SST_O(SLOT, MM, TD) SSTAGE(SLOT, MM, TD, 96, 104, 112, 120, 32, 40, 48, 56)
#define SMM_WA(P) MF("v[64:79]", "v[" #P ":" #P "+3]", "v[" #P "+4:" #P "+7]", "v[64:79]")
#define SMM_WB(P) MF("v[80:95]", "v[" #P "+4:" #P "+7]", "v[" #P ":" #P "+3]", "v[80:95]")
#define SGEMM_ASM(ACC, MM, TD, NLOOP, TAIL, FIN)                                                                       \
    asm volatile(                                                                                                 \
        "s_mov_b32 s44, " #NLOOP "\n\ts_setprio 0\n\t"                                                                       \
        "s_barrier\n\t"                                                                                           \
        SRD3(32, 0, 0, TD) SRD3(40, 0, 1, TD) SRD3(48, 0, 2, TD) SRD3(56, 0, 3, TD)                               \
        "LS_loop%=:\n\t"                                                                                          \
        SST_O(1, MM, TD) SST_E(2, MM, TD) SST_O(3, MM, TD) SST_E(0, MM, TD)                                       \
        "s_sub_u32 s44, s44, 1\n\t"                                                                               \
        "s_cmp_lg_u32 s44, 0\n\t"                                                                                 \
        "s_cbranch_scc1 LS_loop%=\n\t"                                                                            \
        TAIL                                                                                                      \
        "s_waitcnt lgkmcnt(0)\n\t" FIN                                                                            \
        MM(96) MM(104) MM(112) MM(120)                                                                \
        "s_setprio 3\n\ts_nop 15\n\t"                                                                                            \
        : ACC(acc)                                                                                                \
        : [t] "v"(G.a), [dl] "v"(G.b)                                                                             \
        : "memory", "scc", "s44",                                                                                 \
          "v32", "v33", "v34", "v35", "v36", "v37", "v38", "v39", "v40", "v41", "v42", "v43", "v44", "v45", "v46", "v47", \
          "v48", "v49", "v50", "v51", "v52", "v53", "v54", "v55", "v56", "v57", "v58", "v59", "v60", "v61", "v62", "v63", \
          "v96", "v97", "v98", "v99", "v100", "v101", "v102", "v103", "v104", "v105", "v106", "v107", "v108", "v109", "v110", "v111", \
          "v112", "v113", "v114", "v115", "v116", "v117", "v118", "v119", "v120", "v121", "v122", "v123", "v124", "v125", "v126", "v127")

template <int MODE>
DI SG sgemm_setup(const char* wbase, unsigned wbytes, int wrow, const char* dbase, int dblk, int rot, int w) {
    const int lane = lane_id(), row = lane >> 3, lw = w & 3;
    const int q = (lane & 15) >> 2, h2 = lane >> 5;
    SG G;
    G.rw = srd(wbase, wbytes); G.rd = srd(dbase, 2 * dblk);
    G.wst = 64 * wrow; G.de = (MODE == 0 ? 31 : 21) * 4096; G.ow = rot * G.wst; G.od = rot * 4096;
    if (w >= 4) {
        G.a = (16 * lw + row) * wrow + (((lane & 7) ^ (((row >> 1) & 1) << 2)) << 4);
        G.b = G.a + 8 * wrow - 1024;
        G.c = (lw >> 1) * dblk + (lw & 1) * 2048 + lane * 16;
    } else {
        G.a = ((MODE == 0 ? 8 : 4) * h2 + q) * 128 + (((lw & 1) ^ ((q >> 1) & 1)) << 6) + ((lane >> 4) & 1) * 32 + (lane & 3) * 8;
        G.b = 8192 + (lw >> 1) * 4096 + lane * 16;
        G.c = 0;
    }
    asm volatile("" : "+v"(G.a), "+v"(G.b), "+v"(G.c));
    return G;
}
#define ACC0 "+{v[64:79]}"
#define ACC1 "+{v[80:95]}"
template <int MODE>
DI void sgemm_run(f32x16& acc, SG& G) {
    if (MODE == 0) SGEMM_ASM(ACC0, SMM_WA, 512, 7, SST_O(1, SMM_WA, 512) SST_E(2, SMM_WA, 512) SST_O(3, SMM_WA, 512), "s_setprio 3\n\t");
    else SGEMM_ASM(ACC1, SMM_WB, 1024, 5, SST_O(1, SMM_WB, 1024), "s_barrier\n\ts_setprio 3\n\t");
}
DI f32x4 sub4(const f32x16& a, int g) { return f32x4{a[4 * g], a[4 * g + 1], a[4 * g + 2], a[4 * g + 3]}; }

DI unsigned gelu2(float a, float b) {
    constexpr float c1 = -2.f * 0.7978845608028654f * 1.4426950408889634f, c2 = c1 * 0.044715f;
    const f32x2 u = {a, b};
    const f32x2 z = u * (u * u * c2 + c1);
    const f32x2 d = f32x2{__builtin_amdgcn_exp2f(z[0]), __builtin_amdgcn_exp2f(z[1])} + 1.f;
    const f32x2 o = u * f32x2{__builtin_amdgcn_rcpf(d[0]), __builtin_amdgcn_rcpf(d[1])};
    return cvtpk(o[0], o[1]);
}

#define WRD(R0, R1, R2, R3, STEP)                                                    \
    "ds_read_b64_tr_b16 " R0 ", %[t0] offset:" #STEP "*2048\n\t"                      \
    "ds_read_b64_tr_b16 " R1 ", %[t0] offset:" #STEP "*2048+1024\n\t"                 \
    "ds_read_b64_tr_b16 " R2 ", %[t1] offset:" #STEP "*2048\n\t"                      \
    "ds_read_b64_tr_b16 " R3 ", %[t1] offset:" #STEP "*2048+1024\n\t"
#define WRD0(S) WRD("v[32:33]", "v[34:35]", "v[36:37]", "v[38:39]", S)
#define WRD1(S) WRD("v[40:41]", "v[42:43]", "v[44:45]", "v[46:47]", S)
#define WRD2(S) WRD("v[48:49]", "v[50:51]", "v[52:53]", "v[54:55]", S)
#define WRD3(S) WRD("v[56:57]", "v[58:59]", "v[60:61]", "v[62:63]", S)
#define WMM(WAIT, W0, W1, O) "s_waitcnt " WAIT "\n\t" MF("v[96:111]", W0, O, "v[96:111]") MF("v[112:127]", W1, O, "v[112:127]")
#define OLD(K, R, V, OFF)                                             \
    "5:\n\t"                                                           \
    "s_bitcmp1_b32 s40, " #K "\n\t"                                    \
    "s_cbranch_scc1 6f\n\t"                                            \
    "global_load_dwordx2 v[24:25], %[fo], %[f] sc1\n\t"                       \
    "s_sub_u32 s41, s41, 1\n\t"                                        \
    "s_cselect_b32 s42, -1, 0\n\t"                                     \
    "s_waitcnt vmcnt(0)\n\t"                                           \
    "v_cmp_eq_u64 vcc, %[tag], v[24:25]\n\t"                                \
    "s_or_b32 s40, vcc_lo, s42\n\t"                                    \
    "s_bitcmp1_b32 s40, " #K "\n\t"                                    \
    "s_cbranch_scc1 6f\n\t"                                            \
    "s_sleep 1\n\t"                                                    \
    "s_branch 5b\n\t"                                                  \
    "6:\n\t"                                                           \
    "global_load_dwordx4 " R ", " V ", %[o] offset:" #OFF " sc1\n\t"
DI void wo_gemm(f32x16& a0, f32x16& a1, f32x4& k0, f32x4& k1, float& k2, u32x4& k3, const char* osrc, const char* fsrc, unsigned seen, u64 tag,
                unsigned t0, unsigned t1, unsigned d0, unsigned d1, unsigned fo) {
    asm volatile(
        WRD0(0) WRD1(1) WRD2(2) WRD3(3)
        "s_mov_b32 s40, %[seen]\n\t"
        "s_mov_b32 s41, 0x100000\n\t"
        "s_setprio 0\n\t"
        OLD(0, "v[64:67]", "%[d0]", 0) OLD(1, "v[68:71]", "%[d0]", 1024) OLD(2, "v[72:75]", "%[d0]", 2048) OLD(3, "v[76:79]", "%[d0]", 3072)
        OLD(4, "v[80:83]", "%[d1]", 0) OLD(5, "v[84:87]", "%[d1]", 1024) OLD(6, "v[88:91]", "%[d1]", 2048) OLD(7, "v[92:95]", "%[d1]", 3072)
        "s_setprio 3\n\t"
        "s_waitcnt vmcnt(7) lgkmcnt(0)\n\t"
        MF("v[96:111]", "v[32:35]", "v[64:67]", "0") MF("v[112:127]", "v[36:39]", "v[64:67]", "0")
        WRD0(4)
        WMM("vmcnt(6)", "v[40:43]", "v[44:47]", "v[68:71]") WRD1(5)
        WMM("vmcnt(5)", "v[48:51]", "v[52:55]", "v[72:75]") WRD2(6)
        WMM("vmcnt(4)", "v[56:59]", "v[60:63]", "v[76:79]") WRD3(7)
        WMM("vmcnt(3) lgkmcnt(12)", "v[32:35]", "v[36:39]", "v[80:83]")
        WMM("vmcnt(2) lgkmcnt(0)", "v[40:43]", "v[44:47]", "v[84:87]")
        WMM("vmcnt(1)", "v[48:51]", "v[52:55]", "v[88:91]")
        "s_barrier\n\t"
        WMM("vmcnt(0)", "v[56:59]", "v[60:63]", "v[92:95]")
        "s_nop 15\n\t"
        : "={v[96:111]}"(a0), "={v[112:127]}"(a1),
          "+{v[12:15]}"(k0), "+{v[16:19]}"(k1), "+{v20}"(k2), "+{v[28:31]}"(k3)
        : [o] "s"(osrc), [f] "s"(fsrc), [tag] "s"(tag), [seen] "s"(seen), [d0] "v"(d0), [d1] "v"(d1), [t0] "v"(t0), [t1] "v"(t1), [fo] "v"(fo)
        : "memory", "scc", "vcc", "s40", "s41", "s42", "v24", "v25",
          "v32", "v33", "v34", "v35", "v36", "v37", "v38", "v39", "v40", "v41", "v42", "v43", "v44", "v45", "v46", "v47",
          "v48", "v49", "v50", "v51", "v52", "v53", "v54", "v55", "v56", "v57", "v58", "v59", "v60", "v61", "v62", "v63",
          "v64", "v65", "v66", "v67", "v68", "v69", "v70", "v71", "v72", "v73", "v74", "v75", "v76", "v77", "v78", "v79",
          "v80", "v81", "v82", "v83", "v84", "v85", "v86", "v87", "v88", "v89", "v90", "v91", "v92", "v93", "v94", "v95");
}

DI void mixer_role(const char* x, const char* wqkv, const char* gw, const char* wo, const char* w1, const char* b1, const char* w2, const char* b2,
                   char* ws, char* out, char* lds, int bi, int w) {
    const int j = bi & 7, i = bi >> 3;
    __builtin_amdgcn_s_setprio(0);
    const u64 ep = ld_epoch(ws);
    u64 tag;
    {
        const int lane = lane_id(), h2 = lane >> 5, tid = 64 * w + lane;
        const u32x4 xr = *(GAS const u32x4*)(x + (size_t)(8 * bi + w) * 1024 + lane * 16);
        const u32x4 gr = *(GAS const u32x4*)(gw + lane * 16);
        tag = next_tag(ep);
        float v[8], gg[8];
#pragma unroll
        for (int k = 0; k < 4; ++k) { v[2 * k] = bflo(xr[k]); v[2 * k + 1] = bfhi(xr[k]); gg[2 * k] = bflo(gr[k]); gg[2 * k + 1] = bfhi(gr[k]); }
        float s1 = 0.f, s3 = 0.f;
#pragma unroll
        for (int k = 0; k < 8; ++k) { s1 += v[k]; s3 += v[k] * gg[k]; }
        const float mu = wave_sum(s1) * (1.f / 512.f);
        s3 = wave_sum(s3);
        float s2 = 0.f;
#pragma unroll
        for (int k = 0; k < 8; ++k) { v[k] -= mu; s2 += v[k] * v[k]; }
        const float rs = __builtin_amdgcn_rsqf(wave_sum(s2) * (1.f / 512.f) + 1e-5f);
        u32x4 hv;
#pragma unroll
        for (int k = 0; k < 4; ++k) hv[k] = cvtpk(v[2 * k] * rs, v[2 * k + 1] * rs);
        *(LAS u32x4*)((LAS char*)lds + w * 1024 + lane * 16) = hv;
        const float g = __builtin_amdgcn_rcpf(1.f + __builtin_amdgcn_exp2f(-1.4426950408889634f * s3));
        st4(ws + WS_G + (8 * bi + w) * 4, uni(g));
        wgbar();
        LAS const unsigned short* lb = (LAS const unsigned short*)lds + tid;
#pragma unroll
        for (int k = 0; k < 4; ++k) hv[k] = (unsigned)lb[(2 * k) * 512] | ((unsigned)lb[(2 * k + 1) * 512] << 16);
        st16(ws + WS_H + (size_t)(2 * w * 128 + (bi >> 1)) * 1024 + (bi & 1) * 512, h2 * 131072 + (lane & 31) * 16, hv);
        drain();
        wgbar();
        if (w == 0) st_flag(ws + WS_FL + bi * 128, tag);
    }
    __builtin_amdgcn_s_setprio(3);
    const int wb = w & 1, db = (w >> 1) & 1, hf = w >> 2;
    const int L = (j >> 1) * 22 + i, ft = L >> 2, dt1 = 4 * (j & 1) + (L & 3);
    const int st = 8 * (j >> 1) + (i & 7), dt = 4 * (j & 1) + (i >> 3);
    SG G1, G2;
    unsigned ex, l16, og, wof;
    {
        const int lane = lane_id();
        og = (64 * st + 32 * wb + (lane & 31)) * 4;
        G1 = sgemm_setup<0>(w1 + ft * 128, 2048 * 2752 - ft * 128, 2752, ws + WS_H + (size_t)2 * dt1 * 131072, 131072, (4 * dt1 + 10 * ft) & 31, w);
        G2 = sgemm_setup<1>(w2 + st * 128, 1376 * 4096 - st * 128, 4096, ws + WS_U + (size_t)2 * dt * 86 * 1024, 86 * 1024, ((st + dt) & 1) * 11, w);
        l16 = lane * 16;
        ex = 65536 + (w & 3) * 2048 + l16;
        wof = (32 * (w & 3) + (lane >> 3)) * 1024 + (((lane & 7) ^ (((lane >> 4) & 1) << 2)) << 4);
        asm volatile("" : "+v"(ex), "+v"(l16), "+v"(og), "+v"(wof));
    }
    {
        const int row = 16 + 8 * (j & 1) + (i & 7), col = 8 + 8 * (i >> 4) + 2 * (j >> 1) + ((i >> 3) & 1);
        if (w == 0) {
            *(LAS unsigned*)QCNT = 0u;
            wait_flags<2>(ws + WS_FQ + bi * 128, 0, tag);
        }
        wgbar();
        if (w >= 4) qkv2_loader(x, wqkv, row, col, w - 4);
        else qkv2_body(ws, row, col, bi, w, tag);
    }
    if (i >= 22) {
        const int wv = ((i - 22) * 8 + j) * 8 + w;
        __builtin_amdgcn_s_sleep(64);
        __builtin_amdgcn_s_sleep(64);
        warm(w2, 1376, wv, l16 * 4);
        warm(wo, 128, wv, l16 * 4);
    }
    if (i < 22) {
        if (w >= 4) wait_flags<8>(ws + WS_FL + (w - 4) * 8192, l16 * 8, tag);
        wgbar();
        f32x4 v0, v1;
        if (w >= 4) {
            sgemm_load<0>(G1, 32, w - 4, G1.rw, 0);
            v0 = *(LAS const f32x4*)(uintptr_t)ex; v1 = *(LAS const f32x4*)(uintptr_t)(ex + 1024);
        } else {
            f32x16 acc1;
            const char* bsrc = b1 + (64 * ft + (ft < 21 ? 32 * wb : 0)) * 2 + (l16 >> 9) * 8;
#pragma unroll
            for (int k = 0; k < 4; ++k) {
                const u32x2 bv = *(GAS const u32x2*)(bsrc + 16 * k);
                acc1[4 * k] = bflo(bv[0]); acc1[4 * k + 1] = bfhi(bv[0]); acc1[4 * k + 2] = bflo(bv[1]); acc1[4 * k + 3] = bfhi(bv[1]);
            }
            sgemm_run<0>(acc1, G1);
            *(LAS f32x4*)(uintptr_t)ex = sub4(acc1, 2); *(LAS f32x4*)(uintptr_t)(ex + 1024) = sub4(acc1, 3);
            wgbar();
            v0 = sub4(acc1, 0); v1 = sub4(acc1, 1);
        }
        const u32x4 o = {gelu2(v0[0], v0[1]), gelu2(v0[2], v0[3]), gelu2(v1[0], v1[1]), gelu2(v1[2], v1[3])};
        if (ft < 21 || wb == 0) st16(ws + WS_U + (size_t)((2 * dt1 + db) * 86 + 4 * ft + 2 * wb + hf) * 1024, l16, o);
        drain();
        wgbar();
        if (w == 0) st_flag(ws + WS_FU + (dt1 * 32 + ft) * 128, tag);
    }
    if (w >= 4) sgemm_prew(G2, w - 4);
    unsigned t0, t1, d1, fo;
    u32x4 fin;
    {
        const int lane = l16 >> 4, q = (lane & 15) >> 2, kq = 2 * db + hf;
        const unsigned tb = (w >> 1) * 16384 + (4 * (lane >> 5) + q) * 128 + ((lane >> 4) & 1) * 32 + (lane & 3) * 8;
        t0 = tb + ((q >> 1) & 1) * 64; t1 = tb + (((q >> 1) & 1) ^ 1) * 64; d1 = l16 + 4096; fo = (lane & 7) * 128;
        fin = u32x4{w * 8192 + l16, wb * 8192 + kq * 2048 + l16, (unsigned)((64 * st + 32 * wb + (lane & 31)) * 1024 + 128 * dt + kq * 32 + (lane >> 5) * 16), 0u};
        asm volatile("" : "+v"(t0), "+v"(t1), "+v"(d1), "+v"(fo), "+{v[28:31]}"(fin));
    }
    if (bi == 255 && w == 1) {
        wait_flags256(ws + WS_FQ, l16 * 8, tag);
        wait_flags256(ws + WS_FL, l16 * 8, tag);
        st_flag(ws + WS_EPOCH, tag);
    }
    if (w == 0) wait_flags<8>(ws + WS_FU + dt * 4096, (l16 < 22 * 16 ? l16 : 0) * 8, tag);
    wgbar();
    const char* fsrc = ws + WS_FO + ((2 * st + wb) * 32 + 8 * (w >> 1)) * 128;
    u64 f0;
    f32x4 bp0, bp1;
    float g;
    {
        asm volatile("global_load_dword %0, %1, %2 sc1" : "={v20}"(g) : "v"(og), "s"(ws + WS_G) : "memory");
        if (w >= 4) {
            sgemm_load<1>(G2, 22, w - 4, srd(wo + dt * 128, 524288 - dt * 128), wof);
            asm volatile("global_load_dwordx2 %0, %1, %2 sc1" : "={v[22:23]}"(f0) : "v"(fo), "s"(fsrc) : "memory");
            bp0 = *(LAS const f32x4*)(uintptr_t)ex; bp1 = *(LAS const f32x4*)(uintptr_t)(ex + 1024);
        } else {
            const float bb = bflo(*(GAS const unsigned short*)(b2 + (og >> 1)));
            f32x16 acc2 = {bb, bb, bb, bb, bb, bb, bb, bb, bb, bb, bb, bb, bb, bb, bb, bb};
            sgemm_run<1>(acc2, G2);
            asm volatile("global_load_dwordx2 %0, %1, %2 sc1" : "={v[22:23]}"(f0) : "v"(fo), "s"(fsrc) : "memory");
            *(LAS f32x4*)(uintptr_t)ex = sub4(acc2, 2); *(LAS f32x4*)(uintptr_t)(ex + 1024) = sub4(acc2, 3);
            wgbar();
            bp0 = sub4(acc2, 0); bp1 = sub4(acc2, 1);
            drain();
        }
        asm volatile("" : "+{v20}"(g));
        bp0 *= 1.f - g;
        bp1 *= 1.f - g;
        asm volatile("" : "+v"(bp0), "+v"(bp1));
    }
    asm volatile("s_waitcnt vmcnt(0)\n\ts_barrier" : "+{v[22:23]}"(f0)::"memory");
    f32x16 a0, a1;
    wo_gemm(a0, a1, bp0, bp1, g, fin, ws + WS_O + (size_t)((2 * st + wb) * 32 + 8 * (w >> 1)) * 1024,
            fsrc, (unsigned)__ballot(f0 == tag), tag, t0, t1, l16, d1, fo);
    {
        LAS char* ex = (LAS char*)(uintptr_t)fin[0];
#pragma unroll
        for (int k = 0; k < 4; ++k) { *(LAS f32x4*)(ex + k * 1024) = sub4(a0, k); *(LAS f32x4*)(ex + 4096 + k * 1024) = sub4(a1, k); }
        wgbar();
        LAS const char* exr = (LAS const char*)(uintptr_t)fin[1];
        f32x4 s0 = *(LAS const f32x4*)exr, s1 = *(LAS const f32x4*)(exr + 1024);
#pragma unroll
        for (int k = 1; k < 4; ++k) { s0 += *(LAS const f32x4*)(exr + k * 16384); s1 += *(LAS const f32x4*)(exr + k * 16384 + 1024); }
        s0 = s0 * g + bp0;
        s1 = s1 * g + bp1;
        const auto r0 = __builtin_amdgcn_permlane32_swap(cvtpk(s0[0], s0[1]), cvtpk(s1[0], s1[1]), false, false);
        const auto r1 = __builtin_amdgcn_permlane32_swap(cvtpk(s0[2], s0[3]), cvtpk(s1[2], s1[3]), false, false);
        asm volatile("global_store_dwordx4 %0, %1, %2" ::"v"(fin[2]), "v"(u32x4{r0[0], r1[0], r0[1], r1[1]}), "s"(out) : "memory");
    }
}

constexpr int MK_LDS = 81920;
__global__ __launch_bounds__(512, 4) void mk(const char* x, const char* wqkv, const char* wo, const char* w1, const char* b1,
                                             const char* w2, const char* b2, const char* gw, char* ws, char* out) {
    extern __shared__ __attribute__((aligned(16))) char lds[];
    const int w = __builtin_amdgcn_readfirstlane(threadIdx.x >> 6);
    if (blockIdx.x >= 256 && w == 7) {
        asm volatile("s_getpc_b64 s[40:41]\n\t"
                     "Lmk_hot_begin:\n\t"
                     "s_cmp_lt_u32 %[o], Lmk_hot_end-Lmk_hot_begin-4096\n\t"
                     "s_cbranch_scc0 1f\n\t"
                     "s_add_u32 s40, s40, %[o]\n\t"
                     "s_addc_u32 s41, s41, 0\n\t"
                     "s_and_b32 s40, s40, -4\n\t"
                     "s_mov_b32 m0, 0x13e00\n\t"
                     "s_nop 0\n\t"
                     "global_load_lds_dword %[v], s[40:41]\n\t"
                     "1:"
                     :: [o] "s"(((blockIdx.x - 256) >> 3) * 4096), [v] "v"(lane_id() * 64) : "memory", "scc", "s40", "s41");
    }
    if (blockIdx.x < 256) attn_role(x, wqkv, ws, lds, blockIdx.x, w);
    else mixer_role(x, wqkv, gw, wo, w1, b1, w2, b2, ws, out, lds, blockIdx.x - 256, w);
}

extern "C" void kernel_launch(void* const* d_in, const int* in_sizes, int n_in, void* d_out, int out_size, void* d_ws, size_t ws_size,
                              hipStream_t stream) {
    (void)hipFuncSetAttribute((const void*)mk, hipFuncAttributeMaxDynamicSharedMemorySize, MK_LDS);
    mk<<<512, 512, MK_LDS, stream>>>((const char*)d_in[0], (const char*)d_in[1], (const char*)d_in[2], (const char*)d_in[4],
                                     (const char*)d_in[5], (const char*)d_in[6], (const char*)d_in[7], (const char*)d_in[8],
                                     (char*)d_ws, (char*)d_out);
}
```
